```python
import torch

def _softmax(x, axis=-1):
    return torch.softmax(x, dim=axis)
def _gelu(x):
    return torch.nn.functional.gelu(x, approximate="tanh")
def _silu(x):
    return torch.nn.functional.silu(x)
def _sigmoid(x):
    return torch.sigmoid(x)
def _layernorm(x, axis=-1, eps=1e-5):
    m = x.mean(dim=axis, keepdim=True)
    v = x.var(dim=axis, keepdim=True, unbiased=False)
    return (x - m) / torch.sqrt(v + eps)
def _scatter_add(tgt, idx, src, axis=0):
    return tgt.clone().index_add_(axis, idx, src)
def _segment_sum(src, seg, n):
    out = torch.zeros((n,) + src.shape[1:], dtype=src.dtype)
    return out.index_add_(0, seg, src)
def _pad(x, pw):
    flat = tuple(v for pair in reversed(pw) for v in pair)
    return torch.nn.functional.pad(x, flat)

kernel_name = 'sequential_preLN__sliding_local_dilated__glu_gated_geglu__prompt'

def setup_inputs():

    _w = (lambda a: (lambda t: t.to(torch.bfloat16) if t.is_floating_point() else t)(torch.as_tensor(a)))
    def _A_setup():
        B, S, D = 1, 2048, 1024
        return {
            "x": _w((lambda *s: torch.randn(*s, dtype=torch.bfloat16) * 0.05)(B, S, D)),
            "wqkv": _w((lambda *s: torch.randn(*s, dtype=torch.bfloat16) * 0.05)(D, 3 * D)),
            "wo": _w((lambda *s: torch.randn(*s, dtype=torch.bfloat16) * 0.05)(D, D)),
        }
    _A_in = _A_setup()

    def _B_setup():
        B, S, Dm, Df = 1, 2048, 1024, 3584
        return {
            "x": _w((lambda *s: torch.randn(*s, dtype=torch.bfloat16) * 0.05)(B, S, Dm)),
            "w_gate": _w((lambda *s: torch.randn(*s, dtype=torch.bfloat16) * 0.8)(Dm, Df) * (Dm ** -0.5)),
            "w_up": _w((lambda *s: torch.randn(*s, dtype=torch.bfloat16) * 0.8)(Dm, Df) * (Dm ** -0.5)),
            "w_down": _w((lambda *s: torch.randn(*s, dtype=torch.bfloat16) * 0.8)(Df, Dm) * (Df ** -0.5)),
        }
    _B_in = _B_setup()

    return {
        "A_x": _A_in["x"],
        "A_wqkv": _A_in["wqkv"],
        "A_wo": _A_in["wo"],
        "B_x": _B_in["x"],
        "B_w_gate": _B_in["w_gate"],
        "B_w_up": _B_in["w_up"],
        "B_w_down": _B_in["w_down"],
    }

def reference(A_x, A_wqkv, A_wo, B_x, B_w_gate, B_w_up, B_w_down):
    def _A_ref(x, wqkv, wo):
        B, S, D = x.shape
        H = 32; Dh = D // H
        W, dil = 8, 2
        qkv = (lambda x, *d: x.permute(*d))((x @ wqkv).reshape(B, S, 3, H, Dh), 2, 0, 3, 1, 4)
        q, k, v = qkv[0], qkv[1], qkv[2]
        scale = Dh ** -0.5
        pos = torch.arange(S)
        off = torch.arange(W) * dil
        idx = pos[:, None] - off[None, :]
        mask = idx >= 0
        idx_c = torch.where(mask, idx, 0).reshape(-1)
        kg = k[:, :, idx_c, :].reshape(B, H, S, W, Dh)
        vg = v[:, :, idx_c, :].reshape(B, H, S, W, Dh)
        logits = torch.einsum("bhsd,bhswd->bhsw", q, kg) * scale
        logits = torch.where(mask[None, None, :, :], logits, -1e9)
        attn = _softmax(logits, axis=-1)
        o = torch.einsum("bhsw,bhswd->bhsd", attn, vg)
        return (lambda x, *d: x.permute(*d))(o, 0, 2, 1, 3).reshape(B, S, D) @ wo

    def _B_ref(x, w_gate, w_up, w_down):
        g = torch.einsum("bsd,df->bsf", x, w_gate)
        u = torch.einsum("bsd,df->bsf", x, w_up)
        h = _gelu(g) * u
        return torch.einsum("bsf,fd->bsd", h, w_down)

    x = A_x
    h = x + _A_ref(_layernorm(x, axis=-1), A_wqkv, A_wo)
    h2 = h + _B_ref(_layernorm(h, axis=-1), B_w_gate, B_w_up, B_w_down)
    return h2
```

```cpp
#include <hip/hip_runtime.h>
#include <hip/hip_bf16.h>
#include <stdint.h>
#include <type_traits>

typedef unsigned short bf16_t;
typedef short bf16x8 __attribute__((ext_vector_type(8)));
typedef short s16x4 __attribute__((ext_vector_type(4)));
typedef float f32x4 __attribute__((ext_vector_type(4)));
typedef float f32x2 __attribute__((ext_vector_type(2)));
typedef unsigned u32x2 __attribute__((ext_vector_type(2)));
typedef unsigned u32x4 __attribute__((ext_vector_type(4)));
typedef __bf16 bf16x2_t __attribute__((ext_vector_type(2)));
typedef __attribute__((address_space(3))) void* lds_void_ptr;
typedef __attribute__((address_space(3))) char* lds_char_ptr;

constexpr int S_ = 2048, D_ = 1024, DF_ = 3584;
#define KN_HTD 11
#define KN_CT 7
#define KN_HQ 5
#define KN_HK 2
#define KN_KT 4
#define KN_SPLIT 1
#define KN_PO 0
#define KN_PH 0
#define KN_PA 0
constexpr int RBO = D_ / 8 + KN_PO, RBH = D_ / 8 + KN_PH, RBA = DF_ / 8 + KN_PA;

__device__ __forceinline__ float bflo(unsigned v) { return __uint_as_float(v << 16); }
__device__ __forceinline__ float bfhi(unsigned v) { return __uint_as_float(v & 0xffff0000u); }
__device__ __forceinline__ unsigned pk_bf16(float lo, float hi) {
  f32x2 v = {lo, hi};
  bf16x2_t b = __builtin_convertvector(v, bf16x2_t);
  return __builtin_bit_cast(unsigned, b);
}
__device__ __forceinline__ float rbf(float v) { return bflo(pk_bf16(v, v)); }

template <int STRIDE> __device__ __forceinline__ int wswz(int row) {
  if constexpr (STRIDE % 256 == 0) return ((row & 3) | (((row >> 3) & 1) << 2)) << 1;
  else if constexpr (STRIDE % 256 == 128) return (((row >> 1) & 1) | (((row >> 3) & 1) << 1)) << 1;
  else return ((row >> 3) & 1) << 1;
}
template <int STRIDE> constexpr int wcls() { return STRIDE % 256 == 0 ? 8 : (STRIDE % 256 == 128 ? 4 : 2); }

template <int OFF> __device__ __forceinline__ s16x4 ds_tr(unsigned addr) {
  s16x4 r; asm volatile("ds_read_b64_tr_b16 %0, %1 offset:%2" : "=v"(r) : "v"(addr), "n"(OFF)); return r;
}
template <int OFF> __device__ __forceinline__ bf16x8 ds_b128(unsigned addr) {
  bf16x8 r; asm volatile("ds_read_b128 %0, %1 offset:%2" : "=v"(r) : "v"(addr), "n"(OFF)); return r;
}
template <int OFF> __device__ __forceinline__ f32x2 ds_b64(unsigned addr) {
  f32x2 r; asm volatile("ds_read_b64 %0, %1 offset:%2" : "=v"(r) : "v"(addr), "n"(OFF)); return r;
}
__device__ __forceinline__ float sum8(float v) {
  v += __int_as_float(__builtin_amdgcn_update_dpp(0, __float_as_int(v), 0x128, 0xf, 0xf, false));
  auto a = __builtin_amdgcn_permlane16_swap(__float_as_uint(v), __float_as_uint(v), false, false);
  v = __uint_as_float(a[0]) + __uint_as_float(a[1]);
  auto b = __builtin_amdgcn_permlane32_swap(__float_as_uint(v), __float_as_uint(v), false, false);
  return __uint_as_float(b[0]) + __uint_as_float(b[1]);
}
__device__ __forceinline__ float sum8l(float v) {
  v += __int_as_float(__builtin_amdgcn_update_dpp(0, __float_as_int(v), 0xb1, 0xf, 0xf, false));
  v += __int_as_float(__builtin_amdgcn_update_dpp(0, __float_as_int(v), 0x4e, 0xf, 0xf, false));
  return v + __int_as_float(__builtin_amdgcn_update_dpp(0, __float_as_int(v), 0x141, 0xf, 0xf, false));
}
__device__ __forceinline__ int ds_r32(unsigned addr) { int r; asm volatile("ds_read_b32 %0, %1" : "=v"(r) : "v"(addr)); return r; }
__device__ __forceinline__ void ds_w32(unsigned addr, int v) { asm volatile("ds_write_b32 %0, %1" :: "v"(addr), "v"(v) : "memory"); }
template <int N> __device__ __forceinline__ void wait_vm() { asm volatile("s_waitcnt vmcnt(%0)" ::"n"(N) : "memory"); }
__device__ __forceinline__ void raw_barrier() { asm volatile("" ::: "memory"); __builtin_amdgcn_s_barrier(); asm volatile("" ::: "memory"); }
__device__ __forceinline__ void loop_barrier() { raw_barrier(); }
__device__ __forceinline__ void lds_barrier() { asm volatile("s_waitcnt lgkmcnt(0)" ::: "memory"); __builtin_amdgcn_s_barrier(); asm volatile("" ::: "memory"); }

__device__ __forceinline__ u32x4 mk_rsrc(const void* p) { const unsigned long long v = (unsigned long long)p; return (u32x4){(unsigned)v, (unsigned)(v >> 32) & 0xffffu, 0x7fffffffu, 0x00020000u}; }
#define LD1K(v, so, off, sc) "buffer_load_dwordx4 " v ", %1, " so " offen offset:" off sc " lds\n\t"
template <int WAIT> __device__ __forceinline__ void ld_w4(unsigned m, u32x4 r, const unsigned (&v)[4], unsigned s0, unsigned s1) {
  if constexpr (WAIT >= 0) asm volatile("s_mov_b32 m0, %0\n\ts_waitcnt vmcnt(%8)\n\ts_barrier\n\t" LD1K("%2", "%6", "0", "") LD1K("%3", "%7", "1024", "") LD1K("%4", "%6", "2048", "") LD1K("%5", "%7", "3072", "")
                                        :: "s"(m), "s"(r), "v"(v[0]), "v"(v[1]), "v"(v[2]), "v"(v[3]), "s"(s0), "s"(s1), "n"(WAIT) : "memory", "m0");
  else asm volatile("s_mov_b32 m0, %0\n\ts_nop 0\n\t" LD1K("%2", "%6", "0", "") LD1K("%3", "%7", "1024", "") LD1K("%4", "%6", "2048", "") LD1K("%5", "%7", "3072", "")
                    :: "s"(m), "s"(r), "v"(v[0]), "v"(v[1]), "v"(v[2]), "v"(v[3]), "s"(s0), "s"(s1) : "memory", "m0");
}
template <int WAIT> __device__ __forceinline__ void ld_w2(unsigned m, u32x4 r, const unsigned (&v)[4], unsigned s0, unsigned s1) {
  if constexpr (WAIT >= 0) asm volatile("s_mov_b32 m0, %0\n\ts_waitcnt vmcnt(%6)\n\ts_barrier\n\t" LD1K("%2", "%4", "0", "") LD1K("%3", "%5", "1024", "")
                                        :: "s"(m), "s"(r), "v"(v[0]), "v"(v[1]), "s"(s0), "s"(s1), "n"(WAIT) : "memory", "m0");
  else asm volatile("s_mov_b32 m0, %0\n\ts_nop 0\n\t" LD1K("%2", "%4", "0", "") LD1K("%3", "%5", "1024", "") :: "s"(m), "s"(r), "v"(v[0]), "v"(v[1]), "s"(s0), "s"(s1) : "memory", "m0");
}
__device__ __forceinline__ void ld_x4(unsigned m, u32x4 r, unsigned v0, unsigned v1, unsigned so) {
  asm volatile("s_mov_b32 m0, %0\n\ts_nop 0\n\t" LD1K("%2", "%4", "0", " sc1") LD1K("%2", "%4", "1024", " sc1") LD1K("%3", "%4", "2048", " sc1") LD1K("%3", "%4", "3072", " sc1")
               :: "s"(m), "s"(r), "v"(v0), "v"(v1), "s"(so) : "memory", "m0");
}
template <int LO, int SO> __device__ __forceinline__ void ld_c(unsigned mb, u32x4 r, unsigned v) {
  unsigned t;
  asm volatile("s_add_i32 m0, %1, %4\n\ts_mov_b32 %0, %5\n\tbuffer_load_dwordx4 %3, %2, %0 offen lds" : "=&s"(t) : "s"(mb), "s"(r), "v"(v), "n"(LO), "n"(SO) : "memory", "m0", "scc");
}
template <int LO, int SO> __device__ __forceinline__ void ld_cx(unsigned mb, u32x4 r, unsigned v, unsigned sx) {
  unsigned t;
  asm volatile("s_add_i32 m0, %1, %4\n\ts_xor_b32 %0, %6, %5\n\tbuffer_load_dwordx4 %3, %2, %0 offen lds" : "=&s"(t) : "s"(mb), "s"(r), "v"(v), "n"(LO), "n"(SO), "s"(sx) : "memory", "m0", "scc");
}
template <int I, int N, class F> __device__ __forceinline__ void static_for(F&& f) {
  if constexpr (I < N) { f(std::integral_constant<int, I>{}); static_for<I + 1, N>(f); }
}
#define CODE_ADDR(v, sym) asm volatile("s_getpc_b64 s[40:41]\n\ts_add_u32 s40, s40, " sym "@rel32@lo+4\n\ts_addc_u32 s41, s41, " sym "@rel32@hi+12\n\ts_mov_b64 %0, s[40:41]" : "=s"(v) :: "s40", "s41", "scc")
enum { EPI_QKV = 0, EPI_WO = 1, EPI_GLU = 2, EPI_DN = 3 };
#define MERGED 1
constexpr int SNAP = 162752;
constexpr int SY_O = 12288;
#define KN_P 1
#define KN_S1 1
#define KN_S1C 8
#define KN_S1P 3
constexpr int STAB = 786432, STL = 3584;
constexpr unsigned STAG = 0x13572468u;
#define SYO(t) (SY_O + (t) * 32 + (KN_P ? ((((t) >> 3) ^ KN_P) & 1) << 11 : 0))
constexpr unsigned FIN = 65536u;
#define KN_E 6
#define KN_MK 2
#define KN_GW 1
#define KN_TRY 6
#define KN_DM 0
#define KN_LX 0
#define KN_TP 5
__device__ __forceinline__ unsigned snap_get(unsigned lds0, int i) {
  int v = ds_r32(lds0 + SNAP + i * 4);
  asm volatile("s_waitcnt lgkmcnt(0)" : "+v"(v) :: "memory");
  return (unsigned)__builtin_amdgcn_readfirstlane(v);
}
__device__ __forceinline__ bool in_time(unsigned lds0, unsigned level) { return (unsigned)__builtin_amdgcn_s_memrealtime() - snap_get(lds0, 7) < level * 5000000u; }
__device__ __forceinline__ void wait_count(unsigned lds0, const unsigned* cnt, unsigned v0, unsigned n, unsigned level) {
  while (((unsigned)__builtin_amdgcn_readfirstlane(__hip_atomic_load(cnt, __ATOMIC_RELAXED, __HIP_MEMORY_SCOPE_AGENT)) - v0) >> 16 < n && in_time(lds0, level)) {}
}
__device__ __forceinline__ void wait_field(unsigned lds0, const unsigned* cnt, unsigned v0, int sh, unsigned level) {
  while (((((unsigned)__builtin_amdgcn_readfirstlane(__hip_atomic_load(cnt, __ATOMIC_RELAXED, __HIP_MEMORY_SCOPE_AGENT)) - v0) >> sh) & 255u) < 32u && in_time(lds0, level)) {}
}
constexpr int ACNT = SNAP - 60;
__device__ __forceinline__ void lds_inc(unsigned addr) { if (__builtin_amdgcn_mbcnt_hi(~0u, __builtin_amdgcn_mbcnt_lo(~0u, 0u)) == 0) asm volatile("ds_add_u32 %0, %1" :: "v"(addr), "v"(1) : "memory"); }
constexpr int FPAD = SNAP - 704;
__device__ __forceinline__ void lds_wait8(unsigned addr, int n = 8) {
  int dv; unsigned polls = 0;
  do { dv = ds_r32(addr); asm volatile("s_waitcnt lgkmcnt(0)" : "+v"(dv) :: "memory"); } while (__builtin_amdgcn_readfirstlane(dv) < n && ++polls < 40000000u);
}
template <int N, int STR = 4096> __device__ __forceinline__ bool marked(unsigned addr) {
  bf16x8 v[N];
  static_for<0, N>([&](auto ic) { constexpr int i = decltype(ic)::value; v[i] = ds_b128<i * STR>(addr); });
  asm volatile("s_waitcnt lgkmcnt(0)" ::: "memory");
  unsigned m = 0;
#pragma unroll
  for (int i = 0; i < N; ++i) {
    asm volatile("" : "+v"(v[i]));
    const u32x4 u = __builtin_bit_cast(u32x4, v[i]);
    m = max(max(m, u[0]), u[1]); m = max(max(m, u[2]), u[3]);
  }
  return __builtin_amdgcn_ballot_w64(m == 0xffffffffu) != 0;
}

struct GemmArgs {
  const bf16_t* X;
  const bf16_t* W0;
  const bf16_t* W1;
  const bf16_t* R;
  bf16_t* O;
  float* st;
  const bf16_t* CW0;
  const bf16_t* CW1;
  float* cs;
  const bf16_t* PF0;
  const bf16_t* PF1;
  unsigned* sync;
  float* slab;
  unsigned warm;
};
#define STAMP(i)
#define STAMPW(i, w)

#define STAMPV(i) STAMP(i)
#define XAUXD 16
#define XAUXW 16
#define XAUX 16
template <int MF, int NF> struct Frags { bf16x8 x[MF]; s16x4 lo[NF], hi[NF]; };

#define KN_WL 2
#define KN_WA KN_WL
#define KN_WB 2
#define KN_B3 0
constexpr int WOB = 55296;
constexpr int wo_pos(int s) { return s < 6 ? WOB + s * 16384 : (s - 6) * 16384; }
struct NoHook { template <class A> __device__ __forceinline__ void operator()(A) const {} };
template <int EPI, int BM, int NCH, int WM, int WN, int K, int LDW, int TM, int TN, int XA, int XB, int KU, int SX, int SW, int SPK, class HK = NoHook>
__device__ __forceinline__ void gemm_phase(const GemmArgs& a, HK hook = HK{}) {
#if defined(__HIP_DEVICE_COMPILE__)
  constexpr bool SEAM_IN = EPI == EPI_GLU || EPI == EPI_DN, SEAM_OUT = EPI != EPI_DN;
  constexpr bool HALO = EPI == EPI_QKV;
  constexpr bool FX = EPI != EPI_QKV;
  constexpr int HR = HALO ? 16 : 0, XROWS = BM + HR;
  constexpr int RBX = EPI == EPI_WO ? RBO : (EPI == EPI_GLU ? RBH : RBA);
  constexpr bool GLU = EPI == EPI_GLU;
  constexpr int STRIDE = GLU ? 224 : (HALO ? 128 : NCH * 16);
  constexpr int SEGB = GLU ? 16 : 32;
  constexpr int UPOFF = KU * 224 + 16;
  constexpr int XT = XROWS * 128, WU = GLU ? (UPOFF + KU * 224 + 255) / 256 * 256 : KU * NCH * 16;
  constexpr int XBASE = 0, WBASE = SX * XT, RING = WBASE + SW * WU;
  constexpr int MF = BM / 16 / WM, NF = NCH / 2 / WN;
  constexpr int MFX = MF + (HALO ? 1 : 0), NFX = NF;
  constexpr int NT = K / SPK / 64, NU = K / SPK / KU;
  constexpr int C = GLU ? 2 : (HALO ? 1 : (wcls<STRIDE>() < NF ? wcls<STRIDE>() : NF));
  constexpr int NXI = (XROWS / 8) * KU / 64;
  constexpr int NWI = KU * NCH * 16 / 1024;
#define KN_NLW 4
  constexpr int NLW = KN_NLW;
  constexpr int XI = (NXI + NLW - 1) / NLW, XIMIN = NXI / NLW, WI = (NWI + NLW - 1) / NLW;
  extern __shared__ __attribute__((aligned(16))) char smem[];
  STAMP(0);
  const unsigned lds0 = (unsigned)(size_t)(lds_char_ptr)smem;
  const int tid = threadIdx.x, lane = tid & 63;
  const int wave = __builtin_amdgcn_readfirstlane(tid >> 6);
  const int wm = wave / WN, wn = wave % WN;
  const int bid = blockIdx.x, xcd = bid & 7, kh = SPK > 1 ? ((bid >> 3) % SPK) : 0, idx = (bid >> 3) / SPK;
  constexpr int XN = TN / XB;
#define KN_ROT 0
  const int tm = (xcd / XN) * XA + idx / XB, tn = (xcd % XN) * XB + (idx + (EPI == EPI_DN ? KN_ROT : 0)) % XB;
  const int m0 = tm * BM;
  const int krot = (tm >> 2) * 8;
  const int fr = lane & 15, fq = lane >> 4;
  const int wq = (lane >> 2) & 3, wp = lane & 3;

  const int panel = m0 >> 8;
  auto seam_wait = [&]() {
    if (wave == 4) {
      wait_count(lds0, a.sync + panel * 64, snap_get(lds0, 1), 32u, 2u);
    }
    STAMP(9);
    raw_barrier();
    STAMP(6);
  };
  auto seam_signal = [&]() {
    asm volatile("s_waitcnt vmcnt(0)" ::: "memory");
    raw_barrier();
#define KN_SL 0
    if constexpr (EPI == EPI_QKV) { if (wave != 0) { if (KN_SL) __builtin_amdgcn_s_sleep(KN_SL); hook(std::integral_constant<int, 2>{}); } }
    if (tid == 0) __hip_atomic_fetch_add(a.sync + (EPI == EPI_QKV ? SYO(tm) : ((EPI == EPI_WO ? 0 : 8) + panel) * 64), FIN, __ATOMIC_RELAXED, __HIP_MEMORY_SCOPE_AGENT);
    if constexpr (EPI == EPI_QKV) { if (wave == 0) hook(std::integral_constant<int, 2>{}); }
  };
  auto mark_signal = [&]() {
    if constexpr (EPI == EPI_QKV && (KN_E & 4) && KN_MK == 1) {
      if (tid == 64 && (idx >> 4) == ((xcd >> 1) & 1)) __hip_atomic_fetch_add(a.sync + (8 + (xcd >> 2) * 4 + (idx >> 3)) * 64, 4u, __ATOMIC_RELAXED, __HIP_MEMORY_SCOPE_AGENT);
    }
  };
  constexpr int TCH = RING + (EPI == EPI_QKV ? 2304 : (EPI == EPI_GLU ? 5120 : 0));
  constexpr int DONE = TCH + 896;
  auto done_inc = [&]() { if (lane == 0) asm volatile("ds_add_u32 %0, %1" :: "v"(lds0 + DONE), "v"(1) : "memory"); };
  auto touch = [&](const void* base, unsigned voff, unsigned soff, unsigned lim = 0x7fffffffu) {
    __builtin_amdgcn_raw_ptr_buffer_load_lds(__builtin_amdgcn_make_buffer_rsrc((void*)base, 0, lim, 0x00020000), (lds_void_ptr)(size_t)(lds0 + TCH), 4, voff, soff, 0, 0);
  };
  const auto rO = __builtin_amdgcn_make_buffer_rsrc((void*)a.O, 0, 0x7fffffff, 0x00020000);

  unsigned xsrc[XI], wsrc[WI];
  if constexpr (HALO) {
    const int w4 = wave & 3, lr = lane >> 3;
    const unsigned xv = (unsigned)(lr * (K * 2) + (((lane & 7) ^ ((w4 * 4 + (lr >> 1)) & 7)) << 4));
    const int r0 = m0 - HR + w4 * 8;
#pragma unroll
    for (int i = 0; i < XI; ++i) xsrc[i] = xv + (unsigned)((i == 0 ? (r0 < 0 ? 0 : r0) : r0 + 32 * (i < 4 ? i : 4)) * (K * 2));
    const unsigned wv = (unsigned)((w4 * 8 + lr) * (LDW * 2) + tn * 128 + (((lane & 7) ^ wswz<128>(w4 * 8 + lr)) << 4));
#pragma unroll
    for (int i = 0; i < WI; ++i) wsrc[i] = wv + (unsigned)((i & 1) * 32 * (LDW * 2) + (i >> 1) * 2048);
  } else {
#pragma unroll
  for (int i = 0; i < XI; ++i) {
    int j = (wave % NLW) + NLW * i; if (j >= NXI) j = wave % NLW;
    const int r = 8 * j + (lane >> 3), c = (lane & 7) ^ ((r >> 1) & 7);
    int gr = m0 - HR + r; if (gr < 0) gr = 0;
    xsrc[i] = FX ? (unsigned)((((m0 >> 4) + (j >> 1)) * RBX + (j & 1) * 4) * 256 + lane * 16) : (unsigned)(gr * K + kh * (K / SPK) + c * 8) * 2u;
  }
#pragma unroll
  for (int i = 0; i < WI; ++i) {
    int j = (wave % NLW) + NLW * i; if (j >= NWI) j -= NLW;
    if constexpr (GLU) {
      const int jj = j >= 7 ? j - 7 : j;
      const int b = jj * 1024 + lane * 16, p = b / 224, pos = (b % 224) >> 4;
      const int row = 8 * ((p & 15) >> 2) + (p & 3) + 4 * (p >> 4);
      wsrc[i] = (unsigned)(row * LDW + tn * 112 + pos * 8) * 2u;
    } else {
      const int b = j * 1024 + lane * 16, row = b / STRIDE, pos = (b % STRIDE) >> 4;
      const int c = pos ^ wswz<STRIDE>(row);
      wsrc[i] = (unsigned)((row + kh * (K / SPK)) * LDW + tn * (NCH * 8) + c * 8) * 2u;
    }
  }
  }
  auto ldX = [&](int i, int tile, int part, int slot, bool ok = true) {
    const auto rX = __builtin_amdgcn_make_buffer_rsrc((void*)a.X, 0, ok ? 0x7fffffff : 0, 0x00020000);
    if constexpr (GLU) tile = (tile + krot) & 15;
    const int j = wave + NLW * i;
    if (NXI % NLW != 0 && j >= NXI) return;
    __builtin_amdgcn_raw_ptr_buffer_load_lds(rX, (lds_void_ptr)(size_t)(lds0 + XBASE + slot * XT + (part * NXI + j) * 1024), 16, xsrc[i],
                                             tile * (FX ? 2048 : 128) + part * (NXI / 2) * RBX * 256, 0, SEAM_IN ? XAUX : 0);
  };
#define KN_WNT 0
  constexpr int WAUX = ((KN_WNT >> EPI) & 1) ? 2 : 0;
  auto ldW = [&](int i, int unit, int slot, bool ok = true) {
    const auto rW0 = __builtin_amdgcn_make_buffer_rsrc((void*)a.W0, 0, ok ? 0x7fffffff : 0, 0x00020000);
    const auto rW1 = __builtin_amdgcn_make_buffer_rsrc((void*)(GLU ? a.W1 : a.W0), 0, ok ? 0x7fffffff : 0, 0x00020000);
    int j = wave + NLW * i; if (j >= NWI) j -= NLW;
    if constexpr (GLU) {
      unit = (unit + 2 * krot) & 31;
      const bool up = j >= 7;
      __builtin_amdgcn_raw_ptr_buffer_load_lds(up ? rW1 : rW0, (lds_void_ptr)(size_t)(lds0 + WBASE + slot * WU + (up ? UPOFF + (j - 7) * 1024 : j * 1024)), 16, wsrc[i],
                                               unit * KU * LDW * 2, 0, WAUX);
    } else {
      __builtin_amdgcn_raw_ptr_buffer_load_lds(rW0, (lds_void_ptr)(size_t)(lds0 + WBASE + slot * WU + j * 1024), 16, wsrc[i], unit * KU * LDW * 2, 0, WAUX);
    }
  };
  auto issueX = [&](int tile, int part, int slot) {
    if (wave >= NLW) return;
#pragma unroll
    for (int i = 0; i < XI; ++i) ldX(i, tile, part, slot);
  };
  auto issueW = [&](int unit, int slot) {
    if (wave >= NLW) return;
#pragma unroll
    for (int i = 0; i < WI; ++i) ldW(i, unit, slot);
  };

  f32x4 acc[NF][MF];
#pragma unroll
  for (int n = 0; n < NF; ++n)
#pragma unroll
    for (int m = 0; m < MF; ++m) acc[n][m] = (f32x4){0.f, 0.f, 0.f, 0.f};
  f32x4 acch = {0.f, 0.f, 0.f, 0.f};
  f32x4 accs = {0.f, 0.f, 0.f, 0.f};
  f32x4 gram = {0.f, 0.f, 0.f, 0.f}, rsum = {0.f, 0.f, 0.f, 0.f}, gramh = {0.f, 0.f, 0.f, 0.f};
  constexpr int ONES = RING;

  unsigned xa0, xa1, xh0 = 0, xh1 = 0;
  { const unsigned o = fr * 128 + ((fq ^ ((fr >> 1) & 7)) << 4), mo = (HR + wm * MF * 16) * 128;
    xa0 = lds0 + XBASE + mo + o; xa1 = lds0 + XBASE + mo + (o ^ 64); xh0 = lds0 + XBASE + o; xh1 = lds0 + XBASE + (o ^ 64);
    if constexpr (FX) { xa0 = lds0 + XBASE + mo + lane * 16; xa1 = xa0 + 1024; } }
  unsigned wa[C], wha = 0;
#pragma unroll
  for (int c = 0; c < C; ++c) {
    const int row = 8 * fq + wq, seg = HALO ? wn : wn * NF + c;
    if constexpr (GLU) wa[c] = lds0 + WBASE + (4 * fq + wq) * 224 + wn * 112 + (c == 0 ? wp * 8 : 96 + (wp & 1) * 8 + (wp >> 1) * UPOFF);
    else wa[c] = lds0 + WBASE + row * STRIDE + (((seg << 1) ^ wswz<STRIDE>(row)) << 4) + wp * 8;
  }

  using I0 = std::integral_constant<int, 0>; using I1 = std::integral_constant<int, 1>;
  using T = std::true_type; using F = std::false_type;
  typedef Frags<MFX, NFX> FR;
  struct RdCtx { unsigned xa, xh, wb[C], wh; };
  auto mkctx = [&](bool ks1, unsigned xoffs, unsigned woffs) {
    RdCtx c; c.xa = (ks1 ? xa1 : xa0) + xoffs; c.xh = fr == 0 ? lds0 + ONES : (ks1 ? xh1 : xh0) + xoffs; c.wh = wha + woffs;
#pragma unroll
    for (int i = 0; i < C; ++i) c.wb[i] = wa[i] + woffs;
    return c;
  };
  auto rd_item = [&](FR& f, const RdCtx& c, auto KSc, auto kc) {
    constexpr int KS = decltype(KSc)::value, k = decltype(kc)::value;
    if constexpr (k < MF) f.x[k] = ds_b128<k * 2048>(c.xa);
    else if constexpr (k < MFX) f.x[k] = ds_b128<0>(c.xh);
    else if constexpr (GLU) {
      constexpr int n = k - MFX, cc = n == 6, o = n == 6 ? 0 : (n >> 1) * 32 + (n & 1) * UPOFF;
      f.lo[n] = ds_tr<o>(c.wb[cc]); f.hi[n] = ds_tr<o + 16 * 224>(c.wb[cc]);
    } else if constexpr (k < MFX + NF) {
      constexpr int n = k - MFX, cc = n % C, o = (HALO ? n * 8192 : (n / C) * C * SEGB) + KS * 32 * STRIDE;
      f.lo[n] = ds_tr<o>(c.wb[cc]); f.hi[n] = ds_tr<o + 4 * STRIDE>(c.wb[cc]);
    }
  };
  auto wait_frags = [&](FR& f) {
    asm volatile("s_waitcnt lgkmcnt(0)" ::: "memory");
#pragma unroll
    for (int m = 0; m < MFX; ++m) asm volatile("" : "+v"(f.x[m]));
#pragma unroll
    for (int n = 0; n < NFX; ++n) asm volatile("" : "+v"(f.lo[n]), "+v"(f.hi[n]));
  };
  auto step = [&](auto CSc, auto STc, FR& cur, FR& nxt, const RdCtx& c, auto KSc, auto PFc, auto NLc, auto&& ld) {
    constexpr bool PF = decltype(PFc)::value != 0; constexpr int NL = decltype(NLc)::value;
#define KN_RG 1
#define KN_LD 0
    constexpr int R = MFX + NFX, RG = KN_RG == 1 ? (NF > 2 ? NF - 1 : NF) : (KN_RG == 2 ? (NF + 1) / 2 : 1);
    constexpr bool CS = decltype(CSc)::value; constexpr int ST = decltype(STc)::value;
    static_for<0, NF>([&](auto gc) {
      constexpr int g = decltype(gc)::value;
      if constexpr (PF) static_for<0, R>([&](auto kc) { constexpr int k = decltype(kc)::value; if constexpr (k * RG / R == g) rd_item(nxt, c, KSc, kc); });
      static_for<0, NL>([&](auto ic) { constexpr int i = decltype(ic)::value;
        constexpr int gg = KN_LD == 0 ? i * NF / NL : (KN_LD == 1 ? (NF - 1 - (NL - 1 - i) * ((NF + 1) / 2) / NL) : (i * ((NF + 1) / 2) / NL));
        if constexpr (gg == g) ld(i); });
      const bf16x8 w = __builtin_shufflevector(cur.lo[g], cur.hi[g], 0, 1, 2, 3, 4, 5, 6, 7);
#pragma unroll
      for (int m = 0; m < MF; ++m) {
        acc[g][m] = __builtin_amdgcn_mfma_f32_16x16x32_bf16(w, cur.x[m], acc[g][m], 0, 0, 0);
      }
      if constexpr (GLU && CS) {
        const unsigned sv = fr == g ? 0x3f803f80u : 0u;
        const u32x4 s4 = {sv, sv, sv, sv};
        accs = __builtin_amdgcn_mfma_f32_16x16x32_bf16(w, __builtin_bit_cast(bf16x8, s4), accs, 0, 0, 0);
      }
      if constexpr (HALO && g == 1 + ST / WN) acch = __builtin_amdgcn_mfma_f32_16x16x32_bf16(w, cur.x[MF], acch, 0, 0, 0);
      if constexpr (HALO && CS && g == 0) accs = __builtin_amdgcn_mfma_f32_16x16x32_bf16(w, cur.x[MF], accs, 0, 0, 0);
      if constexpr (HALO && ST >= 0 && !KN_S1) {
        if constexpr (g == 1) gram = __builtin_amdgcn_mfma_f32_16x16x32_bf16(cur.x[ST & 3], cur.x[ST & 3], gram, 0, 0, 0);
        if constexpr (g == 2) {
          const u32x4 one4 = {0x3f803f80u, 0x3f803f80u, 0x3f803f80u, 0x3f803f80u};
          rsum = __builtin_amdgcn_mfma_f32_16x16x32_bf16(__builtin_bit_cast(bf16x8, one4), cur.x[ST & 3], rsum, 0, 0, 0);
        }
        if constexpr (g == 1 && ST == 3) gramh = __builtin_amdgcn_mfma_f32_16x16x32_bf16(cur.x[MF], cur.x[MF], gramh, 0, 0, 0);
      }
      __builtin_amdgcn_sched_barrier(0);
    });
    if constexpr (PF && !std::is_same<decltype(PFc), std::integral_constant<int, 2>>::value) wait_frags(nxt);
  };
#define KN_DPP 3
#define KN_FG 1
#define KN_HOLD 0
#define KN_HG 6
#define KN_FB 1
#define KN_FL 22
#define KN_FL2 20
#define KN_FE 25
  auto stepg = [&](auto CSc, FR& cur, FR& nxt, const RdCtx& c, auto R0c, auto IBc, auto&& sync, auto NLc, auto&& ld) {
    if constexpr (GLU) {
      constexpr bool CS = decltype(CSc)::value; constexpr int R0 = decltype(R0c)::value, IB = decltype(IBc)::value, NL = decltype(NLc)::value;
      constexpr int NM = NF * MF, NR = MF + 2 * NF;
      static_for<0, NM>([&](auto ic) {
        constexpr int I = decltype(ic)::value, g = I / MF, m = I % MF;
        constexpr int RL = IB > 8 ? KN_FL2 : KN_FL;
        const bf16x8 w = __builtin_shufflevector(cur.lo[g], cur.hi[g], 0, 1, 2, 3, 4, 5, 6, 7);
        acc[g][m] = __builtin_amdgcn_mfma_f32_16x16x32_bf16(w, cur.x[m], acc[g][m], 0, 0, 0);
        __builtin_amdgcn_sched_barrier(0);
        if constexpr (I == IB) sync();
        static_for<0, NR>([&](auto rc) {
          constexpr int r = decltype(rc)::value;
          if constexpr (R0 + r * (RL - R0) / (NR - 1) == I) {
            if constexpr (r < MF) nxt.x[r] = ds_b128<r * 2048>(c.xa);
            else {
              constexpr int n = (r - MF) >> 1, cc = n == 6, o = n == 6 ? 0 : (n >> 1) * 32 + (n & 1) * UPOFF;
              if constexpr ((r - MF) & 1) nxt.hi[n] = ds_tr<o + 16 * 224>(c.wb[cc]); else nxt.lo[n] = ds_tr<o>(c.wb[cc]);
            }
          }
        });
        static_for<0, NL>([&](auto lc) { constexpr int i = decltype(lc)::value; if constexpr (R0 + i * (NM - 1 - R0) / NL == I) ld(i); });
        if constexpr (CS && m == MF - 1) {
          const unsigned sv = fr == g ? 0x3f803f80u : 0u;
          const u32x4 s4 = {sv, sv, sv, sv};
          accs = __builtin_amdgcn_mfma_f32_16x16x32_bf16(w, __builtin_bit_cast(bf16x8, s4), accs, 0, 0, 0);
        }
        __builtin_amdgcn_sched_barrier(0);
      });
      wait_frags(nxt);
    }
  };
  FR fa, fb, fc, fd;

  f32x4* tb_row = (f32x4*)(smem + RING);
  float* tb_cs = (float*)(tb_row + 256);
  constexpr int RTAB = 524288;
  auto glu_stats_issue = [&]() {
    if constexpr (EPI == EPI_GLU) {
      const auto rS = __builtin_amdgcn_make_buffer_rsrc((void*)a.st, 0, 0x7fffffff, 0x00020000);
      if (wave == 7) __builtin_amdgcn_raw_ptr_buffer_load_lds(rS, (lds_void_ptr)(size_t)(lds0 + RING + 3072), 16, ((lane >> 2) * S_ + m0 + tn * 8 + (lane & 3) * 2) * 8, 0, 0, 16);
    }
  };
  auto glu_stats = [&]() {
    if constexpr (EPI == EPI_GLU) {
      int lx = lane; asm volatile("" : "+v"(lx));
      const unsigned sa = lds0 + RING + 3072 + lx * 8;
      f32x2 p0 = ds_b64<0>(sa), p1 = ds_b64<512>(sa);
      asm volatile("s_waitcnt lgkmcnt(0)" : "+v"(p0), "+v"(p1) :: "memory");
      const float mu = sum8(p0[0] + p1[0]) * (1.f / 16.f);
      const float v = sum8((p0[1] + p1[1]) + 64.f * ((p0[0] - mu) * (p0[0] - mu) + (p1[0] - mu) * (p1[0] - mu))) * (1.f / 1024.f) + 1e-5f;
      const float rs = 1.0f / sqrtf(v);
      const f32x4 rv = {mu, -0.10294324f * rs * rs * rs, -2.3022082f * rs, v};
      __builtin_amdgcn_raw_buffer_store_b128(__builtin_bit_cast(u32x4, rv), __builtin_amdgcn_make_buffer_rsrc((void*)a.cs, 0, 0x7fffffff, 0x00020000),
                                             lx < 8 ? (unsigned)(RTAB + (m0 + tn * 8 + lx) * 16) : 0x80000000u, 0, 16);
    }
  };
  auto epilogue = [&](auto EAc) __attribute__((always_inline)) {
  STAMP(2);

  if constexpr (EPI == EPI_GLU) {
    constexpr bool EARLY = decltype(EAc)::value;
    static_assert(DONE + 4 == ACNT && TCH + 256 == FPAD, "LDS");
#define KN_EP 1
#define KN_HE 0
#define KN_W1 0
#define KN_W2 0
#define KN_WS (KN_W2 ? 4 : 2)
    if constexpr (!EARLY) {
      wait_vm<0>();
      if (KN_HE) hook(std::integral_constant<int, 2>{});
      lds_wait8(lds0 + DONE);
      if (KN_EP) __builtin_amdgcn_s_setprio(2);
      STAMPW(9, 4);
      if (KN_W1 == 0) hook(I0{});
      if (KN_W2 == 4) hook(std::integral_constant<int, 3>{});
      STAMPW(10, 4);
    } else { if (KN_EP) __builtin_amdgcn_s_setprio(3); hook(I1{}); }
    asm volatile("" ::: "memory");
    f32x4 cg[4], cu[4], rw[MF], rw6[2];
    int lz = lane; asm volatile("" : "+v"(lz));
    const int fr = lz & 15, fq = lz >> 4;
#pragma unroll
    for (int p = 0; p < 4; ++p) {
      const int o = wn * 56 + p * 16 + (p == 3 ? (fq & 1) : fq) * 4;
      cg[p] = *(const f32x4*)(tb_cs + o); cu[p] = *(const f32x4*)(tb_cs + 112 + o);
    }
#pragma unroll
    for (int m = 0; m < MF; ++m) rw[m] = tb_row[(wm * MF + m) * 16 + fr];
#pragma unroll
    for (int q = 0; q < 2; ++q) rw6[q] = tb_row[(wm * MF + 2 * q + (fq >> 1)) * 16 + fr];
    const int gz = ((m0 >> 4) + wm * 4) * RBA * 256 + fr * 16, g = tn * 2 + wn;
    const int go0 = gz + ((fq & 1) * RBA + (fq >> 1)) * 256, go6 = gz + ((fq & 1) * 2 + (fq >> 1)) * RBA * 256;
    unsigned hv[MF][NF];
    auto put = [&](unsigned a0, unsigned a1, unsigned b0, unsigned b1, int vo, int so) {
      const auto s0 = __builtin_amdgcn_permlane16_swap(a0, b0, false, false), s1 = __builtin_amdgcn_permlane16_swap(a1, b1, false, false);
      __builtin_amdgcn_raw_buffer_store_b128((u32x4){s0[0], s1[0], s0[1], s1[1]}, rO, vo, so, 16);
      __builtin_amdgcn_sched_barrier(0);
      asm volatile("s_nop 1" ::: "memory");
      __builtin_amdgcn_sched_barrier(0);
    };
#define STAGE(body) _Pragma("unroll") for (int i = 0; i < 2 * NN; ++i) { const int m = MB + i / NN, n = N0 + i % NN; const f32x4 k = n == 6 ? rw6[m >> 1] : rw[m]; body; } __builtin_amdgcn_sched_barrier(0)
    auto grp = [&](auto MBc, auto N0c, auto NNc, auto&& mid) {
      constexpr int MB = decltype(MBc)::value, N0 = decltype(N0c)::value, NN = decltype(NNc)::value;
      f32x2 G[2 * NN], U[2 * NN], P[2 * NN];
      __builtin_amdgcn_sched_barrier(0);
      STAGE({ const int e = (n == 6 ? m & 1 : n & 1) * 2; const f32x4 x = n == 6 ? acc[6][m & 2] : acc[n & 6][m]; G[i] = ((f32x2){x[e], x[e + 1]}) - k[0] * ((f32x2){cg[n >> 1][e], cg[n >> 1][e + 1]}); });
      STAGE(P[i] = G[i] * G[i]);
      STAGE(P[i] = P[i] * k[1] + k[2]);
      STAGE(P[i] = G[i] * P[i]);
      STAGE({ const int e = (n == 6 ? m & 1 : n & 1) * 2; const f32x4 x = n == 6 ? acc[6][m | 1] : acc[n | 1][m]; U[i] = ((f32x2){x[e], x[e + 1]}) - k[0] * ((f32x2){cu[n >> 1][e], cu[n >> 1][e + 1]}); });
      STAGE(P[i] = ((f32x2){__builtin_amdgcn_exp2f(P[i][0]), __builtin_amdgcn_exp2f(P[i][1])}));
      mid();
      __builtin_amdgcn_sched_barrier(0);
      STAGE(P[i] = P[i] * k[3] + k[3]);
      STAGE(P[i] = ((f32x2){__builtin_amdgcn_rcpf(P[i][0]), __builtin_amdgcn_rcpf(P[i][1])}));
      STAGE(U[i] = G[i] * U[i]);
      STAGE(U[i] = U[i] * P[i]);
      STAGE({ hv[m][n] = pk_bf16(U[i][0], U[i][1]); asm volatile("" : "+v"(hv[m][n])); });
    };
#undef STAGE
    using I2 = std::integral_constant<int, 2>; using I3 = std::integral_constant<int, 3>; using I4 = std::integral_constant<int, 4>;
    grp(I0{}, I0{}, I4{}, [&]() { if (EARLY && KN_W1 == 2) { lds_wait8(lds0 + DONE); hook(I0{}); asm volatile("" ::: "memory"); } });
    if (EARLY && KN_W1 == 1) { lds_wait8(lds0 + DONE); hook(I0{}); asm volatile("" ::: "memory"); }
    if (!EARLY && KN_W2 == 3) { hook(I3{}); asm volatile("" ::: "memory"); }
    grp(I2{}, I0{}, I4{}, [&]() {
#pragma unroll
      for (int p = 0; p < 2; ++p) put(hv[0][2 * p], hv[0][2 * p + 1], hv[1][2 * p], hv[1][2 * p + 1], go0 + p * 512, g * 1024); });
#pragma unroll
    for (int p = 0; p < 2; ++p) put(hv[2][2 * p], hv[2][2 * p + 1], hv[3][2 * p], hv[3][2 * p + 1], go0 + p * 512, g * 1024 + 2 * RBA * 256);
    asm volatile("" ::: "memory");
    __builtin_amdgcn_sched_barrier(0);
    STAMP(4);
    STAMPW(11, 4);
    if (KN_EP) __builtin_amdgcn_s_setprio(EARLY ? 1 : 0);
#define KN_AW 2
    auto arrived = [&](int w) { if (!EARLY && w == KN_AW) { wait_vm<0>(); lds_inc(lds0 + ACNT); STAMPW(6, 4); } };
    arrived(0);
    if (EARLY && KN_W2 == 1) { if (KN_W1 == 0) lds_wait8(lds0 + DONE); hook(I3{}); asm volatile("" ::: "memory"); }
#pragma unroll
    for (int q = 0; q < 2; ++q)
#pragma unroll
      for (int e = 0; e < 4; ++e) {
        const float x0 = acc[6][2 * q][e], x1 = acc[6][2 * q + 1][e];
        auto sw = __builtin_amdgcn_permlane32_swap(__float_as_uint(x0), __float_as_uint(x1), false, false);
        acc[6][2 * q][e] = __uint_as_float(sw[0]); acc[6][2 * q + 1][e] = __uint_as_float(sw[1]);
      }
    const int sb = (256 + g * 3) * 256;
    grp(I0{}, I4{}, I3{}, [&]() { arrived(1); });
    arrived(2);
    if (EARLY && KN_W2 == 2) { if (KN_W1 == 0) lds_wait8(lds0 + DONE); hook(I3{}); asm volatile("" ::: "memory"); }
    grp(I2{}, I4{}, I3{}, [&]() { arrived(3); put(hv[0][4], hv[0][5], hv[1][4], hv[1][5], go0, sb); });
    put(hv[2][4], hv[2][5], hv[3][4], hv[3][5], go0, sb + 2 * RBA * 256);
    put(hv[0][6], hv[1][6], hv[2][6], hv[3][6], go6, sb + 512);
    asm volatile("" ::: "memory");
    if constexpr (!EARLY) lds_inc(lds0 + ACNT + 4);
  }
  };
  using IM1 = std::integral_constant<int, -1>;

  if constexpr (KU == 32) {
    static_assert(SX == 3 && SW == 4 && NXI % NLW == 0, "ring");
    constexpr int WR = KN_GW ? 2 : 0;
    constexpr bool EG = (KN_E & 2) != 0;
    if constexpr (!KN_GW) { issueW(0, 0); issueW(1, 1); }
    if constexpr (!EG && KN_GW != 2) { issueW(2, 2 ^ WR); issueW(3, 3 ^ WR); }
    if (wave == 5) { ds_w32(lds0 + DONE, 0); ds_w32(lds0 + DONE + 4, 0); ds_w32(lds0 + DONE + 8, 0); }
    if constexpr (!EG) seam_wait(); else { STAMP(9); STAMP(6); }
    auto check_tile = [&](int t) {
      int lx = lane; asm volatile("" : "+v"(lx));
      const unsigned ad = lds0 + XBASE + t * XT + wave * 1024 + lx * 16;
      if (__builtin_expect(marked<8>(ad), 0)) {
        for (int tries = 0;; ++tries) {
          if (tries >= KN_TRY) wait_count(lds0, a.sync + panel * 64, snap_get(lds0, 1), 32u, 2u);
#pragma unroll 1
          for (int u = t; u < t + 1 + (t == 0); ++u) { issueX(u, 0, u); issueX(u, 1, u); }
          if (t == 0) wait_vm<2 * XI>(); else wait_vm<0>();
          if (tries >= KN_TRY || !marked<8>(ad)) break;
        }
      }
    };
    auto run = [&](auto LDRc, auto STc) __attribute__((always_inline)) {
      constexpr bool LDR = decltype(LDRc)::value;
      if constexpr (LDR) {
        issueX(0, 0, 0); issueX(0, 1, 0); issueX(1, 0, 1); issueX(1, 1, 1);
        if constexpr (EG && KN_GW != 2) { issueW(2, 2 ^ WR); issueW(3, 3 ^ WR); wait_vm<2 * XI + 2 * WI>(); } else wait_vm<2 * XI>();
        if constexpr (EG) check_tile(0);
      } else if constexpr (!EG) glu_stats_issue();
      lds_barrier();
      if constexpr (EG && !LDR) { if (wave == 4) __builtin_amdgcn_raw_ptr_buffer_load_lds(__builtin_amdgcn_make_buffer_rsrc((void*)(a.sync + panel * 64), 0, 4, 0x00020000), (lds_void_ptr)(size_t)(lds0 + TCH + 256), 4, lane * 4, 0, 0, 16); }
      { const RdCtx c = mkctx(false, 0, WR * WU); static_for<0, MFX + NFX>([&](auto kc) { rd_item(fa, c, I0{}, kc); }); }
      wait_frags(fa);
      STAMP(1);
      { const RdCtx c = mkctx(true, 0, (WR + 1) * WU); step(F{}, STc, fa, fb, c, I0{}, T{}, std::integral_constant<int, LDR ? 2 * XI : 0>{}, [&](int i) { ldX(i % XI, 2, i / XI, 2); }); }
      int xs = 1;
#define KN_CTJ (KN_SPLIT ? 11 : 12)
      unsigned long long tb = (unsigned long long)a.PF0; unsigned tv = 0, tl = 0x7fffffffu, ts = KN_SPLIT ? 1835008u : 1048576u, tmask = 0; int tj = 1, tsh = 1;
      if constexpr (!LDR) {
        unsigned long long c0, c1; CODE_ADDR(c0, "k_all"); CODE_ADDR(c1, "k_zend");
#define KN_WJ 1
#define KN_WE 8
#define KN_DJ 6
        tj = KN_DJ;
        if (wave == 4) { tb = c0; tl = c1 - c0 < 262144ull ? (unsigned)(c1 - c0) : 0u; ts = 0; tmask = 1u << KN_CTJ; }
        if (wave == 5) tmask = 0x55u << (KN_DJ + (idx & 1));
        if (wave == 6) { tb = (unsigned long long)((idx & 16) ? a.W1 : a.W0); ts = 64 * LDW * 2; tmask = ((1u << (KN_WE - KN_KT)) - 1u) << KN_WJ; tj = KN_WJ - krot - KN_KT; tsh = 0; }
        const int l4 = lane / 14;
        tv = wave == 4 ? (unsigned)((lane * 32 + idx) * 128)
           : (lane >= 56 ? 0x80000000u : (wave == 5 ? (unsigned)(bid * 7168 + lane * 128) : (unsigned)(((idx & 15) * 4 + l4) * (LDW * 2) + (xcd & 3) * 1792 + (lane - l4 * 14) * 128)));
      }
      const int js = 2 * (tm & 3) + 1;
      const auto rC = __builtin_amdgcn_make_buffer_rsrc((void*)a.cs, 0, 0x7fffffff, 0x00020000);
      unsigned* ccnt = a.sync + 1024 + tn * 32;
#define KN_JR 10
#define KN_JS ((KN_E & 2) ? 2 : 1)
      const int jf = wave == 4 ? NT - 4 : (wave == 7 ? KN_JR : 64);
      const unsigned* fcnt = wave == 4 ? ccnt : a.sync + panel * 64 + 32;
      const bool mrk = (KN_E & 4) && KN_MK == 0 && (idx >> 4) == ((xcd >> 1) & 1);
      const int jm = (idx >> 4) == ((xcd >> 1) & 1) ? 3 : 64;
      static_assert(KN_MK == 2 && (KN_E & 4) && !KN_DM, "marks");
      int mo = (((m0 >> 4) + wave * 4) * RBA + tn * 8) * 256;
      const unsigned evm = tmask | (8u << js) | (mrk ? 0xbfcu : 0u) | (wave == 4 ? 3u << (NT - 4) : 0u) | (wave == 7 ? ((5u << KN_JS) | 2u | (11u << KN_JR)) : 0u);
      constexpr int CFL = TCH + 256, CPB = XBASE + ((NT - 3) % 3) * XT;
      auto iter = [&](int j, auto LWc, auto LXc, auto WAITc, auto CAc, auto CBc) {
        constexpr bool LW = decltype(LWc)::value && LDR, LX = decltype(LXc)::value && LDR, LP = LW && !LX;
        if constexpr (LDR) {
          wait_vm<decltype(WAITc)::value>();
          if constexpr (EG && LX) { if (__builtin_expect(j <= 2, 0)) check_tile(j); }
          if constexpr ((KN_E & 4) && KN_MK == 2 && LX) {
            if ((unsigned)(j - jm) < 8u) {
              int lx = lane; asm volatile("" : "+v"(lx));
              unsigned on = 0xffffffffu; asm volatile("" : "+v"(on));
              __builtin_amdgcn_raw_buffer_store_b128((u32x4){on, on, on, on}, rO, lx * 16, mo, 16);
              mo += (j & 1) ? 1024 : RBA * 256 - 1024;
            }
            if (j == jm + 9) { if (lane == 0) __hip_atomic_fetch_add(a.sync + (8 + panel) * 64, 1u, __ATOMIC_RELAXED, __HIP_MEMORY_SCOPE_AGENT); }
          }
          if constexpr (!KN_FG) loop_barrier();
        }
        if constexpr (!LDR) {
          if (__builtin_expect((evm >> j) & 1, 0)) {
            if (j == js + 3) {
              if ((tmask >> (j - 1)) & 1) wait_vm<1>(); else wait_vm<0>();
              if (lane == 0) __hip_atomic_fetch_add(ccnt, 1u, __ATOMIC_RELAXED, __HIP_MEMORY_SCOPE_AGENT);
            }
            if ((KN_E & 4) && KN_MK == 0) {
              if (mrk && j == 11) {
                if ((tmask >> (j - 1)) & 1) wait_vm<1>(); else wait_vm<0>();
                if (lane == 0) __hip_atomic_fetch_add(a.sync + (8 + panel) * 64, 1u, __ATOMIC_RELAXED, __HIP_MEMORY_SCOPE_AGENT);
              }
            }
            if (EG && j == 1) glu_stats_issue();
            if (j == KN_JS && wave == 7) { wait_vm<0>(); glu_stats(); }
            if (j == KN_JS + 2 && wave == 7) {
              if (j == js + 2) wait_vm<1>(); else wait_vm<0>();
              if (lane == 0) __hip_atomic_fetch_add((unsigned*)fcnt, 1u, __ATOMIC_RELAXED, __HIP_MEMORY_SCOPE_AGENT);
            }
            {
              int lx = lane; asm volatile("" : "+v"(lx));
              if (j == jf) __builtin_amdgcn_raw_ptr_buffer_load_lds(__builtin_amdgcn_make_buffer_rsrc((void*)fcnt, 0, 4, 0x00020000), (lds_void_ptr)(size_t)(lds0 + CFL + (wave & 1) * 256), 4, lx * 4, 0, 0, 16);
              if (j == jf + 1) {
                wait_vm<0>();
                int fv = ds_r32(lds0 + CFL + (wave & 1) * 256);
                const unsigned f0 = snap_get(lds0, 3 - (wave & 1));
                asm volatile("" : "+v"(fv));
                while ((unsigned)__builtin_amdgcn_readfirstlane(fv) - f0 < 32u && in_time(lds0, 3u))
                  asm volatile("global_load_dword %0, %1, %2 sc1\n\ts_waitcnt vmcnt(0)" : "=v"(fv) : "v"(0), "s"(fcnt) : "memory");
                if (wave == 7) {
#pragma unroll
                  for (int i = 0; i < 4; ++i) __builtin_amdgcn_raw_ptr_buffer_load_lds(rC, (lds_void_ptr)(size_t)(lds0 + RING + i * 1024), 16, lx * 16, RTAB + m0 * 16 + i * 1024, 0, 16);
                }
              }
              if (j == jf + 3) wait_vm<0>();
            }
            if (mrk && (unsigned)(j - 2) < 8u) {
              int lx = lane; asm volatile("" : "+v"(lx));
              unsigned on = 0xffffffffu; asm volatile("" : "+v"(on));
              const int g = idx & 7;
#define KN_X 0
              if (KN_X != 2) __builtin_amdgcn_raw_buffer_store_b128((u32x4){on, on, on, on}, rO, KN_X == 1 ? 0x80000000u : lx * 16, (((m0 >> 4) + (wave - 4) * 4 + ((j - 2) >> 1)) * RBA + ((xcd & 3) * 14 + 2 * g - (g >> 2)) * 8 + (j & 1) * 4) * 256, KN_X == 3 ? 0 : 16);
            }
            if ((tmask >> j) & 1) touch((const void*)tb, tv, (((j - tj) >> tsh) & 15) * ts, tl);
          }
          __builtin_amdgcn_sched_barrier(0);
        }
        int xp = xs + 2; if (xp >= 3) xp -= 3;
        const int ws = ((j & 1) * 2) ^ WR;
        auto lda = [&](int i) { ldW(i % WI, 2 * j + 2 + i / WI, (ws ^ 2) + i / WI); };
        auto ldb = [&](int i) {
            if constexpr (LX) ldX(i % XI, j + 2, i / XI, xp);
            else __builtin_amdgcn_raw_ptr_buffer_load_lds(rC, (lds_void_ptr)(size_t)(lds0 + CPB + wave * 8192 + i * 1024), 16, lane * 16, (tn * 2 + wn) * 7168 + i * 1024, 0, 16); };
        using NLA = std::integral_constant<int, LW ? 2 * WI : 0>; using NLB = std::integral_constant<int, LX ? 2 * XI : (LP ? 7 : 0)>;
        if constexpr (KN_FG) {
          { const RdCtx c = mkctx(false, xs * XT, ws * WU); stepg(CAc, fb, fa, c, std::integral_constant<int, LDR ? KN_FB + 1 : 0>{}, std::integral_constant<int, LDR ? KN_FB : -1>{}, [&]() { loop_barrier(); }, NLA{}, lda); }
          { const RdCtx c = mkctx(true, xs * XT, (ws + 1) * WU); stepg(CBc, fa, fb, c, I0{}, std::integral_constant<int, LDR ? -1 : KN_FE>{},
              [&]() { asm volatile("s_waitcnt lgkmcnt(0)" ::: "memory"); if (decltype(WAITc)::value == 0 || j + 1 < NT) loop_barrier();
                      if constexpr (KN_HOLD && decltype(WAITc)::value != 0) { if (j + 2 == NT) { loop_barrier(); if (KN_HOLD & 2) __builtin_amdgcn_s_setprio(3); } } }, NLB{}, ldb); }
        } else {
          { const RdCtx c = mkctx(false, xs * XT, ws * WU); step(CAc, STc, fb, fa, c, I0{}, T{}, NLA{}, lda); }
          { const RdCtx c = mkctx(true, xs * XT, (ws + 1) * WU); step(CBc, STc, fa, fb, c, I0{}, T{}, NLB{}, ldb); }
        }
        xs = xs + 1; if (xs >= 3) xs = 0;
        if constexpr (!LDR && !KN_FG) { if (decltype(WAITc)::value == 0 || j + 1 < NT) loop_barrier(); }
      };
      using WX_ = std::integral_constant<int, 2 * XI>;
      bf16x8 pv[8];
      if constexpr (LDR) {
        for (int j = 1; j < NT - 2; ++j) iter(j, T{}, T{}, WX_{}, F{}, F{});
#define KN_PRI 1
        if (KN_PRI == 2) __builtin_amdgcn_s_setprio(3);
        iter(NT - 2, T{}, F{}, WX_{}, F{}, F{});
        if (KN_PRI == 1) __builtin_amdgcn_s_setprio(3);
        iter(NT - 1, F{}, F{}, std::integral_constant<int, 7>{}, F{}, F{});
        wait_vm<0>();
        int lc = lane; asm volatile("" : "+v"(lc));
        const unsigned ca = lds0 + CPB + wave * 8192 + (lc >> 4) * 1792 + ((lc & 15) < 14 ? lc & 15 : 0) * 16;
        static_for<0, 8>([&](auto ic) { constexpr int i = decltype(ic)::value; pv[i] = ds_b128<i * 224>(ca); });
      } else {
        int j = 1;
        if constexpr (EG) {
          if (wave == 4) {
            wait_vm<0>();
            int fv = ds_r32(lds0 + TCH + 256);
            const unsigned f0 = snap_get(lds0, 1);
            asm volatile("" : "+v"(fv));
            if (((unsigned)__builtin_amdgcn_readfirstlane(fv) - f0) >> 16 < 32u) wait_count(lds0, a.sync + panel * 64, f0, 32u, 2u);
          }
        }
        loop_barrier();
#pragma unroll 1
        for (; j < js; ++j) iter(j, F{}, F{}, I0{}, F{}, F{});
        const int ja = wm == 2 ? js + 2 : js;
#pragma unroll 1
        for (; j < ja; ++j) iter(j, F{}, F{}, I0{}, T{}, F{});
#pragma unroll 1
        for (; j < js + 2; ++j) iter(j, F{}, F{}, I0{}, F{}, T{});
        { int lx = lane; asm volatile("" : "+v"(lx));
          __builtin_amdgcn_raw_buffer_store_b128(__builtin_bit_cast(u32x4, accs), rC, (lx & 15) < NF ? (unsigned)((tn * 4 + wn * 2) * 3584 + (tm * 2 + (wm & 1)) * 224
                  + ((lx & 15) < 6 ? (lx & 1) * 3584 + ((lx & 15) >> 1) * 64 + (lx >> 4) * 16 : (lx >> 5) * 3584 + 192 + (lx & 16))) : 0x80000000u, 0, 16); }
#pragma unroll 1
        for (; j < NT; ++j) iter(j, F{}, F{}, I1{}, F{}, F{});
        done_inc();
      }
      { const RdCtx c = mkctx(false, 0, 0); step(F{}, STc, fb, fa, c, I0{}, F{}, std::integral_constant<int, LDR ? (KN_HOLD ? NF : 2) : 0>{}, [&](int i) {
          if (KN_HOLD && i == KN_HG) loop_barrier();
          if (i == (KN_HOLD ? 3 : 1)) {
            asm volatile("s_waitcnt lgkmcnt(0)" ::: "memory");
#pragma unroll
            for (int k = 0; k < 8; ++k) asm volatile("" : "+v"(pv[k]));
            f32x4 cv = ((__builtin_bit_cast(f32x4, pv[0]) + __builtin_bit_cast(f32x4, pv[1])) + (__builtin_bit_cast(f32x4, pv[2]) + __builtin_bit_cast(f32x4, pv[3])))
                     + ((__builtin_bit_cast(f32x4, pv[4]) + __builtin_bit_cast(f32x4, pv[5])) + (__builtin_bit_cast(f32x4, pv[6]) + __builtin_bit_cast(f32x4, pv[7])));
#pragma unroll
            for (int k = 0; k < 4; ++k) { const float t = cv[k]; auto sw = __builtin_amdgcn_permlane16_swap(__float_as_uint(t), __float_as_uint(t), false, false); cv[k] = __uint_as_float(sw[0]) + __uint_as_float(sw[1]); }
            if ((lane & 31) < 14) *(f32x4*)(tb_cs + (lane >> 5) * 112 + wn * 56 + fr * 4) = cv;
            done_inc();
          } }); }
#define KN_PRE 0
      if constexpr ((LDR || (KN_HOLD & 2)) && KN_PRE == 0) __builtin_amdgcn_s_setprio(0);
      epilogue(LDRc);
      if constexpr (LDR && KN_PRE == 1) __builtin_amdgcn_s_setprio(0);
    };
    if (wave < NLW) run(T{}, IM1{}); else run(F{}, IM1{});
  }
  STAMP(7);
  STAMP(8);
  if constexpr (GLU) STAMPW(12, 4);
  if constexpr (SEAM_OUT && !GLU) seam_signal();
  mark_signal();
  STAMP(3);
#endif
}

#define KN_PT 14
#define KN_QO1 3
#define KN_QO2 6
#define KN_QO3 6
#define KN_QO4 16
#define KN_QP 0
#define KN_QH 0
#define KN_QS 17
#define KN_QLB 4

constexpr int QXT = 144 * 128, QWU = 24576, QWB = 3 * QXT, QRING = QWB + 4 * QWU;
constexpr int QMU = QRING + 64, QRS = QMU + 576, QFL = QRS + 576, QTCH = QRING + 2304;
constexpr int QCS = 2 * QXT, QKH = QCS + 1024, QVH = QKH + 4096;
#define KN_QWC 8
struct SchedQ {
  static constexpr int ns(int j) { return j < 2 ? 2 : 0; }
  static constexpr int nx(int j) { return (j + 2 < 16 ? 4 : 0) + ns(j); }
  static constexpr int wh(int j) { if (j < 0) return 12; int v = wh(j - 1) + KN_QWC - ns(j); if (v > 6 * (j + 4)) v = 6 * (j + 4); return v > 96 ? 96 : v; }
  static constexpr int oh(int j) { return j < 13 ? 0 : 2 * (j == 13 ? KN_QO1 : (j == 14 ? KN_QO2 : KN_QO3)); }
  static constexpr int tot(int j) { return j < 0 ? 20 : tot(j - 1) + nx(j) + wh(j) - wh(j - 1) + oh(j) - oh(j - 1) + (j == 0); }
  static constexpr int cnt(int j) {
    int pos = j < 2 ? 10 * (j + 1) : tot(j - 3) + 4;
    for (int i = 0; i < j; ++i) if (wh(i - 1) < 6 * (j + 1) && wh(i) >= 6 * (j + 1)) { const int q = tot(i - 1) + nx(i) + 6 * (j + 1) - wh(i - 1); if (q > pos) pos = q; }
    return tot(j - 1) - pos;
  }
};
static_assert(SchedQ::cnt(0) == 10 && SchedQ::cnt(8) == 16 && KN_QO1 <= 3 && KN_QO2 <= 6 && KN_QO3 <= 9 && KN_QWC >= 8, "schedule");
static_assert(SchedQ::cnt(KN_S1P) <= SchedQ::tot(KN_S1P - 1) - SchedQ::tot(0) - 6 && KN_S1C > KN_S1P + 2, "statistics");
struct FragQ { bf16x8 x[4]; s16x4 lo[6], hi[6]; };
__device__ __forceinline__ float fma1(float a, float b, float c) { float r; asm("v_fma_f32 %0, %1, %2, %3" : "=v"(r) : "v"(a), "v"(b), "v"(c)); return r; }
__device__ __forceinline__ void k1_phase(const GemmArgs& a) {
#if defined(__HIP_DEVICE_COMPILE__)
  constexpr int K = D_, LDW = 3 * D_, NT = 16, XI = 5, XIMIN = 4, WI = 6;
  extern __shared__ __attribute__((aligned(16))) char smem[];
  STAMP(0);
  const unsigned lds0 = (unsigned)(size_t)(lds_char_ptr)smem;
  const int tid = threadIdx.x, lane = tid & 63;
  const int wave = __builtin_amdgcn_readfirstlane(tid >> 6);
  const int bid = blockIdx.x, xcd = bid & 7, idx = bid >> 3;
  const int tm = (xcd >> 2) * 8 + (idx >> 2), tn = (xcd & 3) * 4 + (idx & 3), m0 = tm * 128;
  const int fr = lane & 15, fq = lane >> 4, wq = (lane >> 2) & 3, wp = lane & 3;
  using I0 = std::integral_constant<int, 0>; using I1 = std::integral_constant<int, 1>; using I2 = std::integral_constant<int, 2>; using I3 = std::integral_constant<int, 3>;
  using T = std::true_type; using F = std::false_type;
  auto touch = [&](const void* base, unsigned voff, unsigned soff, unsigned lim = 0x7fffffffu) {
    __builtin_amdgcn_raw_ptr_buffer_load_lds(__builtin_amdgcn_make_buffer_rsrc((void*)base, 0, lim, 0x00020000), (lds_void_ptr)(size_t)(lds0 + QTCH), 4, voff, soff, 0, 0);
  };
  auto code_touch = [&]() {
    unsigned long long c0, c1; CODE_ADDR(c0, "k_all"); CODE_ADDR(c1, "k_zend");
    const unsigned len = c1 - c0 < 262144ull ? (unsigned)(c1 - c0) : 0u;
    touch((const void*)c0, (unsigned)((lane * 32 + idx) * 128), 0, len);
  };
  int hj1, hj2; unsigned htv;
  auto hset = [&]() {
    const int slc = (3 - ((unsigned)(size_t)a.W0 >> 7) - (xcd & 3) * 4) & 7;
    const int hid = (idx >> 2) * 3 + (idx & 3) - ((idx & 3) > slc ? 1 : 0);
    const bool hlp = slc < 4 && (idx & 3) != slc;
    hj1 = hlp ? hid / 3 - KN_HQ : -100; hj2 = hlp ? hid / 3 + 8 - KN_HQ : -100;
    htv = (unsigned)(lane * (LDW * 2) + (hid % 3) * 2048 + ((xcd & 3) * 4 + slc) * 128);
  };
  const unsigned xo = fr * 128 + ((fq ^ ((fr >> 1) & 7)) << 4);
  auto wfrag = [&](int seg) { const int row = 8 * fq + wq; return lds0 + QWB + row * 128 + (((seg << 1) ^ wswz<128>(row)) << 4) + wp * 8; };
  const f32x4 z4 = {0.f, 0.f, 0.f, 0.f};
  auto cat = [](s16x4 lo, s16x4 hi) { return __builtin_shufflevector(lo, hi, 0, 1, 2, 3, 4, 5, 6, 7); };
  if (wave < 4) {
    unsigned xsrc[XI], wsrc[WI];
    { const int lr = lane >> 3;
      const unsigned xv = (unsigned)(lr * (K * 2) + (((lane & 7) ^ ((wave * 4 + (lr >> 1)) & 7)) << 4));
      const int r0 = m0 - 16 + wave * 8;
#pragma unroll
      for (int i = 0; i < XI; ++i) xsrc[i] = xv + (unsigned)((i == 0 ? (r0 < 0 ? 0 : r0) : r0 + 32 * i) * (K * 2));
      const unsigned wv = (unsigned)((wave * 8 + lr) * (LDW * 2) + tn * 128 + (((lane & 7) ^ wswz<128>(wave * 8 + lr)) << 4));
#pragma unroll
      for (int i = 0; i < WI; ++i) wsrc[i] = wv + (unsigned)((i & 1) * 32 * (LDW * 2) + (i >> 1) * 2048); }
    const unsigned mb = lds0 + wave * 1024;
    asm volatile("s_waitcnt lgkmcnt(0)" :: "s"(a.warm), "v"(wsrc[WI - 1]), "v"(xsrc[XI - 1]));
    const u32x4 qX = mk_rsrc(a.X), qW = mk_rsrc(a.W0);
    auto ldX = [&](auto ic, auto tc) { constexpr int i = decltype(ic)::value, t = decltype(tc)::value; if (i < 4 || wave < 2) ld_c<t % 3 * QXT + i * 4096, t * 128>(mb, qX, xsrc[i]); };
    auto ldW = [&](auto qc) { constexpr int i = decltype(qc)::value % WI, t = decltype(qc)::value / WI; ld_c<QWB + (t & 3) * QWU + i * 4096, t * (64 * LDW * 2)>(mb, qW, wsrc[i]); };
    static_for<0, 2>([&](auto tc) {
      static_for<0, WI>([&](auto ic) { ldW(std::integral_constant<int, decltype(tc)::value * WI + decltype(ic)::value>{}); });
      static_for<0, XI>([&](auto ic) { ldX(ic, tc); }); });
    __builtin_amdgcn_sched_barrier(0);
    unsigned osrc = (unsigned)((wave * 8 + (lane >> 3)) * D_ + ((xcd & 1) * 8 + (idx & 7)) * 64 + ((lane & 7) ^ ((lane >> 3) & 6)) * 8) * 2u;
    asm volatile("" : "+v"(osrc));
    auto ldO = [&](auto qc) { constexpr int i = decltype(qc)::value >> 1, h = decltype(qc)::value & 1;
      ld_cx<wo_pos(i >> 1) + (i & 1) * 8192 + h * 4096, (((i & 1) * 8 + (i >> 1)) * 2 + h) * (32 * D_ * 2)>(mb, mk_rsrc(a.PF1), osrc, (xcd & 3) * 8 * (32 * D_ * 2)); };
    unsigned s1tag, tsrc;
    const unsigned t_start = (unsigned)__builtin_amdgcn_s_memrealtime();
    { const int t2 = (xcd >> 1) * 4 + (idx >> 3), pg = (xcd >> 2) * 4 + (idx >> 3), tg = (xcd & 3) * 8 + (idx & 7), l8 = lane & 7;
      const int w = l8 == 0 ? SYO(t2) : (l8 == 1 ? pg * 64 : (l8 == 2 ? pg * 64 + 32 : (l8 == 3 ? 1024 + tg * 32 : (l8 == 4 ? (8 + (t2 >> 1)) * 64 : (l8 == 6 ? (t2 >> 1) * 64 : 2048 + bid * 32)))));
      tsrc = (unsigned)(((lane & 8) && wave == 0 ? w : SYO((lane & 1) && tm > 0 ? tm - 1 : tm)) * 4); }
    u32x4 sv[4]; f32x4 s1s[2], s1q[2]; float sa[4], sb;
    const char* srows = (const char*)a.X + (size_t)(m0 + tn * 8 + wave * 2) * (K * 2);
    unsigned l16 = lane * 16, sdst = lane < 2 ? (unsigned)(STAB + (m0 + tn * 8 + wave * 2 + lane) * 16) : 0x80000000u;
    asm volatile("" : "+v"(tsrc), "+v"(l16), "+v"(sdst));
    auto dpp = [](float t, auto c) { return __int_as_float(__builtin_amdgcn_update_dpp(0, __float_as_int(t), decltype(c)::value, 0xf, 0xf, false)); };
    auto stat = [&](auto kc) {
      constexpr int k = decltype(kc)::value;
      if constexpr (k == 0) asm volatile("" : "+v"(sv[0]), "+v"(sv[1]), "+v"(sv[2]), "+v"(sv[3]));
      if constexpr (k < 8) {
        constexpr int r = (k >> 1) & 1, h = k >> 2;
        const u32x4 one4 = {0x3f803f80u, 0x3f803f80u, 0x3f803f80u, 0x3f803f80u};
        const bf16x8 x = __builtin_bit_cast(bf16x8, sv[2 * r + h]);
        if constexpr (k & 1) s1q[r] = __builtin_amdgcn_mfma_f32_16x16x32_bf16(x, x, h ? s1q[r] : z4, 0, 0, 0);
        else s1s[r] = __builtin_amdgcn_mfma_f32_16x16x32_bf16(__builtin_bit_cast(bf16x8, one4), x, h ? s1s[r] : z4, 0, 0, 0);
      }
      if constexpr (k == 8 || k == 9) {
        constexpr int r = k - 8;
        const f32x4 g = s1q[r];
        const float d = (lane & 2) ? ((lane & 1) ? g[3] : g[2]) : ((lane & 1) ? g[1] : g[0]);
        sa[r] = fq == 0 ? s1s[r][0] : 0.f;
        sa[2 + r] = fq == wq ? d : 0.f;
        asm volatile("" : "+v"(sa[r]), "+v"(sa[2 + r]));
      }
      if constexpr (k == 10 || k == 11) { constexpr int i = (k - 10) * 2; sa[i] = ((lane & 1) ? sa[i + 1] : sa[i]) + dpp((lane & 1) ? sa[i] : sa[i + 1], std::integral_constant<int, 0xb1>{}); asm volatile("" : "+v"(sa[i])); }
      if constexpr (k == 12) sb = ((lane & 2) ? sa[2] : sa[0]) + dpp((lane & 2) ? sa[0] : sa[2], std::integral_constant<int, 0x4e>{});
      if constexpr (k == 13) { sb += dpp(sb, std::integral_constant<int, 0x124>{}); sb += dpp(sb, std::integral_constant<int, 0x128>{}); }
      if constexpr (k == 14) { auto q = __builtin_amdgcn_permlane16_swap(__float_as_uint(sb), __float_as_uint(sb), false, false); sb = __uint_as_float(q[0]) + __uint_as_float(q[1]); }
      if constexpr (k == 15) { auto q = __builtin_amdgcn_permlane32_swap(__float_as_uint(sb), __float_as_uint(sb), false, false); sb = (__uint_as_float(q[0]) + __uint_as_float(q[1])) * (1.f / 1024.f); }
      if constexpr (k == 16) { const float var = dpp(sb, std::integral_constant<int, 0x4e>{}) - sb * sb; sa[0] = __builtin_amdgcn_rsqf((var > 0.f ? var : 0.f) + 1e-5f); asm volatile("" : "+v"(sa[0])); }
      if constexpr (k >= 12 && k < 16) asm volatile("" : "+v"(sb));
      if constexpr (k == 17) {
        asm volatile("" : "+v"(s1tag));
        const unsigned own = (unsigned)__builtin_amdgcn_readlane((int)s1tag, 0) ^ STAG;
        __builtin_amdgcn_raw_buffer_store_b128((u32x4){__float_as_uint(sb), __float_as_uint(sa[0]), own, ~own}, __builtin_amdgcn_make_buffer_rsrc((void*)a.cs, 0, 0x7fffffff, 0x00020000), sdst, 0, 16);
        asm volatile("s_nop 1" ::: "memory");
      }
    };
    hset();
    if (KN_QH) __builtin_amdgcn_s_setprio(KN_QH);
    const unsigned wa = wfrag(wave);
    f32x4 acq = z4, ack = z4, acv = z4;
    const unsigned long long tb = (unsigned long long)(wave == 3 ? a.PF1 : a.W0);
    unsigned tv = wave == 3 ? (unsigned)((bid * 64 + lane) * 128) : htv;
    const unsigned ts = wave == 3 ? 0u : 64 * LDW * 2;
    const unsigned evm = wave == 3 ? 2u << (idx % 12) : (wave == 2 ? (hj1 > 0 ? 1u << hj1 : 0u) | (hj2 > 0 ? 1u << hj2 : 0u) : 0u);
    const int se = wave * 64 - 64 + lane, sr = m0 - 16 + se;
    const bool sok = (unsigned)se < 144u;
    unsigned ssrc = sok ? (unsigned)(STAB + (sr < 0 ? se : sr) * 16) : 0x80000000u, sld = lds0 + QRING + STL + (se & 255) * 16;
    unsigned smu = lds0 + (sok ? QMU + se * 4 : QTCH), srs = lds0 + (sok ? QRS + (se & 1) * 288 + (se >> 1) * 4 : QTCH);
    unsigned xh0[3], xh1[3];
#pragma unroll
    for (int i = 0; i < 3; ++i) { xh0[i] = fr == 0 ? lds0 + QRING : lds0 + i * QXT + xo; xh1[i] = fr == 0 ? lds0 + QRING : lds0 + i * QXT + (xo ^ 64); asm volatile("" : "+v"(xh0[i]), "+v"(xh1[i])); }
    unsigned wb[2] = {wa, wa + 2 * QWU};
    asm volatile("" : "+v"(tv), "+v"(ssrc), "+v"(sld), "+v"(smu), "+v"(srs), "+v"(wb[0]), "+v"(wb[1]));
    auto fetch = [&]() { __builtin_amdgcn_raw_ptr_buffer_load_lds(__builtin_amdgcn_make_buffer_rsrc((void*)a.cs, 0, 0x7fffffff, 0x00020000), (lds_void_ptr)(size_t)(lds0 + QRING + STL + ((wave + 3) & 3) * 1024), 16, ssrc, 0, 0, 16); };
    unsigned tsq = 0;
    using SQ = SchedQ;
    static_for<0, NT>([&](auto jc) {
      constexpr int j = decltype(jc)::value, NX = (j + 2 < NT ? XI : 0) + SQ::ns(j), NW = SQ::wh(j) - SQ::wh(j - 1), NL = NX + NW + SQ::oh(j) - SQ::oh(j - 1);
      wait_vm<SQ::cnt(j)>();
      lds_barrier();
      bf16x8 xh[2]; s16x4 lo[2][3], hi[2][3]; bf16x8 ent;
      auto rd = [&](auto rc) { constexpr int r = decltype(rc)::value, h = r / 7, q = r % 7 - 1, o = (j & 1) * QWU + h * 4096 + (q >> 1) * 8192;
        if constexpr (q < 0) xh[h] = ds_b128<0>(h ? xh1[j % 3] : xh0[j % 3]);
        else if constexpr (q & 1) hi[h][q >> 1] = ds_tr<o + 512>(wb[(j >> 1) & 1]); else lo[h][q >> 1] = ds_tr<o>(wb[(j >> 1) & 1]); };
      auto mm = [&](auto ic) { constexpr int i = decltype(ic)::value, h = i / 3;
        if constexpr (i % 3 == 0) {
          asm volatile("s_waitcnt lgkmcnt(%0)" :: "n"(h ? 0 : 5) : "memory");
          asm volatile("" : "+v"(xh[h]), "+v"(lo[h][0]), "+v"(hi[h][0]), "+v"(lo[h][1]), "+v"(hi[h][1]), "+v"(lo[h][2]), "+v"(hi[h][2]));
          acq = __builtin_amdgcn_mfma_f32_16x16x32_bf16(cat(lo[h][0], hi[h][0]), xh[h], acq, 0, 0, 0); }
        if constexpr (i % 3 == 1) ack = __builtin_amdgcn_mfma_f32_16x16x32_bf16(cat(lo[h][1], hi[h][1]), xh[h], ack, 0, 0, 0);
        if constexpr (i % 3 == 2) acv = __builtin_amdgcn_mfma_f32_16x16x32_bf16(xh[h], cat(lo[h][2], hi[h][2]), acv, 0, 0, 0); };
      if constexpr (j == KN_S1C + 3) ent = ds_b128<0>(sld);
      static_for<0, (NL > 11 ? NL : 11)>([&](auto gc) {
        constexpr int g = decltype(gc)::value;
        __builtin_amdgcn_sched_barrier(0);
        if constexpr (g < NX - SQ::ns(j)) ldX(gc, std::integral_constant<int, j + 2>{});
        else if constexpr (g < NX) {
          constexpr int q = 2 * j + g - XI;
          asm volatile("s_nop 4\n\tglobal_load_dwordx4 %0, %1, %2 offset:%3" : "=&v"(sv[q]) : "v"(l16), "s"(srows), "n"(q * 1024) : "memory");
        }
        else if constexpr (g < NX + NW) ldW(std::integral_constant<int, SQ::wh(j - 1) + g - NX>{});
        else if constexpr (g < NL) ldO(std::integral_constant<int, SQ::oh(j - 1) + g - NX - NW>{});
        else if constexpr (g < 9) asm volatile("s_nop 7");
        __builtin_amdgcn_sched_barrier(0);
        if constexpr (g < 7) { rd(std::integral_constant<int, 2 * g>{}); rd(std::integral_constant<int, 2 * g + 1>{}); }
        if constexpr (g >= 5 && g < 11) mm(std::integral_constant<int, g - 5>{});
        if constexpr (j == KN_S1P && g < 4) { stat(std::integral_constant<int, 2 * g>{}); stat(std::integral_constant<int, 2 * g + 1>{}); }
        if constexpr (j == KN_S1P && (g == 9 || g == 10)) stat(std::integral_constant<int, g - 1>{});
        if constexpr (j == KN_S1P + 1 && g < 8) stat(std::integral_constant<int, 10 + g>{});
      });
      if constexpr (j == 0) asm volatile("s_nop 4\n\tglobal_load_dword %0, %1, %2 sc1" : "=&v"(s1tag) : "v"(tsrc), "s"(a.sync) : "memory");
      __builtin_amdgcn_sched_barrier(0);
      if constexpr (j >= 1 && j <= 12) { if (__builtin_expect((evm >> j) & 1, 0)) touch((const void*)tb, tv, (j + KN_HQ) * ts); }
      if constexpr (j == KN_S1C - 1) { if (wave == 0 && (lane >> 3) == 1) ds_w32(lds0 + SNAP + (lane & 7) * 4, (int)((lane & 7) == 7 ? t_start : s1tag)); }
      if constexpr (j == KN_S1C) fetch();
      if constexpr (j == KN_S1C + 1) {
        asm volatile("" : "+v"(s1tag));
        const unsigned own = (unsigned)__builtin_amdgcn_readlane((int)s1tag, 0), before = (unsigned)__builtin_amdgcn_readlane((int)s1tag, 1);
        s1tag = (se < 16 ? before : own) ^ STAG;
        asm volatile("" : "+v"(s1tag));
      }
      if constexpr (j == KN_S1C + 3) {
        static_assert(SQ::cnt(j) <= SQ::tot(j - 1) - SQ::tot(KN_S1C), "table");
        asm volatile("" : "+v"(ent));
        u32x4 t = __builtin_bit_cast(u32x4, ent);
        auto stale = [&]() { return __builtin_amdgcn_ballot_w64(sok && (t[2] != s1tag || t[3] != ~s1tag)) != 0; };
        if (__builtin_expect(stale(), 0)) {
          do {
            fetch();
            wait_vm<0>();
            t = __builtin_bit_cast(u32x4, ds_b128<0>(sld));
            asm volatile("s_waitcnt lgkmcnt(0)" : "+v"(t) :: "memory");
          } while (stale() && (unsigned)__builtin_amdgcn_s_memrealtime() - t_start < 2500000u);
        }
        ds_w32(smu, (int)t[0]); ds_w32(srs, (int)t[1]);
      }
      if constexpr (j == NT - 3 && (KN_CT & 2)) { if (wave == 3) code_touch(); }
      if constexpr (j == KN_PT) {
        touch(wave < 2 ? a.CW0 : a.CW1, lane < 56 ? (unsigned)(((wave & 1) * 512 + bid) * (DF_ * 2) + lane * 128) : 0x80000000u, 0);
      }
    });
    {
      int lx = lane; asm volatile("" : "+v"(lx));
      const int fr = lx & 15, fq = lx >> 4;
      if (fr == 0) { *(f32x4*)(smem + QCS + wave * 64 + fq * 16) = acq; *(f32x4*)(smem + QCS + 256 + wave * 64 + fq * 16) = ack; }
      if (fq == 0) *(float*)(smem + QCS + 512 + wave * 64 + fr * 4) = acv[0];
      asm volatile("" ::: "memory");
      const f32x4 ck = __builtin_bit_cast(f32x4, ds_b128<QCS + 256>(lds0 + wave * 64 + fq * 16)), m4 = __builtin_bit_cast(f32x4, ds_b128<0>(lds0 + QMU + fq * 16));
      int cv = ds_r32(lds0 + QCS + 512 + wave * 64 + fr * 4), m1 = ds_r32(lds0 + QMU + fr * 4);
      f32x4 c4 = ck, n4 = m4;
      asm volatile("s_waitcnt lgkmcnt(0)" : "+v"(c4), "+v"(n4), "+v"(cv), "+v"(m1), "+v"(ack), "+v"(acv) :: "memory");
      const float nm = -__int_as_float(m1), nc = -__int_as_float(cv);
      const u32x2 kk = {pk_bf16(fma1(nm, c4[0], ack[0]), fma1(nm, c4[1], ack[1])), pk_bf16(fma1(nm, c4[2], ack[2]), fma1(nm, c4[3], ack[3]))};
      const u32x2 vv = {pk_bf16(fma1(nc, n4[0], acv[0]), fma1(nc, n4[1], acv[1])), pk_bf16(fma1(nc, n4[2], acv[2]), fma1(nc, n4[3], acv[3]))};
      *(u32x2*)(smem + QKH + (wave >> 1) * 1024 + lx * 16 + (wave & 1) * 8) = kk;
      *(u32x2*)(smem + QVH + (wave >> 1) * 1024 + lx * 16 + (wave & 1) * 8) = vv;
    }
    if (KN_QH) __builtin_amdgcn_s_setprio(0);
    lds_barrier();
    if (tid == 64) __hip_atomic_fetch_add(a.sync + ((xcd >> 1) * 2 + (idx >> 4)) * 64, 1u, __ATOMIC_RELAXED, __HIP_MEMORY_SCOPE_AGENT);
    static_for<SQ::oh(NT - 1), 2 * KN_QO4>(ldO);
    wait_vm<2 * (KN_QO4 - 2)>();
    raw_barrier();
    static_for<2 * KN_QO4, 32>(ldO);
  } else {
    const int wm = (wave >> 1) & 1, hd = wave & 1;
    asm volatile("s_waitcnt lgkmcnt(0)" :: "s"(a.warm));
    if (wave == 4) { if (lane < 4) { *(unsigned*)(smem + QRING + lane * 4) = 0x3f803f80u; *(unsigned*)(smem + QFL + lane * 4) = 0u; } }
    if (wave == 5) {
      const int p2 = (xcd >> 1) * 2 + (idx >> 4), mb = (xcd & 1) * 16 + (idx & 15);
#pragma unroll
      for (int i = 0; i < 3; ++i)
        __builtin_amdgcn_raw_buffer_store_b128((u32x4){~0u, ~0u, ~0u, ~0u}, __builtin_amdgcn_make_buffer_rsrc((void*)a.st, 0, 0x7fffffff, 0x00020000), lane * 16 + i * 1024,
                                               ((p2 * 16 + (mb >> 1)) * RBH + (xcd >> 2) * 64 + (mb & 1) * 12) * 256, 16);
    }
    if (wave == 6) { hset(); if (hj1 <= 0 && hj1 > -100) touch(a.W0, htv, (hj1 + KN_HQ) * (64 * LDW * 2)); }
    if ((KN_CT & 1) && wave == 7) code_touch();
    if (KN_QP) __builtin_amdgcn_s_setprio(KN_QP);
    f32x4 acc[6][4];
#pragma unroll
    for (int n = 0; n < 6; ++n)
#pragma unroll
      for (int m = 0; m < 4; ++m) acc[n][m] = z4;
    const unsigned xa[2] = {lds0 + (16 + wm * 64) * 128 + xo, lds0 + (16 + wm * 64) * 128 + (xo ^ 64)}, wa[2] = {wfrag(hd * 2), wfrag(hd * 2 + 1)};
    struct Ctx { unsigned x[2], w[2]; } c = {{xa[0], xa[1]}, {wa[0], wa[1]}};
    auto rd = [&](FragQ& f, auto KSc, auto rc) {
      constexpr int KS = decltype(KSc)::value, r = decltype(rc)::value;
      if constexpr (r >= 2 && r < 6) f.x[r - 2] = ds_b128<(r - 2) * 2048>(c.x[KS]);
      else { constexpr int q = r < 2 ? r : r - 4, n = q >> 1, o = KS * 4096 + (n >> 1) * 8192; if constexpr (q & 1) f.hi[n] = ds_tr<o + 512>(c.w[n & 1]); else f.lo[n] = ds_tr<o>(c.w[n & 1]); }
    };
    auto pin = [&](FragQ& f) {
      asm volatile("s_waitcnt lgkmcnt(0)" ::: "memory");
#pragma unroll
      for (int m = 0; m < 4; ++m) asm volatile("" : "+v"(f.x[m]));
#pragma unroll
      for (int n = 0; n < 6; ++n) asm volatile("" : "+v"(f.lo[n]), "+v"(f.hi[n]));
    };
    auto step = [&](FragQ& cur, auto&& gap) {
      static_for<0, 24>([&](auto ic) {
        constexpr int I = decltype(ic)::value, n = I / 4, m = I % 4;
        if constexpr (n < 4) acc[n][m] = __builtin_amdgcn_mfma_f32_16x16x32_bf16(cat(cur.lo[n], cur.hi[n]), cur.x[m], acc[n][m], 0, 0, 0);
        else acc[n][m] = __builtin_amdgcn_mfma_f32_16x16x32_bf16(cur.x[m], cat(cur.lo[n], cur.hi[n]), acc[n][m], 0, 0, 0);
        __builtin_amdgcn_sched_barrier(0);
        gap(ic);
        __builtin_amdgcn_sched_barrier(0);
      });
    };
    FragQ fa, fb;
    lds_barrier();
    static_for<0, 16>([&](auto rc) { rd(fa, I0{}, rc); });
    pin(fa);
    STAMP(1);
    int sx = 0, sw = 0;
    auto second = [&](auto ic) {
      constexpr int I = decltype(ic)::value;
      static_for<0, 16>([&](auto rc) { if constexpr (decltype(rc)::value * KN_QS / 16 == I) rd(fb, I1{}, rc); });
      if constexpr (I == 18) { sx = sx + QXT; sw = sw + QWU; asm volatile("" : "+s"(sx), "+s"(sw)); }
      if constexpr (I == 19) { sx = sx == 3 * QXT ? 0 : sx; asm volatile("" : "+s"(sx)); }
      if constexpr (I == 20) { sw = sw == 4 * QWU ? 0 : sw; asm volatile("" : "+s"(sw)); }
      if constexpr (I == 21) { c.x[0] = xa[0] + sx; c.x[1] = xa[1] + sx; asm volatile("" : "+v"(c.x[0]), "+v"(c.x[1])); }
      if constexpr (I == 22) { c.w[0] = wa[0] + sw; c.w[1] = wa[1] + sw; asm volatile("" : "+v"(c.w[0]), "+v"(c.w[1])); }
    };
    static_assert(KN_QS <= 18, "addresses");
    step(fa, second); pin(fb);
#pragma unroll 1
    for (int j = 1; j < NT; ++j) {
      step(fb, [&](auto ic) { constexpr int I = decltype(ic)::value;
        if constexpr (I == 0) raw_barrier();
        static_for<0, 16>([&](auto rc) { if constexpr (1 + decltype(rc)::value * KN_QS / 16 == I) rd(fa, I0{}, rc); }); });
      pin(fa);
      step(fa, second); pin(fb);
    }
    int lz = lane; asm volatile("" : "+v"(lz));
    const int gr = lz & 15, gq = lz >> 4;
    const bool par = gr & 1;
    f32x2 st[4], rk[5]; f32x4 m4[4], cs[4]; int cv[2];
    const unsigned ta = lds0 + QRING + STL + (16 + wm * 64 + gr) * 16, tm4 = lds0 + QMU + (16 + wm * 64 + gq * 4) * 4, trk = lds0 + QRS + (gr & 1) * 288 + (wm * 32 + gq * 2) * 4;
    const unsigned tcs = lds0 + QCS + hd * 128 + gq * 16, tcv = lds0 + QCS + 512 + hd * 128 + gr * 4;
    step(fb, [&](auto ic) { constexpr int I = decltype(ic)::value;
      if constexpr (I < 4) st[I] = ds_b64<I * 256>(ta);
      else if constexpr (I < 8) m4[I - 4] = __builtin_bit_cast(f32x4, ds_b128<(I - 4) * 64>(tm4));
      else if constexpr (I < 13) rk[I - 8] = ds_b64<(I - 8) * 32>(trk);
      if constexpr (I == KN_QLB) { wait_vm<0>(); raw_barrier(); }
      if constexpr (I >= 13 && I < 17) cs[I - 13] = __builtin_bit_cast(f32x4, ds_b128<((I - 13) >> 1) * 256 + ((I - 13) & 1) * 64>(tcs));
      if constexpr (I == 17) { cv[0] = ds_r32(tcv); cv[1] = ds_r32(tcv + 64); } });
    static_assert(KN_QLB <= 13, "barrier");
    STAMP(2);
    float bias[4], bias0[4];
    {
      const int srow = m0 + wm * 64 + gr;
      const unsigned mk = 0x5555u & ((2u << (srow < 15 ? srow : 15)) - 1u), sft = (gr & 14) - gq * 4 + 14;
      const unsigned b0 = ~(0x5555u >> sft), b1 = ~(0x55550000u >> sft), c0 = ~(mk >> sft), c1 = ~((mk << 16) >> sft);
#pragma unroll
      for (int j = 0; j < 4; ++j) {
        bias[j] = __uint_as_float((unsigned)__builtin_amdgcn_sbfe((int)(j < 2 ? b0 : b1), 2 - 2 * (j & 1), 1) & 0xff61b1e6u);
        bias0[j] = __uint_as_float((unsigned)__builtin_amdgcn_sbfe((int)(j < 2 ? c0 : c1), 2 - 2 * (j & 1), 1) & 0xff61b1e6u);
      }
    }
    const unsigned pl = par ? 0x01000c0cu : 0x0c0c0100u, ph = par ? 0x03020c0cu : 0x0c0c0302u;
    asm volatile("s_waitcnt lgkmcnt(0)" : "+v"(cs[0]), "+v"(cs[1]), "+v"(cs[2]), "+v"(cs[3]), "+v"(cv[0]), "+v"(cv[1]) :: "memory");
#pragma unroll
    for (int m = 0; m < 4; ++m) asm volatile("" : "+v"(st[m]), "+v"(m4[m]), "+v"(rk[m]));
    asm volatile("" : "+v"(rk[4]));
    u32x4 Q[4], Kf[5], V[5];
    auto nrm = [&](f32x4 x, f32x4 y, float sc) { const f32x4 t = x - sc * y; return (u32x2){pk_bf16(t[0], t[1]), pk_bf16(t[2], t[3])}; };
    auto c4 = [](u32x2 p, u32x2 q) { return (u32x4){p[0], p[1], q[0], q[1]}; };
    auto mkKV = [&](int m) {
      Kf[m + 1] = c4(nrm(acc[2][m], cs[2], st[m][0]), nrm(acc[3][m], cs[3], st[m][0]));
      V[m + 1] = c4(nrm(acc[4][m], m4[m], __int_as_float(cv[0])), nrm(acc[5][m], m4[m], __int_as_float(cv[1])));
    };
    mkKV(3);
    if (wm == 0) {
      *(u32x4*)(smem + QKH + 2048 + hd * 1024 + lz * 16) = Kf[4]; *(u32x4*)(smem + QVH + 2048 + hd * 1024 + lz * 16) = V[4];
      asm volatile("" ::: "memory");
      ds_w32(lds0 + QFL + hd * 4, 1);
    }
    __builtin_amdgcn_sched_barrier(0);
#pragma unroll
    for (int m = 2; m >= 0; --m) mkKV(m);
#pragma unroll
    for (int m = 0; m < 4; ++m) Q[m] = c4(nrm(acc[0][m], cs[0], st[m][0]), nrm(acc[1][m], cs[1], st[m][0]));
    STAMP(4);
    if (wm) lds_wait8(lds0 + QFL + hd * 4, 1);
    { bf16x8 k0 = ds_b128<QKH>(lds0 + wm * 2048 + hd * 1024 + lz * 16), v0 = ds_b128<QVH>(lds0 + wm * 2048 + hd * 1024 + lz * 16);
      Kf[0] = __builtin_bit_cast(u32x4, k0); V[0] = __builtin_bit_cast(u32x4, v0); }
    const float sc2 = 0.17677669529663687f * 1.4426950408889634f;
    f32x4 sc[4][2]; f32x2 lg[4][2]; float mx[4], sum[4];
    auto x16 = [](float v) { auto p = __builtin_amdgcn_permlane16_swap(__float_as_uint(v), __float_as_uint(v), false, false); return (f32x2){__uint_as_float(p[0]), __uint_as_float(p[1])}; };
    auto x32 = [](float v) { auto p = __builtin_amdgcn_permlane32_swap(__float_as_uint(v), __float_as_uint(v), false, false); return (f32x2){__uint_as_float(p[0]), __uint_as_float(p[1])}; };
#pragma unroll
    for (int m = 3; m >= 0; --m) {
      if (m == 0) asm volatile("s_waitcnt lgkmcnt(0)" : "+v"(Kf[0]), "+v"(V[0]) :: "memory");
      sc[m][1] = __builtin_amdgcn_mfma_f32_16x16x32_bf16(__builtin_bit_cast(bf16x8, Kf[m + 1]), __builtin_bit_cast(bf16x8, Q[m]), z4, 0, 0, 0);
      sc[m][0] = __builtin_amdgcn_mfma_f32_16x16x32_bf16(__builtin_bit_cast(bf16x8, Kf[m]), __builtin_bit_cast(bf16x8, Q[m]), z4, 0, 0, 0);
    }
    __builtin_amdgcn_sched_barrier(0);
#pragma unroll
    for (int m = 3; m >= 0; --m) {
      const float f = st[m][1] * sc2;
#pragma unroll
      for (int h = 0; h < 2; ++h) {
        const f32x2 s2 = {par ? sc[m][h][1] : sc[m][h][0], par ? sc[m][h][3] : sc[m][h][2]};
        const f32x2 b2 = m == 0 ? (f32x2){bias0[2 * h], bias0[2 * h + 1]} : (f32x2){bias[2 * h], bias[2 * h + 1]};
        lg[m][h] = s2 * (rk[m + h] * f) + b2;
      }
      mx[m] = fmaxf(fmaxf(lg[m][0][0], lg[m][0][1]), fmaxf(lg[m][1][0], lg[m][1][1]));
    }
    __builtin_amdgcn_sched_barrier(0);
#pragma unroll
    for (int m = 3; m >= 0; --m) { const f32x2 p = x16(mx[m]); mx[m] = fmaxf(p[0], p[1]); }
#pragma unroll
    for (int m = 3; m >= 0; --m) { const f32x2 p = x32(mx[m]); mx[m] = fmaxf(p[0], p[1]); }
    __builtin_amdgcn_sched_barrier(0);
#pragma unroll
    for (int m = 3; m >= 0; --m) {
#pragma unroll
      for (int h = 0; h < 2; ++h) { const f32x2 d = lg[m][h] - mx[m]; lg[m][h] = (f32x2){__builtin_amdgcn_exp2f(d[0]), __builtin_amdgcn_exp2f(d[1])}; }
      const f32x2 t = lg[m][0] + lg[m][1];
      sum[m] = t[0] + t[1];
    }
    __builtin_amdgcn_sched_barrier(0);
#pragma unroll
    for (int m = 3; m >= 0; --m) { const f32x2 p = x16(sum[m]); sum[m] = p[0] + p[1]; lg[m][0] = lg[m][0] * rk[m]; }
#pragma unroll
    for (int m = 3; m >= 0; --m) { const f32x2 p = x32(sum[m]); sum[m] = __builtin_amdgcn_rcpf(p[0] + p[1]); lg[m][1] = lg[m][1] * rk[m + 1]; }
    __builtin_amdgcn_sched_barrier(0);
    auto half = [](u32x4 v, int h) { return __builtin_bit_cast(s16x4, (u32x2){v[2 * h], v[2 * h + 1]}); };
#pragma unroll
    for (int m = 3; m >= 0; --m) {
      s16x4 pf[2];
#pragma unroll
      for (int h = 0; h < 2; ++h) { const f32x2 t = lg[m][h] * sum[m]; const unsigned u = pk_bf16(t[0], t[1]); pf[h] = __builtin_bit_cast(s16x4, (u32x2){__builtin_amdgcn_perm(0u, u, pl), __builtin_amdgcn_perm(0u, u, ph)}); }
#pragma unroll
      for (int f = 0; f < 2; ++f) sc[m][f] = __builtin_amdgcn_mfma_f32_16x16x16bf16_1k(half(V[m], f), pf[0], z4, 0, 0, 0);
#pragma unroll
      for (int f = 0; f < 2; ++f) sc[m][f] = __builtin_amdgcn_mfma_f32_16x16x16bf16_1k(half(V[m + 1], f), pf[1], sc[m][f], 0, 0, 0);
    }
    __builtin_amdgcn_sched_barrier(0);
    STAMP(10);
    const auto rO = __builtin_amdgcn_make_buffer_rsrc((void*)a.O, 0, 0x7fffffff, 0x00020000);
    u32x4 ov[4];
#pragma unroll
    for (int m = 3; m >= 0; --m) {
      ov[m] = (u32x4){pk_bf16(sc[m][0][0], sc[m][0][1]), pk_bf16(sc[m][0][2], sc[m][0][3]), pk_bf16(sc[m][1][0], sc[m][1][1]), pk_bf16(sc[m][1][2], sc[m][1][3])};
      __builtin_amdgcn_raw_buffer_store_b128(ov[m], rO, (((m0 >> 4) + wm * 4 + m) * RBO + tn * 8 + hd * 4) * 256 + lz * 16, 0, 16);
    }
    asm volatile("" :: "v"(ov[0]), "v"(ov[1]), "v"(ov[2]), "v"(ov[3]));
    if (KN_QP) __builtin_amdgcn_s_setprio(0);
    STAMP(7);
    wait_vm<0>();
    STAMP(8);
    raw_barrier();
    if (tid == 256) __hip_atomic_fetch_add(a.sync + SYO(tm), FIN, __ATOMIC_RELAXED, __HIP_MEMORY_SCOPE_AGENT);
  }
  STAMP(3);
#endif
}

struct FragW { s16x4 lo[4], hi[4]; };
__device__ __forceinline__ void wo_phase(const GemmArgs& a, const GemmArgs& nx) {
#if defined(__HIP_DEVICE_COMPILE__)
  extern __shared__ __attribute__((aligned(16))) char smem[];
  STAMP(0);
  const unsigned lds0 = (unsigned)(size_t)(lds_char_ptr)smem;
  const int tid = threadIdx.x, lane = tid & 63;
  const int wave = __builtin_amdgcn_readfirstlane(tid >> 6);
  const int bid = blockIdx.x, xcd = bid & 7, idx = bid >> 3;
  const int tm = (xcd >> 1) * 4 + (idx >> 3), tn = (xcd & 1) * 8 + (idx & 7);
  const int m0 = tm * 128, panel = m0 >> 8, rot = (xcd & 3) * 4;
  {
    constexpr int PAD = QTCH;
    const int g = wave >> 2, rw = wave & 3;
    const int fr = lane & 15, fq = lane >> 4, wq = (lane >> 2) & 3, wp = lane & 3;
    unsigned wa[4];
#pragma unroll
    for (int c = 0; c < 4; ++c) { const int row = 4 * fq + wq; wa[c] = lds0 + g * 8192 + row * 128 + (((c << 1) ^ (row & 6)) << 4) + wp * 8; }
    FragW f[3];
    auto rd = [&](auto ic, auto rc) {
      constexpr int i = decltype(ic)::value, r = decltype(rc)::value, P = wo_pos(i >> 1) + (i & 1) * 4096, B = P & ~32767, o = P & 32767;
      if constexpr (r & 1) f[i % 3].hi[r >> 1] = ds_tr<o + 2048>(wa[r >> 1] + B); else f[i % 3].lo[r >> 1] = ds_tr<o>(wa[r >> 1] + B);
    };
    auto pin = [&](FragW& q) {
#pragma unroll
      for (int n = 0; n < 4; ++n) asm volatile("" : "+v"(q.lo[n]), "+v"(q.hi[n]));
    };
    static_for<0, 16>([&](auto rc) { constexpr int r = decltype(rc)::value; rd(std::integral_constant<int, r / 8>{}, std::integral_constant<int, r % 8>{}); });
    if constexpr (KN_CT & 4) { if (wave == 2) {
      unsigned long long c0, c1; CODE_ADDR(c0, "k_all"); CODE_ADDR(c1, "k_zend");
      wait_vm<0>();
      __builtin_amdgcn_raw_ptr_buffer_load_lds(__builtin_amdgcn_make_buffer_rsrc((void*)c0, 0, c1 - c0 < 262144ull ? (unsigned)(c1 - c0) : 0u, 0x00020000), (lds_void_ptr)(size_t)(lds0 + PAD), 4, (unsigned)((lane * 32 + idx) * 128), 0, 0, 0); } }
    if (wave == 1) wait_count(lds0, a.sync + SYO(tm), snap_get(lds0, 0), 16u, 1u);
    if constexpr (KN_E & 2) {
      if (wave == 3) {
        const unsigned* c2 = a.sync + ((lane & 1) ? (xcd >> 2) * 4 + (idx >> 3) : panel) * 64;
        const unsigned sa = snap_get(lds0, 6), sb = snap_get(lds0, 1), s0 = (lane & 1) ? sb : sa;
        while (__builtin_amdgcn_ballot_w64(((__hip_atomic_load(c2, __ATOMIC_RELAXED, __HIP_MEMORY_SCOPE_AGENT) - s0) & 0xffffu) < 32u) != 0 && in_time(lds0, 1u)) {}
      }
    }
    const auto rX = __builtin_amdgcn_make_buffer_rsrc((void*)a.X, 0, 0x7fffffff, 0x00020000);
    const int rb = (m0 >> 4) + rw * 2 + g;
    unsigned xs[2] = {(unsigned)(rb * RBO * 256 + lane * 16), (unsigned)((rb ^ 1) * RBO * 256 + lane * 16)};
    const int ck = (fq & 1) * 2 + (fq >> 1);
    unsigned orow = (unsigned)((rb * 16 + fr) * (D_ * 2) + tn * 128 + ck * 16), ofrg = (unsigned)((rb * RBH + tn * 8 + ck) * 256 + fr * 16);
    asm volatile("" : "+v"(xs[0]), "+v"(xs[1]), "+v"(orow), "+v"(ofrg));
    STAMP(9);
    if constexpr (!KN_B3) { if (!(KN_CT & 4) || wave != 2) wait_vm<0>(); }
    lds_barrier();
    STAMP(6);
    pin(f[0]); pin(f[1]);
    u32x4 xr[16][2], res[2];
#define KN_XD 4
    auto ldX = [&](int i, int m) {
      if (i < 16) xr[i][m] = __builtin_amdgcn_raw_buffer_load_b128(rX, xs[m] + (i & 1) * 1024, ((g * 8 + (i >> 1)) ^ rot) * 2048, XAUXW);
      else if (i == 16) res[m] = __builtin_amdgcn_raw_buffer_load_b128(__builtin_amdgcn_make_buffer_rsrc((void*)a.R, 0, 0x7fffffff, 0x00020000), orow + m * 64, 0, 0);
    };
#pragma unroll
    for (int i = 0; i < KN_XD; ++i) { ldX(i, 0); ldX(i, 1); __builtin_amdgcn_sched_barrier(0); }
    unsigned gsrc[2];
#pragma unroll
    for (int i = 0; i < 2; ++i) {
      const int j = wave + 8 * i, jj = j >= 7 ? j - 7 : j;
      const int b = jj * 1024 + lane * 16, q = b / 224, pos = (b % 224) >> 4;
      gsrc[i] = (unsigned)((8 * ((q & 15) >> 2) + (q & 3) + 4 * (q >> 4)) * DF_ + ((xcd & 3) * 8 + (idx & 7)) * 112 + pos * 8) * 2u;
      asm volatile("" : "+v"(gsrc[i]));
    }
    f32x4 acc[4][2];
#pragma unroll
    for (int n = 0; n < 4; ++n) { acc[n][0] = (f32x4){0.f, 0.f, 0.f, 0.f}; acc[n][1] = acc[n][0]; }
    __builtin_amdgcn_sched_barrier(0);
    static_for<0, 16>([&](auto ic) {
      constexpr int i = decltype(ic)::value;
      if constexpr (i >= 2) { asm volatile("s_waitcnt lgkmcnt(%0)" :: "n"(i < 15 ? 8 : 0) : "memory"); pin(f[i % 3]); }
      if constexpr (KN_B3 && i == (KN_WB & ~1) - 2) raw_barrier();
      static_for<0, 8>([&](auto jc) {
        constexpr int j = decltype(jc)::value, n = j >> 1, m = j & 1;
        const bf16x8 w = __builtin_shufflevector(f[i % 3].lo[n], f[i % 3].hi[n], 0, 1, 2, 3, 4, 5, 6, 7);
        acc[n][m] = __builtin_amdgcn_mfma_f32_16x16x32_bf16(w, __builtin_bit_cast(bf16x8, xr[i][m]), acc[n][m], 0, 0, 0);
        if constexpr (i + 2 < 16) rd(std::integral_constant<int, i + 2>{}, jc);
        if constexpr (j == 1 || j == 5) ldX(i + KN_XD, j >> 2);
        __builtin_amdgcn_sched_barrier(0);
      });
      if constexpr (i == 0) STAMP(1);
    });
    STAMP(2);
    auto slot = [&](int w) { return 32768 + w * 4096 + lane * 16; };
#pragma unroll
    for (int n = 0; n < 4; ++n) *(f32x4*)(smem + slot(wave) + n * 1024) = acc[n][1];
    asm volatile("" : "+v"(res[0]), "+v"(res[1]));
    lds_barrier();
    STAMP(4);
    f32x4 pa[4];
    static_for<0, 4>([&](auto nc) { constexpr int n = decltype(nc)::value; pa[n] = __builtin_bit_cast(f32x4, ds_b128<n * 1024>(lds0 + slot(wave ^ 4))); });
    if constexpr (KN_GW) {
      auto ldG = [&](int i, int u) { const int j = wave + 8 * i;
        __builtin_amdgcn_raw_ptr_buffer_load_lds(__builtin_amdgcn_make_buffer_rsrc((void*)(j >= 7 ? nx.W1 : nx.W0), 0, 0x7fffffff, 0x00020000),
                          (lds_void_ptr)(size_t)(lds0 + 98304 + (u ^ 2) * 14592 + (j >= 7 ? 7184 + (j - 7) * 1024 : j * 1024)), 16, gsrc[i], (u + (xcd >> 2) * 16) * 32 * DF_ * 2, 0, 0); };
      ldG(0, 0); ldG(0, 1);
      if (wave < 6) { ldG(1, 0); ldG(1, 1); }
    }
    asm volatile("s_waitcnt lgkmcnt(0)" : "+v"(pa[0]), "+v"(pa[1]), "+v"(pa[2]), "+v"(pa[3]) :: "memory");
    const auto rO = __builtin_amdgcn_make_buffer_rsrc((void*)a.O, 0, 0x7fffffff, 0x00020000);
    u32x4 o[2];
#pragma unroll
    for (int i = 0; i < 2; ++i) {
      const f32x4 u = acc[2 * i][0] + pa[2 * i], v = acc[2 * i + 1][0] + pa[2 * i + 1];
      const auto s0 = __builtin_amdgcn_permlane16_swap(pk_bf16(u[0], u[1]), pk_bf16(v[0], v[1]), false, false);
      const auto s1 = __builtin_amdgcn_permlane16_swap(pk_bf16(u[2], u[3]), pk_bf16(v[2], v[3]), false, false);
      const u32x4 q4 = {s0[0], s1[0], s0[1], s1[1]};
#pragma unroll
      for (int j = 0; j < 4; ++j) { const unsigned r = res[i][j], q = q4[j]; o[i][j] = pk_bf16(bflo(r) + bflo(q), bfhi(r) + bfhi(q)); }
      __builtin_amdgcn_raw_buffer_store_b128(o[i], rO, ofrg + i * 1024, 0, 16);
    }
    auto sum4 = [](float v) {
      auto p = __builtin_amdgcn_permlane16_swap(__float_as_uint(v), __float_as_uint(v), false, false); v = __uint_as_float(p[0]) + __uint_as_float(p[1]);
      auto q = __builtin_amdgcn_permlane32_swap(__float_as_uint(v), __float_as_uint(v), false, false); return __uint_as_float(q[0]) + __uint_as_float(q[1]); };
    f32x2 e[8], sm = {0.f, 0.f};
#pragma unroll
    for (int j = 0; j < 8; ++j) { const unsigned q = o[j >> 2][j & 3]; e[j] = (f32x2){bflo(q), bfhi(q)}; sm += e[j]; }
    const float mu = sum4(sm[0] + sm[1]) * (1.f / 64.f);
    f32x2 t2 = {0.f, 0.f};
#pragma unroll
    for (int j = 0; j < 8; ++j) { const f32x2 d = e[j] - mu; t2 += d * d; }
    const float q2 = sum4(t2[0] + t2[1]);
    __builtin_amdgcn_raw_buffer_store_b64((u32x2){__float_as_uint(mu), __float_as_uint(q2)}, __builtin_amdgcn_make_buffer_rsrc((void*)a.st, 0, 0x7fffffff, 0x00020000),
                                          lane < 16 ? (unsigned)((tn * S_ + rb * 16 + lane) * 8) : 0x80000000u, 0, 16);
    STAMP(7);
    STAMP(8);
    asm volatile("s_waitcnt vmcnt(0) lgkmcnt(0)" ::: "memory");
    raw_barrier();
    if (tid == 0) __hip_atomic_fetch_add(a.sync + panel * 64, FIN, __ATOMIC_RELAXED, __HIP_MEMORY_SCOPE_AGENT);
    STAMP(3);
  }
#endif
}

struct FragE { bf16x8 x[4]; s16x4 lo[4], hi[4]; };
template <int A, int NT, int DX, int DW, int PW> struct Sched2 {
  static constexpr int nw(int t) { return (t + DW >= PW && t + DW < NT) ? (t + DW < A ? 4 : 2) : 0; }
  static constexpr int nx(int t) { return t + DX < NT ? 4 : 0; }
  static constexpr int cnt(int t) {
    if (t + 1 >= NT) return 63;
    int tot = 0, pos = 0;
    for (int i = 0; i < DX - 1; ++i) { tot += 4; if (i == t + 1) pos = tot; }
    for (int i = -1; i < t; ++i) {
      tot += nw(i); if (nw(i) && i + DW == t + 1) pos = tot;
      tot += nx(i); if (nx(i) && i + DX == t + 1) pos = tot;
    }
    return tot - pos;
  }
  static constexpr bool same(int t, int r) { return nw(t) == nw(r) && nx(t) == nx(r) && cnt(t) == cnt(r); }
  static constexpr int lo(int r) { int t = r; while (t > 0 && same(t - 1, r)) --t; return t; }
  static constexpr int hi(int r) { int t = r; while (same(t + 1, r)) ++t; return t + 1; }
};
template <int MODE, int A, int DX, int DW, int PW, int HTD, int TA, int TB, int TL = 0>
__device__ __forceinline__ void dn_phase(const GemmArgs& a) {
#if defined(__HIP_DEVICE_COMPILE__)
  constexpr int NT = DF_ / 64 - A, SX = DX + 1, SW = 4, WB = SX * 16384, PAD = WB + SW * 16384;
  static_assert(A % 2 == 0 && NT % 2 == 0 && PW <= SW && PW <= A && TA % 2 == 0 && TB % 2 == 0 && TA >= 2 && TB >= TA && A + TB + 6 <= NT, "schedule");
  using SC = Sched2<A, NT, DX, DW, PW>;
  extern __shared__ __attribute__((aligned(16))) char smem[];
  if constexpr (MODE != 1) STAMP(0);
  const unsigned lds0 = (unsigned)(size_t)(lds_char_ptr)smem;
  const int tid = threadIdx.x;
  int lane = tid & 63; asm volatile("" : "+v"(lane));
  const int wave = __builtin_amdgcn_readfirstlane(tid >> 6);
  const int bid = blockIdx.x, xcd = bid & 7, idx = bid >> 3;
  const int tm = (xcd >> 1) * 4 + (idx >> 3), tn = (xcd & 1) * 8 + (idx & 7);
  const int m0 = tm * 128, panel = m0 >> 8;
  unsigned* pcnt = a.sync + (8 + (xcd >> 2) * 4 + (idx >> 3)) * 64;
  const unsigned orow = (unsigned)((m0 + wave * 16 + (lane >> 3)) * (D_ * 2) + tn * 128 + (lane & 7) * 16);
  const unsigned ofrg = (unsigned)((((m0 >> 4) + wave) * RBH + tn * 8 + (lane & 7)) * 256 + (lane >> 3) * 16);
  u32x4 res[2];
  auto res_loads = [&]() {
    const auto rR = __builtin_amdgcn_make_buffer_rsrc((void*)a.R, 0, 0x7fffffff, 0x00020000);
#pragma unroll
    for (int i = 0; i < 2; ++i) res[i] = __builtin_amdgcn_raw_buffer_load_b128(rR, ofrg, i * 128, 16);
  };
  constexpr int STG = ((NT - 2) % SX) * 16384;
  if (MODE == 1 || wave < 4) {
    const int lw = wave & 3;
    static_assert((KN_E & 4) && A == 24, "walk");
    constexpr int V = 4;
    const int ra = (xcd & 3) * 8, rb = (xcd & 3) * 6;
    auto ktile2 = [&](int t, int e) {
      const int v1 = (ra + t + 4 + 12 * e - (((t - 4) >> 31) & (4 + 8 * e))) & 31, q = rb + t - 8 - (((t - 24) >> 31) & (8 - 8 * e)), v2 = q + 8 + (((q - 24) >> 31) & 24), m = (t - 16) >> 31;
      return (v1 & m) | (v2 & ~m); };
    auto ktile = [&](int t) { return ktile2(t, idx & 1); };
    auto wrow = [&](int kt, int j) { const int p = kt * 8 + j, b = p - 256, v1 = p + 3 * (p >> 2), v2 = b + 4 * ((b * 21846) >> 16) + 4, m = b >> 31; return ((v1 & m) | (v2 & ~m)) * 8; };
    const auto rW = __builtin_amdgcn_make_buffer_rsrc((void*)a.W0, 0, 0x7fffffff, 0x00020000);
    const u32x4 qX = mk_rsrc(a.X), qW = mk_rsrc((const char*)a.W0 - 4096);
    const unsigned xv0 = (unsigned)(((m0 >> 4) + lw * 2) * RBA * 256 + lane * 16), xv1 = xv0 + (RBA * 256 - 2048);
    unsigned wv[4];
    { const unsigned w0 = (unsigned)((lane >> 3) * D_ + tn * 64 + ((lane & 7) ^ wswz<128>(lane >> 3)) * 8) * 2u;
#pragma unroll
      for (int i = 0; i < 4; ++i) wv[i] = (w0 ^ (i * 64)) + (4096 - i * 1024); }
    const unsigned mX = lds0 + lw * 4096, mW = mX + WB;
    auto ldXo = [&](int so, int slot) { ld_x4(mX + slot * 16384, qX, xv0, xv1, so); };
    int tbx = 0, tb0 = 0, tb1 = 0;
    auto tables = [&]() { const int kt = ktile(lane & 31); tbx = kt * 2048; tb0 = wrow(kt, 2 * lw) * (D_ * 2); tb1 = wrow(kt, 2 * lw + 1) * (D_ * 2); asm volatile("" : "+v"(tbx), "+v"(tb0), "+v"(tb1)); };
    auto ldX = [&](int t, int slot) { int so = __builtin_amdgcn_readlane(tbx, t); asm volatile("s_nop 3" : "+s"(so)); ldXo(so, slot); };
    int o0, o1, ox;
    auto offs = [&](int t) { o0 = __builtin_amdgcn_readlane(tb0, t + DW); o1 = __builtin_amdgcn_readlane(tb1, t + DW); ox = __builtin_amdgcn_readlane(tbx, t + DX); asm volatile("s_nop 1" : "+s"(o0), "+s"(o1), "+s"(ox)); };
    auto ldW = [&](int slot, auto P1c) { if constexpr (decltype(P1c)::value) ld_w4<-1>(mW + slot * 16384, qW, wv, o0, o1); else ld_w2<-1>(mW + slot * 16384, qW, wv, o0, o1); };
    const unsigned* fcnt = a.sync + (8 + panel) * 64;
    auto ldF = [&](int o) { __builtin_amdgcn_raw_ptr_buffer_load_lds(__builtin_amdgcn_make_buffer_rsrc((void*)fcnt, 0, 4, 0x00020000), (lds_void_ptr)(size_t)(lds0 + PAD + o), 4, lane * 4, 0, 0, 16); };
    auto rdF = [&](int o) { int fv = ds_r32(lds0 + PAD + o); asm volatile("s_waitcnt lgkmcnt(0)" : "+v"(fv) :: "memory"); return (unsigned)__builtin_amdgcn_readfirstlane(fv); };
    const int kf = ra + (idx & 1) * 4;
    int ht; unsigned htv;
    auto hset = [&]() {
      int idh = idx; asm volatile("" : "+s"(idh));
      const int slc = (3 - ((unsigned)(size_t)a.W0 >> 7)) & 7, pr = (idh & 7) >> 1;
      const bool hlp = pr != (slc >> 1) && lw < 3;
      const int hk = ((idh >> 3) * 3 + pr - (pr > (slc >> 1) ? 1 : 0)) * 2 + (idh & 1) + lw * 24;
      const int htau = hk < 48 ? hk >> 1 : hk - 24, hkt = ktile2(htau < 32 ? htau : 0, hk & 1);
      ht = (hlp && hk < DF_ / 64 && htau >= KN_WS) ? htau - HTD : 1000;
      htv = (unsigned)((wrow(hkt, lane >> 3) + (lane & 7)) * (D_ * 2) + ((xcd & 1) * 8 + slc) * 128);
    };
    auto help = [&]() { __builtin_amdgcn_raw_ptr_buffer_load_lds(rW, (lds_void_ptr)(size_t)(lds0 + FPAD - 256), 4, htv, 0, 0, 0); };
    auto ldW0 = [&](int i) { const int so = ((kf + i) * 112 + (lw >> 1) * 56 + (lw & 1) * 16) * (D_ * 2); ld_w4<-1>(mW + i * 16384, qW, wv, so, so + 8 * D_ * 2); };
    if constexpr (MODE == 1) {
      static_assert(PW == 4, "first tiles");
      if constexpr (TL == 1) ldF(FPAD - PAD);
      if constexpr (TL == 2) { if (lw == 0) { hset(); if (ht < -1) help(); } }
      if constexpr (TL == 0 || TL == 3) { ldW0(TL ? 2 : 0); ldW0(TL ? 3 : 1); }
      return;
    }
#define TS0
#define TS1(T)
    int f0v = ds_r32(lds0 + SNAP + 16);
    wait_vm<3>();
    lds_inc(lds0 + ACNT);
    STAMPW(4, 0);
    const unsigned f1 = rdF(FPAD - PAD);
    asm volatile("" : "+v"(f0v));
    const unsigned f0 = __builtin_amdgcn_readfirstlane(f0v);
    const bool early = ((f1 - f0) & 0xffffu) >= 64u;
    lds_wait8(lds0 + ACNT);
    STAMPW(9, 0);
#define KN_X0 0
    if (tid == 192) __hip_atomic_fetch_add(pcnt, 1u << 16, __ATOMIC_RELAXED, __HIP_MEMORY_SCOPE_AGENT);
    asm volatile("" : "+v"(lane) :: "memory");
    hset();
    tables();
    asm volatile("" :: "s"(ht), "v"(htv));
    lds_wait8(lds0 + ACNT + 4, 4);
    STAMPW(5, 0);
#pragma unroll
    for (int i = 0; i < 3; ++i) ldXo((kf + i) * 2048, i);
#pragma unroll
    for (int i = KN_WS; i < 4; ++i) ldW0(i);
    asm volatile("" ::: "memory");
    static_assert(HTD <= 11, "only the first loader wave helps that early");
    if (!KN_HE && ht < -1) help();
    {
      static_assert(DX == 4 && DW == 4 && PW == 4 && KN_WS == 2 && SC::cnt(3) == 16 && SC::cnt(4) == 16 && V == DX, "schedule");
      int x4 = 0;
#pragma unroll 1
      for (int t = -1; t < V - 1; ++t) {
        static_assert(KN_WS == 4 || KN_WS == 2, "W tiles");
        offs(t);
        if (t == 1) wait_vm<8>(); else wait_vm<16>();
#ifndef KN_FL
#define KN_FL 0
#endif
        if (!KN_FL && t == -1 && lw == 0) ldF(256);
        if (__builtin_expect(marked<4, 1024>(mX + (t + 1) * 16384 + lane * 16) || !early, 0)) {
          for (int tries = early ? 0 : KN_TRY;; ++tries) {
            if (tries >= KN_TRY) wait_field(lds0, fcnt, f0, 16, 4u);
#pragma unroll 1
            for (int u = t + 1; u < V && u < t + DX; ++u) ldX(u, u);
            if (KN_WS == 4 && t < 1) wait_vm<8>(); else wait_vm<0>();
            if (tries >= KN_TRY || !marked<4, 1024>(mX + (t + 1) * 16384 + lane * 16)) break;
          }
        }
        if (t == -1) STAMPW(6, 0);
        if (t == 1 && lw == 0) {
          if ((((rdF(256) - f0) >> 16) & 255u) < 32u) wait_field(lds0, fcnt, f0, 16, 4u);
        }
        raw_barrier(); TS0;
        if (KN_FL && t == -1 && lw == 0) { ldF(256); asm volatile("" ::: "memory"); }
        if (t == 1) ldXo(x4, 4);
        x4 = ox;
        if (t >= 0) ldW((t + DW) & 3, std::true_type{});
        if (t != 0) ldXo(ox, t >= 1 ? t - 1 : t + DX);
        if (t == ht) help();
        TS1(t + 1);
      }
    }
    auto fixed = [&](auto tc) {
      constexpr int t = decltype(tc)::value;
#define KN_TR 4
      if constexpr (SC::nw(t) > 0) offs(t);
      wait_vm<(t > NT - KN_TR && t < NT - 1) ? SC::cnt(t) + 2 : SC::cnt(t)>();
      raw_barrier(); TS0;
      static_assert(SC::nx(t) == 0 || SC::nw(t) > 0, "offsets");
      if constexpr (SC::nw(t) > 0) ldW((t + DW) % SW, std::integral_constant<bool, (t + DW < A)>{});
      if constexpr (SC::nx(t) > 0) ldXo(ox, (t + DX) % SX);
      if constexpr (t + HTD < NT) { if (t == ht) help(); }
      TS1(t + 1);
      if constexpr (t == NT - KN_TR) { static_assert(SC::nw(t + 1) == 0 && SC::nx(t + 1) == 0, "last loads"); asm volatile("" ::: "memory"); res_loads(); }
    };
    const int tf = lw == 0 ? 13 - DX : 1000;
    auto steady = [&](auto Lc, auto Hc) {
      constexpr int L = decltype(Lc)::value, H = decltype(Hc)::value;
      unsigned sx = mX + (L + DX) % SX * 16384, sw = (L + DW) % SW * 16384;
      constexpr bool P1 = L + DW < A;
      static_assert(DX == DW && SW == 4, "offsets");
      offs(L);
#pragma unroll 1
      for (int t = L; t < H; ++t) {
        if constexpr (L <= 8) { static_assert(H > 16 - DX, "check"); if (__builtin_expect(t == tf + 3, 0)) { wait_vm<SC::cnt(L)>(); if ((rdF(256) - f0) >> 24 < 32u) wait_field(lds0, fcnt, f0, 24, 5u); } }
        if constexpr (P1) ld_w4<SC::cnt(L)>(mW + sw, qW, wv, o0, o1); else ld_w2<SC::cnt(L)>(mW + sw, qW, wv, o0, o1);
        TS0;
        __builtin_amdgcn_sched_barrier(0);
        if (__builtin_expect(t == ht, 0)) help();
        if constexpr (L <= 8) { if (__builtin_expect(t == tf, 0)) ldF(256); }
        const int oy = ox; const unsigned sy = sx;
        offs(t + 1);
        sw = (sw + 16384) & 65535; sx = sx + 16384; sx = sx >= mX + SX * 16384 ? mX : sx;
        asm volatile("" : "+s"(sw), "+s"(sx));
        TS1(t + 1);
        __builtin_amdgcn_sched_barrier(0);
        ld_x4(sy, qX, xv0, xv1, oy);
      }
    };
    constexpr int L1 = V ? V - 1 : SC::lo(A / 2), H1 = SC::hi(A / 2), R2 = NT - (DX > DW ? DX : DW) - 1, L2 = SC::lo(R2), H2 = SC::hi(R2);
    static_assert(SC::nw(L1) == 4 && SC::nx(L1) == 4 && SC::nw(L2) == 2 && SC::nx(L2) == 4 && H1 <= L2, "steady states");
    if constexpr (!V) static_for<0, L1 + 1>([&](auto ic) { fixed(std::integral_constant<int, decltype(ic)::value - 1>{}); });
    steady(std::integral_constant<int, L1>{}, std::integral_constant<int, H1>{});
    static_for<H1, L2>(fixed);
    steady(std::integral_constant<int, L2>{}, std::integral_constant<int, H2>{});
    static_for<H2, NT>(fixed);
    asm volatile("" : "+v"(res[0]), "+v"(res[1]));
  } else {
    if constexpr (MODE == 1) return;
    const int fr = lane & 15, fq = lane >> 4, wq = (lane >> 2) & 3, wp = lane & 3, cw = wave & 3, wm = cw >> 1, wn = cw & 1;
    const unsigned f0 = snap_get(lds0, 5);
#define KN_DP 0
    if (KN_DP) __builtin_amdgcn_s_setprio(KN_DP);
    f32x4 acc[4][4];
#pragma unroll
    for (int n = 0; n < 4; ++n)
#pragma unroll
      for (int m = 0; m < 4; ++m) acc[n][m] = (f32x4){0.f, 0.f, 0.f, 0.f};
    const unsigned xa = lds0 + wm * 8192 + lane * 16;
    unsigned wa[2];
#pragma unroll
    for (int c = 0; c < 2; ++c) { const int row = 8 * fq + wq; wa[c] = lds0 + WB + (row >> 4) * 4096 + (row & 15) * 128 + ((((wn * 2 + c) << 1) ^ wswz<128>(row)) << 4) + wp * 8; }
    struct Ctx { unsigned x, w[2]; };
    auto mk = [&](unsigned xo, unsigned wo) { Ctx c; c.x = xa + xo; c.w[0] = wa[0] + wo; c.w[1] = wa[1] + wo; return c; };
    auto rd = [&](FragE& f, const Ctx& c, auto KSc, auto kc) {
      constexpr int KS = decltype(KSc)::value, k = decltype(kc)::value;
      if constexpr (k < 4) f.x[k] = ds_b128<KS * 1024 + k * 2048>(c.x);
      else { constexpr int n = k - 4, o = KS * 8192 + (n >> 1) * 2048; f.lo[n] = ds_tr<o>(c.w[n & 1]); f.hi[n] = ds_tr<o + 512>(c.w[n & 1]); }
    };
    auto pin = [&](FragE& f, auto Nc) {
#pragma unroll
      for (int m = 0; m < 4; ++m) asm volatile("" : "+v"(f.x[m]));
#pragma unroll
      for (int n = 0; n < decltype(Nc)::value; ++n) asm volatile("" : "+v"(f.lo[n]), "+v"(f.hi[n]));
    };
    using I0 = std::integral_constant<int, 0>; using I1 = std::integral_constant<int, 1>; using I2 = std::integral_constant<int, 2>; using I4 = std::integral_constant<int, 4>;
    FragE fa, fb, fc, fd;
    raw_barrier();
    { const Ctx c = mk(0, 0); static_for<0, 8>([&](auto kc) { rd(fa, c, I0{}, kc); }); asm volatile("s_waitcnt lgkmcnt(4)" ::: "memory"); static_for<0, 8>([&](auto kc) { rd(fb, c, I1{}, kc); }); }
    asm volatile("s_waitcnt lgkmcnt(0)" ::: "memory");
    pin(fa, I4{}); pin(fb, I4{});
    STAMP(1);
    static_assert((SW & (SW - 1)) == 0, "W ring");
    int sx = 16384, sw = 16384;
    Ctx cn = mk(sx, sw);
#define KN_SP4 26
#define KN_IL 1
#define KN_IB 1
    auto rd1 = [&](FragE& f, const Ctx& c, auto KSc, auto rc) {
      constexpr int KS = decltype(KSc)::value, r = decltype(rc)::value;
      if constexpr (r < 4) f.x[r] = ds_b128<KS * 1024 + r * 2048>(c.x);
      else { constexpr int n = (r - 4) >> 1, o = KS * 8192 + (n >> 1) * 2048; if constexpr (r & 1) f.hi[n] = ds_tr<o + 512>(c.w[n & 1]); else f.lo[n] = ds_tr<o>(c.w[n & 1]); }
    };
    auto tile = [&](FragE& c0, FragE& c1, FragE& n0, FragE& n1, auto NCc, auto NNc, auto&& post) {
      constexpr int NC = decltype(NCc)::value, NN = decltype(NNc)::value, RI = 4 + NN, RG = NC == 4 ? 3 : 2;
      constexpr int R1 = 4 + 2 * NN, NM = 8 * NC;
      const Ctx c = cn;
      static_for<0, NM>([&](auto ic) {
        constexpr int I = decltype(ic)::value, G = I / 4, m = I % 4, ks = G / NC, n = G % NC;
        FragE& cur = ks ? c1 : c0;
        constexpr int SP = NC == 4 ? (NN == 4 ? KN_SP4 : 20) : 12;
        constexpr int before = I <= KN_IB ? 0 : ((I - KN_IB) * 2 * R1 / SP < 2 * R1 ? (I - KN_IB) * 2 * R1 / SP : 2 * R1);
        constexpr int after = I + 1 <= KN_IB ? 0 : ((I + 1 - KN_IB) * 2 * R1 / SP < 2 * R1 ? (I + 1 - KN_IB) * 2 * R1 / SP : 2 * R1);
        if constexpr (I == NM / 2) { asm volatile("s_waitcnt lgkmcnt(%0)" :: "n"(before < 15 ? before : 15) : "memory"); pin(c1, NCc); }
        const bf16x8 w = __builtin_shufflevector(cur.lo[n], cur.hi[n], 0, 1, 2, 3, 4, 5, 6, 7);
        acc[n][m] = __builtin_amdgcn_mfma_f32_16x16x32_bf16(w, cur.x[m], acc[n][m], 0, 0, 0);
        __builtin_amdgcn_sched_barrier(0);
        if constexpr (I + 1 == KN_IB) { asm volatile("s_waitcnt lgkmcnt(0)" ::: "memory"); raw_barrier(); post(); }
        static_for<before, after>([&](auto rc) { constexpr int r = decltype(rc)::value;
          if constexpr (r < R1) rd1(n0, c, I0{}, rc); else rd1(n1, c, I1{}, std::integral_constant<int, r - R1>{}); });
        if constexpr (I == NM - 4) { sw = (sw + 16384) & (SW * 16384 - 1); asm volatile("" : "+s"(sw)); }
        if constexpr (I == NM - 3) { cn.w[0] = wa[0] + sw; cn.w[1] = wa[1] + sw; asm volatile("" : "+v"(cn.w[0]), "+v"(cn.w[1])); }
        if constexpr (I == NM - 5) { sx = sx + 16384; asm volatile("" : "+s"(sx)); }
        if constexpr (I == NM - 2) { sx = sx == SX * 16384 ? 0 : sx; asm volatile("" : "+s"(sx)); }
        if constexpr (I == NM - 1) { cn.x = xa + sx; asm volatile("" : "+v"(cn.x)); }
        __builtin_amdgcn_sched_barrier(0);
      });
      asm volatile("s_waitcnt lgkmcnt(%0)" :: "n"(R1) : "memory");
      pin(n0, NNc);
    };
    auto nop = [&]() {};
    for (int t = 0; t < A - 2; t += 2) {
      tile(fa, fb, fc, fd, I4{}, I4{}, [&]() { if (t == 2) { if (tid == 256) __hip_atomic_fetch_add(pcnt, 1u << 24, __ATOMIC_RELAXED, __HIP_MEMORY_SCOPE_AGENT); } });
      tile(fc, fd, fa, fb, I4{}, I4{}, nop);
      if (t == 0) wait_vm<0>();
    }
    tile(fa, fb, fc, fd, I4{}, I4{}, nop); tile(fc, fd, fa, fb, I4{}, I2{}, nop);
    const auto rS = __builtin_amdgcn_make_buffer_rsrc((void*)a.slab, 0, 0x7fffffff, 0x00020000);
    STAMP(10);
#pragma unroll
    for (int m = 0; m < 4; ++m)
      __builtin_amdgcn_raw_buffer_store_b128((u32x4){pk_bf16(acc[2][m][0], acc[2][m][1]), pk_bf16(acc[2][m][2], acc[2][m][3]), pk_bf16(acc[3][m][0], acc[3][m][1]), pk_bf16(acc[3][m][2], acc[3][m][3])},
                                             rS, (((tm * 16 + (tn ^ 1)) * 4 + cw) * 4 + m) * 1024 + lane * 16, 0, (KN_LX & 1) ? 0 : 16);
    auto pairs = [&](int n) {
#pragma unroll 1
      for (int t = 0; t < n; t += 2) { tile(fa, fb, fc, fd, I2{}, I2{}, nop); tile(fc, fd, fa, fb, I2{}, I2{}, nop); } };
    const unsigned* flag = a.sync + 2048 + bid * 32;
    u32x4 pp[4];
    auto ldP = [&]() {
#pragma unroll
      for (int i = 0; i < 4; ++i) pp[i] = __builtin_amdgcn_raw_buffer_load_b128(rS, (((tm * 16 + tn) * 4 + cw) * 4 + i) * 1024 + lane * 16, 0, 16);
    };
    if constexpr (KN_DM) {
      static_assert(KN_E & 4, "snapshot");
      pairs(KN_TP & ~1);
      STAMPV(4);
      static_for<(KN_TP & ~1), NT - A>([&](auto tc) {
        constexpr int t = decltype(tc)::value;
        auto post = [&]() { if constexpr (t == KN_TP) { ldP(); res_loads(); } };
        if constexpr (t & 1) tile(fc, fd, fa, fb, I2{}, I2{}, post); else tile(fa, fb, fc, fd, I2{}, I2{}, post);
      });
    } else {
    pairs(TA);
    STAMPV(11);
    wait_vm<0>();
    STAMPV(12);
    tile(fa, fb, fc, fd, I2{}, I2{}, [&]() { if (tid == 256) __hip_atomic_fetch_add(a.sync + 2048 + (bid ^ 8) * 32, 1u, __ATOMIC_RELAXED, (KN_LX & 2) ? __HIP_MEMORY_SCOPE_WORKGROUP : __HIP_MEMORY_SCOPE_AGENT); });
    tile(fc, fd, fa, fb, I2{}, I2{}, nop);
    if constexpr (TB > TA) pairs(TB - TA);
    unsigned fv = 1u;
    if (wave == 4) fv = __hip_atomic_load(flag, __ATOMIC_RELAXED, __HIP_MEMORY_SCOPE_AGENT);
    tile(fa, fb, fc, fd, I2{}, I2{}, nop);
    tile(fc, fd, fa, fb, I2{}, I2{}, nop);
    if (wave == 4) {
      while (__builtin_amdgcn_readfirstlane(fv) == f0 && in_time(lds0, 6u)) fv = __hip_atomic_load(flag, __ATOMIC_RELAXED, __HIP_MEMORY_SCOPE_AGENT);
    }
    STAMPV(4);
    tile(fa, fb, fc, fd, I2{}, I2{}, [&]() {
#pragma unroll
      for (int i = 0; i < 4; ++i) pp[i] = __builtin_amdgcn_raw_buffer_load_b128(rS, (((tm * 16 + tn) * 4 + cw) * 4 + i) * 1024 + lane * 16, 0, 16);
      res_loads();
    });
    tile(fc, fd, fa, fb, I2{}, I2{}, nop);
    if constexpr (NT - A - TB - 6 > 0) pairs(NT - A - TB - 6);
    }
    asm volatile("s_waitcnt lgkmcnt(0)" ::: "memory");
    pin(fa, I2{}); pin(fb, I2{});
    STAMP(2);
    if constexpr (KN_DM) {
      auto anym = [&]() {
        unsigned m = 0;
#pragma unroll
        for (int i = 0; i < 4; ++i) { m = max(max(m, pp[i][0]), pp[i][1]); m = max(max(m, pp[i][2]), pp[i][3]); }
        return __builtin_amdgcn_ballot_w64(m == 0xffffffffu) != 0;
      };
      asm volatile("s_waitcnt vmcnt(0)" : "+v"(res[0]), "+v"(res[1]) :: "memory");
      if (lane == 0) __hip_atomic_fetch_add(a.sync + 2048 + (bid ^ 8) * 32, FIN, __ATOMIC_RELAXED, __HIP_MEMORY_SCOPE_AGENT);
      if (__builtin_expect(anym(), 0)) {
        for (int tries = 0;; ++tries) {
          if (tries >= KN_TRY) wait_count(lds0, flag, f0, 4u, 6u);
          ldP();
          asm volatile("" : "+v"(pp[0]), "+v"(pp[1]), "+v"(pp[2]), "+v"(pp[3]));
          if (tries >= KN_TRY || !anym()) break;
        }
      }
      STAMPV(12);
    }
#pragma unroll
    for (int m = 0; m < 4; ++m)
#pragma unroll
      for (int n = 0; n < 2; ++n) {
        const unsigned q0 = pp[m][n * 2], q1 = pp[m][n * 2 + 1];
        const f32x4 v = acc[n][m] + (f32x4){bflo(q0), bfhi(q0), bflo(q1), bfhi(q1)};
        *(u32x2*)(smem + STG + (wm * 64 + m * 16 + fr) * 128 + ((((wn * 2 + n) * 2 + (fq >> 1)) ^ (fr & 7)) << 4) + (fq & 1) * 8) = (u32x2){pk_bf16(v[0], v[1]), pk_bf16(v[2], v[3])};
      }
    STAMPV(5);
    asm volatile("" : "+v"(res[0]), "+v"(res[1]));
  }
  lds_barrier();
  {
    const auto rO = __builtin_amdgcn_make_buffer_rsrc((void*)a.O, 0, 0x7fffffff, 0x00020000);
    u32x4 o[2];
    int lz = lane; asm volatile("" : "+v"(lz));
#pragma unroll
    for (int i = 0; i < 2; ++i) {
      const u32x4 v = *(const u32x4*)(smem + STG + (wave * 16 + i * 8 + (lz >> 3)) * 128 + (((lz & 7) ^ (lz >> 3)) << 4));
#pragma unroll
      for (int j = 0; j < 4; ++j) { const unsigned r = res[i][j], q = v[j]; o[i][j] = pk_bf16(bflo(r) + bflo(q), bfhi(r) + bfhi(q)); }
    }
    asm volatile("" : "+v"(o[0]), "+v"(o[1]));
#pragma unroll
#define KN_OAUX 16
    for (int i = 0; i < 2; ++i) __builtin_amdgcn_raw_buffer_store_b128(o[i], rO, orow, i * 8 * D_ * 2, KN_OAUX);
  }
  STAMP(7);
  STAMP(8);
  STAMP(3);
#endif
}

struct Ptrs { const bf16_t *x, *wqkv, *wo, *wg, *wu, *wd; bf16_t* out; char* ws; };
#define DBG(i)
#define WS_O ((bf16_t*)(p.ws + (8u << 20)))
#define WS_SL (KN_DM == 1 ? (float*)(p.ws + (12u << 20)) : (float*)WS_O)
#define WS_H ((bf16_t*)(p.ws + (16u << 20)))
#define WS_ST ((float*)(p.ws + (24u << 20)))
#define WS_CS ((float*)(p.ws + (25u << 20)))
#define WS_SY ((unsigned*)(p.ws + (26u << 20)))
#define WS_A ((bf16_t*)(p.ws + (28u << 20)))
#if defined(__HIP_DEVICE_COMPILE__) && !defined(KN_NOWARM)
#define ARG_WARM(v) do { asm volatile("s_load_dword %0, %1, 0x0" : "=s"(v) : "s"(__builtin_amdgcn_kernarg_segment_ptr())); __builtin_amdgcn_sched_barrier(0); } while (0)
#else
#define ARG_WARM(v)
#endif
struct Args4 { GemmArgs q, b, c, d; };
extern "C" __global__ __launch_bounds__(512, 1) void k_all(Ptrs p) {
  unsigned warm = 0;
  ARG_WARM(warm);
  const Args4 a{GemmArgs{p.x, p.wqkv, nullptr, nullptr, WS_O, (float*)WS_H, p.wg, p.wu, WS_CS, p.wd, p.wo, WS_SY, (float*)WS_A, warm DBG(0)},
    GemmArgs{WS_O, p.wo, nullptr, p.x, WS_H, WS_ST, nullptr, nullptr, nullptr, p.wd, nullptr, WS_SY, nullptr, 0u DBG(1)},
    GemmArgs{WS_H, p.wg, p.wu, p.out, WS_A, WS_ST, nullptr, nullptr, WS_CS, p.wd, nullptr, WS_SY, WS_SL, 0u DBG(2)},
    GemmArgs{WS_A, p.wd, nullptr, WS_H, p.out, nullptr, nullptr, nullptr, nullptr, nullptr, nullptr, WS_SY, WS_SL, 0u DBG(3)}};
#define KN_WDX 4
#define KN_WHT 8
#define KN_DDX 4
#define KN_DDW 8
#define KN_DPW 9
#define KN_DHT 14
#define KN_WPW 5
  k1_phase(a.q);
  wo_phase(a.b, a.c);
#define KN_A 24
#define KN_EX 4
#define KN_EW 4
#define KN_EH 10
#define KN_TA 2
#define KN_TB 2
  auto pre = [&](auto tc) { dn_phase<1, KN_A, KN_EX, KN_EW, 4, KN_EH, KN_TA, KN_TB, decltype(tc)::value>(a.d); };
  gemm_phase<EPI_GLU, 256, 28, 4, 2, 1024, 3584, 8, 32, 4, 8, 32, 3, 4, 1>(a.c, pre);
  dn_phase<2, KN_A, KN_EX, KN_EW, 4, KN_EH, KN_TA, KN_TB>(a.d);
}

extern "C" __global__ void k_zend() {}

extern "C" void kernel_launch(void* const* d_in, const int* in_sizes, int n_in, void* d_out, int out_size, void* d_ws,
                              size_t ws_size, hipStream_t stream) {
  const Ptrs p{(const bf16_t*)d_in[0], (const bf16_t*)d_in[1], (const bf16_t*)d_in[2], (const bf16_t*)d_in[4], (const bf16_t*)d_in[5], (const bf16_t*)d_in[6], (bf16_t*)d_out, (char*)d_ws};
  constexpr int L_all = SNAP + 64;
  static_assert(SNAP >= 3 * 256 * 128 + 4 * 14592 + 4096 + 1024 + 256 + 512 + 128 + 64 && L_all <= 163840, "LDS");
  (void)hipFuncSetAttribute((const void*)k_all, hipFuncAttributeMaxDynamicSharedMemorySize, L_all);
  k_all<<<256, 512, L_all, stream>>>(p);
}
```

```cpp
#include <hip/hip_runtime.h>
#include <hip/hip_bf16.h>
#include <stdint.h>
#include <type_traits>

typedef unsigned short bf16_t;
typedef short bf16x8 __attribute__((ext_vector_type(8)));
typedef short s16x4 __attribute__((ext_vector_type(4)));
typedef float f32x4 __attribute__((ext_vector_type(4)));
typedef float f32x2 __attribute__((ext_vector_type(2)));
typedef unsigned u32x2 __attribute__((ext_vector_type(2)));
typedef unsigned u32x4 __attribute__((ext_vector_type(4)));
typedef __bf16 bf16x2_t __attribute__((ext_vector_type(2)));
typedef __attribute__((address_space(3))) void* lds_void_ptr;
typedef __attribute__((address_space(3))) char* lds_char_ptr;

constexpr int S_ = 2048, D_ = 1024, DF_ = 3584;
#define KN_HTD 11
#define KN_CT 7
#define KN_HQ 5
#define KN_HK 2
#define KN_KT 4
#define KN_SPLIT 1
#define KN_PO 0
#define KN_PH 0
#define KN_PA 0
#define KN_NUMA 3
constexpr int RSH = (KN_NUMA & 1) ? 65536 : 32768, RSA = (KN_NUMA & 2) ? 262144 : 114688;
__device__ __forceinline__ int rbH(int rb, int sd) { return (KN_NUMA & 1) ? (rb & 63) * RSH + (((sd ^ (rb >> 5)) & 1) << 13) : rb * RSH; }
__device__ __forceinline__ int rbA(int rb, int sd) { return (KN_NUMA & 2) ? (rb & 63) * RSA + (((sd ^ (rb >> 3)) & 1) << 13) : rb * RSA; }
__device__ __forceinline__ constexpr int omH(int o) { return (KN_NUMA & 1) ? o + (o & ~8191) : o; }
__device__ __forceinline__ constexpr int omA(int o) { return (KN_NUMA & 2) ? o + (o & ~8191) : o; }

__device__ __forceinline__ float bflo(unsigned v) { return __uint_as_float(v << 16); }
__device__ __forceinline__ float bfhi(unsigned v) { return __uint_as_float(v & 0xffff0000u); }
__device__ __forceinline__ unsigned pk_bf16(float lo, float hi) {
  f32x2 v = {lo, hi};
  bf16x2_t b = __builtin_convertvector(v, bf16x2_t);
  return __builtin_bit_cast(unsigned, b);
}
__device__ __forceinline__ float rbf(float v) { return bflo(pk_bf16(v, v)); }

template <int STRIDE> __device__ __forceinline__ int wswz(int row) {
  if constexpr (STRIDE % 256 == 0) return ((row & 3) | (((row >> 3) & 1) << 2)) << 1;
  else if constexpr (STRIDE % 256 == 128) return (((row >> 1) & 1) | (((row >> 3) & 1) << 1)) << 1;
  else return ((row >> 3) & 1) << 1;
}
template <int STRIDE> constexpr int wcls() { return STRIDE % 256 == 0 ? 8 : (STRIDE % 256 == 128 ? 4 : 2); }

template <int OFF> __device__ __forceinline__ s16x4 ds_tr(unsigned addr) {
  s16x4 r; asm volatile("ds_read_b64_tr_b16 %0, %1 offset:%2" : "=v"(r) : "v"(addr), "n"(OFF)); return r;
}
template <int OFF> __device__ __forceinline__ bf16x8 ds_b128(unsigned addr) {
  bf16x8 r; asm volatile("ds_read_b128 %0, %1 offset:%2" : "=v"(r) : "v"(addr), "n"(OFF)); return r;
}
template <int OFF> __device__ __forceinline__ f32x2 ds_b64(unsigned addr) {
  f32x2 r; asm volatile("ds_read_b64 %0, %1 offset:%2" : "=v"(r) : "v"(addr), "n"(OFF)); return r;
}
__device__ __forceinline__ float sum8(float v) {
  v += __int_as_float(__builtin_amdgcn_update_dpp(0, __float_as_int(v), 0x128, 0xf, 0xf, false));
  auto a = __builtin_amdgcn_permlane16_swap(__float_as_uint(v), __float_as_uint(v), false, false);
  v = __uint_as_float(a[0]) + __uint_as_float(a[1]);
  auto b = __builtin_amdgcn_permlane32_swap(__float_as_uint(v), __float_as_uint(v), false, false);
  return __uint_as_float(b[0]) + __uint_as_float(b[1]);
}
__device__ __forceinline__ float sum8l(float v) {
  v += __int_as_float(__builtin_amdgcn_update_dpp(0, __float_as_int(v), 0xb1, 0xf, 0xf, false));
  v += __int_as_float(__builtin_amdgcn_update_dpp(0, __float_as_int(v), 0x4e, 0xf, 0xf, false));
  return v + __int_as_float(__builtin_amdgcn_update_dpp(0, __float_as_int(v), 0x141, 0xf, 0xf, false));
}
__device__ __forceinline__ int ds_r32(unsigned addr) { int r; asm volatile("ds_read_b32 %0, %1" : "=v"(r) : "v"(addr)); return r; }
__device__ __forceinline__ void ds_w32(unsigned addr, int v) { asm volatile("ds_write_b32 %0, %1" :: "v"(addr), "v"(v) : "memory"); }
template <int N> __device__ __forceinline__ void wait_vm() { asm volatile("s_waitcnt vmcnt(%0)" ::"n"(N) : "memory"); }
__device__ __forceinline__ void raw_barrier() { asm volatile("" ::: "memory"); __builtin_amdgcn_s_barrier(); asm volatile("" ::: "memory"); }
__device__ __forceinline__ void loop_barrier() { raw_barrier(); }
__device__ __forceinline__ void lds_barrier() { asm volatile("s_waitcnt lgkmcnt(0)" ::: "memory"); __builtin_amdgcn_s_barrier(); asm volatile("" ::: "memory"); }

__device__ __forceinline__ u32x4 mk_rsrc(const void* p) { const unsigned long long v = (unsigned long long)p; return (u32x4){(unsigned)v, (unsigned)(v >> 32) & 0xffffu, 0x7fffffffu, 0x00020000u}; }
#define LD1K(v, so, off, sc) "buffer_load_dwordx4 " v ", %1, " so " offen offset:" off sc " lds\n\t"
template <int WAIT> __device__ __forceinline__ void ld_w4(unsigned m, u32x4 r, const unsigned (&v)[4], unsigned s0, unsigned s1) {
  if constexpr (WAIT >= 0) asm volatile("s_mov_b32 m0, %0\n\ts_waitcnt vmcnt(%8)\n\ts_barrier\n\t" LD1K("%2", "%6", "0", "") LD1K("%3", "%7", "1024", "") LD1K("%4", "%6", "2048", "") LD1K("%5", "%7", "3072", "")
                                        :: "s"(m), "s"(r), "v"(v[0]), "v"(v[1]), "v"(v[2]), "v"(v[3]), "s"(s0), "s"(s1), "n"(WAIT) : "memory", "m0");
  else asm volatile("s_mov_b32 m0, %0\n\ts_nop 0\n\t" LD1K("%2", "%6", "0", "") LD1K("%3", "%7", "1024", "") LD1K("%4", "%6", "2048", "") LD1K("%5", "%7", "3072", "")
                    :: "s"(m), "s"(r), "v"(v[0]), "v"(v[1]), "v"(v[2]), "v"(v[3]), "s"(s0), "s"(s1) : "memory", "m0");
}
template <int WAIT> __device__ __forceinline__ void ld_w2(unsigned m, u32x4 r, const unsigned (&v)[4], unsigned s0, unsigned s1) {
  if constexpr (WAIT >= 0) asm volatile("s_mov_b32 m0, %0\n\ts_waitcnt vmcnt(%6)\n\ts_barrier\n\t" LD1K("%2", "%4", "0", "") LD1K("%3", "%5", "1024", "")
                                        :: "s"(m), "s"(r), "v"(v[0]), "v"(v[1]), "s"(s0), "s"(s1), "n"(WAIT) : "memory", "m0");
  else asm volatile("s_mov_b32 m0, %0\n\ts_nop 0\n\t" LD1K("%2", "%4", "0", "") LD1K("%3", "%5", "1024", "") :: "s"(m), "s"(r), "v"(v[0]), "v"(v[1]), "s"(s0), "s"(s1) : "memory", "m0");
}
__device__ __forceinline__ void ld_x4(unsigned m, u32x4 r, unsigned v0, unsigned v1, unsigned so) {
  asm volatile("s_mov_b32 m0, %0\n\ts_nop 0\n\t" LD1K("%2", "%4", "0", " sc1") LD1K("%2", "%4", "1024", " sc1") LD1K("%3", "%4", "2048", " sc1") LD1K("%3", "%4", "3072", " sc1")
               :: "s"(m), "s"(r), "v"(v0), "v"(v1), "s"(so) : "memory", "m0");
}
template <int LO, int SO, bool SUB> __device__ __forceinline__ void ld_ca(unsigned mb, u32x4 r, unsigned v, unsigned sd) {
  unsigned t;
  if constexpr (SUB) asm volatile("s_add_i32 m0, %1, %4\n\ts_sub_i32 %0, %5, %6\n\tbuffer_load_dwordx4 %3, %2, %0 offen lds" : "=&s"(t) : "s"(mb), "s"(r), "v"(v), "n"(LO), "n"(SO), "s"(sd) : "memory", "m0", "scc");
  else asm volatile("s_add_i32 m0, %1, %4\n\ts_add_i32 %0, %6, %5\n\tbuffer_load_dwordx4 %3, %2, %0 offen lds" : "=&s"(t) : "s"(mb), "s"(r), "v"(v), "n"(LO), "n"(SO), "s"(sd) : "memory", "m0", "scc");
}
template <int LO, int SO> __device__ __forceinline__ void ld_cx(unsigned mb, u32x4 r, unsigned v, unsigned sx) {
  unsigned t;
  asm volatile("s_add_i32 m0, %1, %4\n\ts_xor_b32 %0, %6, %5\n\tbuffer_load_dwordx4 %3, %2, %0 offen lds" : "=&s"(t) : "s"(mb), "s"(r), "v"(v), "n"(LO), "n"(SO), "s"(sx) : "memory", "m0", "scc");
}
template <int I, int N, class F> __device__ __forceinline__ void static_for(F&& f) {
  if constexpr (I < N) { f(std::integral_constant<int, I>{}); static_for<I + 1, N>(f); }
}
#define CODE_ADDR(v, sym) asm volatile("s_getpc_b64 s[40:41]\n\ts_add_u32 s40, s40, " sym "@rel32@lo+4\n\ts_addc_u32 s41, s41, " sym "@rel32@hi+12\n\ts_mov_b64 %0, s[40:41]" : "=s"(v) :: "s40", "s41", "scc")
enum { EPI_QKV = 0, EPI_WO = 1, EPI_GLU = 2, EPI_DN = 3 };
#define MERGED 1
constexpr int SNAP = 162752;
#define KN_P 1
#define KN_S1 1
#define KN_S1C 8
#define KN_S1P 3
constexpr int STAB = 786432, STL = 3584;
#define FHM(p) ((p) * 256)
#define FRC(p) ((p) * 256 + 64)
#define FAM(p) ((p) * 256 + 128)
#define FCS(t) (2048 + (t) * 32)
#define FLN(k, i) ((1 + 2 * (k) + (((i) >> 6) & 1)) * 4096 + ((~sd & 1) << 11) + ((i) & 63) * 32)
#define FLO(t) FLN(0, (t) * 16)
#define FHF(p) FLN(1, (p) * 32)
#define FAE(p) FLN(2, (p) * 32)
#define FPR(b) FLN(3, ((b) & 3) * 32 + ((b) >> 3))
#define SIDE (((xcd >> 2) ^ tag) & 1)
constexpr int VTAB = STAB + 32768, PRB = 1048576;
constexpr int FEP = 3072;
constexpr unsigned long long LO32 = 0xffffffffull, HI32 = LO32 << 32;
#define KN_E 6
#define KN_MK 2
#define KN_GW 1
#define KN_TRY 6
#define KN_DM 0
#define KN_LX 0
#define KN_TP 5
__device__ __forceinline__ void set_flag(unsigned* p, unsigned tag) { __hip_atomic_store(p, tag, __ATOMIC_RELAXED, __HIP_MEMORY_SCOPE_AGENT); }
__device__ __forceinline__ bool missing(unsigned v, unsigned tag, unsigned long long mask) { return (__builtin_amdgcn_ballot_w64(v != tag) & mask) != 0; }
__device__ __forceinline__ void wait_flags(const unsigned* p, unsigned off, unsigned tag, unsigned long long mask) {
  unsigned v, polls = 0;
  do { asm volatile("s_nop 4\n\tglobal_load_dword %0, %1, %2 sc1\n\ts_waitcnt vmcnt(0)" : "=v"(v) : "v"(off), "s"(p) : "memory"); } while (missing(v, tag, mask) && ++polls < 4000000u);
}
__device__ __forceinline__ unsigned lane4() { unsigned l = __builtin_amdgcn_mbcnt_hi(~0u, __builtin_amdgcn_mbcnt_lo(~0u, 0u)) * 4u; asm volatile("" : "+v"(l)); return l; }
__device__ __forceinline__ bool lds_missing(unsigned addr, unsigned tag, unsigned long long mask) {
  int v = ds_r32(addr + lane4());
  asm volatile("s_waitcnt lgkmcnt(0)" : "+v"(v) :: "memory");
  return missing((unsigned)v, tag, mask);
}
constexpr int ACNT = SNAP - 60;
__device__ __forceinline__ void lds_inc(unsigned addr) { if (__builtin_amdgcn_mbcnt_hi(~0u, __builtin_amdgcn_mbcnt_lo(~0u, 0u)) == 0) asm volatile("ds_add_u32 %0, %1" :: "v"(addr), "v"(1) : "memory"); }
constexpr int FPAD = SNAP - 704;
__device__ __forceinline__ void lds_wait8(unsigned addr, int n = 8) {
  int dv; unsigned polls = 0;
  do { dv = ds_r32(addr); asm volatile("s_waitcnt lgkmcnt(0)" : "+v"(dv) :: "memory"); } while (__builtin_amdgcn_readfirstlane(dv) < n && ++polls < 40000000u);
}
template <int N, int STR = 4096> __device__ __forceinline__ bool marked(unsigned addr) {
  bf16x8 v[N];
  static_for<0, N>([&](auto ic) { constexpr int i = decltype(ic)::value; v[i] = ds_b128<i * STR>(addr); });
  asm volatile("s_waitcnt lgkmcnt(0)" ::: "memory");
  unsigned m = 0;
#pragma unroll
  for (int i = 0; i < N; ++i) {
    asm volatile("" : "+v"(v[i]));
    const u32x4 u = __builtin_bit_cast(u32x4, v[i]);
    m = max(max(m, u[0]), u[1]); m = max(max(m, u[2]), u[3]);
  }
  return __builtin_amdgcn_ballot_w64(m == 0xffffffffu) != 0;
}

struct GemmArgs {
  const bf16_t* X;
  const bf16_t* W0;
  const bf16_t* W1;
  const bf16_t* R;
  bf16_t* O;
  float* st;
  const bf16_t* CW0;
  const bf16_t* CW1;
  float* cs;
  const bf16_t* PF0;
  const bf16_t* PF1;
  unsigned* sync;
  float* slab;
  unsigned warm;
};
#define STAMP(i)
#define STAMPW(i, w)

#define STAMPV(i) STAMP(i)
#define XAUXD 16
#define XAUXW 16
#define XAUX 16
template <int MF, int NF> struct Frags { bf16x8 x[MF]; s16x4 lo[NF], hi[NF]; };

#define KN_WL 2
#define KN_WA KN_WL
#define KN_WB 2
#define KN_B3 0
constexpr int WOB = 55296;
constexpr int wo_pos(int s) { return s < 6 ? WOB + s * 16384 : (s - 6) * 16384; }
#define KN_GR 24
#define KN_GE 26
template <int NF, int GR> struct SchedP {
  static constexpr int NP = 4 * NF, NR = NP + 8;
  static constexpr int pa(int p) { return (p + NP - 12) % NP; }
  static constexpr int pn(int a) { return a < 8 ? (a ^ (a >> 1)) & 1 : (a < NP - 8 ? a / 4 : NF - 2 + ((a ^ (a >> 1)) & 1)); }
  static constexpr int pm(int a) { return a < 8 ? a >> 1 : (a < NP - 8 ? ((a & 4) ? a & 3 : 3 - (a & 3)) : (a - NP + 8) >> 1); }
  static constexpr int gap(int r) { return r < 8 ? 2 + r : (r < 12 ? 12 + 4 * (r - 8) : (r < NP ? 26 + (r - 12) * GR / (NP - 12) : 11 + 2 * ((r - NP) & ~1) - (r & 1))); }
  static constexpr int need(int r) { return r < 8 ? 12 : (r < NP ? (12 + (r & ~3) < NP ? 12 + (r & ~3) : NP) : 12 + ((r - NP) & ~1)); }
  static constexpr int cnt(int p) {
    int g = -1, q = -1;
    for (int r = 0; r < NR; ++r) if (need(r) == p && (gap(r) > g || (gap(r) == g && r > q))) { g = gap(r); q = r; }
    if (g < 0) return -1;
    int c = 0;
    for (int r = 0; r < NR; ++r) if (gap(r) < 2 * p && (gap(r) > g || (gap(r) == g && r > q))) ++c;
    return c < 15 ? c : 15;
  }
  static constexpr bool last(int a) { for (int b = a + 1; b < NP; ++b) if (pn(b) == pn(a)) return false; return true; }
};
using SchedG = SchedP<7, KN_GR>;
struct NoHook { template <class A> __device__ __forceinline__ void operator()(A) const {} };
template <int EPI, int BM, int NCH, int WM, int WN, int K, int LDW, int TM, int TN, int XA, int XB, int KU, int SX, int SW, int SPK, class HK = NoHook>
__device__ __forceinline__ void gemm_phase(const GemmArgs& a, unsigned tag, HK hook = HK{}) {
#if defined(__HIP_DEVICE_COMPILE__)
  constexpr bool SEAM_IN = EPI == EPI_GLU || EPI == EPI_DN, SEAM_OUT = EPI != EPI_DN;
  constexpr bool HALO = EPI == EPI_QKV;
  constexpr bool FX = EPI != EPI_QKV;
  constexpr int HR = HALO ? 16 : 0, XROWS = BM + HR;
  constexpr bool GLU = EPI == EPI_GLU;
  constexpr int STRIDE = GLU ? 224 : (HALO ? 128 : NCH * 16);
  constexpr int SEGB = GLU ? 16 : 32;
  constexpr int UPOFF = KU * 224 + 16;
  constexpr int XT = XROWS * 128, WU = GLU ? (UPOFF + KU * 224 + 255) / 256 * 256 : KU * NCH * 16;
  constexpr int XBASE = 0, WBASE = SX * XT, RING = WBASE + SW * WU;
  constexpr int MF = BM / 16 / WM, NF = NCH / 2 / WN;
  constexpr int MFX = MF + (HALO ? 1 : 0), NFX = NF;
  constexpr int NT = K / SPK / 64, NU = K / SPK / KU;
  constexpr int C = GLU ? 2 : (HALO ? 1 : (wcls<STRIDE>() < NF ? wcls<STRIDE>() : NF));
  constexpr int NXI = (XROWS / 8) * KU / 64;
  constexpr int NWI = KU * NCH * 16 / 1024;
#define KN_NLW 4
  constexpr int NLW = KN_NLW;
  constexpr int XI = (NXI + NLW - 1) / NLW, XIMIN = NXI / NLW, WI = (NWI + NLW - 1) / NLW;
  extern __shared__ __attribute__((aligned(16))) char smem[];
  STAMP(0);
  const unsigned lds0 = (unsigned)(size_t)(lds_char_ptr)smem;
  const int tid = threadIdx.x, lane = tid & 63;
  const int wave = __builtin_amdgcn_readfirstlane(tid >> 6);
  const int wm = wave / WN, wn = wave % WN;
  const int bid = blockIdx.x, xcd = bid & 7, kh = SPK > 1 ? ((bid >> 3) % SPK) : 0, idx = (bid >> 3) / SPK;
  constexpr int XN = TN / XB;
#define KN_ROT 0
  const int tm = (xcd / XN) * XA + idx / XB, tn = (xcd % XN) * XB + (idx + (EPI == EPI_DN ? KN_ROT : 0)) % XB;
  const int m0 = tm * BM, sd = SIDE;
  const int krot = (tm >> 2) * 8;
  const int fr = lane & 15, fq = lane >> 4;
  const int wq = (lane >> 2) & 3, wp = lane & 3;

  const int panel = m0 >> 8;
  constexpr int TCH = RING + (EPI == EPI_QKV ? 2304 : (EPI == EPI_GLU ? 5120 : 0));
  constexpr int DONE = TCH + 896;
  auto done_inc = [&]() { if (lane == 0) asm volatile("ds_add_u32 %0, %1" :: "v"(lds0 + DONE), "v"(1) : "memory"); };
  auto touch = [&](const void* base, unsigned voff, unsigned soff, unsigned lim = 0x7fffffffu) {
    __builtin_amdgcn_raw_ptr_buffer_load_lds(__builtin_amdgcn_make_buffer_rsrc((void*)base, 0, lim, 0x00020000), (lds_void_ptr)(size_t)(lds0 + TCH), 4, voff, soff, 0, 0);
  };
  const auto rO = __builtin_amdgcn_make_buffer_rsrc((void*)a.O, 0, 0x7fffffff, 0x00020000);

  unsigned xsrc[XI], wsrc[WI];
  if constexpr (HALO) {
    const int w4 = wave & 3, lr = lane >> 3;
    const unsigned xv = (unsigned)(lr * (K * 2) + (((lane & 7) ^ ((w4 * 4 + (lr >> 1)) & 7)) << 4));
    const int r0 = m0 - HR + w4 * 8;
#pragma unroll
    for (int i = 0; i < XI; ++i) xsrc[i] = xv + (unsigned)((i == 0 ? (r0 < 0 ? 0 : r0) : r0 + 32 * (i < 4 ? i : 4)) * (K * 2));
    const unsigned wv = (unsigned)((w4 * 8 + lr) * (LDW * 2) + tn * 128 + (((lane & 7) ^ wswz<128>(w4 * 8 + lr)) << 4));
#pragma unroll
    for (int i = 0; i < WI; ++i) wsrc[i] = wv + (unsigned)((i & 1) * 32 * (LDW * 2) + (i >> 1) * 2048);
  } else {
#pragma unroll
  for (int i = 0; i < XI; ++i) {
    int j = (wave % NLW) + NLW * i; if (j >= NXI) j = wave % NLW;
    const int r = 8 * j + (lane >> 3), c = (lane & 7) ^ ((r >> 1) & 7);
    int gr = m0 - HR + r; if (gr < 0) gr = 0;
    xsrc[i] = FX ? (unsigned)(rbH(m0 >> 4, sd) + (j >> 1) * RSH + (j & 1) * 1024 + lane * 16) : (unsigned)(gr * K + kh * (K / SPK) + c * 8) * 2u;
  }
#pragma unroll
  for (int i = 0; i < WI; ++i) {
    int j = (wave % NLW) + NLW * i; if (j >= NWI) j -= NLW;
    if constexpr (GLU) {
      const int jj = j >= 7 ? j - 7 : j;
      const int b = jj * 1024 + lane * 16, p = b / 224, pos = (b % 224) >> 4;
      const int row = 8 * ((p & 15) >> 2) + (p & 3) + 4 * (p >> 4);
      wsrc[i] = (unsigned)(row * LDW + tn * 112 + pos * 8) * 2u;
    } else {
      const int b = j * 1024 + lane * 16, row = b / STRIDE, pos = (b % STRIDE) >> 4;
      const int c = pos ^ wswz<STRIDE>(row);
      wsrc[i] = (unsigned)((row + kh * (K / SPK)) * LDW + tn * (NCH * 8) + c * 8) * 2u;
    }
  }
  }
  auto ldX = [&](int i, int tile, int part, int slot, bool ok = true) {
    const auto rX = __builtin_amdgcn_make_buffer_rsrc((void*)a.X, 0, ok ? 0x7fffffff : 0, 0x00020000);
    if constexpr (GLU) tile = (tile + krot) & 15;
    const int j = wave + NLW * i;
    if (NXI % NLW != 0 && j >= NXI) return;
    __builtin_amdgcn_raw_ptr_buffer_load_lds(rX, (lds_void_ptr)(size_t)(lds0 + XBASE + slot * XT + (part * NXI + j) * 1024), 16, xsrc[i],
                                             omH(tile * 2048) + part * (NXI / 2) * RSH, 0, SEAM_IN ? XAUX : 0);
  };
#define KN_WNT 0
  constexpr int WAUX = ((KN_WNT >> EPI) & 1) ? 2 : 0;
  auto ldW = [&](int i, int unit, int slot, bool ok = true) {
    const auto rW0 = __builtin_amdgcn_make_buffer_rsrc((void*)a.W0, 0, ok ? 0x7fffffff : 0, 0x00020000);
    const auto rW1 = __builtin_amdgcn_make_buffer_rsrc((void*)(GLU ? a.W1 : a.W0), 0, ok ? 0x7fffffff : 0, 0x00020000);
    int j = wave + NLW * i; if (j >= NWI) j -= NLW;
    if constexpr (GLU) {
      unit = (unit + 2 * krot) & 31;
      const bool up = j >= 7;
      __builtin_amdgcn_raw_ptr_buffer_load_lds(up ? rW1 : rW0, (lds_void_ptr)(size_t)(lds0 + WBASE + slot * WU + (up ? UPOFF + (j - 7) * 1024 : j * 1024)), 16, wsrc[i],
                                               unit * KU * LDW * 2, 0, WAUX);
    } else {
      __builtin_amdgcn_raw_ptr_buffer_load_lds(rW0, (lds_void_ptr)(size_t)(lds0 + WBASE + slot * WU + j * 1024), 16, wsrc[i], unit * KU * LDW * 2, 0, WAUX);
    }
  };
  auto issueX = [&](int tile, int part, int slot) {
    if (wave >= NLW) return;
#pragma unroll
    for (int i = 0; i < XI; ++i) ldX(i, tile, part, slot);
  };
  auto issueW = [&](int unit, int slot) {
    if (wave >= NLW) return;
#pragma unroll
    for (int i = 0; i < WI; ++i) ldW(i, unit, slot);
  };

  f32x4 acc[NF][MF];
#pragma unroll
  for (int n = 0; n < NF; ++n)
#pragma unroll
    for (int m = 0; m < MF; ++m) acc[n][m] = (f32x4){0.f, 0.f, 0.f, 0.f};
  f32x4 acch = {0.f, 0.f, 0.f, 0.f};
  f32x4 accs = {0.f, 0.f, 0.f, 0.f};
  f32x4 gram = {0.f, 0.f, 0.f, 0.f}, rsum = {0.f, 0.f, 0.f, 0.f}, gramh = {0.f, 0.f, 0.f, 0.f};
  constexpr int ONES = RING;

  unsigned xa0, xa1, xh0 = 0, xh1 = 0;
  { const unsigned o = fr * 128 + ((fq ^ ((fr >> 1) & 7)) << 4), mo = (HR + wm * MF * 16) * 128;
    xa0 = lds0 + XBASE + mo + o; xa1 = lds0 + XBASE + mo + (o ^ 64); xh0 = lds0 + XBASE + o; xh1 = lds0 + XBASE + (o ^ 64);
    if constexpr (FX) { xa0 = lds0 + XBASE + mo + lane * 16; xa1 = xa0 + 1024; } }
  unsigned wa[C], wha = 0;
#pragma unroll
  for (int c = 0; c < C; ++c) {
    const int row = 8 * fq + wq, seg = HALO ? wn : wn * NF + c;
    if constexpr (GLU) wa[c] = lds0 + WBASE + (4 * fq + wq) * 224 + wn * 112 + (c == 0 ? wp * 8 : 96 + (wp & 1) * 8 + (wp >> 1) * UPOFF);
    else wa[c] = lds0 + WBASE + row * STRIDE + (((seg << 1) ^ wswz<STRIDE>(row)) << 4) + wp * 8;
  }

  using I0 = std::integral_constant<int, 0>; using I1 = std::integral_constant<int, 1>;
  using T = std::true_type; using F = std::false_type;
  typedef Frags<MFX, NFX> FR;
  struct RdCtx { unsigned xa, xh, wb[C], wh; };
  auto mkctx = [&](bool ks1, unsigned xoffs, unsigned woffs) {
    RdCtx c; c.xa = (ks1 ? xa1 : xa0) + xoffs; c.xh = fr == 0 ? lds0 + ONES : (ks1 ? xh1 : xh0) + xoffs; c.wh = wha + woffs;
#pragma unroll
    for (int i = 0; i < C; ++i) c.wb[i] = wa[i] + woffs;
    return c;
  };
  auto rd_item = [&](FR& f, const RdCtx& c, auto KSc, auto kc) {
    constexpr int KS = decltype(KSc)::value, k = decltype(kc)::value;
    if constexpr (k < MF) f.x[k] = ds_b128<k * 2048>(c.xa);
    else if constexpr (k < MFX) f.x[k] = ds_b128<0>(c.xh);
    else if constexpr (GLU) {
      constexpr int n = k - MFX, cc = n == 6, o = n == 6 ? 0 : (n >> 1) * 32 + (n & 1) * UPOFF;
      f.lo[n] = ds_tr<o>(c.wb[cc]); f.hi[n] = ds_tr<o + 16 * 224>(c.wb[cc]);
    } else if constexpr (k < MFX + NF) {
      constexpr int n = k - MFX, cc = n % C, o = (HALO ? n * 8192 : (n / C) * C * SEGB) + KS * 32 * STRIDE;
      f.lo[n] = ds_tr<o>(c.wb[cc]); f.hi[n] = ds_tr<o + 4 * STRIDE>(c.wb[cc]);
    }
  };
  auto wait_frags = [&](FR& f) {
    asm volatile("s_waitcnt lgkmcnt(0)" ::: "memory");
#pragma unroll
    for (int m = 0; m < MFX; ++m) asm volatile("" : "+v"(f.x[m]));
#pragma unroll
    for (int n = 0; n < NFX; ++n) asm volatile("" : "+v"(f.lo[n]), "+v"(f.hi[n]));
  };
#define KN_DPP 3
#define KN_FG 1
#define KN_HOLD 0
#define KN_HG 6
#define KN_FB 1
#define KN_FL 22
#define KN_FL2 20
#define KN_FE 25
  FR fa, fb;
  auto rdg = [&](const RdCtx& c, auto rc) {
    constexpr int r = decltype(rc)::value;
    if constexpr (r >= 28) { constexpr int m = (r - 28) >> 1; if constexpr (r & 1) fb.x[m] = ds_b128<m * 2048 + 1024>(c.xa); else fa.x[m] = ds_b128<m * 2048>(c.xa); }
    else {
      constexpr int n = r >> 2, cc = n == 6, o = (n == 6 ? 0 : (n >> 1) * 32 + (n & 1) * UPOFF) + ((r >> 1) & 1) * WU + (r & 1) * 16 * 224;
      FR& f = (r & 2) ? fb : fa;
      if constexpr (r & 1) f.hi[n] = ds_tr<o>(c.wb[cc]); else f.lo[n] = ds_tr<o>(c.wb[cc]);
    }
  };
  auto ping = [&](auto pc) {
    static_for<0, 36>([&](auto rc) { constexpr int r = decltype(rc)::value;
      if constexpr (SchedG::need(r) == decltype(pc)::value) {
        FR& f = (r >= 28 ? r & 1 : r & 2) ? fb : fa;
        if constexpr (r >= 28) { bf16x8& q = f.x[(r - 28) >> 1]; asm volatile("" : "+v"(q)); } else { s16x4& q = (r & 1) ? f.hi[r >> 2] : f.lo[r >> 2]; asm volatile("" : "+v"(q)); } } });
  };
  auto pairs = [&](auto PBc, auto PEc, auto RDc, auto CSc, const RdCtx& c, auto&& gapf) {
    constexpr bool RD = decltype(RDc)::value; constexpr int CS = decltype(CSc)::value;
    static_for<decltype(PBc)::value, decltype(PEc)::value>([&](auto pc) {
      constexpr int p = decltype(pc)::value, a = (p + 16) % 28, n = SchedG::pn(a), m = SchedG::pm(a);
      if constexpr (SchedG::cnt(p) >= 0) { asm volatile("s_waitcnt lgkmcnt(%0)" :: "n"(SchedG::cnt(p)) : "memory"); ping(pc); }
      static_for<0, 2>([&](auto hc) {
        constexpr int g = 2 * p + decltype(hc)::value;
        FR& u = ((a ^ g) & 1) ? fb : fa;
        acc[n][m] = __builtin_amdgcn_mfma_f32_16x16x32_bf16(__builtin_shufflevector(u.lo[n], u.hi[n], 0, 1, 2, 3, 4, 5, 6, 7), u.x[m], acc[n][m], 0, 0, 0);
        __builtin_amdgcn_sched_barrier(0);
        if constexpr (CS >= 0 && SchedG::last(a) && (g & 1)) {
          const unsigned sv = fr == n ? 0x3f803f80u : 0u;
          const u32x4 s4 = {sv, sv, sv, sv};
          FR& w = CS ? fb : fa;
          accs = __builtin_amdgcn_mfma_f32_16x16x32_bf16(__builtin_shufflevector(w.lo[n], w.hi[n], 0, 1, 2, 3, 4, 5, 6, 7), __builtin_bit_cast(bf16x8, s4), accs, 0, 0, 0);
          __builtin_amdgcn_sched_barrier(0);
        }
        if constexpr (RD) static_for<0, 36>([&](auto rc) { if constexpr (SchedG::gap(decltype(rc)::value) == g) rdg(c, rc); });
        if constexpr (g & 1) gapf(pc);
        __builtin_amdgcn_sched_barrier(0);
      });
    });
    if constexpr (RD) { asm volatile("s_waitcnt lgkmcnt(0)" ::: "memory"); ping(std::integral_constant<int, 28>{}); }
  };

  f32x4* tb_row = (f32x4*)(smem + RING);
  float* tb_cs = (float*)(tb_row + 256);
  constexpr int RTAB = 524288;
  auto glu_stats_issue = [&]() {
    if constexpr (EPI == EPI_GLU) {
      const auto rS = __builtin_amdgcn_make_buffer_rsrc((void*)a.st, 0, 0x7fffffff, 0x00020000);
      if (wave == 7) __builtin_amdgcn_raw_ptr_buffer_load_lds(rS, (lds_void_ptr)(size_t)(lds0 + RING + 3072), 16, ((lane >> 2) * S_ + m0 + tn * 8 + (lane & 3) * 2) * 8, 0, 0, 16);
    }
  };
  auto glu_stats = [&]() {
    if constexpr (EPI == EPI_GLU) {
      int lx = lane; asm volatile("" : "+v"(lx));
      const unsigned sa = lds0 + RING + 3072 + lx * 8;
      f32x2 p0 = ds_b64<0>(sa), p1 = ds_b64<512>(sa);
      asm volatile("s_waitcnt lgkmcnt(0)" : "+v"(p0), "+v"(p1) :: "memory");
      const float mu = sum8(p0[0] + p1[0]) * (1.f / 16.f);
      const float v = sum8((p0[1] + p1[1]) + 64.f * ((p0[0] - mu) * (p0[0] - mu) + (p1[0] - mu) * (p1[0] - mu))) * (1.f / 1024.f) + 1e-5f;
      const float rs = 1.0f / sqrtf(v);
      const f32x4 rv = {mu, -0.10294324f * rs * rs * rs, -2.3022082f * rs, v};
      __builtin_amdgcn_raw_buffer_store_b128(__builtin_bit_cast(u32x4, rv), __builtin_amdgcn_make_buffer_rsrc((void*)a.cs, 0, 0x7fffffff, 0x00020000),
                                             lx < 8 ? (unsigned)(RTAB + (m0 + tn * 8 + lx) * 16) : 0x80000000u, 0, 16);
    }
  };
  auto epilogue = [&](auto EAc) __attribute__((always_inline)) {
  STAMP(2);

  if constexpr (EPI == EPI_GLU) {
    constexpr bool EARLY = decltype(EAc)::value;
    static_assert(DONE + 4 == ACNT && TCH + 256 == FPAD, "LDS");
#define KN_EP 1
#define KN_HE 0
#define KN_W1 0
#define KN_W2 0
#define KN_WS (KN_W2 ? 4 : 2)
    if constexpr (!EARLY) {
      wait_vm<0>();
      if (KN_HE) hook(std::integral_constant<int, 2>{});
      lds_wait8(lds0 + DONE);
      if (KN_EP) __builtin_amdgcn_s_setprio(2);
      STAMPW(9, 4);
      if (KN_W1 == 0) hook(I0{});
      if (KN_W2 == 4) hook(std::integral_constant<int, 3>{});
      STAMPW(10, 4);
    } else { if (KN_EP) __builtin_amdgcn_s_setprio(3); hook(I1{}); }
    asm volatile("" ::: "memory");
    f32x4 cg[4], cu[4], rw[MF], rw6[2];
    int lz = lane; asm volatile("" : "+v"(lz));
    const int fr = lz & 15, fq = lz >> 4;
#pragma unroll
    for (int p = 0; p < 4; ++p) {
      const int o = wn * 56 + p * 16 + (p == 3 ? (fq & 1) : fq) * 4;
      cg[p] = *(const f32x4*)(tb_cs + o); cu[p] = *(const f32x4*)(tb_cs + 112 + o);
    }
#pragma unroll
    for (int m = 0; m < MF; ++m) rw[m] = tb_row[(wm * MF + m) * 16 + fr];
#pragma unroll
    for (int q = 0; q < 2; ++q) rw6[q] = tb_row[(wm * MF + 2 * q + (fq >> 1)) * 16 + fr];
    const int gz = rbA((m0 >> 4) + wm * 4, sd) + fr * 16, g = tn * 2 + wn, g1 = omA(g * 1024);
    const int go0 = gz + (fq & 1) * RSA + (fq >> 1) * 256, go6 = gz + ((fq & 1) * 2 + (fq >> 1)) * RSA;
    unsigned hv[MF][NF];
    auto put = [&](unsigned a0, unsigned a1, unsigned b0, unsigned b1, int vo, int so) {
      const auto s0 = __builtin_amdgcn_permlane16_swap(a0, b0, false, false), s1 = __builtin_amdgcn_permlane16_swap(a1, b1, false, false);
      __builtin_amdgcn_raw_buffer_store_b128((u32x4){s0[0], s1[0], s0[1], s1[1]}, rO, vo, so, 16);
      __builtin_amdgcn_sched_barrier(0);
      asm volatile("s_nop 1" ::: "memory");
      __builtin_amdgcn_sched_barrier(0);
    };
#define STAGE(body) _Pragma("unroll") for (int i = 0; i < 2 * NN; ++i) { const int m = MB + i / NN, n = N0 + i % NN; const f32x4 k = n == 6 ? rw6[m >> 1] : rw[m]; body; } __builtin_amdgcn_sched_barrier(0)
    auto grp = [&](auto MBc, auto N0c, auto NNc, auto&& mid) {
      constexpr int MB = decltype(MBc)::value, N0 = decltype(N0c)::value, NN = decltype(NNc)::value;
      f32x2 G[2 * NN], U[2 * NN], P[2 * NN];
      __builtin_amdgcn_sched_barrier(0);
      STAGE({ const int e = (n == 6 ? m & 1 : n & 1) * 2; const f32x4 x = n == 6 ? acc[6][m & 2] : acc[n & 6][m]; G[i] = ((f32x2){x[e], x[e + 1]}) - k[0] * ((f32x2){cg[n >> 1][e], cg[n >> 1][e + 1]}); });
      STAGE(P[i] = G[i] * G[i]);
      STAGE(P[i] = P[i] * k[1] + k[2]);
      STAGE(P[i] = G[i] * P[i]);
      STAGE({ const int e = (n == 6 ? m & 1 : n & 1) * 2; const f32x4 x = n == 6 ? acc[6][m | 1] : acc[n | 1][m]; U[i] = ((f32x2){x[e], x[e + 1]}) - k[0] * ((f32x2){cu[n >> 1][e], cu[n >> 1][e + 1]}); });
      STAGE(P[i] = ((f32x2){__builtin_amdgcn_exp2f(P[i][0]), __builtin_amdgcn_exp2f(P[i][1])}));
      mid();
      __builtin_amdgcn_sched_barrier(0);
      STAGE(P[i] = P[i] * k[3] + k[3]);
      STAGE(P[i] = ((f32x2){__builtin_amdgcn_rcpf(P[i][0]), __builtin_amdgcn_rcpf(P[i][1])}));
      STAGE(U[i] = G[i] * U[i]);
      STAGE(U[i] = U[i] * P[i]);
      STAGE({ hv[m][n] = pk_bf16(U[i][0], U[i][1]); asm volatile("" : "+v"(hv[m][n])); });
    };
#undef STAGE
    using I2 = std::integral_constant<int, 2>; using I3 = std::integral_constant<int, 3>; using I4 = std::integral_constant<int, 4>;
    grp(I0{}, I0{}, I4{}, [&]() { if (EARLY && KN_W1 == 2) { lds_wait8(lds0 + DONE); hook(I0{}); asm volatile("" ::: "memory"); } });
    if (EARLY && KN_W1 == 1) { lds_wait8(lds0 + DONE); hook(I0{}); asm volatile("" ::: "memory"); }
    if (!EARLY && KN_W2 == 3) { hook(I3{}); asm volatile("" ::: "memory"); }
    grp(I2{}, I0{}, I4{}, [&]() {
#pragma unroll
      for (int p = 0; p < 2; ++p) put(hv[0][2 * p], hv[0][2 * p + 1], hv[1][2 * p], hv[1][2 * p + 1], go0 + p * 512, g1); });
#pragma unroll
    for (int p = 0; p < 2; ++p) put(hv[2][2 * p], hv[2][2 * p + 1], hv[3][2 * p], hv[3][2 * p + 1], go0 + p * 512, g1 + 2 * RSA);
    asm volatile("" ::: "memory");
    __builtin_amdgcn_sched_barrier(0);
    STAMP(4);
    STAMPW(11, 4);
    if (KN_EP) __builtin_amdgcn_s_setprio(EARLY ? 1 : 0);
#define KN_AW 2
    auto arrived = [&](int w) { if (!EARLY && w == KN_AW) { wait_vm<0>(); lds_inc(lds0 + ACNT); STAMPW(6, 4); } };
    arrived(0);
    if (EARLY && KN_W2 == 1) { if (KN_W1 == 0) lds_wait8(lds0 + DONE); hook(I3{}); asm volatile("" ::: "memory"); }
#pragma unroll
    for (int q = 0; q < 2; ++q)
#pragma unroll
      for (int e = 0; e < 4; ++e) {
        const float x0 = acc[6][2 * q][e], x1 = acc[6][2 * q + 1][e];
        auto sw = __builtin_amdgcn_permlane32_swap(__float_as_uint(x0), __float_as_uint(x1), false, false);
        acc[6][2 * q][e] = __uint_as_float(sw[0]); acc[6][2 * q + 1][e] = __uint_as_float(sw[1]);
      }
    const int sb = omA((256 + g * 3) * 256), go4 = go0 + (fq >> 1) * (omA((257 + g * 3) * 256) - sb - 256);
    grp(I0{}, I4{}, I3{}, [&]() { arrived(1); });
    arrived(2);
    if (EARLY && KN_W2 == 2) { if (KN_W1 == 0) lds_wait8(lds0 + DONE); hook(I3{}); asm volatile("" ::: "memory"); }
    grp(I2{}, I4{}, I3{}, [&]() { arrived(3); put(hv[0][4], hv[0][5], hv[1][4], hv[1][5], go4, sb); });
    put(hv[2][4], hv[2][5], hv[3][4], hv[3][5], go4, sb + 2 * RSA);
    put(hv[0][6], hv[1][6], hv[2][6], hv[3][6], go6, omA((258 + g * 3) * 256));
    asm volatile("" ::: "memory");
    if constexpr (!EARLY) lds_inc(lds0 + ACNT + 4);
  }
  };
  using IM1 = std::integral_constant<int, -1>;

  if constexpr (KU == 32) {
    static_assert(SX == 3 && SW == 4 && NXI % NLW == 0, "ring");
    constexpr int WR = KN_GW ? 2 : 0;
    constexpr bool EG = (KN_E & 2) != 0;
    if constexpr (!KN_GW) { issueW(0, 0); issueW(1, 1); }
    if constexpr (!EG && KN_GW != 2) { issueW(2, 2 ^ WR); issueW(3, 3 ^ WR); }
    if (wave == 5) { ds_w32(lds0 + DONE, 0); ds_w32(lds0 + DONE + 4, 0); ds_w32(lds0 + DONE + 8, 0); }
    static_assert(EG, "early start");
    STAMP(9); STAMP(6);
    auto check_tile = [&](int t) {
      int lx = lane; asm volatile("" : "+v"(lx));
      const unsigned ad = lds0 + XBASE + t * XT + wave * 1024 + lx * 16;
      if (__builtin_expect(marked<8>(ad), 0)) {
        for (int tries = 0;; ++tries) {
          if (tries >= KN_TRY) wait_flags(a.sync + FHF(panel), (lane4() & 127) * 32, tag, LO32);
#pragma unroll 1
          for (int u = t; u < t + 1 + (t == 0); ++u) { issueX(u, 0, u); issueX(u, 1, u); }
          if (t == 0) wait_vm<2 * XI>(); else wait_vm<0>();
          if (tries >= KN_TRY || !marked<8>(ad)) break;
        }
      }
    };
    auto run = [&](auto LDRc, auto STc) __attribute__((always_inline)) {
      constexpr bool LDR = decltype(LDRc)::value;
      if constexpr (LDR) {
        issueX(0, 0, 0); issueX(0, 1, 0); issueX(1, 0, 1); issueX(1, 1, 1);
        if constexpr (EG && KN_GW != 2) { issueW(2, 2 ^ WR); issueW(3, 3 ^ WR); wait_vm<2 * XI + 2 * WI>(); } else wait_vm<2 * XI>();
        if constexpr (EG) check_tile(0);
      } else if constexpr (!EG) glu_stats_issue();
      lds_barrier();
      if constexpr (EG && !LDR) { if (wave == 4) __builtin_amdgcn_raw_ptr_buffer_load_lds(__builtin_amdgcn_make_buffer_rsrc((void*)(a.sync + FHF(panel)), 0, 4096, 0x00020000), (lds_void_ptr)(size_t)(lds0 + TCH + 256), 4, lane * 128, 0, 0, 16); }
      using I12 = std::integral_constant<int, 12>; using I28 = std::integral_constant<int, 28>;
      { const RdCtx c = mkctx(false, 0, WR * WU);
        static_for<0, 24>([&](auto gc) { static_for<0, 36>([&](auto rc) { if constexpr (SchedG::gap(decltype(rc)::value) == decltype(gc)::value) rdg(c, rc); }); });
        STAMP(1);
        pairs(I12{}, I28{}, T{}, IM1{}, c, [&](auto pc) { constexpr int i = (decltype(pc)::value - 12) >> 1; if constexpr (LDR && !(decltype(pc)::value & 1)) ldX(i % XI, 2, i / XI, 2); }); }
      int xs = 1;
#define KN_CTJ (KN_SPLIT ? 11 : 12)
      unsigned long long tb = (unsigned long long)a.PF0; unsigned tv = 0, tl = 0x7fffffffu, ts = KN_SPLIT ? 1835008u : 1048576u, tmask = 0; int tj = 1, tsh = 1;
      if constexpr (!LDR) {
        unsigned long long c0, c1; CODE_ADDR(c0, "k_all"); CODE_ADDR(c1, "k_zend");
#define KN_WJ 1
#define KN_WE 8
#define KN_DJ 6
        tj = KN_DJ;
        if (wave == 4) { tb = c0; tl = c1 - c0 < 262144ull ? (unsigned)(c1 - c0) : 0u; ts = 0; tmask = 1u << KN_CTJ; }
        if (wave == 5) tmask = 0x55u << (KN_DJ + (idx & 1));
        if (wave == 6) { tb = (unsigned long long)((idx & 16) ? a.W1 : a.W0); ts = 64 * LDW * 2; tmask = ((1u << (KN_WE - KN_KT)) - 1u) << KN_WJ; tj = KN_WJ - krot - KN_KT; tsh = 0; }
        const int l4 = lane / 14;
        tv = wave == 4 ? (unsigned)((lane * 32 + idx) * 128)
           : (lane >= 56 ? 0x80000000u : (wave == 5 ? (unsigned)(bid * 7168 + lane * 128) : (unsigned)(((idx & 15) * 4 + l4) * (LDW * 2) + (xcd & 3) * 1792 + (lane - l4 * 14) * 128)));
      }
      const int js = 2 * (tm & 3) + 1;
      const auto rC = __builtin_amdgcn_make_buffer_rsrc((void*)a.cs, 0, 0x7fffffff, 0x00020000);
      unsigned* ccnt = a.sync + FCS(tn);
#define KN_JR 10
#define KN_JS ((KN_E & 2) ? 2 : 1)
      const int jf = wave == 4 ? NT - 4 : (wave == 7 ? KN_JR : 64);
      const unsigned* fcnt = wave == 4 ? ccnt : a.sync + FRC(panel);
      const bool mrk = (KN_E & 4) && KN_MK == 0 && (idx >> 4) == ((xcd >> 1) & 1);
      const int jm = (idx >> 4) == ((xcd >> 1) & 1) ? 3 : 64;
      static_assert(KN_MK == 2 && (KN_E & 4) && !KN_DM, "marks");
      int mo = rbA((m0 >> 4) + wave * 4, sd) + omA(tn * 2048);
      const unsigned evm = tmask | (8u << js) | (mrk ? 0xbfcu : 0u) | (wave == 4 ? 3u << (NT - 4) : 0u) | (wave == 7 ? ((5u << KN_JS) | 2u | (11u << KN_JR)) : 0u);
      constexpr int CFL = TCH + 256, CPB = XBASE + ((NT - 3) % 3) * XT;
      auto iter = [&](int j, auto LWc, auto LXc, auto WAITc, auto CSc) {
        constexpr bool LW = decltype(LWc)::value && LDR, LX = decltype(LXc)::value && LDR, LP = LW && !LX;
        if constexpr (LDR) {
          wait_vm<decltype(WAITc)::value>();
          if constexpr (EG && LX) { if (__builtin_expect(j <= 2, 0)) check_tile(j); }
          if constexpr ((KN_E & 4) && KN_MK == 2 && LX) {
            if ((unsigned)(j - jm) < 8u) {
              int lx = lane; asm volatile("" : "+v"(lx));
              unsigned on = 0xffffffffu; asm volatile("" : "+v"(on));
              __builtin_amdgcn_raw_buffer_store_b128((u32x4){on, on, on, on}, rO, lx * 16, mo, 16);
              mo += (j & 1) ? 1024 : RSA - 1024;
            }
            if (j == jm + 9) { if (lane == 0) set_flag(a.sync + FAM(panel) + (tn & 15) * 4 + wave, tag); }
          }
        }
        if constexpr (!LDR) {
          if (__builtin_expect((evm >> j) & 1, 0)) {
            if (j == js + 3) {
              if ((tmask >> (j - 1)) & 1) wait_vm<1>(); else wait_vm<0>();
              if (lane == 0) set_flag(ccnt + tm * 4 + wave - 4, tag);
            }
            if (EG && j == 1) glu_stats_issue();
            if (j == KN_JS && wave == 7) { wait_vm<0>(); glu_stats(); }
            if (j == KN_JS + 2 && wave == 7) {
              if (j == js + 2) wait_vm<1>(); else wait_vm<0>();
              if (lane == 0) set_flag((unsigned*)fcnt + tn, tag);
            }
            {
              int lx = lane; asm volatile("" : "+v"(lx));
              if (j == jf) __builtin_amdgcn_raw_ptr_buffer_load_lds(__builtin_amdgcn_make_buffer_rsrc((void*)fcnt, 0, 128, 0x00020000), (lds_void_ptr)(size_t)(lds0 + CFL + (wave & 1) * 256), 4, lx * 4, 0, 0, 16);
              if (j == jf + 1) {
                wait_vm<0>();
                if (lds_missing(lds0 + CFL + (wave & 1) * 256, tag, LO32)) wait_flags(fcnt, lx * 4, tag, LO32);
                if (wave == 7) {
#pragma unroll
                  for (int i = 0; i < 4; ++i) __builtin_amdgcn_raw_ptr_buffer_load_lds(rC, (lds_void_ptr)(size_t)(lds0 + RING + i * 1024), 16, lx * 16, RTAB + m0 * 16 + i * 1024, 0, 16);
                }
              }
              if (j == jf + 3) wait_vm<0>();
            }
            if ((tmask >> j) & 1) touch((const void*)tb, tv, (((j - tj) >> tsh) & 15) * ts, tl);
          }
          __builtin_amdgcn_sched_barrier(0);
        }
        int xp = xs + 2; if (xp >= 3) xp -= 3;
        const int ws = ((j & 1) * 2) ^ WR;
        auto lda = [&](int i) { ldW(i % WI, 2 * j + 2 + i / WI, (ws ^ 2) + i / WI); };
        auto ldb = [&](int i) {
            if constexpr (LX) ldX(i % XI, j + 2, i / XI, xp);
            else __builtin_amdgcn_raw_ptr_buffer_load_lds(rC, (lds_void_ptr)(size_t)(lds0 + CPB + wave * 8192 + i * 1024), 16, lane * 16, (tn * 2 + wn) * 7168 + i * 1024, 0, 16); };
        using NLA = std::integral_constant<int, LW ? 2 * WI : 0>; using NLB = std::integral_constant<int, LX ? 2 * XI : (LP ? 7 : 0)>;
        { const RdCtx c = mkctx(false, xs * XT, ws * WU);
          pairs(I0{}, I28{}, T{}, CSc, c, [&](auto pc) { constexpr int p = decltype(pc)::value;
            if constexpr (LDR && p == 0) loop_barrier();
            if constexpr (!LDR && p == KN_GE) { asm volatile("s_waitcnt lgkmcnt(0)" ::: "memory"); if (decltype(WAITc)::value == 0 || j + 1 < NT) loop_barrier(); }
            static_for<0, NLA::value>([&](auto ic) { constexpr int i = decltype(ic)::value; if constexpr (i * 13 / 8 == p) lda(i); });
            static_for<0, NLB::value>([&](auto ic) { constexpr int i = decltype(ic)::value; if constexpr (14 + i * 13 / 8 == p) ldb(i); }); }); }
        xs = xs + 1; if (xs >= 3) xs = 0;
      };
      using WX_ = std::integral_constant<int, 2 * XI>;
      bf16x8 pv[8];
      if constexpr (LDR) {
        for (int j = 1; j < NT - 2; ++j) iter(j, T{}, T{}, WX_{}, IM1{});
#define KN_PRI 1
        if (KN_PRI == 2) __builtin_amdgcn_s_setprio(3);
        iter(NT - 2, T{}, F{}, WX_{}, IM1{});
        if (KN_PRI == 1) __builtin_amdgcn_s_setprio(3);
        iter(NT - 1, F{}, F{}, std::integral_constant<int, 7>{}, IM1{});
        wait_vm<0>();
        int lc = lane; asm volatile("" : "+v"(lc));
        const unsigned ca = lds0 + CPB + wave * 8192 + (lc >> 4) * 1792 + ((lc & 15) < 14 ? lc & 15 : 0) * 16;
        static_for<0, 8>([&](auto ic) { constexpr int i = decltype(ic)::value; pv[i] = ds_b128<i * 224>(ca); });
      } else {
        int j = 1;
        if constexpr (EG) {
          if (wave == 4) {
            wait_vm<0>();
            if (lds_missing(lds0 + TCH + 256, tag, LO32)) wait_flags(a.sync + FHF(panel), (lane4() & 127) * 32, tag, LO32);
          }
        }
        loop_barrier();
#pragma unroll 1
        for (; j < js; ++j) iter(j, F{}, F{}, I0{}, IM1{});
        const int ja = wm == 2 ? js + 2 : js;
#pragma unroll 1
        for (; j < ja; ++j) iter(j, F{}, F{}, I0{}, I0{});
#pragma unroll 1
        for (; j < js + 2; ++j) iter(j, F{}, F{}, I0{}, I1{});
        { int lx = lane; asm volatile("" : "+v"(lx));
          __builtin_amdgcn_raw_buffer_store_b128(__builtin_bit_cast(u32x4, accs), rC, (lx & 15) < NF ? (unsigned)((tn * 4 + wn * 2) * 3584 + (tm * 2 + (wm & 1)) * 224
                  + ((lx & 15) < 6 ? (lx & 1) * 3584 + ((lx & 15) >> 1) * 64 + (lx >> 4) * 16 : (lx >> 5) * 3584 + 192 + (lx & 16))) : 0x80000000u, 0, 16); }
#pragma unroll 1
        for (; j < NT; ++j) iter(j, F{}, F{}, I1{}, IM1{});
        done_inc();
      }
      { const RdCtx c = mkctx(false, 0, 0); pairs(I0{}, I12{}, F{}, IM1{}, c, [&](auto pc) {
          if constexpr (LDR && decltype(pc)::value == 4) {
            asm volatile("s_waitcnt lgkmcnt(0)" ::: "memory");
#pragma unroll
            for (int k = 0; k < 8; ++k) { bf16x8& q = pv[k]; asm volatile("" : "+v"(q)); }
            f32x4 cv = ((__builtin_bit_cast(f32x4, pv[0]) + __builtin_bit_cast(f32x4, pv[1])) + (__builtin_bit_cast(f32x4, pv[2]) + __builtin_bit_cast(f32x4, pv[3])))
                     + ((__builtin_bit_cast(f32x4, pv[4]) + __builtin_bit_cast(f32x4, pv[5])) + (__builtin_bit_cast(f32x4, pv[6]) + __builtin_bit_cast(f32x4, pv[7])));
#pragma unroll
            for (int k = 0; k < 4; ++k) { const float t = cv[k]; auto sw = __builtin_amdgcn_permlane16_swap(__float_as_uint(t), __float_as_uint(t), false, false); cv[k] = __uint_as_float(sw[0]) + __uint_as_float(sw[1]); }
            if ((lane & 31) < 14) *(f32x4*)(tb_cs + (lane >> 5) * 112 + wn * 56 + fr * 4) = cv;
            done_inc();
          } }); }
#define KN_PRE 0
      if constexpr ((LDR || (KN_HOLD & 2)) && KN_PRE == 0) __builtin_amdgcn_s_setprio(0);
      epilogue(LDRc);
      if constexpr (LDR && KN_PRE == 1) __builtin_amdgcn_s_setprio(0);
    };
    if (wave < NLW) run(T{}, IM1{}); else run(F{}, IM1{});
  }
  STAMP(7);
  STAMP(8);
  if constexpr (GLU) STAMPW(12, 4);
  STAMP(3);
#endif
}

#define KN_PT 13
#define KN_QR 0
#define KN_PS 0
#define KN_PG -1
#define KN_FW 1
#define KN_GQ 6
#define KN_G0 4
#define KN_OC 12
#define KN_TG 10
#define KN_PQ 1
#define KN_PZ 0
#define KN_PN 4
#define KN_QO1 3
#define KN_QO2 3
#define KN_QO3 3
#define KN_QO4 16
#define KN_QP 0
#define KN_QH 0
#define KN_QS 18
#define KN_QLB 13

constexpr int QXT = 144 * 128, QWU = 24576, QWB = 3 * QXT, QRING = QWB + 4 * QWU;
constexpr int QMU = QRING + 64, QRS = QMU + 576, QFL = QRS + 576, QTCH = QRING + 2304;
constexpr int QCS = 2 * QXT, QKH = QCS + 1024, QVH = QKH + 4096;
#define KN_QWC 8
struct SchedQ {
  static constexpr int ns(int j) { return j < 2 ? 2 : 0; }
  static constexpr int nx(int j) { return (j + 2 < 16 ? 4 : 0) + ns(j); }
  static constexpr int wh(int j) { if (j < 0) return 12; int v = wh(j - 1) + KN_QWC - ns(j); if (v > 6 * (j + 4)) v = 6 * (j + 4); return v > 96 ? 96 : v; }
  static constexpr int oh(int j) { return j < 13 ? 0 : 2 * (j == 13 ? KN_QO1 : (j == 14 ? KN_QO2 : KN_QO3)); }
  static constexpr int tot(int j) { return j < 0 ? 20 : tot(j - 1) + nx(j) + wh(j) - wh(j - 1) + oh(j) - oh(j - 1); }
  static constexpr int cnt(int j) {
    int pos = j < 2 ? 10 * (j + 1) : tot(j - 3) + 4;
    for (int i = 0; i < j; ++i) if (wh(i - 1) < 6 * (j + 1) && wh(i) >= 6 * (j + 1)) { const int q = tot(i - 1) + nx(i) + 6 * (j + 1) - wh(i - 1); if (q > pos) pos = q; }
    return tot(j - 1) - pos;
  }
};
static_assert(SchedQ::cnt(0) == 10 && SchedQ::cnt(8) == 16 && KN_QO1 <= 3 && KN_QO2 <= 6 && KN_QO3 <= 9 && KN_QWC >= 8, "schedule");
static_assert(SchedQ::cnt(KN_S1P) <= SchedQ::tot(KN_S1P - 1) - SchedQ::tot(0) - 6 && KN_S1C > KN_S1P + 2, "statistics");
struct FragQ { bf16x8 x[4]; s16x4 lo[6], hi[6]; };
__device__ __forceinline__ f32x2 ds_r2(unsigned addr) { f32x2 r; asm volatile("ds_read2_b32 %0, %1 offset1:16" : "=v"(r) : "v"(addr)); return r; }
__device__ __forceinline__ float fma1(float a, float b, float c) { float r; asm("v_fma_f32 %0, %1, %2, %3" : "=v"(r) : "v"(a), "v"(b), "v"(c)); return r; }
__device__ __forceinline__ unsigned k1_phase(const GemmArgs& a) {
  unsigned tag = 0;
#if defined(__HIP_DEVICE_COMPILE__)
  constexpr int K = D_, LDW = 3 * D_, NT = 16, XI = 5, XIMIN = 4, WI = 6;
  extern __shared__ __attribute__((aligned(16))) char smem[];
  STAMP(0);
  const unsigned lds0 = (unsigned)(size_t)(lds_char_ptr)smem;
  const int tid = threadIdx.x, lane = tid & 63;
  const int wave = __builtin_amdgcn_readfirstlane(tid >> 6);
  const int bid = blockIdx.x, xcd = bid & 7, idx = bid >> 3;
  const int tm = (xcd >> 2) * 8 + (idx >> 2), tn = (xcd & 3) * 4 + (idx & 3), m0 = tm * 128;
  const int fr = lane & 15, fq = lane >> 4, wq = (lane >> 2) & 3, wp = lane & 3;
  using I0 = std::integral_constant<int, 0>; using I1 = std::integral_constant<int, 1>; using I2 = std::integral_constant<int, 2>; using I3 = std::integral_constant<int, 3>;
  using T = std::true_type; using F = std::false_type;
  auto touch = [&](const void* base, unsigned voff, unsigned soff, unsigned lim = 0x7fffffffu) {
    __builtin_amdgcn_raw_ptr_buffer_load_lds(__builtin_amdgcn_make_buffer_rsrc((void*)base, 0, lim, 0x00020000), (lds_void_ptr)(size_t)(lds0 + QTCH), 4, voff, soff, 0, 0);
  };
  auto code_touch = [&](int line) {
    unsigned long long c0, c1; CODE_ADDR(c0, "k_all"); CODE_ADDR(c1, "k_zend");
    const unsigned len = c1 - c0 < 262144ull ? (unsigned)(c1 - c0) : 0u;
    touch((const void*)c0, (unsigned)(line * 128), 0, len);
  };
  int hj1, hj2; unsigned htv;
  auto hset = [&]() {
    const int slc = (3 - ((unsigned)(size_t)a.W0 >> 7) - (xcd & 3) * 4) & 7;
    const int hid = (idx >> 2) * 3 + (idx & 3) - ((idx & 3) > slc ? 1 : 0);
    const bool hlp = slc < 4 && (idx & 3) != slc;
    hj1 = hlp ? hid / 3 - KN_HQ : -100; hj2 = hlp ? hid / 3 + 8 - KN_HQ : -100;
    htv = (unsigned)(lane * (LDW * 2) + (hid % 3) * 2048 + ((xcd & 3) * 4 + slc) * 128);
  };
  const unsigned xo = fr * 128 + ((fq ^ ((fr >> 1) & 7)) << 4);
  auto wfrag = [&](int seg) { const int row = 8 * fq + wq; return lds0 + QWB + row * 128 + (((seg << 1) ^ wswz<128>(row)) << 4) + wp * 8; };
  const f32x4 z4 = {0.f, 0.f, 0.f, 0.f};
  auto cat = [](s16x4 lo, s16x4 hi) { return __builtin_shufflevector(lo, hi, 0, 1, 2, 3, 4, 5, 6, 7); };
  unsigned xsrc[XI], wsrc[WI];
  { const int lr = lane >> 3, w4 = wave & 3;
    const unsigned xv = (unsigned)(lr * (K * 2) + (((lane & 7) ^ ((w4 * 4 + (lr >> 1)) & 7)) << 4));
    const int r0 = m0 - 16 + w4 * 8;
#pragma unroll
    for (int i = 0; i < XI; ++i) xsrc[i] = xv + (unsigned)((i == 0 ? (r0 < 0 ? 0 : r0) : r0 + 32 * i) * (K * 2));
    const unsigned wv = (unsigned)((w4 * 8 + lr) * (LDW * 2) + tn * 128 + (((lane & 7) ^ wswz<128>(w4 * 8 + lr)) << 4));
#pragma unroll
    for (int i = 0; i < WI; ++i) wsrc[i] = wv + (unsigned)((i & 1) * 32 * (LDW * 2) + (i >> 1) * 2048); }
  unsigned mb = lds0 + wave * 1024, rx = KN_QR ? ((tm ^ tn) & 1) * 1024 : 0, rw = rx * (64 * LDW * 2 / 128);
  asm volatile("s_waitcnt lgkmcnt(0)" : "+s"(mb), "+s"(rx), "+s"(rw) : "s"(a.warm), "v"(wsrc[0]), "v"(wsrc[1]), "v"(wsrc[2]), "v"(wsrc[3]), "v"(wsrc[4]), "v"(wsrc[5]), "v"(xsrc[0]), "v"(xsrc[1]), "v"(xsrc[2]), "v"(xsrc[3]), "v"(xsrc[4]));
  __builtin_amdgcn_sched_barrier(0);
  const u32x4 qX = mk_rsrc(a.X), qW = mk_rsrc(a.W0);
  auto ldX = [&](auto ic, auto tc) { constexpr int i = decltype(ic)::value, t = decltype(tc)::value; if (i < 4 || wave < 2) ld_cx<t % 3 * QXT + i * 4096, t * 128>(mb, qX, xsrc[i], rx); };
  auto ldW = [&](auto qc) { constexpr int i = decltype(qc)::value % WI, t = decltype(qc)::value / WI; ld_ca<QWB + (t & 3) * QWU + i * 4096, t * (64 * LDW * 2), (t >= 8)>(mb, qW, wsrc[i], rw); };
  if (__builtin_expect(wave < 4, 1)) {
    static_for<0, WI>(ldW);
    static_for<0, XI>([&](auto ic) { ldX(ic, I0{}); });
  }
  __builtin_amdgcn_sched_barrier(0);
  if (__builtin_expect(wave < 4, 1)) {
    unsigned seed;
    asm volatile("s_load_dword %0, %1, %2" : "=s"(seed) : "s"(a.sync), "n"(FEP * 4));
    static_for<WI, 2 * WI>(ldW);
    static_for<0, XI>([&](auto ic) { ldX(ic, I1{}); });
    __builtin_amdgcn_sched_barrier(0);
    unsigned osrc = (unsigned)((wave * 8 + (lane >> 3)) * D_ + ((xcd & 1) * 8 + (idx & 7)) * 64 + ((lane & 7) ^ ((lane >> 3) & 6)) * 8) * 2u;
    asm volatile("" : "+v"(osrc));
    auto ldO = [&](auto qc) { constexpr int i = decltype(qc)::value >> 1, h = decltype(qc)::value & 1;
      ld_cx<wo_pos(i >> 1) + (i & 1) * 8192 + h * 4096, (((i & 1) * 8 + (i >> 1)) * 2 + h) * (32 * D_ * 2)>(mb, mk_rsrc(a.PF1), osrc, (xcd & 3) * 8 * (32 * D_ * 2)); };
    asm volatile("s_waitcnt lgkmcnt(0)\n\ts_mul_i32 %0, %1, 0x9e3779b1\n\ts_add_u32 %0, %0, 0x7f4a7c15" : "=s"(tag) : "s"(seed) : "scc");
    u32x4 sv[4]; f32x4 s1s[2], s1q[2]; float sa[4], sb;
    const char* srows = (const char*)a.X + (size_t)(m0 + tn * 8 + wave * 2) * (K * 2);
    unsigned l16 = lane * 16, sdst = lane < 2 ? (unsigned)(STAB + (m0 + tn * 8 + wave * 2 + lane) * 16) : 0x80000000u;
    asm volatile("" : "+v"(l16), "+v"(sdst));
    auto dpp = [](float t, auto c) { return __int_as_float(__builtin_amdgcn_update_dpp(0, __float_as_int(t), decltype(c)::value, 0xf, 0xf, false)); };
    auto stat = [&](auto kc) {
      constexpr int k = decltype(kc)::value;
      if constexpr (k == 0) asm volatile("" : "+v"(sv[0]), "+v"(sv[1]), "+v"(sv[2]), "+v"(sv[3]));
      if constexpr (k < 8) {
        constexpr int r = (k >> 1) & 1, h = k >> 2;
        const u32x4 one4 = {0x3f803f80u, 0x3f803f80u, 0x3f803f80u, 0x3f803f80u};
        const bf16x8 x = __builtin_bit_cast(bf16x8, sv[2 * r + h]);
        if constexpr (k & 1) s1q[r] = __builtin_amdgcn_mfma_f32_16x16x32_bf16(x, x, h ? s1q[r] : z4, 0, 0, 0);
        else s1s[r] = __builtin_amdgcn_mfma_f32_16x16x32_bf16(__builtin_bit_cast(bf16x8, one4), x, h ? s1s[r] : z4, 0, 0, 0);
      }
      if constexpr (k == 8 || k == 9) {
        constexpr int r = k - 8;
        const f32x4 g = s1q[r];
        const float d = (lane & 2) ? ((lane & 1) ? g[3] : g[2]) : ((lane & 1) ? g[1] : g[0]);
        sa[r] = fq == 0 ? s1s[r][0] : 0.f;
        sa[2 + r] = fq == wq ? d : 0.f;
        asm volatile("" : "+v"(sa[r]), "+v"(sa[2 + r]));
      }
      if constexpr (k == 10 || k == 11) { constexpr int i = (k - 10) * 2; sa[i] = ((lane & 1) ? sa[i + 1] : sa[i]) + dpp((lane & 1) ? sa[i] : sa[i + 1], std::integral_constant<int, 0xb1>{}); asm volatile("" : "+v"(sa[i])); }
      if constexpr (k == 12) sb = ((lane & 2) ? sa[2] : sa[0]) + dpp((lane & 2) ? sa[0] : sa[2], std::integral_constant<int, 0x4e>{});
      if constexpr (k == 13) { sb += dpp(sb, std::integral_constant<int, 0x124>{}); sb += dpp(sb, std::integral_constant<int, 0x128>{}); }
      if constexpr (k == 14) { auto q = __builtin_amdgcn_permlane16_swap(__float_as_uint(sb), __float_as_uint(sb), false, false); sb = __uint_as_float(q[0]) + __uint_as_float(q[1]); }
      if constexpr (k == 15) { auto q = __builtin_amdgcn_permlane32_swap(__float_as_uint(sb), __float_as_uint(sb), false, false); sb = (__uint_as_float(q[0]) + __uint_as_float(q[1])) * (1.f / 1024.f); }
      if constexpr (k == 16) { const float var = dpp(sb, std::integral_constant<int, 0x4e>{}) - sb * sb; sa[0] = __builtin_amdgcn_rsqf((var > 0.f ? var : 0.f) + 1e-5f); asm volatile("" : "+v"(sa[0])); }
      if constexpr (k >= 12 && k < 16) asm volatile("" : "+v"(sb));
      if constexpr (k == 17) {
        __builtin_amdgcn_raw_buffer_store_b128((u32x4){__float_as_uint(-sb), __float_as_uint(sa[0]), tag, ~tag}, __builtin_amdgcn_make_buffer_rsrc((void*)a.cs, 0, 0x7fffffff, 0x00020000), sdst, 0, 16);
        asm volatile("s_nop 1" ::: "memory");
      }
    };
    hset();
    if (KN_QH) __builtin_amdgcn_s_setprio(KN_QH);
    const unsigned wa = wfrag(wave);
    f32x4 acq = z4, ack = z4, acv = z4;
    const unsigned long long tb = (unsigned long long)((wave == 3 || wave == 0) ? a.PF1 : a.W0);
    unsigned tv = (wave & 1) ? (unsigned)((bid * 64 + lane) * 128) : htv, ts = 8 * 64 * LDW * 2, to = ((hj1 >= 0 ? hj1 : hj2) + KN_HQ) * (64 * LDW * 2);
    unsigned evm = (hj1 >= 0 ? 1u << hj1 : 0u) | (hj2 > 0 ? 1u << hj2 : 0u);
    if (wave == 3) { evm = 2u << (idx % 12); to = 0; }
    if (wave == 1) {
      const int t1 = (bid + 256) / 48 - KN_GQ, t2 = (bid + 512) / 48 - KN_GQ;
      evm = KN_GQ ? (1u << (t1 > 0 ? t1 : 0)) | (1u << t2) : 0u; ts = to = 2u << 20;
    }
    if (wave == 0) {
      evm = KN_OC ? 1u << KN_OC : 0u; to = 0;
      tv = lane < 32 ? (unsigned)((idx * 32 + lane) * 2048 + (xcd & 1) * 1024 + ((3 - ((unsigned)(size_t)a.PF1 >> 7)) & 7) * 128) : 0x80000000u;
    }
    const int se = wave * 64 - 64 + lane, sr = m0 - 16 + se;
    const bool sok = (unsigned)se < 144u;
    unsigned ssrc = sok ? (unsigned)(STAB + (sr < 0 ? se : sr) * 16) : (wave == 0 ? (unsigned)(VTAB + lane * 16) : 0x80000000u), sld = lds0 + QRING + STL + (se & 255) * 16;
    unsigned smu = lds0 + (sok ? QMU + se * 4 : QTCH), srs = lds0 + (sok ? QRS + (se & 1) * 288 + (se >> 1) * 4 : QTCH);
    unsigned xh0[3], xh1[3];
#pragma unroll
    for (int i = 0; i < 3; ++i) { xh0[i] = fr == 0 ? lds0 + QRING : lds0 + i * QXT + xo; xh1[i] = fr == 0 ? lds0 + QRING : lds0 + i * QXT + (xo ^ 64); asm volatile("" : "+v"(xh0[i]), "+v"(xh1[i])); }
    unsigned wb[2] = {wa, wa + 2 * QWU};
    unsigned gv = lane < 56 ? (unsigned)(((bid & 1) * 512 + (wave >> 1) * 128 + (bid >> 1)) * (DF_ * 2) + lane * 128) : 0x80000000u;
    asm volatile("" : "+v"(gv), "+v"(tv), "+v"(ssrc), "+v"(sld), "+v"(smu), "+v"(srs), "+v"(wb[0]), "+v"(wb[1]));
    auto fetch = [&]() { __builtin_amdgcn_raw_ptr_buffer_load_lds(__builtin_amdgcn_make_buffer_rsrc((void*)a.cs, 0, 0x7fffffff, 0x00020000), (lds_void_ptr)(size_t)(lds0 + QRING + STL + ((wave + 3) & 3) * 1024), 16, ssrc, 0, 0, 16); };
    u32x4 one1; { const unsigned o = fr == 0 ? 0x3f803f80u : 0u; one1 = (u32x4){o, o, o, o}; asm volatile("" : "+v"(one1)); }
    unsigned tsq = 0;
    using SQ = SchedQ;
    static_for<0, NT>([&](auto jc) {
      constexpr int j = decltype(jc)::value, NX = (j + 2 < NT ? XI : 0) + SQ::ns(j), NW = SQ::wh(j) - SQ::wh(j - 1), NL = NX + NW + SQ::oh(j) - SQ::oh(j - 1);
      wait_vm<SQ::cnt(j)>();
      lds_barrier();
      bf16x8 xh[2]; s16x4 lo[2][3], hi[2][3]; bf16x8 ent;
      auto rd = [&](auto rc) { constexpr int r = decltype(rc)::value, q = r < 4 ? r : r - 2, n = q >> 2, h = (q >> 1) & 1, o = (j & 1) * QWU + h * 4096 + n * 8192;
        if constexpr (r == 4 || r == 5) xh[r - 4] = ds_b128<0>(r == 5 ? xh1[j % 3] : xh0[j % 3]);
        else if constexpr (q & 1) hi[h][n] = ds_tr<o + 512>(wb[(j >> 1) & 1]); else lo[h][n] = ds_tr<o>(wb[(j >> 1) & 1]); };
      auto mm = [&](auto ic) { constexpr int n = decltype(ic)::value;
        asm volatile("s_waitcnt lgkmcnt(%0)" :: "n"(n == 0 ? 6 : (n == 1 ? 4 : 0)) : "memory");
        asm volatile("" : "+v"(lo[0][n]), "+v"(hi[0][n]), "+v"(lo[1][n]), "+v"(hi[1][n]));
        if constexpr (n == 1) asm volatile("" : "+v"(xh[0]), "+v"(xh[1]));
#pragma unroll
        for (int h = 0; h < 2; ++h) {
          if constexpr (n == 0) acq = __builtin_amdgcn_mfma_f32_16x16x32_bf16(cat(lo[h][0], hi[h][0]), __builtin_bit_cast(bf16x8, one1), acq, 0, 0, 0);
          if constexpr (n == 1) ack = __builtin_amdgcn_mfma_f32_16x16x32_bf16(cat(lo[h][1], hi[h][1]), xh[h], ack, 0, 0, 0);
          if constexpr (n == 2) acv = __builtin_amdgcn_mfma_f32_16x16x32_bf16(xh[h], cat(lo[h][2], hi[h][2]), acv, 0, 0, 0);
        } };
      if constexpr (j == KN_S1C + 3) ent = ds_b128<0>(sld);
      static_for<0, (NL > 11 ? NL : 11)>([&](auto gc) {
        constexpr int g = decltype(gc)::value;
        __builtin_amdgcn_sched_barrier(0);
        if constexpr (g < NX - SQ::ns(j)) ldX(gc, std::integral_constant<int, j + 2>{});
        else if constexpr (g < NX) {
          constexpr int q = 2 * j + g - XI;
          asm volatile("s_nop 4\n\tglobal_load_dwordx4 %0, %1, %2 offset:%3" : "=&v"(sv[q]) : "v"(l16), "s"(srows), "n"(q * 1024) : "memory");
        }
        else if constexpr (g < NX + NW) ldW(std::integral_constant<int, SQ::wh(j - 1) + g - NX>{});
        else if constexpr (g < NL) ldO(std::integral_constant<int, SQ::oh(j - 1) + g - NX - NW>{});
        else if constexpr (g < 9 && j < NT - 1) asm volatile("s_nop 7");
        if constexpr (j <= 12 && g == KN_TG) { if (__builtin_expect((evm >> j) & 1, 0)) { touch((const void*)tb, tv, to); to += ts; } }
        if constexpr (g == (KN_PG < 0 ? (NL > 11 ? NL : 11) - 1 : KN_PG) && j <= KN_PT && j >= KN_PT - 3 * KN_PS && (KN_PS == 0 || (KN_PT - j) % (KN_PS ? KN_PS : 1) == 0)) {
          if (KN_PS == 0 || wave == 3 - (KN_PT - j) / (KN_PS ? KN_PS : 1)) touch(wave & 1 ? a.CW1 : a.CW0, gv, 0);
        }
        __builtin_amdgcn_sched_barrier(0);
        if constexpr (g < 7) { rd(std::integral_constant<int, 2 * g>{}); rd(std::integral_constant<int, 2 * g + 1>{}); }
        if constexpr (g == 4 || g == 7 || g == 9) mm(std::integral_constant<int, g == 4 ? 0 : (g == 7 ? 1 : 2)>{});
        if constexpr (j == KN_S1P && g < 4) { stat(std::integral_constant<int, 2 * g>{}); stat(std::integral_constant<int, 2 * g + 1>{}); }
        if constexpr (j == KN_S1P && (g == 9 || g == 10)) stat(std::integral_constant<int, g - 1>{});
        if constexpr (j == KN_S1P + 1 && g < 8) stat(std::integral_constant<int, 10 + g>{});
      });
      __builtin_amdgcn_sched_barrier(0);
      if constexpr (j == KN_S1C) fetch();
      if constexpr (j == KN_S1C + 3) {
        static_assert(SQ::cnt(j) <= SQ::tot(j - 1) - SQ::tot(KN_S1C), "table");
        asm volatile("" : "+v"(ent));
        u32x4 t = __builtin_bit_cast(u32x4, ent);
        auto stale = [&]() { return __builtin_amdgcn_ballot_w64(wave == 0 ? ((t[0] ^ tag) | (t[1] ^ tag) | (t[2] ^ tag) | (t[3] ^ tag)) > 1u : sok && (t[2] != tag || t[3] != ~tag)) != 0; };
        if (__builtin_expect(stale(), 0)) {
          unsigned polls = 0;
          do {
            fetch();
            wait_vm<0>();
            t = __builtin_bit_cast(u32x4, ds_b128<0>(sld));
            asm volatile("s_waitcnt lgkmcnt(0)" : "+v"(t) :: "memory");
          } while (stale() && ++polls < 4000000u);
        }
        ds_w32(smu, (int)t[0]); ds_w32(srs, (int)t[1]);
        if (wave == 0) {
          int pc = 0;
#pragma unroll
          for (int i = 0; i < 4; ++i) pc += __builtin_popcountll(__builtin_amdgcn_ballot_w64(t[i] & 1));
          tag = (tag & ~1u) | (pc > 128 ? 1u : 0u);
          ds_w32(lds0 + QFL + 8, (int)tag);
          const int p2 = (xcd >> 1) * 2 + (idx >> 4), mb = (xcd & 1) * 16 + (idx & 15);
#pragma unroll
          for (int i = 0; i < 3; ++i)
            __builtin_amdgcn_raw_buffer_store_b128((u32x4){~0u, ~0u, ~0u, ~0u}, __builtin_amdgcn_make_buffer_rsrc((void*)a.st, 0, 0x7fffffff, 0x00020000), l16 + i * 1024,
                                                   rbH(p2 * 16 + (mb >> 1), SIDE) + omH((xcd >> 2) * 16384) + (mb & 1) * 3072, 16);
          asm volatile("s_nop 1" ::: "memory");
        }
      }
      if constexpr (j == KN_S1C + 6) {
        static_assert(SQ::cnt(j) <= SQ::tot(j - 1) - SQ::tot(KN_S1C + 3), "marks");
        if (tid == 0) set_flag(a.sync + FHM((xcd >> 1) * 2 + (idx >> 4)) + (xcd & 1) * 16 + (idx & 15), tag);
      }
      if constexpr (j == 1) {
        if (wave == 0) {
          u32x4 d = __builtin_bit_cast(u32x4, ds_b128<0>(lds0 + QFL + 16));
          asm volatile("s_waitcnt lgkmcnt(0)" : "+v"(d) :: "memory");
          const unsigned vote = ((d[0] + d[2] > d[1] + d[3] ? 0u : 1u) ^ (xcd >> 2)) & 1u;
          __builtin_amdgcn_raw_buffer_store_b32((tag & ~1u) | vote, __builtin_amdgcn_make_buffer_rsrc((void*)a.cs, 0, 0x7fffffff, 0x00020000), lane == 0 ? (unsigned)(VTAB + bid * 4) : 0x80000000u, 0, 16);
        }
      }
      if constexpr (j == NT - 3 && (KN_CT & 2)) { if (wave == 3) code_touch(lane * 32 + idx); }
    });
    {
      int lx = lane; asm volatile("" : "+v"(lx));
      const int fr = lx & 15, fq = lx >> 4;
      if (fr == 0) { *(f32x4*)(smem + QCS + wave * 64 + fq * 16) = acq; *(f32x4*)(smem + QCS + 256 + wave * 64 + fq * 16) = ack; }
      if (fq == 0) *(float*)(smem + QCS + 512 + wave * 64 + fr * 4) = acv[0];
      asm volatile("" ::: "memory");
      const f32x4 ck = __builtin_bit_cast(f32x4, ds_b128<QCS + 256>(lds0 + wave * 64 + fq * 16)), m4 = __builtin_bit_cast(f32x4, ds_b128<0>(lds0 + QMU + fq * 16));
      int cv = ds_r32(lds0 + QCS + 512 + wave * 64 + fr * 4), m1 = ds_r32(lds0 + QMU + fr * 4), tg = ds_r32(lds0 + QFL + 8);
      f32x4 c4 = ck, n4 = m4;
      asm volatile("s_waitcnt lgkmcnt(0)" : "+v"(c4), "+v"(n4), "+v"(cv), "+v"(m1), "+v"(ack), "+v"(acv), "+v"(tg) :: "memory");
      tag = __builtin_amdgcn_readfirstlane(tg);
      const float nm = __int_as_float(m1), nc = __int_as_float(cv);
      const u32x2 kk = {pk_bf16(fma1(nm, c4[0], ack[0]), fma1(nm, c4[1], ack[1])), pk_bf16(fma1(nm, c4[2], ack[2]), fma1(nm, c4[3], ack[3]))};
      const u32x2 vv = {pk_bf16(fma1(nc, n4[0], acv[0]), fma1(nc, n4[1], acv[1])), pk_bf16(fma1(nc, n4[2], acv[2]), fma1(nc, n4[3], acv[3]))};
      *(u32x2*)(smem + QKH + (wave >> 1) * 1024 + lx * 16 + (wave & 1) * 8) = kk;
      *(u32x2*)(smem + QVH + (wave >> 1) * 1024 + lx * 16 + (wave & 1) * 8) = vv;
    }
    if (KN_QH) __builtin_amdgcn_s_setprio(0);
    lds_barrier();
    static_for<SQ::oh(NT - 1), 2 * KN_QO4>(ldO);
    wait_vm<2 * (KN_QO4 - 2)>();
    const int sd = SIDE;
    unsigned fo = tid == 0 ? (unsigned)(FLO(tm) + tn * 32) * 4u : 0x80000000u, ft = tag;
    const auto rF = __builtin_amdgcn_make_buffer_rsrc((void*)a.sync, 0, 147456, 0x00020000);
    asm volatile("" : "+v"(fo), "+v"(ft) : "s"(rF));
    raw_barrier();
    if (wave == 0) __builtin_amdgcn_raw_buffer_store_b32(ft, rF, fo, 0, 16);
    static_for<2 * KN_QO4, 32>(ldO);
  } else {
    const int wm = (wave >> 1) & 1, hd = wave & 1;
    if (wave == 4) {
      if (lane < 4) { *(unsigned*)(smem + QRING + lane * 4) = 0x3f803f80u; if (lane < 2) *(unsigned*)(smem + QFL + lane * 4) = 0u; }
      if (KN_FW) __builtin_amdgcn_raw_ptr_buffer_load_lds(__builtin_amdgcn_make_buffer_rsrc((void*)a.sync, 0, 147456, 0x00020000), (lds_void_ptr)(size_t)(lds0 + QTCH), 4, lane * 32768 + bid * 128, 0, 0, 16);
    }
    if (wave == 6) { hset(); if (hj1 < 0 && hj1 > -100) touch(a.W0, htv, (hj1 + KN_HQ) * (64 * LDW * 2));
      if (KN_G0 && bid >= KN_G0 * 48) touch(a.W0, (bid * 64 + lane) * 128, 0); }
    if ((KN_CT & 1) && wave == 7) code_touch(lane * 32 + idx);
    {
      const int id = bid * 2 + wm;
      const char* pb = (const char*)a.sync + PRB + (id >> 3) * 16384 + hd * 8192 + (id & 7) * (KN_PN == 8 ? 128 : 256);
      unsigned long long p0, p1; unsigned t;
#define SL4(a, b) "s_load_dword %1, %3, 0x" a "00\n\ts_load_dword %1, %3, 0x" b "00\n\ts_load_dword %1, %3, 0x1" a "00\n\ts_load_dword %1, %3, 0x1" b "00\n\t"
      if (KN_PN == 8) asm volatile("s_sleep %5\n\ts_memtime %0\n\ts_waitcnt lgkmcnt(0)\n\t" SL4("0", "8") SL4("4", "c") "s_waitcnt lgkmcnt(%4)\n\ts_memtime %2\n\ts_waitcnt lgkmcnt(0)" : "=&s"(p0), "=&s"(t), "=&s"(p1) : "s"(pb), "n"(KN_PQ), "n"(KN_PZ));
      else asm volatile("s_sleep %5\n\ts_memtime %0\n\ts_waitcnt lgkmcnt(0)\n\t" SL4("0", "8") "s_waitcnt lgkmcnt(%4)\n\ts_memtime %2\n\ts_waitcnt lgkmcnt(0)" : "=&s"(p0), "=&s"(t), "=&s"(p1) : "s"(pb), "n"(KN_PQ), "n"(KN_PZ));
      ds_w32(lds0 + QFL + 16 + (wave - 4) * 4, (int)((unsigned)p1 - (unsigned)p0));
    }
    f32x2 bias[2], bias0[2];
    {
      const int srow = m0 + wm * 64 + fr;
      const unsigned mk = 0x5555u & ((2u << (srow < 15 ? srow : 15)) - 1u), sft = (fr & 14) - fq * 4 + 14;
      const unsigned b0 = ~(0x5555u >> sft), b1 = ~(0x55550000u >> sft), c0 = ~(mk >> sft), c1 = ~((mk << 16) >> sft);
#pragma unroll
      for (int j = 0; j < 4; ++j) {
        bias[j >> 1][j & 1] = __uint_as_float((unsigned)__builtin_amdgcn_sbfe((int)(j < 2 ? b0 : b1), 2 - 2 * (j & 1), 1) & 0xff61b1e6u);
        bias0[j >> 1][j & 1] = __uint_as_float((unsigned)__builtin_amdgcn_sbfe((int)(j < 2 ? c0 : c1), 2 - 2 * (j & 1), 1) & 0xff61b1e6u);
      }
    }
    unsigned pl = (fr & 1) ? 0x01000c0cu : 0x0c0c0100u, ph = (fr & 1) ? 0x03020c0cu : 0x0c0c0302u;
    unsigned ta = lds0 + QRING + STL + (16 + wm * 64 + fr) * 16, tm4 = lds0 + QMU + (16 + wm * 64 + fq * 4) * 4, trk = lds0 + QRS + (fr & 1) * 288 + (wm * 32 + fq * 2) * 4;
    unsigned tcs = lds0 + hd * 128 + fq * 16, tcv = lds0 + QCS + 512 + hd * 128 + fr * 4, tex = lds0 + hd * 1024 + lane * 16, tfl = lds0 + QFL + hd * 4, odst = rbH((m0 >> 4) + wm * 4, xcd >> 2) + omH(tn * 2048) + hd * 1024 + lane * 16;
    asm volatile("" : "+v"(bias[0]), "+v"(bias[1]), "+v"(bias0[0]), "+v"(bias0[1]), "+v"(pl), "+v"(ph), "+v"(ta), "+v"(tm4), "+v"(trk), "+v"(tcs), "+v"(tcv), "+v"(tex), "+v"(tfl), "+v"(odst));
    if (KN_QP) __builtin_amdgcn_s_setprio(KN_QP);
    f32x4 acc[6][4];
#pragma unroll
    for (int n = 0; n < 6; ++n)
#pragma unroll
      for (int m = 0; m < 4; ++m) acc[n][m] = z4;
    const unsigned xa[2] = {lds0 + (16 + wm * 64) * 128 + xo, lds0 + (16 + wm * 64) * 128 + (xo ^ 64)}, wa[2] = {wfrag(hd * 2), wfrag(hd * 2 + 1)};
    struct Ctx { unsigned x[2], w[2]; } c = {{xa[0], xa[1]}, {wa[0], wa[1]}};
    using SP = SchedP<6, KN_QS>;
    FragQ fa, fb;
    auto rd = [&](auto rc) {
      constexpr int r = decltype(rc)::value;
      if constexpr (r >= 24) { constexpr int m = (r - 24) >> 1; if constexpr (r & 1) fb.x[m] = ds_b128<m * 2048>(c.x[1]); else fa.x[m] = ds_b128<m * 2048>(c.x[0]); }
      else { constexpr int n = r >> 2, o = ((r >> 1) & 1) * 4096 + (n >> 1) * 8192 + (r & 1) * 512; FragQ& f = (r & 2) ? fb : fa;
        if constexpr (r & 1) f.hi[n] = ds_tr<o>(c.w[n & 1]); else f.lo[n] = ds_tr<o>(c.w[n & 1]); }
    };
    auto pin = [&](auto pc) {
      static_for<0, 32>([&](auto rc) { constexpr int r = decltype(rc)::value;
        if constexpr (SP::need(r) == decltype(pc)::value) {
          FragQ& f = (r >= 24 ? r & 1 : r & 2) ? fb : fa;
          if constexpr (r >= 24) { bf16x8& q = f.x[(r - 24) >> 1]; asm volatile("" : "+v"(q)); } else { s16x4& q = (r & 1) ? f.hi[r >> 2] : f.lo[r >> 2]; asm volatile("" : "+v"(q)); } } });
    };
    int sx = QXT, sw = QWU;
    auto pairs = [&](auto PBc, auto PEc, auto RDc, auto&& gap) {
      constexpr bool RD = decltype(RDc)::value;
      static_for<decltype(PBc)::value, decltype(PEc)::value>([&](auto pc) {
        constexpr int p = decltype(pc)::value, A = SP::pa(p), n = SP::pn(A), m = SP::pm(A);
        if constexpr (SP::cnt(p) >= 0) { asm volatile("s_waitcnt lgkmcnt(%0)" :: "n"(SP::cnt(p)) : "memory"); pin(pc); }
        static_for<2 * p, 2 * p + 2>([&](auto gc) {
          constexpr int g = decltype(gc)::value;
          FragQ& u = ((A ^ g) & 1) ? fb : fa;
          if constexpr (n < 4) acc[n][m] = __builtin_amdgcn_mfma_f32_16x16x32_bf16(cat(u.lo[n], u.hi[n]), u.x[m], acc[n][m], 0, 0, 0);
          else acc[n][m] = __builtin_amdgcn_mfma_f32_16x16x32_bf16(u.x[m], cat(u.lo[n], u.hi[n]), acc[n][m], 0, 0, 0);
          __builtin_amdgcn_sched_barrier(0);
          gap(gc);
          if constexpr (RD) {
            static_for<0, 32>([&](auto rc) { if constexpr (SP::gap(decltype(rc)::value) == g) rd(rc); });
            if constexpr (g == 13) { sx = sx + QXT; sw = sw + QWU; asm volatile("" : "+s"(sx), "+s"(sw)); }
            if constexpr (g == 17) { sx = sx == 3 * QXT ? 0 : sx; asm volatile("" : "+s"(sx)); }
            if constexpr (g == 21) { sw = sw == 4 * QWU ? 0 : sw; asm volatile("" : "+s"(sw)); }
            if constexpr (g == 44) { c.x[0] = xa[0] + sx; c.x[1] = xa[1] + sx; asm volatile("" : "+v"(c.x[0]), "+v"(c.x[1])); }
            if constexpr (g == 45) { c.w[0] = wa[0] + sw; c.w[1] = wa[1] + sw; asm volatile("" : "+v"(c.w[0]), "+v"(c.w[1])); }
          }
          __builtin_amdgcn_sched_barrier(0);
        });
      });
      if constexpr (RD) { asm volatile("s_waitcnt lgkmcnt(0)" ::: "memory"); pin(std::integral_constant<int, 24>{}); }
    };
    static_assert(SP::gap(23) <= 44, "addresses");
    using I12 = std::integral_constant<int, 12>; using I24 = std::integral_constant<int, 24>;
    lds_barrier();
    static_for<0, 24>([&](auto gc) { static_for<0, 32>([&](auto rc) { if constexpr (SP::gap(decltype(rc)::value) == decltype(gc)::value) rd(rc); }); });
    STAMP(1);
    pairs(I12{}, I24{}, T{}, [&](auto) {});
#pragma unroll 1
    for (int j = 1; j < NT; ++j) pairs(I0{}, I24{}, T{}, [&](auto pc) { if constexpr (decltype(pc)::value == 1) raw_barrier(); });
    f32x2 st[4], rk[5], cv, fs[4][2]; f32x4 m4[4], cs[4]; int tg;
    pairs(I0{}, I12{}, F{}, [&](auto ic) { constexpr int I = decltype(ic)::value;
      if constexpr (I < 4) st[I] = ds_b64<I * 256>(ta);
      if constexpr (I == 4) tg = ds_r32(lds0 + QFL + 8);
      if constexpr (I < 5) rk[I] = ds_b64<I * 32>(trk);
      if constexpr (I >= 5 && I < 9) m4[I - 5] = __builtin_bit_cast(f32x4, ds_b128<(I - 5) * 64>(tm4));
      if constexpr (I == 9) { asm volatile("s_waitcnt lgkmcnt(4)" : "+v"(st[0]), "+v"(st[1]), "+v"(st[2]), "+v"(st[3]), "+v"(rk[0]), "+v"(rk[1]), "+v"(rk[2]), "+v"(rk[3]), "+v"(rk[4]) :: "memory"); }
      if constexpr (I == 9 || I == 10) { constexpr int m = (I - 9) * 2; const float sc2 = 0.17677669529663687f * 1.4426950408889634f; st[m][1] *= sc2; st[m + 1][1] *= sc2; asm volatile("" : "+v"(st[m]), "+v"(st[m + 1])); }
      if constexpr (I >= 11 && I < 19) { constexpr int m = (I - 11) >> 1, h = (I - 11) & 1; fs[m][h] = rk[m + h] * st[m][1]; asm volatile("" : "+v"(fs[m][h])); }
      if constexpr (I == 19) { int& q = tg; asm volatile("" : "+v"(q)); tag = __builtin_amdgcn_readfirstlane(tg); if (KN_NUMA & 1) odst ^= ((SIDE ^ (xcd >> 2)) & 1) << 13; asm volatile("" : "+v"(odst)); }
      if constexpr (I == KN_QLB) raw_barrier();
      if constexpr (I >= 14 && I < 18) { constexpr int q = (I - 14) ^ 2; cs[q] = __builtin_bit_cast(f32x4, ds_b128<QCS + (q >> 1) * 256 + (q & 1) * 64>(tcs)); }
      if constexpr (I == 18) cv = ds_r2(tcv); });
    static_assert(KN_QLB <= 14, "barrier");
    STAMP(2);
    asm volatile("s_waitcnt lgkmcnt(0)" : "+v"(cs[0]), "+v"(cs[1]), "+v"(cs[2]), "+v"(cs[3]), "+v"(cv), "+v"(m4[0]), "+v"(m4[1]), "+v"(m4[2]), "+v"(m4[3]) :: "memory");
    u32x4 Q[4], Kf[5], V[5];
    auto nrm = [&](f32x4 x, f32x4 y, float sc) {
      const f32x2 p = (f32x2){y[0], y[1]} * sc + (f32x2){x[0], x[1]}, q = (f32x2){y[2], y[3]} * sc + (f32x2){x[2], x[3]};
      return (u32x2){pk_bf16(p[0], p[1]), pk_bf16(q[0], q[1])}; };
    auto c4 = [](u32x2 p, u32x2 q) { return (u32x4){p[0], p[1], q[0], q[1]}; };
    auto mkK = [&](int m) { Kf[m + 1] = c4(nrm(acc[2][m], cs[2], st[m][0]), nrm(acc[3][m], cs[3], st[m][0])); };
    auto mkQ = [&](int m) { Q[m] = c4(nrm(acc[0][m], cs[0], st[m][0]), nrm(acc[1][m], cs[1], st[m][0])); };
    auto mkV = [&](int m) { V[m + 1] = c4(nrm(acc[4][m], m4[m], cv[0]), nrm(acc[5][m], m4[m], cv[1])); };
    mkK(3); mkV(3);
    if (wm == 0) asm volatile("ds_write_b128 %0, %1 offset:%5\n\tds_write_b128 %0, %2 offset:%6\n\tds_write_b32 %3, %4" :: "v"(tex), "v"(Kf[4]), "v"(V[4]), "v"(tfl), "v"(pl), "n"(QKH + 2048), "n"(QVH + 2048) : "memory");
    __builtin_amdgcn_sched_barrier(0);
    f32x4 sc[4][2]; f32x2 lg[4][2]; float mx[4];
    auto qk = [&](int m, int h) { sc[m][h] = __builtin_amdgcn_mfma_f32_16x16x32_bf16(__builtin_bit_cast(bf16x8, Kf[m + h]), __builtin_bit_cast(bf16x8, Q[m]), z4, 0, 0, 0); };
    mkQ(3); mkK(2); qk(3, 1); qk(3, 0); mkQ(2); mkK(1); qk(2, 1); qk(2, 0); mkQ(1); mkK(0); qk(1, 1); qk(1, 0);
    __builtin_amdgcn_sched_barrier(0);
    int fl; bf16x8 k0, v0;
    tex += wm * 2048;
    auto front = [&]() { fl = ds_r32(tfl); k0 = ds_b128<QKH>(tex); v0 = ds_b128<QVH>(tex); };
    front();
    __builtin_amdgcn_sched_barrier(0);
    mkQ(0); qk(0, 1); mkV(2); mkV(1);
    __builtin_amdgcn_sched_barrier(0);
    asm volatile("s_waitcnt lgkmcnt(0)" : "+v"(fl), "+v"(k0), "+v"(v0), "+v"(V[3]), "+v"(V[2]) :: "memory");
    if (__builtin_expect(wm && __builtin_amdgcn_readfirstlane(fl) == 0, 0)) {
      unsigned polls = 0;
      do { front(); asm volatile("s_waitcnt lgkmcnt(0)" : "+v"(fl), "+v"(k0), "+v"(v0) :: "memory"); } while (__builtin_amdgcn_readfirstlane(fl) == 0 && ++polls < 40000000u);
    }
    Kf[0] = __builtin_bit_cast(u32x4, k0); V[0] = __builtin_bit_cast(u32x4, v0);
    qk(0, 0); mkV(0);
    STAMP(4);
    __builtin_amdgcn_sched_barrier(0);
    const bool par = pl & 4;
#pragma unroll
    for (int m = 3; m >= 0; --m) {
#pragma unroll
      for (int h = 0; h < 2; ++h) {
        float e[4] = {sc[m][h][0], sc[m][h][1], sc[m][h][2], sc[m][h][3]};
        asm("" : "+v"(e[0]), "+v"(e[2]));
        lg[m][h] = (f32x2){par ? e[1] : e[0], par ? e[3] : e[2]} * fs[m][h] + (m == 0 ? bias0[h] : bias[h]);
      }
      mx[m] = fmaxf(fmaxf(lg[m][0][0], lg[m][0][1]), fmaxf(lg[m][1][0], lg[m][1][1]));
    }
    __builtin_amdgcn_sched_barrier(0);
    auto s16 = [](float x, float y) { auto p = __builtin_amdgcn_permlane16_swap(__float_as_uint(x), __float_as_uint(y), false, false); return (f32x2){__uint_as_float(p[0]), __uint_as_float(p[1])}; };
    auto s32 = [](float x, float y) { auto p = __builtin_amdgcn_permlane32_swap(__float_as_uint(x), __float_as_uint(y), false, false); return (f32x2){__uint_as_float(p[0]), __uint_as_float(p[1])}; };
    auto spread = [&](float r) { const f32x2 t = s32(r, r), u = s16(t[0], t[0]), w = s16(t[1], t[1]); mx[0] = u[0]; mx[1] = u[1]; mx[2] = w[0]; mx[3] = w[1]; };
    { const f32x2 p = s16(mx[0], mx[1]), q = s16(mx[2], mx[3]), r = s32(fmaxf(p[0], p[1]), fmaxf(q[0], q[1])); spread(fmaxf(r[0], r[1])); }
    __builtin_amdgcn_sched_barrier(0);
#pragma unroll
    for (int m = 3; m >= 0; --m) {
#pragma unroll
      for (int h = 0; h < 2; ++h) { const f32x2 d = lg[m][h] - mx[m]; lg[m][h] = (f32x2){__builtin_amdgcn_exp2f(d[0]), __builtin_amdgcn_exp2f(d[1])}; }
      const f32x2 t = lg[m][0] + lg[m][1];
      mx[m] = t[0] + t[1];
    }
    __builtin_amdgcn_sched_barrier(0);
    { const f32x2 p = s16(mx[0], mx[1]), q = s16(mx[2], mx[3]);
#pragma unroll
      for (int m = 3; m >= 0; --m) lg[m][0] = lg[m][0] * rk[m];
      const f32x2 r = s32(p[0] + p[1], q[0] + q[1]);
#pragma unroll
      for (int m = 3; m >= 0; --m) lg[m][1] = lg[m][1] * rk[m + 1];
      spread(__builtin_amdgcn_rcpf(r[0] + r[1])); }
    __builtin_amdgcn_sched_barrier(0);
    auto half = [](u32x4 v, int h) { return __builtin_bit_cast(s16x4, (u32x2){v[2 * h], v[2 * h + 1]}); };
#pragma unroll
    for (int m = 3; m >= 0; --m) {
      s16x4 pf[2];
#pragma unroll
      for (int h = 0; h < 2; ++h) { const f32x2 t = lg[m][h] * mx[m]; const unsigned u = pk_bf16(t[0], t[1]); pf[h] = __builtin_bit_cast(s16x4, (u32x2){__builtin_amdgcn_perm(0u, u, pl), __builtin_amdgcn_perm(0u, u, ph)}); }
#pragma unroll
      for (int f = 0; f < 2; ++f) sc[m][f] = __builtin_amdgcn_mfma_f32_16x16x16bf16_1k(half(V[m], f), pf[0], z4, 0, 0, 0);
#pragma unroll
      for (int f = 0; f < 2; ++f) sc[m][f] = __builtin_amdgcn_mfma_f32_16x16x16bf16_1k(half(V[m + 1], f), pf[1], sc[m][f], 0, 0, 0);
    }
    __builtin_amdgcn_sched_barrier(0);
    STAMP(10);
    const auto rO = __builtin_amdgcn_make_buffer_rsrc((void*)a.O, 0, 0x7fffffff, 0x00020000);
    u32x4 ov[4];
#pragma unroll
    for (int m = 3; m >= 0; --m) {
      ov[m] = (u32x4){pk_bf16(sc[m][0][0], sc[m][0][1]), pk_bf16(sc[m][0][2], sc[m][0][3]), pk_bf16(sc[m][1][0], sc[m][1][1]), pk_bf16(sc[m][1][2], sc[m][1][3])};
      __builtin_amdgcn_raw_buffer_store_b128(ov[m], rO, odst, m * RSH, 16);
    }
    asm volatile("" :: "v"(ov[0]), "v"(ov[1]), "v"(ov[2]), "v"(ov[3]));
    if (KN_QP) __builtin_amdgcn_s_setprio(0);
    STAMP(7);
    wait_vm<0>();
    STAMP(8);
    raw_barrier();
  }
  STAMP(3);
#endif
  return tag;
}

struct FragW { s16x4 lo[4], hi[4]; };
__device__ __forceinline__ void wo_phase(const GemmArgs& a, const GemmArgs& nx, unsigned tag) {
#if defined(__HIP_DEVICE_COMPILE__)
  extern __shared__ __attribute__((aligned(16))) char smem[];
  STAMP(0);
  const unsigned lds0 = (unsigned)(size_t)(lds_char_ptr)smem;
  const int tid = threadIdx.x, lane = tid & 63;
  const int wave = __builtin_amdgcn_readfirstlane(tid >> 6);
  const int bid = blockIdx.x, xcd = bid & 7, idx = bid >> 3;
  const int tm = (xcd >> 1) * 4 + (idx >> 3), tn = (xcd & 1) * 8 + (idx & 7);
  const int m0 = tm * 128, panel = m0 >> 8, rot = (xcd & 3) * 4, sd = SIDE;
  {
    constexpr int PAD = QTCH;
    const int g = wave >> 2, rw = wave & 3;
    const int fr = lane & 15, fq = lane >> 4, wq = (lane >> 2) & 3, wp = lane & 3;
    unsigned wa[4];
#pragma unroll
    for (int c = 0; c < 4; ++c) { const int row = 4 * fq + wq; wa[c] = lds0 + g * 8192 + row * 128 + (((c << 1) ^ (row & 6)) << 4) + wp * 8; }
    FragW f[3];
    auto rd = [&](auto ic, auto rc) {
      constexpr int i = decltype(ic)::value, r = decltype(rc)::value, P = wo_pos(i >> 1) + (i & 1) * 4096, B = P & ~32767, o = P & 32767;
      if constexpr (r & 1) f[i % 3].hi[r >> 1] = ds_tr<o + 2048>(wa[r >> 1] + B); else f[i % 3].lo[r >> 1] = ds_tr<o>(wa[r >> 1] + B);
    };
    auto pin = [&](FragW& q) {
#pragma unroll
      for (int n = 0; n < 4; ++n) asm volatile("" : "+v"(q.lo[n]), "+v"(q.hi[n]));
    };
    static_for<0, 16>([&](auto rc) { constexpr int r = decltype(rc)::value; rd(std::integral_constant<int, r / 8>{}, std::integral_constant<int, r % 8>{}); });
    if constexpr (KN_CT & 4) { if (wave == 2) {
      unsigned long long c0, c1; CODE_ADDR(c0, "k_all"); CODE_ADDR(c1, "k_zend");
      wait_vm<0>();
      __builtin_amdgcn_raw_ptr_buffer_load_lds(__builtin_amdgcn_make_buffer_rsrc((void*)c0, 0, c1 - c0 < 262144ull ? (unsigned)(c1 - c0) : 0u, 0x00020000), (lds_void_ptr)(size_t)(lds0 + PAD), 4, (unsigned)((lane * 32 + idx) * 128), 0, 0, 0); } }
    if (wave == 1) wait_flags(a.sync + FLO(tm), (lane & 15) * 128, tag, 0xffffull);
    if constexpr (KN_E & 2) {
      if (wave == 3) {
        wait_flags(a.sync, (FHM(lane >= 32 ? (xcd >> 2) * 4 + (idx >> 3) : panel) + (lane & 31)) * 4, tag, ~0ull);
      }
    }
    const auto rX = __builtin_amdgcn_make_buffer_rsrc((void*)a.X, 0, 0x7fffffff, 0x00020000);
    const int rb = (m0 >> 4) + rw * 2 + g;
    unsigned xs[2] = {(unsigned)(rbH(rb, sd) + lane * 16), (unsigned)(rbH(rb ^ 1, sd) + lane * 16)};
    const int ck = (fq & 1) * 2 + (fq >> 1);
    unsigned orow = (unsigned)((rb * 16 + fr) * (D_ * 2) + tn * 128 + ck * 16), ofrg = (unsigned)(rbH(rb, sd) + omH(tn * 2048) + ck * 256 + fr * 16);
    asm volatile("" : "+v"(xs[0]), "+v"(xs[1]), "+v"(orow), "+v"(ofrg));
    STAMP(9);
    if constexpr (!KN_B3) { if (!(KN_CT & 4) || wave != 2) wait_vm<0>(); }
    lds_barrier();
    STAMP(6);
    pin(f[0]); pin(f[1]);
    u32x4 xr[16][2], res[2];
#define KN_XD 4
    auto ldX = [&](int i, int m) {
      if (i < 16) xr[i][m] = __builtin_amdgcn_raw_buffer_load_b128(rX, xs[m] + (i & 1) * 1024, omH((i >> 1) * 2048) ^ omH(((g * 8) ^ rot) * 2048), XAUXW);
      else if (i == 16) res[m] = __builtin_amdgcn_raw_buffer_load_b128(__builtin_amdgcn_make_buffer_rsrc((void*)a.R, 0, 0x7fffffff, 0x00020000), orow + m * 64, 0, 0);
    };
#pragma unroll
    for (int i = 0; i < KN_XD; ++i) { ldX(i, 0); ldX(i, 1); __builtin_amdgcn_sched_barrier(0); }
    unsigned gsrc[2];
#pragma unroll
    for (int i = 0; i < 2; ++i) {
      const int j = wave + 8 * i, jj = j >= 7 ? j - 7 : j;
      const int b = jj * 1024 + lane * 16, q = b / 224, pos = (b % 224) >> 4;
      gsrc[i] = (unsigned)((8 * ((q & 15) >> 2) + (q & 3) + 4 * (q >> 4)) * DF_ + ((xcd & 3) * 8 + (idx & 7)) * 112 + pos * 8) * 2u;
      asm volatile("" : "+v"(gsrc[i]));
    }
    f32x4 acc[4][2];
#pragma unroll
    for (int n = 0; n < 4; ++n) { acc[n][0] = (f32x4){0.f, 0.f, 0.f, 0.f}; acc[n][1] = acc[n][0]; }
    __builtin_amdgcn_sched_barrier(0);
    static_for<0, 16>([&](auto ic) {
      constexpr int i = decltype(ic)::value;
      if constexpr (i >= 2) { asm volatile("s_waitcnt lgkmcnt(%0)" :: "n"(i < 15 ? 8 : 0) : "memory"); pin(f[i % 3]); }
      if constexpr (KN_B3 && i == (KN_WB & ~1) - 2) raw_barrier();
      static_for<0, 8>([&](auto jc) {
        constexpr int j = decltype(jc)::value, n = j >> 1, m = j & 1;
        const bf16x8 w = __builtin_shufflevector(f[i % 3].lo[n], f[i % 3].hi[n], 0, 1, 2, 3, 4, 5, 6, 7);
        acc[n][m] = __builtin_amdgcn_mfma_f32_16x16x32_bf16(w, __builtin_bit_cast(bf16x8, xr[i][m]), acc[n][m], 0, 0, 0);
        if constexpr (i + 2 < 16) rd(std::integral_constant<int, i + 2>{}, jc);
        if constexpr (j == 1 || j == 5) ldX(i + KN_XD, j >> 2);
        __builtin_amdgcn_sched_barrier(0);
      });
      if constexpr (i == 0) STAMP(1);
    });
    STAMP(2);
    auto slot = [&](int w) { return 32768 + w * 4096 + lane * 16; };
#pragma unroll
    for (int n = 0; n < 4; ++n) *(f32x4*)(smem + slot(wave) + n * 1024) = acc[n][1];
    asm volatile("" : "+v"(res[0]), "+v"(res[1]));
    lds_barrier();
    STAMP(4);
    f32x4 pa[4];
    static_for<0, 4>([&](auto nc) { constexpr int n = decltype(nc)::value; pa[n] = __builtin_bit_cast(f32x4, ds_b128<n * 1024>(lds0 + slot(wave ^ 4))); });
    if constexpr (KN_GW) {
      auto ldG = [&](int i, int u) { const int j = wave + 8 * i;
        __builtin_amdgcn_raw_ptr_buffer_load_lds(__builtin_amdgcn_make_buffer_rsrc((void*)(j >= 7 ? nx.W1 : nx.W0), 0, 0x7fffffff, 0x00020000),
                          (lds_void_ptr)(size_t)(lds0 + 98304 + (u ^ 2) * 14592 + (j >= 7 ? 7184 + (j - 7) * 1024 : j * 1024)), 16, gsrc[i], (u + (xcd >> 2) * 16) * 32 * DF_ * 2, 0, 0); };
      ldG(0, 0); ldG(0, 1);
      if (wave < 6) { ldG(1, 0); ldG(1, 1); }
    }
    asm volatile("s_waitcnt lgkmcnt(0)" : "+v"(pa[0]), "+v"(pa[1]), "+v"(pa[2]), "+v"(pa[3]) :: "memory");
    const auto rO = __builtin_amdgcn_make_buffer_rsrc((void*)a.O, 0, 0x7fffffff, 0x00020000);
    u32x4 o[2];
#pragma unroll
    for (int i = 0; i < 2; ++i) {
      const f32x4 u = acc[2 * i][0] + pa[2 * i], v = acc[2 * i + 1][0] + pa[2 * i + 1];
      const auto s0 = __builtin_amdgcn_permlane16_swap(pk_bf16(u[0], u[1]), pk_bf16(v[0], v[1]), false, false);
      const auto s1 = __builtin_amdgcn_permlane16_swap(pk_bf16(u[2], u[3]), pk_bf16(v[2], v[3]), false, false);
      const u32x4 q4 = {s0[0], s1[0], s0[1], s1[1]};
#pragma unroll
      for (int j = 0; j < 4; ++j) { const unsigned r = res[i][j], q = q4[j]; o[i][j] = pk_bf16(bflo(r) + bflo(q), bfhi(r) + bfhi(q)); }
      __builtin_amdgcn_raw_buffer_store_b128(o[i], rO, ofrg + i * 1024, 0, 16);
    }
    auto sum4 = [](float v) {
      auto p = __builtin_amdgcn_permlane16_swap(__float_as_uint(v), __float_as_uint(v), false, false); v = __uint_as_float(p[0]) + __uint_as_float(p[1]);
      auto q = __builtin_amdgcn_permlane32_swap(__float_as_uint(v), __float_as_uint(v), false, false); return __uint_as_float(q[0]) + __uint_as_float(q[1]); };
    f32x2 e[8], sm = {0.f, 0.f};
#pragma unroll
    for (int j = 0; j < 8; ++j) { const unsigned q = o[j >> 2][j & 3]; e[j] = (f32x2){bflo(q), bfhi(q)}; sm += e[j]; }
    const float mu = sum4(sm[0] + sm[1]) * (1.f / 64.f);
    f32x2 t2 = {0.f, 0.f};
#pragma unroll
    for (int j = 0; j < 8; ++j) { const f32x2 d = e[j] - mu; t2 += d * d; }
    const float q2 = sum4(t2[0] + t2[1]);
    __builtin_amdgcn_raw_buffer_store_b64((u32x2){__float_as_uint(mu), __float_as_uint(q2)}, __builtin_amdgcn_make_buffer_rsrc((void*)a.st, 0, 0x7fffffff, 0x00020000),
                                          lane < 16 ? (unsigned)((tn * S_ + rb * 16 + lane) * 8) : 0x80000000u, 0, 16);
    STAMP(7);
    STAMP(8);
    asm volatile("s_waitcnt vmcnt(0) lgkmcnt(0)" ::: "memory");
    raw_barrier();
    if (tid == 0) set_flag(a.sync + FHF(panel) + ((tm & 1) * 16 + tn) * 32, tag);
    STAMP(3);
  }
#endif
}

struct FragE { bf16x8 x[4]; s16x4 lo[4], hi[4]; };
template <int A, int NT, int DX, int DW, int PW> struct Sched2 {
  static constexpr int nw(int t) { return (t + DW >= PW && t + DW < NT) ? (t + DW < A ? 4 : 2) : 0; }
  static constexpr int nx(int t) { return t + DX < NT ? 4 : 0; }
  static constexpr int cnt(int t) {
    if (t + 1 >= NT) return 63;
    int tot = 0, pos = 0;
    for (int i = 0; i < DX - 1; ++i) { tot += 4; if (i == t + 1) pos = tot; }
    for (int i = -1; i < t; ++i) {
      tot += nw(i); if (nw(i) && i + DW == t + 1) pos = tot;
      tot += nx(i); if (nx(i) && i + DX == t + 1) pos = tot;
    }
    return tot - pos;
  }
  static constexpr bool same(int t, int r) { return nw(t) == nw(r) && nx(t) == nx(r) && cnt(t) == cnt(r); }
  static constexpr int lo(int r) { int t = r; while (t > 0 && same(t - 1, r)) --t; return t; }
  static constexpr int hi(int r) { int t = r; while (same(t + 1, r)) ++t; return t + 1; }
};
template <int MODE, int A, int DX, int DW, int PW, int HTD, int TA, int TB, int TL = 0>
__device__ __forceinline__ void dn_phase(const GemmArgs& a, unsigned tag) {
#if defined(__HIP_DEVICE_COMPILE__)
  constexpr int NT = DF_ / 64 - A, SX = DX + 1, SW = 4, WB = SX * 16384, PAD = WB + SW * 16384;
  static_assert(A % 2 == 0 && NT % 2 == 0 && PW <= SW && PW <= A && TA % 2 == 0 && TB % 2 == 0 && TA >= 2 && TB >= TA && A + TB + 6 <= NT, "schedule");
  using SC = Sched2<A, NT, DX, DW, PW>;
  extern __shared__ __attribute__((aligned(16))) char smem[];
  if constexpr (MODE != 1) STAMP(0);
  const unsigned lds0 = (unsigned)(size_t)(lds_char_ptr)smem;
  const int tid = threadIdx.x;
  int lane = tid & 63; asm volatile("" : "+v"(lane));
  const int wave = __builtin_amdgcn_readfirstlane(tid >> 6);
  const int bid = blockIdx.x, xcd = bid & 7, idx = bid >> 3;
  const int tm = (xcd >> 1) * 4 + (idx >> 3), tn = (xcd & 1) * 8 + (idx & 7);
  const int m0 = tm * 128, panel = m0 >> 8, sd = SIDE;
  unsigned* pcnt = a.sync + FAE((xcd >> 2) * 4 + (idx >> 3)) + ((xcd & 3) * 8 + (idx & 7)) * 32;
  const unsigned orow = (unsigned)((m0 + wave * 16 + (lane >> 3)) * (D_ * 2) + tn * 128 + (lane & 7) * 16);
  const unsigned ofrg = (unsigned)(rbH((m0 >> 4) + wave, sd) + omH(tn * 2048) + (lane & 7) * 256 + (lane >> 3) * 16);
  u32x4 res[2];
  auto res_loads = [&]() {
    const auto rR = __builtin_amdgcn_make_buffer_rsrc((void*)a.R, 0, 0x7fffffff, 0x00020000);
#pragma unroll
    for (int i = 0; i < 2; ++i) res[i] = __builtin_amdgcn_raw_buffer_load_b128(rR, ofrg, i * 128, 16);
  };
  constexpr int STG = ((NT - 2) % SX) * 16384;
  if (MODE == 1 || wave < 4) {
    const int lw = wave & 3;
    static_assert((KN_E & 4) && A == 24, "walk");
    constexpr int V = 4;
    const int ra = (xcd & 3) * 8, rb = (xcd & 3) * 6;
    auto ktile2 = [&](int t, int e) {
      const int v1 = (ra + t + 4 + 12 * e - (((t - 4) >> 31) & (4 + 8 * e))) & 31, q = rb + t - 8 - (((t - 24) >> 31) & (8 - 8 * e)), v2 = q + 8 + (((q - 24) >> 31) & 24), m = (t - 16) >> 31;
      return (v1 & m) | (v2 & ~m); };
    auto ktile = [&](int t) { return ktile2(t, idx & 1); };
    auto wrow = [&](int kt, int j) { const int p = kt * 8 + j, b = p - 256, v1 = p + 3 * (p >> 2), v2 = b + 4 * ((b * 21846) >> 16) + 4, m = b >> 31; return ((v1 & m) | (v2 & ~m)) * 8; };
    const auto rW = __builtin_amdgcn_make_buffer_rsrc((void*)a.W0, 0, 0x7fffffff, 0x00020000);
    const u32x4 qX = mk_rsrc(a.X), qW = mk_rsrc((const char*)a.W0 - 4096);
    const unsigned xv0 = (unsigned)(rbA((m0 >> 4) + lw * 2, sd) + lane * 16), xv1 = xv0 + (RSA - 2048);
    unsigned wv[4];
    { const unsigned w0 = (unsigned)((lane >> 3) * D_ + tn * 64 + ((lane & 7) ^ wswz<128>(lane >> 3)) * 8) * 2u;
#pragma unroll
      for (int i = 0; i < 4; ++i) wv[i] = (w0 ^ (i * 64)) + (4096 - i * 1024); }
    const unsigned mX = lds0 + lw * 4096, mW = mX + WB;
    auto ldXo = [&](int so, int slot) { ld_x4(mX + slot * 16384, qX, xv0, xv1, so); };
    int tbx = 0, tb0 = 0, tb1 = 0;
    auto tables = [&]() { const int kt = ktile(lane & 31); tbx = omA(kt * 2048); tb0 = wrow(kt, 2 * lw) * (D_ * 2); tb1 = wrow(kt, 2 * lw + 1) * (D_ * 2); asm volatile("" : "+v"(tbx), "+v"(tb0), "+v"(tb1)); };
    auto ldX = [&](int t, int slot) { int so = __builtin_amdgcn_readlane(tbx, t); asm volatile("s_nop 3" : "+s"(so)); ldXo(so, slot); };
    int o0, o1, ox;
    auto offs = [&](int t) { o0 = __builtin_amdgcn_readlane(tb0, t + DW); o1 = __builtin_amdgcn_readlane(tb1, t + DW); ox = __builtin_amdgcn_readlane(tbx, t + DX); asm volatile("s_nop 1" : "+s"(o0), "+s"(o1), "+s"(ox)); };
    auto ldW = [&](int slot, auto P1c) { if constexpr (decltype(P1c)::value) ld_w4<-1>(mW + slot * 16384, qW, wv, o0, o1); else ld_w2<-1>(mW + slot * 16384, qW, wv, o0, o1); };
    const unsigned* fcnt = a.sync + FAE(panel);
    const unsigned fof = (lane & 31) * 128 + (lane >> 5) * 4;
    auto ldF = [&]() { __builtin_amdgcn_raw_ptr_buffer_load_lds(__builtin_amdgcn_make_buffer_rsrc((void*)fcnt, 0, 4096, 0x00020000), (lds_void_ptr)(size_t)(lds0 + PAD + 256), 4, fof, 0, 0, 16); };
    const int kf = ra + (idx & 1) * 4;
    int ht; unsigned htv;
    auto hset = [&]() {
      int idh = idx; asm volatile("" : "+s"(idh));
      const int slc = (3 - ((unsigned)(size_t)a.W0 >> 7)) & 7, pr = (idh & 7) >> 1;
      const bool hlp = pr != (slc >> 1) && lw < 3;
      const int hk = ((idh >> 3) * 3 + pr - (pr > (slc >> 1) ? 1 : 0)) * 2 + (idh & 1) + lw * 24;
      const int htau = hk < 48 ? hk >> 1 : hk - 24, hkt = ktile2(htau < 32 ? htau : 0, hk & 1);
      ht = (hlp && hk < DF_ / 64 && htau >= KN_WS) ? htau - HTD : 1000;
      htv = (unsigned)((wrow(hkt, lane >> 3) + (lane & 7)) * (D_ * 2) + ((xcd & 1) * 8 + slc) * 128);
    };
    auto help = [&]() { __builtin_amdgcn_raw_ptr_buffer_load_lds(rW, (lds_void_ptr)(size_t)(lds0 + FPAD - 256), 4, htv, 0, 0, 0); };
    auto ldW0 = [&](int i) { const int so = ((kf + i) * 112 + (lw >> 1) * 56 + (lw & 1) * 16) * (D_ * 2); ld_w4<-1>(mW + i * 16384, qW, wv, so, so + 8 * D_ * 2); };
    if constexpr (MODE == 1) {
      static_assert(PW == 4, "first tiles");
      if constexpr (TL == 1) __builtin_amdgcn_raw_ptr_buffer_load_lds(__builtin_amdgcn_make_buffer_rsrc((void*)(a.sync + FAM(panel)), 0, 256, 0x00020000), (lds_void_ptr)(size_t)(lds0 + FPAD), 4, lane * 4, 0, 0, 16);
      if constexpr (TL == 2) { if (lw == 0) { hset(); if (ht < -1) help(); } }
      if constexpr (TL == 0 || TL == 3) { ldW0(TL ? 2 : 0); ldW0(TL ? 3 : 1); }
      return;
    }
#define TS0
#define TS1(T)
    wait_vm<3>();
    lds_inc(lds0 + ACNT);
    STAMPW(4, 0);
    const bool early = !lds_missing(lds0 + FPAD, tag, ~0ull);
    lds_wait8(lds0 + ACNT);
    STAMPW(9, 0);
#define KN_X0 0
    if (tid == 192) set_flag(pcnt, tag);
    asm volatile("" : "+v"(lane) :: "memory");
    hset();
    tables();
    asm volatile("" :: "s"(ht), "v"(htv));
    lds_wait8(lds0 + ACNT + 4, 4);
    STAMPW(5, 0);
#pragma unroll
    for (int i = 0; i < 3; ++i) ldXo(omA(kf * 2048) + i * 2048, i);
#pragma unroll
    for (int i = KN_WS; i < 4; ++i) ldW0(i);
    asm volatile("" ::: "memory");
    static_assert(HTD <= 11, "only the first loader wave helps that early");
    if (!KN_HE && ht < -1) help();
    {
      static_assert(DX == 4 && DW == 4 && PW == 4 && KN_WS == 2 && SC::cnt(3) == 16 && SC::cnt(4) == 16 && V == DX, "schedule");
      int x4 = 0;
#pragma unroll 1
      for (int t = -1; t < V - 1; ++t) {
        static_assert(KN_WS == 4 || KN_WS == 2, "W tiles");
        offs(t);
        if (t == 1) wait_vm<8>(); else wait_vm<16>();
#ifndef KN_FL
#define KN_FL 0
#endif
        if (!KN_FL && t == -1 && lw == 0) ldF();
        if (__builtin_expect(marked<4, 1024>(mX + (t + 1) * 16384 + lane * 16) || !early, 0)) {
          for (int tries = early ? 0 : KN_TRY;; ++tries) {
            if (tries >= KN_TRY) wait_flags(fcnt, fof, tag, LO32);
#pragma unroll 1
            for (int u = t + 1; u < V && u < t + DX; ++u) ldX(u, u);
            if (KN_WS == 4 && t < 1) wait_vm<8>(); else wait_vm<0>();
            if (tries >= KN_TRY || !marked<4, 1024>(mX + (t + 1) * 16384 + lane * 16)) break;
          }
        }
        if (t == -1) STAMPW(6, 0);
        if (t == 1 && lw == 0) {
          if (lds_missing(lds0 + PAD + 256, tag, LO32)) wait_flags(fcnt, fof, tag, LO32);
        }
        raw_barrier(); TS0;
        if (KN_FL && t == -1 && lw == 0) { ldF(); asm volatile("" ::: "memory"); }
        if (t == 1) ldXo(x4, 4);
        x4 = ox;
        if (t >= 0) ldW((t + DW) & 3, std::true_type{});
        if (t != 0) ldXo(ox, t >= 1 ? t - 1 : t + DX);
        if (t == ht) help();
        TS1(t + 1);
      }
    }
    auto fixed = [&](auto tc) {
      constexpr int t = decltype(tc)::value;
#define KN_TR 4
      if constexpr (SC::nw(t) > 0) offs(t);
      wait_vm<(t > NT - KN_TR && t < NT - 1) ? SC::cnt(t) + 2 : SC::cnt(t)>();
      raw_barrier(); TS0;
      static_assert(SC::nx(t) == 0 || SC::nw(t) > 0, "offsets");
      if constexpr (SC::nw(t) > 0) ldW((t + DW) % SW, std::integral_constant<bool, (t + DW < A)>{});
      if constexpr (SC::nx(t) > 0) ldXo(ox, (t + DX) % SX);
      if constexpr (t + HTD < NT) { if (t == ht) help(); }
      TS1(t + 1);
      if constexpr (t == NT - KN_TR) { static_assert(SC::nw(t + 1) == 0 && SC::nx(t + 1) == 0, "last loads"); asm volatile("" ::: "memory"); res_loads(); }
    };
    const int tf = lw == 0 ? 13 - DX : 1000;
    auto steady = [&](auto Lc, auto Hc) {
      constexpr int L = decltype(Lc)::value, H = decltype(Hc)::value;
      unsigned sx = mX + (L + DX) % SX * 16384, sw = (L + DW) % SW * 16384;
      constexpr bool P1 = L + DW < A;
      static_assert(DX == DW && SW == 4, "offsets");
      offs(L);
#pragma unroll 1
      for (int t = L; t < H; ++t) {
        if constexpr (L <= 8) { static_assert(H > 16 - DX, "check"); if (__builtin_expect(t == tf + 3, 0)) { wait_vm<SC::cnt(L)>(); if (lds_missing(lds0 + PAD + 256, tag, HI32)) wait_flags(fcnt, fof, tag, HI32); } }
        if constexpr (P1) ld_w4<SC::cnt(L)>(mW + sw, qW, wv, o0, o1); else ld_w2<SC::cnt(L)>(mW + sw, qW, wv, o0, o1);
        TS0;
        __builtin_amdgcn_sched_barrier(0);
        if (__builtin_expect(t == ht, 0)) help();
        if constexpr (L <= 8) { if (__builtin_expect(t == tf, 0)) ldF(); }
        const int oy = ox; const unsigned sy = sx;
        offs(t + 1);
        sw = (sw + 16384) & 65535; sx = sx + 16384; sx = sx >= mX + SX * 16384 ? mX : sx;
        asm volatile("" : "+s"(sw), "+s"(sx));
        TS1(t + 1);
        __builtin_amdgcn_sched_barrier(0);
        ld_x4(sy, qX, xv0, xv1, oy);
      }
    };
    constexpr int L1 = V ? V - 1 : SC::lo(A / 2), H1 = SC::hi(A / 2), R2 = NT - (DX > DW ? DX : DW) - 1, L2 = SC::lo(R2), H2 = SC::hi(R2);
    static_assert(SC::nw(L1) == 4 && SC::nx(L1) == 4 && SC::nw(L2) == 2 && SC::nx(L2) == 4 && H1 <= L2, "steady states");
    if constexpr (!V) static_for<0, L1 + 1>([&](auto ic) { fixed(std::integral_constant<int, decltype(ic)::value - 1>{}); });
    steady(std::integral_constant<int, L1>{}, std::integral_constant<int, H1>{});
    static_for<H1, L2>(fixed);
    steady(std::integral_constant<int, L2>{}, std::integral_constant<int, H2>{});
    static_for<H2, NT>(fixed);
    asm volatile("" : "+v"(res[0]), "+v"(res[1]));
  } else {
    if constexpr (MODE == 1) return;
    const int fr = lane & 15, fq = lane >> 4, wq = (lane >> 2) & 3, wp = lane & 3, cw = wave & 3, wm = cw >> 1, wn = cw & 1;
#define KN_DP 0
    if (KN_DP) __builtin_amdgcn_s_setprio(KN_DP);
    f32x4 acc[4][4];
#pragma unroll
    for (int n = 0; n < 4; ++n)
#pragma unroll
      for (int m = 0; m < 4; ++m) acc[n][m] = (f32x4){0.f, 0.f, 0.f, 0.f};
    const unsigned xa = lds0 + wm * 8192 + lane * 16;
    unsigned wa[2];
#pragma unroll
    for (int c = 0; c < 2; ++c) { const int row = 8 * fq + wq; wa[c] = lds0 + WB + (row >> 4) * 4096 + (row & 15) * 128 + ((((wn * 2 + c) << 1) ^ wswz<128>(row)) << 4) + wp * 8; }
    struct Ctx { unsigned x, w[2]; };
    auto mk = [&](unsigned xo, unsigned wo) { Ctx c; c.x = xa + xo; c.w[0] = wa[0] + wo; c.w[1] = wa[1] + wo; return c; };
    auto rd = [&](FragE& f, const Ctx& c, auto KSc, auto kc) {
      constexpr int KS = decltype(KSc)::value, k = decltype(kc)::value;
      if constexpr (k < 4) f.x[k] = ds_b128<KS * 1024 + k * 2048>(c.x);
      else { constexpr int n = k - 4, o = KS * 8192 + (n >> 1) * 2048; f.lo[n] = ds_tr<o>(c.w[n & 1]); f.hi[n] = ds_tr<o + 512>(c.w[n & 1]); }
    };
    auto pin = [&](FragE& f, auto Nc) {
#pragma unroll
      for (int m = 0; m < 4; ++m) asm volatile("" : "+v"(f.x[m]));
#pragma unroll
      for (int n = 0; n < decltype(Nc)::value; ++n) asm volatile("" : "+v"(f.lo[n]), "+v"(f.hi[n]));
    };
    using I0 = std::integral_constant<int, 0>; using I1 = std::integral_constant<int, 1>; using I2 = std::integral_constant<int, 2>; using I4 = std::integral_constant<int, 4>;
    FragE fa, fb, fc, fd;
    raw_barrier();
    { const Ctx c = mk(0, 0); static_for<0, 8>([&](auto kc) { rd(fa, c, I0{}, kc); }); asm volatile("s_waitcnt lgkmcnt(4)" ::: "memory"); static_for<0, 8>([&](auto kc) { rd(fb, c, I1{}, kc); }); }
    asm volatile("s_waitcnt lgkmcnt(0)" ::: "memory");
    pin(fa, I4{}); pin(fb, I4{});
    STAMP(1);
    static_assert((SW & (SW - 1)) == 0, "W ring");
    int sx = 16384, sw = 16384;
    Ctx cn = mk(sx, sw);
#define KN_SP4 26
#define KN_IL 1
#define KN_IB 1
    auto rd1 = [&](FragE& f, const Ctx& c, auto KSc, auto rc) {
      constexpr int KS = decltype(KSc)::value, r = decltype(rc)::value;
      if constexpr (r < 4) f.x[r] = ds_b128<KS * 1024 + r * 2048>(c.x);
      else { constexpr int n = (r - 4) >> 1, o = KS * 8192 + (n >> 1) * 2048; if constexpr (r & 1) f.hi[n] = ds_tr<o + 512>(c.w[n & 1]); else f.lo[n] = ds_tr<o>(c.w[n & 1]); }
    };
    auto tile = [&](FragE& c0, FragE& c1, FragE& n0, FragE& n1, auto NCc, auto NNc, auto&& post) {
      constexpr int NC = decltype(NCc)::value, NN = decltype(NNc)::value;
      constexpr int R2 = 8 + 4 * NN, NP = 4 * NC;
      const Ctx c = cn;
      static_for<0, 2 * NP>([&](auto ic) {
        constexpr int I = decltype(ic)::value, P = I >> 1, n = P / 4, m = (n & 1) ? 3 - P % 4 : P % 4;
        constexpr int SP = NC == 4 ? (NN == 4 ? KN_SP4 : 20) : 12;
        constexpr int before = I < 1 ? 0 : ((I - 1) * R2 / SP < R2 ? (I - 1) * R2 / SP : R2), after = I * R2 / SP < R2 ? I * R2 / SP : R2;
        FragE& u = ((P ^ I) & 1) ? c1 : c0;
        acc[n][m] = __builtin_amdgcn_mfma_f32_16x16x32_bf16(__builtin_shufflevector(u.lo[n], u.hi[n], 0, 1, 2, 3, 4, 5, 6, 7), u.x[m], acc[n][m], 0, 0, 0);
        __builtin_amdgcn_sched_barrier(0);
        if constexpr (I == 1) { asm volatile("s_waitcnt lgkmcnt(0)" ::: "memory"); pin(c0, NCc); pin(c1, NCc); raw_barrier(); post(); }
        static_for<before, after>([&](auto rc) { constexpr int r = decltype(rc)::value;
          constexpr int h = r < 6 ? r / 3 : (r < 12 ? r & 1 : (r >> 1) & 1), q = r < 6 ? (r % 3 == 2 ? 0 : 4 + r % 3) : (r < 12 ? (r - 4) >> 1 : 4 + ((r - 8) >> 2) * 2 + (r & 1));
          if constexpr (h) rd1(n1, c, I1{}, std::integral_constant<int, q>{}); else rd1(n0, c, I0{}, std::integral_constant<int, q>{}); });
        if constexpr (I == 2 * NP - 4) { sw = (sw + 16384) & (SW * 16384 - 1); asm volatile("" : "+s"(sw)); }
        if constexpr (I == 2 * NP - 3) { cn.w[0] = wa[0] + sw; cn.w[1] = wa[1] + sw; asm volatile("" : "+v"(cn.w[0]), "+v"(cn.w[1])); }
        if constexpr (I == 2 * NP - 5) { sx = sx + 16384; asm volatile("" : "+s"(sx)); }
        if constexpr (I == 2 * NP - 2) { sx = sx == SX * 16384 ? 0 : sx; asm volatile("" : "+s"(sx)); }
        if constexpr (I == 2 * NP - 1) { cn.x = xa + sx; asm volatile("" : "+v"(cn.x)); }
        __builtin_amdgcn_sched_barrier(0);
      });
      asm volatile("s_waitcnt lgkmcnt(%0)" :: "n"(R2 - 6 < 15 ? R2 - 6 : 15) : "memory");
      asm volatile("" : "+v"(n0.x[0]), "+v"(n0.lo[0]), "+v"(n0.hi[0]), "+v"(n1.x[0]), "+v"(n1.lo[0]), "+v"(n1.hi[0]));
    };
    auto nop = [&]() {};
    for (int t = 0; t < A - 2; t += 2) {
      tile(fa, fb, fc, fd, I4{}, I4{}, [&]() { if (t == 2) { if (tid == 256) set_flag(pcnt + 1, tag); } });
      tile(fc, fd, fa, fb, I4{}, I4{}, nop);
      if (t == 0) wait_vm<0>();
    }
    tile(fa, fb, fc, fd, I4{}, I4{}, nop); tile(fc, fd, fa, fb, I4{}, I2{}, nop);
    const auto rS = __builtin_amdgcn_make_buffer_rsrc((void*)a.slab, 0, 0x7fffffff, 0x00020000);
    STAMP(10);
#pragma unroll
    for (int m = 0; m < 4; ++m)
      __builtin_amdgcn_raw_buffer_store_b128((u32x4){pk_bf16(acc[2][m][0], acc[2][m][1]), pk_bf16(acc[2][m][2], acc[2][m][3]), pk_bf16(acc[3][m][0], acc[3][m][1]), pk_bf16(acc[3][m][2], acc[3][m][3])},
                                             rS, rbH(tm * 8 + (tn >> 1), sd) + omH((~tn & 1) * 16384 + cw * 4096) + m * 1024 + lane * 16, 0, 16);
    auto pairs = [&](int n) {
#pragma unroll 1
      for (int t = 0; t < n; t += 2) { tile(fa, fb, fc, fd, I2{}, I2{}, nop); tile(fc, fd, fa, fb, I2{}, I2{}, nop); } };
    const unsigned* flag = a.sync + FPR(bid);
    u32x4 pp[4];
    auto ldP = [&]() {
#pragma unroll
      for (int i = 0; i < 4; ++i) pp[i] = __builtin_amdgcn_raw_buffer_load_b128(rS, rbH(tm * 8 + (tn >> 1), sd) + omH((tn & 1) * 16384 + cw * 4096) + i * 1024 + lane * 16, 0, 16);
    };
    {
    pairs(TA);
    STAMPV(11);
    wait_vm<0>();
    STAMPV(12);
    tile(fa, fb, fc, fd, I2{}, I2{}, [&]() { if (tid == 256) set_flag(a.sync + FPR(bid ^ 8), tag); });
    tile(fc, fd, fa, fb, I2{}, I2{}, nop);
    if constexpr (TB > TA) pairs(TB - TA);
    unsigned fv = 1u;
    if (wave == 4) fv = __hip_atomic_load(flag, __ATOMIC_RELAXED, __HIP_MEMORY_SCOPE_AGENT);
    tile(fa, fb, fc, fd, I2{}, I2{}, nop);
    tile(fc, fd, fa, fb, I2{}, I2{}, nop);
    if (wave == 4) {
      if (__builtin_amdgcn_readfirstlane(fv) != tag) wait_flags(flag, 0, tag, 1ull);
    }
    STAMPV(4);
    tile(fa, fb, fc, fd, I2{}, I2{}, [&]() {
#pragma unroll
      for (int i = 0; i < 4; ++i) pp[i] = __builtin_amdgcn_raw_buffer_load_b128(rS, rbH(tm * 8 + (tn >> 1), sd) + omH((tn & 1) * 16384 + cw * 4096) + i * 1024 + lane * 16, 0, 16);
      res_loads();
    });
    tile(fc, fd, fa, fb, I2{}, I2{}, nop);
    if constexpr (NT - A - TB - 6 > 0) pairs(NT - A - TB - 6);
    }
    asm volatile("s_waitcnt lgkmcnt(0)" ::: "memory");
    pin(fa, I2{}); pin(fb, I2{});
    STAMP(2);
#pragma unroll
    for (int m = 0; m < 4; ++m)
#pragma unroll
      for (int n = 0; n < 2; ++n) {
        const unsigned q0 = pp[m][n * 2], q1 = pp[m][n * 2 + 1];
        const f32x4 v = acc[n][m] + (f32x4){bflo(q0), bfhi(q0), bflo(q1), bfhi(q1)};
        *(u32x2*)(smem + STG + (wm * 64 + m * 16 + fr) * 128 + ((((wn * 2 + n) * 2 + (fq >> 1)) ^ (fr & 7)) << 4) + (fq & 1) * 8) = (u32x2){pk_bf16(v[0], v[1]), pk_bf16(v[2], v[3])};
      }
    STAMPV(5);
    asm volatile("" : "+v"(res[0]), "+v"(res[1]));
  }
  lds_barrier();
  {
    const auto rO = __builtin_amdgcn_make_buffer_rsrc((void*)a.O, 0, 0x7fffffff, 0x00020000);
    u32x4 o[2];
    int lz = lane; asm volatile("" : "+v"(lz));
#pragma unroll
    for (int i = 0; i < 2; ++i) {
      const u32x4 v = *(const u32x4*)(smem + STG + (wave * 16 + i * 8 + (lz >> 3)) * 128 + (((lz & 7) ^ (lz >> 3)) << 4));
#pragma unroll
      for (int j = 0; j < 4; ++j) { const unsigned r = res[i][j], q = v[j]; o[i][j] = pk_bf16(bflo(r) + bflo(q), bfhi(r) + bfhi(q)); }
    }
    asm volatile("" : "+v"(o[0]), "+v"(o[1]));
#pragma unroll
#define KN_OAUX 16
    for (int i = 0; i < 2; ++i) __builtin_amdgcn_raw_buffer_store_b128(o[i], rO, orow, i * 8 * D_ * 2, KN_OAUX);
    if (bid == 0 && tid == 0) set_flag(a.sync + FEP, tag);
  }
  STAMP(7);
  STAMP(8);
  STAMP(3);
#endif
}

struct Ptrs { const bf16_t *x, *wqkv, *wo, *wg, *wu, *wd; bf16_t* out; char* ws; };
#define DBG(i)
#define WS_O ((bf16_t*)(p.ws + (8u << 20)))
#define WS_SL (KN_DM == 1 ? (float*)(p.ws + (12u << 20)) : (float*)WS_O)
#define WS_H ((bf16_t*)(p.ws + (16u << 20)))
#define WS_ST ((float*)(p.ws + (24u << 20)))
#define WS_CS ((float*)(p.ws + (25u << 20)))
#define WS_SY ((unsigned*)(p.ws + (26u << 20)))
#define WS_A ((bf16_t*)(p.ws + (28u << 20)))
#if defined(__HIP_DEVICE_COMPILE__) && !defined(KN_NOWARM)
#define ARG_WARM(v) do { asm volatile("s_load_dword %0, %1, 0x0" : "=s"(v) : "s"(__builtin_amdgcn_kernarg_segment_ptr())); __builtin_amdgcn_sched_barrier(0); } while (0)
#else
#define ARG_WARM(v)
#endif
struct Args4 { GemmArgs q, b, c, d; };
extern "C" __global__ __launch_bounds__(512, 1) void k_all(Ptrs p) {
  unsigned warm = 0;
  ARG_WARM(warm);
  p.ws = (char*)(((unsigned long long)p.ws + 0x1fffffull) & ~0x1fffffull);
  const Args4 a{GemmArgs{p.x, p.wqkv, nullptr, nullptr, WS_O, (float*)WS_H, p.wg, p.wu, WS_CS, p.wd, p.wo, WS_SY, (float*)WS_A, warm DBG(0)},
    GemmArgs{WS_O, p.wo, nullptr, p.x, WS_H, WS_ST, nullptr, nullptr, nullptr, p.wd, nullptr, WS_SY, nullptr, 0u DBG(1)},
    GemmArgs{WS_H, p.wg, p.wu, p.out, WS_A, WS_ST, nullptr, nullptr, WS_CS, p.wd, nullptr, WS_SY, WS_SL, 0u DBG(2)},
    GemmArgs{WS_A, p.wd, nullptr, WS_H, p.out, nullptr, nullptr, nullptr, nullptr, nullptr, nullptr, WS_SY, WS_SL, 0u DBG(3)}};
#define KN_WDX 4
#define KN_WHT 8
#define KN_DDX 4
#define KN_DDW 8
#define KN_DPW 9
#define KN_DHT 14
#define KN_WPW 5
  const unsigned tag = k1_phase(a.q);
  wo_phase(a.b, a.c, tag);
#define KN_A 24
#define KN_EX 4
#define KN_EW 4
#define KN_EH 10
#define KN_TA 2
#define KN_TB 2
  auto pre = [&](auto tc) { dn_phase<1, KN_A, KN_EX, KN_EW, 4, KN_EH, KN_TA, KN_TB, decltype(tc)::value>(a.d, tag); };
  gemm_phase<EPI_GLU, 256, 28, 4, 2, 1024, 3584, 8, 32, 4, 8, 32, 3, 4, 1>(a.c, tag, pre);
  dn_phase<2, KN_A, KN_EX, KN_EW, 4, KN_EH, KN_TA, KN_TB>(a.d, tag);
}

extern "C" __global__ void k_zend() {}

extern "C" void kernel_launch(void* const* d_in, const int* in_sizes, int n_in, void* d_out, int out_size, void* d_ws,
                              size_t ws_size, hipStream_t stream) {
  const Ptrs p{(const bf16_t*)d_in[0], (const bf16_t*)d_in[1], (const bf16_t*)d_in[2], (const bf16_t*)d_in[4], (const bf16_t*)d_in[5], (const bf16_t*)d_in[6], (bf16_t*)d_out, (char*)d_ws};
  constexpr int L_all = SNAP + 64;
  static_assert(SNAP >= 3 * 256 * 128 + 4 * 14592 + 4096 + 1024 + 256 + 512 + 128 + 64 && L_all <= 163840, "LDS");
  (void)hipFuncSetAttribute((const void*)k_all, hipFuncAttributeMaxDynamicSharedMemorySize, L_all);
  k_all<<<256, 512, L_all, stream>>>(p);
}
```

```cpp
#include <hip/hip_runtime.h>
#include <hip/hip_bf16.h>
#include <stdint.h>
#include <type_traits>

typedef unsigned short bf16_t;
typedef short bf16x8 __attribute__((ext_vector_type(8)));
typedef short s16x4 __attribute__((ext_vector_type(4)));
typedef float f32x4 __attribute__((ext_vector_type(4)));
typedef float f32x2 __attribute__((ext_vector_type(2)));
typedef unsigned u32x2 __attribute__((ext_vector_type(2)));
typedef unsigned u32x4 __attribute__((ext_vector_type(4)));
typedef __bf16 bf16x2_t __attribute__((ext_vector_type(2)));
typedef __attribute__((address_space(3))) void* lds_void_ptr;
typedef __attribute__((address_space(3))) char* lds_char_ptr;

constexpr int S_ = 2048, D_ = 1024, DF_ = 3584;
#define KN_HTD 11
#define KN_CT 7
#define KN_HQ 5
#define KN_HK 2
#define KN_KT 4
#define KN_SPLIT 1
#define KN_PO 0
#define KN_PH 0
#define KN_PA 0
#define KN_NUMA 3
constexpr int RSH = (KN_NUMA & 1) ? 65536 : 32768, RSA = (KN_NUMA & 2) ? 262144 : 114688;
__device__ __forceinline__ int rbH(int rb, int sd) { return (KN_NUMA & 1) ? (rb & 63) * RSH + (((sd ^ (rb >> 5)) & 1) << 13) : rb * RSH; }
__device__ __forceinline__ int rbA(int rb, int sd) { return (KN_NUMA & 2) ? (rb & 63) * RSA + (((sd ^ (rb >> 3)) & 1) << 13) : rb * RSA; }
__device__ __forceinline__ constexpr int omH(int o) { return (KN_NUMA & 1) ? o + (o & ~8191) : o; }
__device__ __forceinline__ constexpr int omA(int o) { return (KN_NUMA & 2) ? o + (o & ~8191) : o; }

__device__ __forceinline__ float bflo(unsigned v) { return __uint_as_float(v << 16); }
__device__ __forceinline__ float bfhi(unsigned v) { return __uint_as_float(v & 0xffff0000u); }
__device__ __forceinline__ unsigned pk_bf16(float lo, float hi) {
  f32x2 v = {lo, hi};
  bf16x2_t b = __builtin_convertvector(v, bf16x2_t);
  return __builtin_bit_cast(unsigned, b);
}
__device__ __forceinline__ float rbf(float v) { return bflo(pk_bf16(v, v)); }

template <int STRIDE> __device__ __forceinline__ int wswz(int row) {
  if constexpr (STRIDE % 256 == 0) return ((row & 3) | (((row >> 3) & 1) << 2)) << 1;
  else if constexpr (STRIDE % 256 == 128) return (((row >> 1) & 1) | (((row >> 3) & 1) << 1)) << 1;
  else return ((row >> 3) & 1) << 1;
}
template <int STRIDE> constexpr int wcls() { return STRIDE % 256 == 0 ? 8 : (STRIDE % 256 == 128 ? 4 : 2); }

template <int OFF> __device__ __forceinline__ s16x4 ds_tr(unsigned addr) {
  s16x4 r; asm volatile("ds_read_b64_tr_b16 %0, %1 offset:%2" : "=v"(r) : "v"(addr), "n"(OFF)); return r;
}
template <int OFF> __device__ __forceinline__ bf16x8 ds_b128(unsigned addr) {
  bf16x8 r; asm volatile("ds_read_b128 %0, %1 offset:%2" : "=v"(r) : "v"(addr), "n"(OFF)); return r;
}
template <int OFF> __device__ __forceinline__ f32x2 ds_b64(unsigned addr) {
  f32x2 r; asm volatile("ds_read_b64 %0, %1 offset:%2" : "=v"(r) : "v"(addr), "n"(OFF)); return r;
}
__device__ __forceinline__ float sum8(float v) {
  v += __int_as_float(__builtin_amdgcn_update_dpp(0, __float_as_int(v), 0x128, 0xf, 0xf, false));
  auto a = __builtin_amdgcn_permlane16_swap(__float_as_uint(v), __float_as_uint(v), false, false);
  v = __uint_as_float(a[0]) + __uint_as_float(a[1]);
  auto b = __builtin_amdgcn_permlane32_swap(__float_as_uint(v), __float_as_uint(v), false, false);
  return __uint_as_float(b[0]) + __uint_as_float(b[1]);
}
__device__ __forceinline__ float sum8l(float v) {
  v += __int_as_float(__builtin_amdgcn_update_dpp(0, __float_as_int(v), 0xb1, 0xf, 0xf, false));
  v += __int_as_float(__builtin_amdgcn_update_dpp(0, __float_as_int(v), 0x4e, 0xf, 0xf, false));
  return v + __int_as_float(__builtin_amdgcn_update_dpp(0, __float_as_int(v), 0x141, 0xf, 0xf, false));
}
__device__ __forceinline__ int ds_r32(unsigned addr) { int r; asm volatile("ds_read_b32 %0, %1" : "=v"(r) : "v"(addr)); return r; }
__device__ __forceinline__ void ds_w32(unsigned addr, int v) { asm volatile("ds_write_b32 %0, %1" :: "v"(addr), "v"(v) : "memory"); }
template <int N> __device__ __forceinline__ void wait_vm() { asm volatile("s_waitcnt vmcnt(%0)" ::"n"(N) : "memory"); }
__device__ __forceinline__ void raw_barrier() { asm volatile("" ::: "memory"); __builtin_amdgcn_s_barrier(); asm volatile("" ::: "memory"); }
__device__ __forceinline__ void loop_barrier() { raw_barrier(); }
__device__ __forceinline__ void lds_barrier() { asm volatile("s_waitcnt lgkmcnt(0)" ::: "memory"); __builtin_amdgcn_s_barrier(); asm volatile("" ::: "memory"); }

__device__ __forceinline__ u32x4 mk_rsrc(const void* p) { const unsigned long long v = (unsigned long long)p; return (u32x4){(unsigned)v, (unsigned)(v >> 32) & 0xffffu, 0x7fffffffu, 0x00020000u}; }
#define LD1K(v, so, off, sc) "buffer_load_dwordx4 " v ", %1, " so " offen offset:" off sc " lds\n\t"
template <int WAIT> __device__ __forceinline__ void ld_w4(unsigned m, u32x4 r, const unsigned (&v)[4], unsigned s0, unsigned s1) {
  if constexpr (WAIT >= 0) asm volatile("s_mov_b32 m0, %0\n\ts_waitcnt vmcnt(%8)\n\ts_barrier\n\t" LD1K("%2", "%6", "0", "") LD1K("%3", "%7", "1024", "") LD1K("%4", "%6", "2048", "") LD1K("%5", "%7", "3072", "")
                                        :: "s"(m), "s"(r), "v"(v[0]), "v"(v[1]), "v"(v[2]), "v"(v[3]), "s"(s0), "s"(s1), "n"(WAIT) : "memory", "m0");
  else asm volatile("s_mov_b32 m0, %0\n\ts_nop 0\n\t" LD1K("%2", "%6", "0", "") LD1K("%3", "%7", "1024", "") LD1K("%4", "%6", "2048", "") LD1K("%5", "%7", "3072", "")
                    :: "s"(m), "s"(r), "v"(v[0]), "v"(v[1]), "v"(v[2]), "v"(v[3]), "s"(s0), "s"(s1) : "memory", "m0");
}
template <int WAIT> __device__ __forceinline__ void ld_w2(unsigned m, u32x4 r, const unsigned (&v)[4], unsigned s0, unsigned s1) {
  if constexpr (WAIT >= 0) asm volatile("s_mov_b32 m0, %0\n\ts_waitcnt vmcnt(%6)\n\ts_barrier\n\t" LD1K("%2", "%4", "0", "") LD1K("%3", "%5", "1024", "")
                                        :: "s"(m), "s"(r), "v"(v[0]), "v"(v[1]), "s"(s0), "s"(s1), "n"(WAIT) : "memory", "m0");
  else asm volatile("s_mov_b32 m0, %0\n\ts_nop 0\n\t" LD1K("%2", "%4", "0", "") LD1K("%3", "%5", "1024", "") :: "s"(m), "s"(r), "v"(v[0]), "v"(v[1]), "s"(s0), "s"(s1) : "memory", "m0");
}
__device__ __forceinline__ void ld_x4(unsigned m, u32x4 r, unsigned v0, unsigned v1, unsigned so) {
  asm volatile("s_mov_b32 m0, %0\n\ts_nop 0\n\t" LD1K("%2", "%4", "0", " sc1") LD1K("%2", "%4", "1024", " sc1") LD1K("%3", "%4", "2048", " sc1") LD1K("%3", "%4", "3072", " sc1")
               :: "s"(m), "s"(r), "v"(v0), "v"(v1), "s"(so) : "memory", "m0");
}
__device__ __forceinline__ unsigned long long ld_x4c(unsigned m, u32x4 r, unsigned v0, unsigned v1, unsigned so, unsigned ca) {
  u32x4 a, b, c, d; unsigned long long f;
  asm volatile("ds_read_b128 %1, %10\n\tds_read_b128 %2, %10 offset:1024\n\tds_read_b128 %3, %10 offset:2048\n\tds_read_b128 %4, %10 offset:3072\n\ts_mov_b32 m0, %5\n\ts_nop 0\n\t"
               "buffer_load_dwordx4 %7, %6, %9 offen sc1 lds\n\tbuffer_load_dwordx4 %7, %6, %9 offen offset:1024 sc1 lds\n\ts_waitcnt lgkmcnt(0)"
               : "=s"(f), "=&v"(a), "=&v"(b), "=&v"(c), "=&v"(d) : "s"(m), "s"(r), "v"(v0), "v"(v1), "s"(so), "v"(ca) : "memory", "m0");
  unsigned x = max(max(a[0], a[1]), a[2]), y = max(max(b[0], b[1]), b[2]); x = max(max(x, a[3]), c[0]); y = max(max(y, b[3]), d[0]);
  asm volatile("s_mov_b32 m0, %2\n\ts_nop 0\n\tbuffer_load_dwordx4 %4, %3, %5 offen offset:2048 sc1 lds" : "+v"(x), "+v"(y) : "s"(m), "s"(r), "v"(v1), "s"(so) : "memory", "m0");
  x = max(max(x, c[1]), c[2]); y = max(max(y, d[1]), d[2]); x = max(max(x, c[3]), d[3]);
  f = __builtin_amdgcn_ballot_w64(max(x, y) == 0xffffffffu);
  asm volatile("s_mov_b32 m0, %1\n\ts_nop 0\n\tbuffer_load_dwordx4 %3, %2, %4 offen offset:3072 sc1 lds" : "+s"(f) : "s"(m), "s"(r), "v"(v1), "s"(so) : "memory", "m0");
  return f;
}
template <int LO, int SO, bool SUB> __device__ __forceinline__ void ld_ca(unsigned mb, u32x4 r, unsigned v, unsigned sd) {
  unsigned t;
  if constexpr (SUB) asm volatile("s_add_i32 m0, %1, %4\n\ts_sub_i32 %0, %5, %6\n\tbuffer_load_dwordx4 %3, %2, %0 offen lds" : "=&s"(t) : "s"(mb), "s"(r), "v"(v), "n"(LO), "n"(SO), "s"(sd) : "memory", "m0", "scc");
  else asm volatile("s_add_i32 m0, %1, %4\n\ts_add_i32 %0, %6, %5\n\tbuffer_load_dwordx4 %3, %2, %0 offen lds" : "=&s"(t) : "s"(mb), "s"(r), "v"(v), "n"(LO), "n"(SO), "s"(sd) : "memory", "m0", "scc");
}
template <int LO, int SO> __device__ __forceinline__ void ld_cx(unsigned mb, u32x4 r, unsigned v, unsigned sx) {
  unsigned t;
  asm volatile("s_add_i32 m0, %1, %4\n\ts_xor_b32 %0, %6, %5\n\tbuffer_load_dwordx4 %3, %2, %0 offen lds" : "=&s"(t) : "s"(mb), "s"(r), "v"(v), "n"(LO), "n"(SO), "s"(sx) : "memory", "m0", "scc");
}
template <int I, int N, class F> __device__ __forceinline__ void static_for(F&& f) {
  if constexpr (I < N) { f(std::integral_constant<int, I>{}); static_for<I + 1, N>(f); }
}
#define CODE_ADDR(v, sym) asm volatile("s_getpc_b64 s[40:41]\n\ts_add_u32 s40, s40, " sym "@rel32@lo+4\n\ts_addc_u32 s41, s41, " sym "@rel32@hi+12\n\ts_mov_b64 %0, s[40:41]" : "=s"(v) :: "s40", "s41", "scc")
enum { EPI_QKV = 0, EPI_WO = 1, EPI_GLU = 2, EPI_DN = 3 };
#define MERGED 1
constexpr int SNAP = 162752;
#define KN_P 1
#define KN_S1 1
#define KN_S1C 8
#define KN_S1P 3
constexpr int STAB = 786432, STL = 3584;
#define FHM(p) ((p) * 256)
#define FRC(p) ((p) * 256 + 64)
#define FAM(p) ((p) * 256 + 128)
#define FCS(t) (2048 + (t) * 32)
#define FLN(k, i) ((1 + 2 * (k) + (((i) >> 6) & 1)) * 4096 + ((~sd & 1) << 11) + ((i) & 63) * 32)
#define FLO(t) FLN(0, (t) * 16)
#define FHF(p) FLN(1, (p) * 32)
#define FAE(p) FLN(2, (p) * 32)
#define FPR(b) FLN(3, ((b) & 3) * 32 + ((b) >> 3))
#define SIDE (((xcd >> 2) ^ tag) & 1)
constexpr int VTAB = STAB + 32768, PRB = 1048576;
constexpr int FEP = 3072;
constexpr unsigned long long LO32 = 0xffffffffull, HI32 = LO32 << 32;
#define KN_E 6
#define KN_MK 2
#define KN_GW 1
#define KN_V 5
#define KN_D 1
#define KN_PF 0
#define KN_TRY 6
#define KN_DM 0
#define KN_LX 0
#define KN_TP 5
typedef __attribute__((address_space(1))) unsigned* gu32;
__device__ __forceinline__ void set_flag(unsigned* p, unsigned tag) { __hip_atomic_store((gu32)p, tag, __ATOMIC_RELAXED, __HIP_MEMORY_SCOPE_AGENT); }
__device__ __forceinline__ bool missing(unsigned v, unsigned tag, unsigned long long mask) { return (__builtin_amdgcn_ballot_w64(v != tag) & mask) != 0; }
__device__ __forceinline__ void wait_flags(const unsigned* p, unsigned off, unsigned tag, unsigned long long mask) {
  unsigned v, polls = 0;
  do { asm volatile("s_nop 4\n\tglobal_load_dword %0, %1, %2 sc1\n\ts_waitcnt vmcnt(0)" : "=v"(v) : "v"(off), "s"(p) : "memory"); } while (missing(v, tag, mask) && ++polls < 4000000u);
}
__device__ __forceinline__ unsigned lane4() { unsigned l = __builtin_amdgcn_mbcnt_hi(~0u, __builtin_amdgcn_mbcnt_lo(~0u, 0u)) * 4u; asm volatile("" : "+v"(l)); return l; }
__device__ __forceinline__ bool lds_missing(unsigned addr, unsigned tag, unsigned long long mask) {
  int v = ds_r32(addr + lane4());
  asm volatile("s_waitcnt lgkmcnt(0)" : "+v"(v) :: "memory");
  return missing((unsigned)v, tag, mask);
}
constexpr int ACNT = SNAP - 60;
__device__ __forceinline__ void lds_inc(unsigned addr) { if (__builtin_amdgcn_mbcnt_hi(~0u, __builtin_amdgcn_mbcnt_lo(~0u, 0u)) == 0) asm volatile("ds_add_u32 %0, %1" :: "v"(addr), "v"(1) : "memory"); }
constexpr int FPAD = SNAP - 704;
__device__ __forceinline__ void lds_wait8(unsigned addr, int n = 8) {
  int dv; unsigned polls = 0;
  do { dv = ds_r32(addr); asm volatile("s_waitcnt lgkmcnt(0)" : "+v"(dv) :: "memory"); } while (__builtin_amdgcn_readfirstlane(dv) < n && ++polls < 40000000u);
}
template <int N> __device__ __forceinline__ bool marked_in(bf16x8 (&v)[N]) {
  asm volatile("s_waitcnt lgkmcnt(0)" ::: "memory");
  unsigned m = 0;
#pragma unroll
  for (int i = 0; i < N; ++i) {
    asm volatile("" : "+v"(v[i]));
    const u32x4 u = __builtin_bit_cast(u32x4, v[i]);
    m = max(max(m, u[0]), u[1]); m = max(max(m, u[2]), u[3]);
  }
  return __builtin_amdgcn_ballot_w64(m == 0xffffffffu) != 0;
}
template <int N, int STR = 4096> __device__ __forceinline__ bool marked(unsigned addr) {
  bf16x8 v[N];
  static_for<0, N>([&](auto ic) { constexpr int i = decltype(ic)::value; v[i] = ds_b128<i * STR>(addr); });
  return marked_in<N>(v);
}

struct GemmArgs {
  const bf16_t* X;
  const bf16_t* W0;
  const bf16_t* W1;
  const bf16_t* R;
  bf16_t* O;
  float* st;
  const bf16_t* CW0;
  const bf16_t* CW1;
  float* cs;
  const bf16_t* PF0;
  const bf16_t* PF1;
  unsigned* sync;
  float* slab;
  unsigned warm;
};
#define STAMP(i)
#define STAMPW(i, w)

#define STAMPV(i) STAMP(i)
#define XAUXD 16
#define XAUXW 16
#define XAUX 16
template <int MF, int NF> struct Frags { bf16x8 x[MF]; s16x4 lo[NF], hi[NF]; };

#define KN_WL 2
#define KN_WA KN_WL
#define KN_WB 2
#define KN_B3 0
constexpr int WOB = 55296;
constexpr int wo_pos(int s) { return s < 6 ? WOB + s * 16384 : (s - 6) * 16384; }
#define KN_GR 24
#define KN_GE 26
template <int NF, int GR> struct SchedP {
  static constexpr int NP = 4 * NF, NR = NP + 8;
  static constexpr int pa(int p) { return (p + NP - 12) % NP; }
  static constexpr int pn(int a) { return a < 8 ? (a ^ (a >> 1)) & 1 : (a < NP - 8 ? a / 4 : NF - 2 + ((a ^ (a >> 1)) & 1)); }
  static constexpr int pm(int a) { return a < 8 ? a >> 1 : (a < NP - 8 ? ((a & 4) ? a & 3 : 3 - (a & 3)) : (a - NP + 8) >> 1); }
  static constexpr int gap(int r) { return r < 8 ? 2 + r : (r < 12 ? 12 + 4 * (r - 8) : (r < NP ? 26 + (r - 12) * GR / (NP - 12) : 11 + 2 * ((r - NP) & ~1) - (r & 1))); }
  static constexpr int need(int r) { return r < 8 ? 12 : (r < NP ? (12 + (r & ~3) < NP ? 12 + (r & ~3) : NP) : 12 + ((r - NP) & ~1)); }
  static constexpr int cnt(int p) {
    int g = -1, q = -1;
    for (int r = 0; r < NR; ++r) if (need(r) == p && (gap(r) > g || (gap(r) == g && r > q))) { g = gap(r); q = r; }
    if (g < 0) return -1;
    int c = 0;
    for (int r = 0; r < NR; ++r) if (gap(r) < 2 * p && (gap(r) > g || (gap(r) == g && r > q))) ++c;
    return c < 15 ? c : 15;
  }
  static constexpr bool last(int a) { for (int b = a + 1; b < NP; ++b) if (pn(b) == pn(a)) return false; return true; }
};
using SchedG = SchedP<7, KN_GR>;
struct NoHook { template <class A> __device__ __forceinline__ void operator()(A) const {} };
template <int EPI, int BM, int NCH, int WM, int WN, int K, int LDW, int TM, int TN, int XA, int XB, int KU, int SX, int SW, int SPK, class HK = NoHook>
__device__ __forceinline__ void gemm_phase(const GemmArgs& a, unsigned tag, HK hook = HK{}) {
#if defined(__HIP_DEVICE_COMPILE__)
  constexpr bool SEAM_IN = EPI == EPI_GLU || EPI == EPI_DN, SEAM_OUT = EPI != EPI_DN;
  constexpr bool HALO = EPI == EPI_QKV;
  constexpr bool FX = EPI != EPI_QKV;
  constexpr int HR = HALO ? 16 : 0, XROWS = BM + HR;
  constexpr bool GLU = EPI == EPI_GLU;
  constexpr int STRIDE = GLU ? 224 : (HALO ? 128 : NCH * 16);
  constexpr int SEGB = GLU ? 16 : 32;
  constexpr int UPOFF = KU * 224 + 16;
  constexpr int XT = XROWS * 128, WU = GLU ? (UPOFF + KU * 224 + 255) / 256 * 256 : KU * NCH * 16;
  constexpr int XBASE = 0, WBASE = SX * XT, RING = WBASE + SW * WU;
  constexpr int MF = BM / 16 / WM, NF = NCH / 2 / WN;
  constexpr int MFX = MF + (HALO ? 1 : 0), NFX = NF;
  constexpr int NT = K / SPK / 64, NU = K / SPK / KU;
  constexpr int C = GLU ? 2 : (HALO ? 1 : (wcls<STRIDE>() < NF ? wcls<STRIDE>() : NF));
  constexpr int NXI = (XROWS / 8) * KU / 64;
  constexpr int NWI = KU * NCH * 16 / 1024;
#define KN_NLW 4
  constexpr int NLW = KN_NLW;
  constexpr int XI = (NXI + NLW - 1) / NLW, XIMIN = NXI / NLW, WI = (NWI + NLW - 1) / NLW;
  extern __shared__ __attribute__((aligned(16))) char smem[];
  STAMP(0);
  const unsigned lds0 = (unsigned)(size_t)(lds_char_ptr)smem;
  const int tid = threadIdx.x, lane = tid & 63;
  const int wave = __builtin_amdgcn_readfirstlane(tid >> 6);
  const int wm = wave / WN, wn = wave % WN;
  const int bid = blockIdx.x, xcd = bid & 7, kh = SPK > 1 ? ((bid >> 3) % SPK) : 0, idx = (bid >> 3) / SPK;
  constexpr int XN = TN / XB;
#define KN_ROT 0
  const int tm = (xcd / XN) * XA + idx / XB, tn = (xcd % XN) * XB + (idx + (EPI == EPI_DN ? KN_ROT : 0)) % XB;
  const int m0 = tm * BM, sd = SIDE;
  const int krot = (tm >> 2) * 8;
  const int fr = lane & 15, fq = lane >> 4;
  const int wq = (lane >> 2) & 3, wp = lane & 3;

  const int panel = m0 >> 8;
  constexpr int TCH = RING + (EPI == EPI_QKV ? 2304 : (EPI == EPI_GLU ? 5120 : 0));
  constexpr int DONE = TCH + 896;
  auto done_inc = [&]() { if (lane == 0) asm volatile("ds_add_u32 %0, %1" :: "v"(lds0 + DONE), "v"(1) : "memory"); };
  auto touch = [&](const void* base, unsigned voff, unsigned soff, unsigned lim = 0x7fffffffu) {
    __builtin_amdgcn_raw_ptr_buffer_load_lds(__builtin_amdgcn_make_buffer_rsrc((void*)base, 0, lim, 0x00020000), (lds_void_ptr)(size_t)(lds0 + TCH), 4, voff, soff, 0, 0);
  };
  const auto rO = __builtin_amdgcn_make_buffer_rsrc((void*)a.O, 0, 0x7fffffff, 0x00020000);

  unsigned xsrc[XI], wsrc[WI];
  if constexpr (HALO) {
    const int w4 = wave & 3, lr = lane >> 3;
    const unsigned xv = (unsigned)(lr * (K * 2) + (((lane & 7) ^ ((w4 * 4 + (lr >> 1)) & 7)) << 4));
    const int r0 = m0 - HR + w4 * 8;
#pragma unroll
    for (int i = 0; i < XI; ++i) xsrc[i] = xv + (unsigned)((i == 0 ? (r0 < 0 ? 0 : r0) : r0 + 32 * (i < 4 ? i : 4)) * (K * 2));
    const unsigned wv = (unsigned)((w4 * 8 + lr) * (LDW * 2) + tn * 128 + (((lane & 7) ^ wswz<128>(w4 * 8 + lr)) << 4));
#pragma unroll
    for (int i = 0; i < WI; ++i) wsrc[i] = wv + (unsigned)((i & 1) * 32 * (LDW * 2) + (i >> 1) * 2048);
  } else {
#pragma unroll
  for (int i = 0; i < XI; ++i) {
    int j = (wave % NLW) + NLW * i; if (j >= NXI) j = wave % NLW;
    const int r = 8 * j + (lane >> 3), c = (lane & 7) ^ ((r >> 1) & 7);
    int gr = m0 - HR + r; if (gr < 0) gr = 0;
    xsrc[i] = FX ? (unsigned)(rbH(m0 >> 4, sd) + (j >> 1) * RSH + (j & 1) * 1024 + lane * 16) : (unsigned)(gr * K + kh * (K / SPK) + c * 8) * 2u;
  }
#pragma unroll
  for (int i = 0; i < WI; ++i) {
    int j = (wave % NLW) + NLW * i; if (j >= NWI) j -= NLW;
    if constexpr (GLU) {
      const int jj = j >= 7 ? j - 7 : j;
      const int b = jj * 1024 + lane * 16, p = b / 224, pos = (b % 224) >> 4;
      const int row = 8 * ((p & 15) >> 2) + (p & 3) + 4 * (p >> 4);
      wsrc[i] = (unsigned)(row * LDW + tn * 112 + pos * 8) * 2u;
    } else {
      const int b = j * 1024 + lane * 16, row = b / STRIDE, pos = (b % STRIDE) >> 4;
      const int c = pos ^ wswz<STRIDE>(row);
      wsrc[i] = (unsigned)((row + kh * (K / SPK)) * LDW + tn * (NCH * 8) + c * 8) * 2u;
    }
  }
  }
  auto ldX = [&](int i, int tile, int part, int slot, bool ok = true) {
    const auto rX = __builtin_amdgcn_make_buffer_rsrc((void*)a.X, 0, ok ? 0x7fffffff : 0, 0x00020000);
    if constexpr (GLU) tile = (tile + krot) & 15;
    const int j = wave + NLW * i;
    if (NXI % NLW != 0 && j >= NXI) return;
    __builtin_amdgcn_raw_ptr_buffer_load_lds(rX, (lds_void_ptr)(size_t)(lds0 + XBASE + slot * XT + (part * NXI + j) * 1024), 16, xsrc[i],
                                             omH(tile * 2048) + part * (NXI / 2) * RSH, 0, SEAM_IN ? XAUX : 0);
  };
#define KN_WNT 0
  constexpr int WAUX = ((KN_WNT >> EPI) & 1) ? 2 : 0;
  auto ldW = [&](int i, int unit, int slot, bool ok = true) {
    const auto rW0 = __builtin_amdgcn_make_buffer_rsrc((void*)a.W0, 0, ok ? 0x7fffffff : 0, 0x00020000);
    const auto rW1 = __builtin_amdgcn_make_buffer_rsrc((void*)(GLU ? a.W1 : a.W0), 0, ok ? 0x7fffffff : 0, 0x00020000);
    int j = wave + NLW * i; if (j >= NWI) j -= NLW;
    if constexpr (GLU) {
      unit = (unit + 2 * krot) & 31;
      const bool up = j >= 7;
      __builtin_amdgcn_raw_ptr_buffer_load_lds(up ? rW1 : rW0, (lds_void_ptr)(size_t)(lds0 + WBASE + slot * WU + (up ? UPOFF + (j - 7) * 1024 : j * 1024)), 16, wsrc[i],
                                               unit * KU * LDW * 2, 0, WAUX);
    } else {
      __builtin_amdgcn_raw_ptr_buffer_load_lds(rW0, (lds_void_ptr)(size_t)(lds0 + WBASE + slot * WU + j * 1024), 16, wsrc[i], unit * KU * LDW * 2, 0, WAUX);
    }
  };
  auto issueX = [&](int tile, int part, int slot) {
    if (wave >= NLW) return;
#pragma unroll
    for (int i = 0; i < XI; ++i) ldX(i, tile, part, slot);
  };
  auto issueW = [&](int unit, int slot) {
    if (wave >= NLW) return;
#pragma unroll
    for (int i = 0; i < WI; ++i) ldW(i, unit, slot);
  };

  f32x4 acc[NF][MF];
#pragma unroll
  for (int n = 0; n < NF; ++n)
#pragma unroll
    for (int m = 0; m < MF; ++m) acc[n][m] = (f32x4){0.f, 0.f, 0.f, 0.f};
  f32x4 acch = {0.f, 0.f, 0.f, 0.f};
  f32x4 accs = {0.f, 0.f, 0.f, 0.f};
  f32x4 gram = {0.f, 0.f, 0.f, 0.f}, rsum = {0.f, 0.f, 0.f, 0.f}, gramh = {0.f, 0.f, 0.f, 0.f};
  constexpr int ONES = RING;

  unsigned xa0, xa1, xh0 = 0, xh1 = 0;
  { const unsigned o = fr * 128 + ((fq ^ ((fr >> 1) & 7)) << 4), mo = (HR + wm * MF * 16) * 128;
    xa0 = lds0 + XBASE + mo + o; xa1 = lds0 + XBASE + mo + (o ^ 64); xh0 = lds0 + XBASE + o; xh1 = lds0 + XBASE + (o ^ 64);
    if constexpr (FX) { xa0 = lds0 + XBASE + mo + lane * 16; xa1 = xa0 + 1024; } }
  unsigned wa[C], wha = 0;
#pragma unroll
  for (int c = 0; c < C; ++c) {
    const int row = 8 * fq + wq, seg = HALO ? wn : wn * NF + c;
    if constexpr (GLU) wa[c] = lds0 + WBASE + (4 * fq + wq) * 224 + wn * 112 + (c == 0 ? wp * 8 : 96 + (wp & 1) * 8 + (wp >> 1) * UPOFF);
    else wa[c] = lds0 + WBASE + row * STRIDE + (((seg << 1) ^ wswz<STRIDE>(row)) << 4) + wp * 8;
  }

  using I0 = std::integral_constant<int, 0>; using I1 = std::integral_constant<int, 1>;
  using T = std::true_type; using F = std::false_type;
  typedef Frags<MFX, NFX> FR;
  struct RdCtx { unsigned xa, xh, wb[C], wh; };
  auto mkctx = [&](bool ks1, unsigned xoffs, unsigned woffs) {
    RdCtx c; c.xa = (ks1 ? xa1 : xa0) + xoffs; c.xh = fr == 0 ? lds0 + ONES : (ks1 ? xh1 : xh0) + xoffs; c.wh = wha + woffs;
#pragma unroll
    for (int i = 0; i < C; ++i) c.wb[i] = wa[i] + woffs;
    return c;
  };
  auto rd_item = [&](FR& f, const RdCtx& c, auto KSc, auto kc) {
    constexpr int KS = decltype(KSc)::value, k = decltype(kc)::value;
    if constexpr (k < MF) f.x[k] = ds_b128<k * 2048>(c.xa);
    else if constexpr (k < MFX) f.x[k] = ds_b128<0>(c.xh);
    else if constexpr (GLU) {
      constexpr int n = k - MFX, cc = n == 6, o = n == 6 ? 0 : (n >> 1) * 32 + (n & 1) * UPOFF;
      f.lo[n] = ds_tr<o>(c.wb[cc]); f.hi[n] = ds_tr<o + 16 * 224>(c.wb[cc]);
    } else if constexpr (k < MFX + NF) {
      constexpr int n = k - MFX, cc = n % C, o = (HALO ? n * 8192 : (n / C) * C * SEGB) + KS * 32 * STRIDE;
      f.lo[n] = ds_tr<o>(c.wb[cc]); f.hi[n] = ds_tr<o + 4 * STRIDE>(c.wb[cc]);
    }
  };
  auto wait_frags = [&](FR& f) {
    asm volatile("s_waitcnt lgkmcnt(0)" ::: "memory");
#pragma unroll
    for (int m = 0; m < MFX; ++m) asm volatile("" : "+v"(f.x[m]));
#pragma unroll
    for (int n = 0; n < NFX; ++n) asm volatile("" : "+v"(f.lo[n]), "+v"(f.hi[n]));
  };
#define KN_DPP 3
#define KN_FG 1
#define KN_HOLD 0
#define KN_HG 6
#define KN_FB 1
#define KN_FL 22
#define KN_FL2 20
#define KN_FE 25
  FR fa, fb;
  auto rdg = [&](const RdCtx& c, auto rc) {
    constexpr int r = decltype(rc)::value;
    if constexpr (r >= 28) { constexpr int m = (r - 28) >> 1; if constexpr (r & 1) fb.x[m] = ds_b128<m * 2048 + 1024>(c.xa); else fa.x[m] = ds_b128<m * 2048>(c.xa); }
    else {
      constexpr int n = r >> 2, cc = n == 6, o = (n == 6 ? 0 : (n >> 1) * 32 + (n & 1) * UPOFF) + ((r >> 1) & 1) * WU + (r & 1) * 16 * 224;
      FR& f = (r & 2) ? fb : fa;
      if constexpr (r & 1) f.hi[n] = ds_tr<o>(c.wb[cc]); else f.lo[n] = ds_tr<o>(c.wb[cc]);
    }
  };
  auto ping = [&](auto pc) {
    static_for<0, 36>([&](auto rc) { constexpr int r = decltype(rc)::value;
      if constexpr (SchedG::need(r) == decltype(pc)::value) {
        FR& f = (r >= 28 ? r & 1 : r & 2) ? fb : fa;
        if constexpr (r >= 28) { bf16x8& q = f.x[(r - 28) >> 1]; asm volatile("" : "+v"(q)); } else { s16x4& q = (r & 1) ? f.hi[r >> 2] : f.lo[r >> 2]; asm volatile("" : "+v"(q)); } } });
  };
  auto pairs = [&](auto PBc, auto PEc, auto RDc, auto CSc, const RdCtx& c, auto&& gapf) {
    constexpr bool RD = decltype(RDc)::value; constexpr int CS = decltype(CSc)::value;
    static_for<decltype(PBc)::value, decltype(PEc)::value>([&](auto pc) {
      constexpr int p = decltype(pc)::value, a = (p + 16) % 28, n = SchedG::pn(a), m = SchedG::pm(a);
      if constexpr (SchedG::cnt(p) >= 0) { asm volatile("s_waitcnt lgkmcnt(%0)" :: "n"(SchedG::cnt(p)) : "memory"); ping(pc); }
      static_for<0, 2>([&](auto hc) {
        constexpr int g = 2 * p + decltype(hc)::value;
        FR& u = ((a ^ g) & 1) ? fb : fa;
        acc[n][m] = __builtin_amdgcn_mfma_f32_16x16x32_bf16(__builtin_shufflevector(u.lo[n], u.hi[n], 0, 1, 2, 3, 4, 5, 6, 7), u.x[m], acc[n][m], 0, 0, 0);
        __builtin_amdgcn_sched_barrier(0);
        if constexpr (CS >= 0 && SchedG::last(a) && (g & 1)) {
          const unsigned sv = fr == n ? 0x3f803f80u : 0u;
          const u32x4 s4 = {sv, sv, sv, sv};
          FR& w = CS ? fb : fa;
          accs = __builtin_amdgcn_mfma_f32_16x16x32_bf16(__builtin_shufflevector(w.lo[n], w.hi[n], 0, 1, 2, 3, 4, 5, 6, 7), __builtin_bit_cast(bf16x8, s4), accs, 0, 0, 0);
          __builtin_amdgcn_sched_barrier(0);
        }
        if constexpr (RD) static_for<0, 36>([&](auto rc) { if constexpr (SchedG::gap(decltype(rc)::value) == g) rdg(c, rc); });
        if constexpr (g & 1) gapf(pc);
        __builtin_amdgcn_sched_barrier(0);
      });
    });
    if constexpr (RD) { asm volatile("s_waitcnt lgkmcnt(0)" ::: "memory"); ping(std::integral_constant<int, 28>{}); }
  };

  f32x4* tb_row = (f32x4*)(smem + RING);
  float* tb_cs = (float*)(tb_row + 256);
  constexpr int RTAB = 524288;
  auto glu_stats_issue = [&]() {
    if constexpr (EPI == EPI_GLU) {
      const auto rS = __builtin_amdgcn_make_buffer_rsrc((void*)a.st, 0, 0x7fffffff, 0x00020000);
      if (wave == 7) __builtin_amdgcn_raw_ptr_buffer_load_lds(rS, (lds_void_ptr)(size_t)(lds0 + RING + 3072), 16, ((lane >> 2) * S_ + m0 + tn * 8 + (lane & 3) * 2) * 8, 0, 0, 16);
    }
  };
  auto glu_stats = [&]() {
    if constexpr (EPI == EPI_GLU) {
      int lx = lane; asm volatile("" : "+v"(lx));
      const unsigned sa = lds0 + RING + 3072 + lx * 8;
      f32x2 p0 = ds_b64<0>(sa), p1 = ds_b64<512>(sa);
      asm volatile("s_waitcnt lgkmcnt(0)" : "+v"(p0), "+v"(p1) :: "memory");
      float s0 = p0[0] + p1[0]; asm volatile("" : "+v"(s0), "+v"(p0));
      const float mu = sum8(s0) * (1.f / 16.f);
      const float v = sum8((p0[1] + p1[1]) + 64.f * ((p0[0] - mu) * (p0[0] - mu) + (p1[0] - mu) * (p1[0] - mu))) * (1.f / 1024.f) + 1e-5f;
      float rs = __builtin_amdgcn_rsqf(v), k2 = -2.3022082f * rs; asm volatile("" : "+v"(k2), "+v"(rs));
      const f32x4 rv = {mu, -0.10294324f * rs * rs * rs, k2, v};
      __builtin_amdgcn_raw_buffer_store_b128(__builtin_bit_cast(u32x4, rv), __builtin_amdgcn_make_buffer_rsrc((void*)a.cs, 0, 0x7fffffff, 0x00020000),
                                             lx < 8 ? (unsigned)(RTAB + (m0 + tn * 8 + lx) * 16) : 0x80000000u, 0, 16);
    }
  };
  auto epilogue = [&](auto EAc) __attribute__((always_inline)) {
  STAMP(2);

  if constexpr (EPI == EPI_GLU) {
    constexpr bool EARLY = decltype(EAc)::value;
    static_assert(DONE + 4 == ACNT && TCH + 256 == FPAD, "LDS");
#define KN_EP 1
#define KN_HE 0
#define KN_W1 0
#define KN_W2 0
#define KN_WS (KN_W2 ? 4 : 2)
    if constexpr (!EARLY) {
      wait_vm<0>();
      if (KN_HE) hook(std::integral_constant<int, 2>{});
      lds_wait8(lds0 + DONE);
      if (KN_EP) __builtin_amdgcn_s_setprio(2);
      STAMPW(9, 4);
      if (KN_W1 == 0) hook(I0{});
      if (KN_W2 == 4) hook(std::integral_constant<int, 3>{});
      STAMPW(10, 4);
    } else { if (KN_EP) __builtin_amdgcn_s_setprio(3); hook(I1{}); }
    asm volatile("" ::: "memory");
    f32x4 cg[4], cu[4], rw[MF], rw6[2];
    int lz = lane; asm volatile("" : "+v"(lz));
    const int fr = lz & 15, fq = lz >> 4;
#pragma unroll
    for (int p = 0; p < 4; ++p) {
      const int o = wn * 56 + p * 16 + (p == 3 ? (fq & 1) : fq) * 4;
      cg[p] = *(const f32x4*)(tb_cs + o); cu[p] = *(const f32x4*)(tb_cs + 112 + o);
    }
#pragma unroll
    for (int m = 0; m < MF; ++m) rw[m] = tb_row[(wm * MF + m) * 16 + fr];
#pragma unroll
    for (int q = 0; q < 2; ++q) rw6[q] = tb_row[(wm * MF + 2 * q + (fq >> 1)) * 16 + fr];
    const int gz = rbA((m0 >> 4) + wm * 4, sd) + fr * 16, g = tn * 2 + wn, g1 = omA(g * 1024);
    const int go0 = gz + (fq & 1) * RSA + (fq >> 1) * 256, go6 = gz + ((fq & 1) * 2 + (fq >> 1)) * RSA;
    unsigned hv[MF][NF];
    auto put = [&](unsigned a0, unsigned a1, unsigned b0, unsigned b1, int vo, int so) {
      const auto s0 = __builtin_amdgcn_permlane16_swap(a0, b0, false, false), s1 = __builtin_amdgcn_permlane16_swap(a1, b1, false, false);
      __builtin_amdgcn_raw_buffer_store_b128((u32x4){s0[0], s1[0], s0[1], s1[1]}, rO, vo, so, 16);
      __builtin_amdgcn_sched_barrier(0);
      asm volatile("s_nop 1" ::: "memory");
      __builtin_amdgcn_sched_barrier(0);
    };
#define STAGE(body) _Pragma("unroll") for (int i = 0; i < 2 * NN; ++i) { const int m = MB + i / NN, n = N0 + i % NN; const f32x4 k = n == 6 ? rw6[m >> 1] : rw[m]; body; } __builtin_amdgcn_sched_barrier(0)
    auto grp = [&](auto MBc, auto N0c, auto NNc, auto&& mid) {
      constexpr int MB = decltype(MBc)::value, N0 = decltype(N0c)::value, NN = decltype(NNc)::value;
      f32x2 G[2 * NN], U[2 * NN], P[2 * NN];
      __builtin_amdgcn_sched_barrier(0);
      STAGE({ const int e = (n == 6 ? m & 1 : n & 1) * 2; const f32x4 x = n == 6 ? acc[6][m & 2] : acc[n & 6][m]; G[i] = ((f32x2){x[e], x[e + 1]}) - k[0] * ((f32x2){cg[n >> 1][e], cg[n >> 1][e + 1]}); });
      STAGE(P[i] = G[i] * G[i]);
      STAGE(P[i] = P[i] * k[1] + k[2]);
      STAGE(P[i] = G[i] * P[i]);
      STAGE({ const int e = (n == 6 ? m & 1 : n & 1) * 2; const f32x4 x = n == 6 ? acc[6][m | 1] : acc[n | 1][m]; U[i] = ((f32x2){x[e], x[e + 1]}) - k[0] * ((f32x2){cu[n >> 1][e], cu[n >> 1][e + 1]}); });
      STAGE(P[i] = ((f32x2){__builtin_amdgcn_exp2f(P[i][0]), __builtin_amdgcn_exp2f(P[i][1])}));
      mid();
      __builtin_amdgcn_sched_barrier(0);
      STAGE(P[i] = P[i] * k[3] + k[3]);
      STAGE(P[i] = ((f32x2){__builtin_amdgcn_rcpf(P[i][0]), __builtin_amdgcn_rcpf(P[i][1])}));
      STAGE(U[i] = G[i] * U[i]);
      STAGE(U[i] = U[i] * P[i]);
      STAGE({ hv[m][n] = pk_bf16(U[i][0], U[i][1]); asm volatile("" : "+v"(hv[m][n])); });
    };
#undef STAGE
    using I2 = std::integral_constant<int, 2>; using I3 = std::integral_constant<int, 3>; using I4 = std::integral_constant<int, 4>;
    grp(I0{}, I0{}, I4{}, [&]() { if (EARLY && KN_W1 == 2) { lds_wait8(lds0 + DONE); hook(I0{}); asm volatile("" ::: "memory"); } });
    if (EARLY && KN_W1 == 1) { lds_wait8(lds0 + DONE); hook(I0{}); asm volatile("" ::: "memory"); }
    if (!EARLY && KN_W2 == 3) { hook(I3{}); asm volatile("" ::: "memory"); }
    grp(I2{}, I0{}, I4{}, [&]() {
#pragma unroll
      for (int p = 0; p < 2; ++p) put(hv[0][2 * p], hv[0][2 * p + 1], hv[1][2 * p], hv[1][2 * p + 1], go0 + p * 512, g1); });
#pragma unroll
    for (int p = 0; p < 2; ++p) put(hv[2][2 * p], hv[2][2 * p + 1], hv[3][2 * p], hv[3][2 * p + 1], go0 + p * 512, g1 + 2 * RSA);
    asm volatile("" ::: "memory");
    __builtin_amdgcn_sched_barrier(0);
    STAMP(4);
    STAMPW(11, 4);
    if (KN_EP) __builtin_amdgcn_s_setprio(EARLY ? 1 : 0);
#define KN_AW 2
    auto arrived = [&](int w) { if (!EARLY && w == KN_AW) { wait_vm<0>(); lds_inc(lds0 + ACNT); STAMPW(6, 4); } };
    arrived(0);
    if (EARLY && KN_W2 == 1) { if (KN_W1 == 0) lds_wait8(lds0 + DONE); hook(I3{}); asm volatile("" ::: "memory"); }
#pragma unroll
    for (int q = 0; q < 2; ++q)
#pragma unroll
      for (int e = 0; e < 4; ++e) {
        const float x0 = acc[6][2 * q][e], x1 = acc[6][2 * q + 1][e];
        auto sw = __builtin_amdgcn_permlane32_swap(__float_as_uint(x0), __float_as_uint(x1), false, false);
        acc[6][2 * q][e] = __uint_as_float(sw[0]); acc[6][2 * q + 1][e] = __uint_as_float(sw[1]);
      }
    const int sb = omA((256 + g * 3) * 256), go4 = go0 + (fq >> 1) * (omA((257 + g * 3) * 256) - sb - 256);
    grp(I0{}, I4{}, I3{}, [&]() { arrived(1); });
    arrived(2);
    if (EARLY && KN_W2 == 2) { if (KN_W1 == 0) lds_wait8(lds0 + DONE); hook(I3{}); asm volatile("" ::: "memory"); }
    grp(I2{}, I4{}, I3{}, [&]() { arrived(3); put(hv[0][4], hv[0][5], hv[1][4], hv[1][5], go4, sb); });
    put(hv[2][4], hv[2][5], hv[3][4], hv[3][5], go4, sb + 2 * RSA);
    put(hv[0][6], hv[1][6], hv[2][6], hv[3][6], go6, omA((258 + g * 3) * 256));
    asm volatile("" ::: "memory");
    if constexpr (!EARLY) lds_inc(lds0 + ACNT + 4);
  }
  };
  using IM1 = std::integral_constant<int, -1>;

  if constexpr (KU == 32) {
    static_assert(SX == 3 && SW == 4 && NXI % NLW == 0, "ring");
    constexpr int WR = KN_GW ? 2 : 0;
    constexpr bool EG = (KN_E & 2) != 0;
    if constexpr (!KN_GW) { issueW(0, 0); issueW(1, 1); }
    if constexpr (!EG && KN_GW != 2) { issueW(2, 2 ^ WR); issueW(3, 3 ^ WR); }
    if (wave == 5) { ds_w32(lds0 + DONE, 0); ds_w32(lds0 + DONE + 4, 0); ds_w32(lds0 + DONE + 8, 0); }
    static_assert(EG, "early start");
    STAMP(9); STAMP(6);
    auto check_tile = [&](int t) {
      int lx = lane; asm volatile("" : "+v"(lx));
      const unsigned ad = lds0 + XBASE + t * XT + wave * 1024 + lx * 16;
      if (__builtin_expect(marked<8>(ad), 0)) {
        for (int tries = 0;; ++tries) {
          if (tries >= KN_TRY) wait_flags(a.sync + FHF(panel), (lane4() & 127) * 32, tag, LO32);
#pragma unroll 1
          for (int u = t; u < t + 1 + (t == 0); ++u) { issueX(u, 0, u); issueX(u, 1, u); }
          if (t == 0) wait_vm<2 * XI>(); else wait_vm<0>();
          if (tries >= KN_TRY || !marked<8>(ad)) break;
        }
      }
    };
    auto run = [&](auto LDRc, auto STc) __attribute__((always_inline)) {
      constexpr bool LDR = decltype(LDRc)::value;
      if constexpr (LDR) {
        issueX(0, 0, 0); issueX(0, 1, 0); issueX(1, 0, 1); issueX(1, 1, 1);
        if constexpr (EG && KN_GW != 2) { issueW(2, 2 ^ WR); issueW(3, 3 ^ WR); wait_vm<2 * XI + 2 * WI>(); } else wait_vm<2 * XI>();
        if constexpr (EG) check_tile(0);
      } else if constexpr (!EG) glu_stats_issue();
      lds_barrier();
      if constexpr (EG && !LDR) { if (wave == 4) __builtin_amdgcn_raw_ptr_buffer_load_lds(__builtin_amdgcn_make_buffer_rsrc((void*)(a.sync + FHF(panel)), 0, 4096, 0x00020000), (lds_void_ptr)(size_t)(lds0 + TCH + 256), 4, lane * 128, 0, 0, 16); }
      using I12 = std::integral_constant<int, 12>; using I28 = std::integral_constant<int, 28>;
      { const RdCtx c = mkctx(false, 0, WR * WU);
        static_for<0, 24>([&](auto gc) { static_for<0, 36>([&](auto rc) { if constexpr (SchedG::gap(decltype(rc)::value) == decltype(gc)::value) rdg(c, rc); }); });
        STAMP(1);
        pairs(I12{}, I28{}, T{}, IM1{}, c, [&](auto pc) { constexpr int i = (decltype(pc)::value - 12) >> 1; if constexpr (LDR && !(decltype(pc)::value & 1)) ldX(i % XI, 2, i / XI, 2); }); }
      int xs = 1;
#define KN_CTJ (KN_SPLIT ? 11 : 12)
      unsigned long long tb = (unsigned long long)a.PF0; unsigned tv = 0, tl = 0x7fffffffu, ts = KN_SPLIT ? 1835008u : 1048576u, tmask = 0; int tj = 1, tsh = 1;
      if constexpr (!LDR) {
        unsigned long long c0, c1; CODE_ADDR(c0, "k_all"); CODE_ADDR(c1, "k_zend");
#define KN_WJ 1
#define KN_WE 8
#define KN_DJ 6
        tj = KN_DJ;
        if (wave == 4) { tb = c0; tl = c1 - c0 < 262144ull ? (unsigned)(c1 - c0) : 0u; ts = 0; tmask = 1u << KN_CTJ; }
        if (wave == 5) tmask = 0x55u << (KN_DJ + (idx & 1));
        if (wave == 6) { tb = (unsigned long long)((idx & 16) ? a.W1 : a.W0); ts = 64 * LDW * 2; tmask = ((1u << (KN_WE - KN_KT)) - 1u) << KN_WJ; tj = KN_WJ - krot - KN_KT; tsh = 0; }
        const int l4 = lane / 14;
        tv = wave == 4 ? (unsigned)((lane * 32 + idx) * 128)
           : (lane >= 56 ? 0x80000000u : (wave == 5 ? (unsigned)(bid * 7168 + lane * 128) : (unsigned)(((idx & 15) * 4 + l4) * (LDW * 2) + (xcd & 3) * 1792 + (lane - l4 * 14) * 128)));
      }
      const int js = 2 * (tm & 3) + 1;
      const auto rC = __builtin_amdgcn_make_buffer_rsrc((void*)a.cs, 0, 0x7fffffff, 0x00020000);
      unsigned* ccnt = a.sync + FCS(tn);
#define KN_JR 10
#define KN_JS ((KN_E & 2) ? 2 : 1)
      const int jf = wave == 4 ? NT - 4 : (wave == 7 ? KN_JR : 64);
      const unsigned* fcnt = wave == 4 ? ccnt : a.sync + FRC(panel);
      const bool mrk = (KN_E & 4) && KN_MK == 0 && (idx >> 4) == ((xcd >> 1) & 1);
      const int jm = (idx >> 4) == ((xcd >> 1) & 1) && (idx & 7) < KN_V ? 3 : 64;
      static_assert(KN_MK == 2 && (KN_E & 4) && !KN_DM, "marks");
      int mo = rbA((m0 >> 4) + wave * 4, sd) + omA(tn * 2048);
      const unsigned evm = tmask | (8u << js) | (mrk ? 0xbfcu : 0u) | (wave == 4 ? 3u << (NT - 4) : 0u) | (wave == 7 ? ((5u << KN_JS) | 2u | (11u << KN_JR)) : 0u);
      constexpr int CFL = TCH + 256, CPB = XBASE + ((NT - 3) % 3) * XT;
      auto iter = [&](int j, auto LWc, auto LXc, auto WAITc, auto CSc) {
        constexpr bool LW = decltype(LWc)::value && LDR, LX = decltype(LXc)::value && LDR, LP = LW && !LX;
        if constexpr (LDR) {
          wait_vm<decltype(WAITc)::value>();
          if constexpr (EG && LX) { if (__builtin_expect(j <= 2, 0)) check_tile(j); }
          if constexpr ((KN_E & 4) && KN_MK == 2 && LX) {
            if ((unsigned)(j - jm) < 8u) {
              int lx = lane; asm volatile("" : "+v"(lx));
              unsigned on = 0xffffffffu; asm volatile("" : "+v"(on));
              __builtin_amdgcn_raw_buffer_store_b128((u32x4){on, on, on, on}, rO, lx * 16, mo, 16);
              mo += (j & 1) ? 1024 : RSA - 1024;
            }
            if (j == jm + 9) { if (lane == 0) set_flag(a.sync + FAM(panel) + (tn & 15) * 4 + wave, tag); }
          }
        }
        if constexpr (!LDR) {
          if (__builtin_expect((evm >> j) & 1, 0)) {
            if (j == js + 3) {
              if ((tmask >> (j - 1)) & 1) wait_vm<1>(); else wait_vm<0>();
              if (lane == 0) set_flag(ccnt + tm * 4 + wave - 4, tag);
            }
            if (EG && j == 1) glu_stats_issue();
            if (j == KN_JS && wave == 7) { wait_vm<0>(); glu_stats(); }
            if (j == KN_JS + 2 && wave == 7) {
              if (j == js + 2) wait_vm<1>(); else wait_vm<0>();
              if (lane == 0) set_flag((unsigned*)fcnt + tn, tag);
            }
            {
              int lx = lane; asm volatile("" : "+v"(lx));
              if (j == jf) __builtin_amdgcn_raw_ptr_buffer_load_lds(__builtin_amdgcn_make_buffer_rsrc((void*)fcnt, 0, 128, 0x00020000), (lds_void_ptr)(size_t)(lds0 + CFL + (wave & 1) * 256), 4, lx * 4, 0, 0, 16);
              if (j == jf + 1) {
                wait_vm<0>();
                if (lds_missing(lds0 + CFL + (wave & 1) * 256, tag, LO32)) wait_flags(fcnt, lx * 4, tag, LO32);
                if (wave == 7) {
#pragma unroll
                  for (int i = 0; i < 4; ++i) __builtin_amdgcn_raw_ptr_buffer_load_lds(rC, (lds_void_ptr)(size_t)(lds0 + RING + i * 1024), 16, lx * 16, RTAB + m0 * 16 + i * 1024, 0, 16);
                }
              }
              if (j == jf + 3) wait_vm<0>();
            }
            if ((tmask >> j) & 1) touch((const void*)tb, tv, (((j - tj) >> tsh) & 15) * ts, tl);
          }
          __builtin_amdgcn_sched_barrier(0);
        }
        int xp = xs + 2; if (xp >= 3) xp -= 3;
        const int ws = ((j & 1) * 2) ^ WR;
        auto lda = [&](int i) { ldW(i % WI, 2 * j + 2 + i / WI, (ws ^ 2) + i / WI); };
        auto ldb = [&](int i) {
            if constexpr (LX) ldX(i % XI, j + 2, i / XI, xp);
            else __builtin_amdgcn_raw_ptr_buffer_load_lds(rC, (lds_void_ptr)(size_t)(lds0 + CPB + wave * 8192 + i * 1024), 16, lane * 16, (tn * 2 + wn) * 7168 + i * 1024, 0, 16); };
        using NLA = std::integral_constant<int, LW ? 2 * WI : 0>; using NLB = std::integral_constant<int, LX ? 2 * XI : (LP ? 7 : 0)>;
        { const RdCtx c = mkctx(false, xs * XT, ws * WU);
          pairs(I0{}, I28{}, T{}, CSc, c, [&](auto pc) { constexpr int p = decltype(pc)::value;
            if constexpr (LDR && p == 0) loop_barrier();
            if constexpr (!LDR && p == KN_GE) { asm volatile("s_waitcnt lgkmcnt(0)" ::: "memory"); if (decltype(WAITc)::value == 0 || j + 1 < NT) loop_barrier(); }
            static_for<0, NLA::value>([&](auto ic) { constexpr int i = decltype(ic)::value; if constexpr (i * 13 / 8 == p) lda(i); });
            static_for<0, NLB::value>([&](auto ic) { constexpr int i = decltype(ic)::value; if constexpr (14 + i * 13 / 8 == p) ldb(i); }); }); }
        xs = xs + 1; if (xs >= 3) xs = 0;
      };
      using WX_ = std::integral_constant<int, 2 * XI>;
      bf16x8 pv[8];
      if constexpr (LDR) {
        for (int j = 1; j < NT - 2; ++j) iter(j, T{}, T{}, WX_{}, IM1{});
#define KN_PRI 1
        if (KN_PRI == 2) __builtin_amdgcn_s_setprio(3);
        iter(NT - 2, T{}, F{}, WX_{}, IM1{});
        if (KN_PRI == 1) __builtin_amdgcn_s_setprio(3);
        iter(NT - 1, F{}, F{}, std::integral_constant<int, 7>{}, IM1{});
        wait_vm<0>();
        int lc = lane; asm volatile("" : "+v"(lc));
        const unsigned ca = lds0 + CPB + wave * 8192 + (lc >> 4) * 1792 + ((lc & 15) < 14 ? lc & 15 : 0) * 16;
        static_for<0, 8>([&](auto ic) { constexpr int i = decltype(ic)::value; pv[i] = ds_b128<i * 224>(ca); });
      } else {
        int j = 1;
        if constexpr (EG) {
          if (wave == 4) {
            wait_vm<0>();
            if (lds_missing(lds0 + TCH + 256, tag, LO32)) wait_flags(a.sync + FHF(panel), (lane4() & 127) * 32, tag, LO32);
          }
        }
        loop_barrier();
#pragma unroll 1
        for (; j < js; ++j) iter(j, F{}, F{}, I0{}, IM1{});
        const int ja = wm == 2 ? js + 2 : js;
#pragma unroll 1
        for (; j < ja; ++j) iter(j, F{}, F{}, I0{}, I0{});
#pragma unroll 1
        for (; j < js + 2; ++j) iter(j, F{}, F{}, I0{}, I1{});
        { int lx = lane; asm volatile("" : "+v"(lx));
          __builtin_amdgcn_raw_buffer_store_b128(__builtin_bit_cast(u32x4, accs), rC, (lx & 15) < NF ? (unsigned)((tn * 4 + wn * 2) * 3584 + (tm * 2 + (wm & 1)) * 224
                  + ((lx & 15) < 6 ? (lx & 1) * 3584 + ((lx & 15) >> 1) * 64 + (lx >> 4) * 16 : (lx >> 5) * 3584 + 192 + (lx & 16))) : 0x80000000u, 0, 16); }
#pragma unroll 1
        for (; j < NT; ++j) iter(j, F{}, F{}, I1{}, IM1{});
        done_inc();
      }
      { const RdCtx c = mkctx(false, 0, 0); pairs(I0{}, I12{}, F{}, IM1{}, c, [&](auto pc) {
          if constexpr (LDR && decltype(pc)::value == 4) {
            asm volatile("s_waitcnt lgkmcnt(0)" ::: "memory");
#pragma unroll
            for (int k = 0; k < 8; ++k) { bf16x8& q = pv[k]; asm volatile("" : "+v"(q)); }
            f32x4 cv = ((__builtin_bit_cast(f32x4, pv[0]) + __builtin_bit_cast(f32x4, pv[1])) + (__builtin_bit_cast(f32x4, pv[2]) + __builtin_bit_cast(f32x4, pv[3])))
                     + ((__builtin_bit_cast(f32x4, pv[4]) + __builtin_bit_cast(f32x4, pv[5])) + (__builtin_bit_cast(f32x4, pv[6]) + __builtin_bit_cast(f32x4, pv[7])));
#pragma unroll
            for (int k = 0; k < 4; ++k) { const float t = cv[k]; auto sw = __builtin_amdgcn_permlane16_swap(__float_as_uint(t), __float_as_uint(t), false, false); cv[k] = __uint_as_float(sw[0]) + __uint_as_float(sw[1]); }
            if ((lane & 31) < 14) *(f32x4*)(tb_cs + (lane >> 5) * 112 + wn * 56 + fr * 4) = cv;
            done_inc();
          } }); }
#define KN_PRE 0
      if constexpr ((LDR || (KN_HOLD & 2)) && KN_PRE == 0) __builtin_amdgcn_s_setprio(0);
      epilogue(LDRc);
      if constexpr (LDR && KN_PRE == 1) __builtin_amdgcn_s_setprio(0);
    };
    if (wave < NLW) run(T{}, IM1{}); else run(F{}, IM1{});
  }
  STAMP(7);
  STAMP(8);
  if constexpr (GLU) STAMPW(12, 4);
  STAMP(3);
#endif
}

#define KN_PT 13
#define KN_QR 0
#define KN_PS 0
#define KN_PG -1
#define KN_FW 1
#define KN_GQ 6
#define KN_G0 4
#define KN_OC 12
#define KN_TG 10
#define KN_PQ 1
#define KN_PZ 0
#define KN_PN 4
#define KN_H0 0
#define KN_HS 0
#define KN_HN 7
#define KN_QO1 3
#define KN_QO2 3
#define KN_QO3 3
#define KN_QO4 16
#define KN_QP 2
#define KN_QH 0
#define KN_QS 18
#define KN_QLB 13

constexpr int QXT = 144 * 128, QWU = 24576, QWB = 3 * QXT, QRING = QWB + 4 * QWU;
constexpr int QMU = QRING + 64, QRS = QMU + 576, QFL = QRS + 576, QTCH = QRING + 2304;
constexpr int QCS = 2 * QXT, QKH = QCS + 2560, QVH = QKH + 4096;
#define KN_QWC 8
struct SchedQ {
  static constexpr int ns(int j) { return j < 2 ? 2 : 0; }
  static constexpr int nx(int j) { return (j + 2 < 16 ? 4 : 0) + ns(j); }
  static constexpr int wh(int j) { if (j < 0) return 12; int v = wh(j - 1) + KN_QWC - ns(j); if (v > 6 * (j + 4)) v = 6 * (j + 4); return v > 96 ? 96 : v; }
  static constexpr int oh(int j) { return j < 13 ? 0 : 2 * (j == 13 ? KN_QO1 : (j == 14 ? KN_QO2 : KN_QO3)); }
  static constexpr int tot(int j) { return j < 0 ? 20 : tot(j - 1) + nx(j) + wh(j) - wh(j - 1) + oh(j) - oh(j - 1); }
  static constexpr int cnt(int j) {
    int pos = j < 2 ? 10 * (j + 1) : tot(j - 3) + 4;
    for (int i = 0; i < j; ++i) if (wh(i - 1) < 6 * (j + 1) && wh(i) >= 6 * (j + 1)) { const int q = tot(i - 1) + nx(i) + 6 * (j + 1) - wh(i - 1); if (q > pos) pos = q; }
    return tot(j - 1) - pos;
  }
};
static_assert(SchedQ::cnt(0) == 10 && SchedQ::cnt(8) == 16 && KN_QO1 <= 3 && KN_QO2 <= 6 && KN_QO3 <= 9 && KN_QWC >= 8, "schedule");
static_assert(SchedQ::cnt(KN_S1P) <= SchedQ::tot(KN_S1P - 1) - SchedQ::tot(0) - 6 && KN_S1C > KN_S1P + 2, "statistics");
struct FragQ { bf16x8 x[4]; s16x4 lo[6], hi[6]; };
__device__ __forceinline__ f32x2 ds_r2(unsigned addr) { f32x2 r; asm volatile("ds_read2_b32 %0, %1 offset1:16" : "=v"(r) : "v"(addr)); return r; }
__device__ __forceinline__ float fma1(float a, float b, float c) { float r; asm("v_fma_f32 %0, %1, %2, %3" : "=v"(r) : "v"(a), "v"(b), "v"(c)); return r; }
__device__ __forceinline__ unsigned k1_phase(const GemmArgs& a) {
  unsigned tag = 0;
#if defined(__HIP_DEVICE_COMPILE__)
  constexpr int K = D_, LDW = 3 * D_, NT = 16, XI = 5, XIMIN = 4, WI = 6;
  extern __shared__ __attribute__((aligned(16))) char smem[];
  STAMP(0);
  const unsigned lds0 = (unsigned)(size_t)(lds_char_ptr)smem;
  const int tid = threadIdx.x, lane = tid & 63;
  const int wave = __builtin_amdgcn_readfirstlane(tid >> 6);
  const int bid = blockIdx.x, xcd = bid & 7, idx = bid >> 3;
  const int tm = (xcd >> 2) * 8 + (idx >> 2), tn = (xcd & 3) * 4 + (idx & 3), m0 = tm * 128;
  const int fr = lane & 15, fq = lane >> 4, wq = (lane >> 2) & 3, wp = lane & 3;
  using I0 = std::integral_constant<int, 0>; using I1 = std::integral_constant<int, 1>; using I2 = std::integral_constant<int, 2>; using I3 = std::integral_constant<int, 3>;
  using T = std::true_type; using F = std::false_type;
  auto touch = [&](const void* base, unsigned voff, unsigned soff, unsigned lim = 0x7fffffffu) {
    __builtin_amdgcn_raw_ptr_buffer_load_lds(__builtin_amdgcn_make_buffer_rsrc((void*)base, 0, lim, 0x00020000), (lds_void_ptr)(size_t)(lds0 + QTCH), 4, voff, soff, 0, 0);
  };
  auto code_touch = [&](int line) {
    unsigned long long c0, c1; CODE_ADDR(c0, "k_all"); CODE_ADDR(c1, "k_zend");
    const unsigned len = c1 - c0 < 262144ull ? (unsigned)(c1 - c0) : 0u;
    touch((const void*)c0, (unsigned)(line * 128), 0, len);
  };
  int hj1, hj2; unsigned htv;
  auto hset = [&]() {
    const int slc = (3 - ((unsigned)(size_t)a.W0 >> 7) - (xcd & 3) * 4) & 7;
    const int hid = (idx >> 2) * 3 + (idx & 3) - ((idx & 3) > slc ? 1 : 0);
    const bool hlp = slc < 4 && (idx & 3) != slc;
    hj1 = hlp ? hid / 3 - KN_HQ : -100; hj2 = hlp ? hid / 3 + 8 - KN_HQ : -100;
    htv = (unsigned)(lane * (LDW * 2) + (hid % 3) * 2048 + ((xcd & 3) * 4 + slc) * 128);
  };
  const unsigned xo = fr * 128 + ((fq ^ ((fr >> 1) & 7)) << 4);
  auto wfrag = [&](int seg) { const int row = 8 * fq + wq; return lds0 + QWB + row * 128 + (((seg << 1) ^ wswz<128>(row)) << 4) + wp * 8; };
  const f32x4 z4 = {0.f, 0.f, 0.f, 0.f};
  auto cat = [](s16x4 lo, s16x4 hi) { return __builtin_shufflevector(lo, hi, 0, 1, 2, 3, 4, 5, 6, 7); };
  unsigned xsrc[XI], wsrc[WI];
  { const int lr = lane >> 3, w4 = wave & 3;
    const unsigned xv = (unsigned)(lr * (K * 2) + (((lane & 7) ^ ((w4 * 4 + (lr >> 1)) & 7)) << 4));
    const int r0 = m0 - 16 + w4 * 8;
#pragma unroll
    for (int i = 0; i < XI; ++i) xsrc[i] = xv + (unsigned)((i == 0 ? (r0 < 0 ? 0 : r0) : r0 + 32 * i) * (K * 2));
    const unsigned wv = (unsigned)((w4 * 8 + lr) * (LDW * 2) + tn * 128 + (((lane & 7) ^ wswz<128>(w4 * 8 + lr)) << 4));
#pragma unroll
    for (int i = 0; i < WI; ++i) wsrc[i] = wv + (unsigned)((i & 1) * 32 * (LDW * 2) + (i >> 1) * 2048); }
  unsigned mb = lds0 + wave * 1024, rx = KN_QR ? ((tm ^ tn) & 1) * 1024 : 0, rw = rx * (64 * LDW * 2 / 128);
  asm volatile("s_waitcnt lgkmcnt(0)" : "+s"(mb), "+s"(rx), "+s"(rw) : "s"(a.warm), "v"(wsrc[0]), "v"(wsrc[1]), "v"(wsrc[2]), "v"(wsrc[3]), "v"(wsrc[4]), "v"(wsrc[5]), "v"(xsrc[0]), "v"(xsrc[1]), "v"(xsrc[2]), "v"(xsrc[3]), "v"(xsrc[4]));
  __builtin_amdgcn_sched_barrier(0);
  const u32x4 qX = mk_rsrc(a.X), qW = mk_rsrc(a.W0);
  auto ldX = [&](auto ic, auto tc) { constexpr int i = decltype(ic)::value, t = decltype(tc)::value; if (i < 4 || wave < 2) ld_cx<t % 3 * QXT + i * 4096, t * 128>(mb, qX, xsrc[i], rx); };
  auto ldW = [&](auto qc) { constexpr int i = decltype(qc)::value % WI, t = decltype(qc)::value / WI; ld_ca<QWB + (t & 3) * QWU + i * 4096, t * (64 * LDW * 2), (t >= 8)>(mb, qW, wsrc[i], rw); };
  if (__builtin_expect(wave < 4, 1)) {
    static_for<0, WI>(ldW);
    static_for<0, XI>([&](auto ic) { ldX(ic, I0{}); });
  }
  __builtin_amdgcn_sched_barrier(0);
  if (__builtin_expect(wave < 4, 1)) {
    unsigned seed;
    asm volatile("s_load_dword %0, %1, %2" : "=s"(seed) : "s"(a.sync), "n"(FEP * 4));
    auto sb0 = []() { __builtin_amdgcn_sched_barrier(0); };
    static_for<WI, WI + 2>(ldW); sb0();
    unsigned xh0[3], xh1[3], wb[2], l16 = lane * 16;
#pragma unroll
    for (int i = 0; i < 3; ++i) { xh0[i] = fr == 0 ? lds0 + QRING : lds0 + i * QXT + xo; xh1[i] = fr == 0 ? lds0 + QRING : lds0 + i * QXT + (xo ^ 64); asm volatile("" : "+v"(xh0[i]), "+v"(xh1[i])); }
    sb0(); static_for<WI + 2, WI + 4>(ldW); sb0();
    wb[0] = wfrag(wave); wb[1] = wb[0] + 2 * QWU;
    u32x4 one1; { const unsigned o = fr == 0 ? 0x3f803f80u : 0u; one1 = (u32x4){o, o, o, o}; }
    const char* srows = (const char*)a.X + (size_t)(m0 + tn * 8 + wave * 2) * (K * 2);
    asm volatile("" : "+v"(wb[0]), "+v"(wb[1]), "+v"(one1), "+v"(l16), "+s"(srows));
    sb0(); static_for<WI + 4, 2 * WI>(ldW); sb0();
    hset();
    asm volatile("" : "+s"(hj1), "+s"(hj2), "+v"(htv));
    sb0(); static_for<0, 2>([&](auto ic) { ldX(ic, I1{}); }); sb0();
    unsigned long long tb = (unsigned long long)((wave == 3 || wave == 0) ? a.PF1 : a.W0);
    unsigned tv, ts, to, evm;
    { const int t1 = (bid + 256) / 48 - KN_GQ, t2 = (bid + 512) / 48 - KN_GQ;
      const unsigned e0 = KN_OC ? 1u << KN_OC : 0u, e1 = KN_GQ ? (1u << (t1 > 0 ? t1 : 0)) | (1u << t2) : 0u, e2 = (hj1 >= 0 ? 1u << hj1 : 0u) | (hj2 > 0 ? 1u << hj2 : 0u), e3 = 2u << (idx % 12);
      evm = wave == 0 ? e0 : (wave == 1 ? e1 : (wave == 2 ? e2 : e3));
      ts = wave == 1 ? 2u << 20 : 8 * 64 * LDW * 2;
      to = wave == 1 ? 2u << 20 : (wave == 2 ? ((hj1 >= 0 ? hj1 : hj2) + KN_HQ) * (64 * LDW * 2) : 0); }
    asm volatile("" : "+s"(evm), "+s"(ts), "+s"(to), "+s"(tb));
    sb0(); static_for<2, 4>([&](auto ic) { ldX(ic, I1{}); }); sb0();
    tv = (wave & 1) ? (unsigned)((bid * 64 + lane) * 128)
       : (wave ? htv : (lane < 32 ? (unsigned)((idx * 32 + lane) * 2048 + (xcd & 1) * 1024 + ((3 - ((unsigned)(size_t)a.PF1 >> 7)) & 7) * 128) : 0x80000000u));
    asm volatile("" : "+v"(tv));
    sb0(); ldX(std::integral_constant<int, 4>{}, I1{}); sb0();
    asm volatile("s_waitcnt lgkmcnt(0)\n\ts_mul_i32 %0, %1, 0x9e3779b1\n\ts_add_u32 %0, %0, 0x7f4a7c15" : "=s"(tag) : "s"(seed) : "scc");
    unsigned osrc, gv, sdst, ssrc, sld, smu, srs, fa0, fa1, fa2, mko;
    auto ldO = [&](auto qc) { constexpr int i = decltype(qc)::value >> 1, h = decltype(qc)::value & 1;
      ld_cx<wo_pos(i >> 1) + (i & 1) * 8192 + h * 4096, (((i & 1) * 8 + (i >> 1)) * 2 + h) * (32 * D_ * 2)>(mb, mk_rsrc(a.PF1), osrc, (xcd & 3) * 8 * (32 * D_ * 2)); };
    u32x4 sv[4]; f32x4 s1s[2], s1q[2]; float sa[4], sb;
    auto dpp = [](float t, auto c) { return __int_as_float(__builtin_amdgcn_update_dpp(0, __float_as_int(t), decltype(c)::value, 0xf, 0xf, false)); };
    auto stat = [&](auto kc) {
      constexpr int k = decltype(kc)::value;
      if constexpr (k == 0) asm volatile("" : "+v"(sv[0]), "+v"(sv[1]), "+v"(sv[2]), "+v"(sv[3]));
      if constexpr (k < 8) {
        constexpr int r = (k >> 1) & 1, h = k >> 2;
        const u32x4 one4 = {0x3f803f80u, 0x3f803f80u, 0x3f803f80u, 0x3f803f80u};
        const bf16x8 x = __builtin_bit_cast(bf16x8, sv[2 * r + h]);
        if constexpr (k & 1) s1q[r] = __builtin_amdgcn_mfma_f32_16x16x32_bf16(x, x, h ? s1q[r] : z4, 0, 0, 0);
        else s1s[r] = __builtin_amdgcn_mfma_f32_16x16x32_bf16(__builtin_bit_cast(bf16x8, one4), x, h ? s1s[r] : z4, 0, 0, 0);
      }
      if constexpr (k == 8 || k == 9) {
        constexpr int r = k - 8;
        const f32x4 g = s1q[r];
        const float d = (lane & 2) ? ((lane & 1) ? g[3] : g[2]) : ((lane & 1) ? g[1] : g[0]);
        sa[r] = fq == 0 ? s1s[r][0] : 0.f;
        sa[2 + r] = fq == wq ? d : 0.f;
        asm volatile("" : "+v"(sa[r]), "+v"(sa[2 + r]));
      }
      if constexpr (k == 10 || k == 11) { constexpr int i = (k - 10) * 2; sa[i] = ((lane & 1) ? sa[i + 1] : sa[i]) + dpp((lane & 1) ? sa[i] : sa[i + 1], std::integral_constant<int, 0xb1>{}); asm volatile("" : "+v"(sa[i])); }
      if constexpr (k == 12) sb = ((lane & 2) ? sa[2] : sa[0]) + dpp((lane & 2) ? sa[0] : sa[2], std::integral_constant<int, 0x4e>{});
      if constexpr (k == 13) { sb += dpp(sb, std::integral_constant<int, 0x124>{}); sb += dpp(sb, std::integral_constant<int, 0x128>{}); }
      if constexpr (k == 14) { auto q = __builtin_amdgcn_permlane16_swap(__float_as_uint(sb), __float_as_uint(sb), false, false); sb = __uint_as_float(q[0]) + __uint_as_float(q[1]); }
      if constexpr (k == 15) { auto q = __builtin_amdgcn_permlane32_swap(__float_as_uint(sb), __float_as_uint(sb), false, false); sb = (__uint_as_float(q[0]) + __uint_as_float(q[1])) * (1.f / 1024.f); }
      if constexpr (k == 16) { const float var = dpp(sb, std::integral_constant<int, 0x4e>{}) - sb * sb; sa[0] = __builtin_amdgcn_rsqf((var > 0.f ? var : 0.f) + 1e-5f); asm volatile("" : "+v"(sa[0])); }
      if constexpr (k >= 12 && k < 16) asm volatile("" : "+v"(sb));
      if constexpr (k == 17) {
        __builtin_amdgcn_raw_buffer_store_b128((u32x4){__float_as_uint(-sb), __float_as_uint(sa[0]), tag, ~tag}, __builtin_amdgcn_make_buffer_rsrc((void*)a.cs, 0, 0x7fffffff, 0x00020000), sdst, 0, 16);
        asm volatile("s_nop 1" ::: "memory");
      }
    };
    f32x4 acq = z4, ack = z4, acv = z4;
    const auto rC = __builtin_amdgcn_make_buffer_rsrc((void*)a.cs, 0, 0x7fffffff, 0x00020000);
    auto fetch = [&]() { __builtin_amdgcn_raw_ptr_buffer_load_lds(rC, (lds_void_ptr)(size_t)(lds0 + QRING + STL + wave * 1024), 16, ssrc, 0, 0, 16); };
    u32x4 ent; f32x4 hm4; int hm1, htg;
    auto olane = [&]() { int lx = lane; asm volatile("" : "+v"(lx)); return lx; };
    auto chore = [&](auto jc, auto gc) {
      constexpr int j = decltype(jc)::value, g = decltype(gc)::value;
      if constexpr (j == 1 && g == -1) ent = __builtin_bit_cast(u32x4, ds_b128<0>(lds0 + QFL + 16));
      if constexpr (j == 1 && g == 5) {
        asm volatile("" : "+v"(ent));
        if (wave == 3) __builtin_amdgcn_raw_buffer_store_b32((tag & ~1u) | (((ent[0] + ent[2] > ent[1] + ent[3] ? 0u : 1u) ^ (xcd >> 2)) & 1u), rC, lane == 0 ? (unsigned)(VTAB + bid * 4) : 0x80000000u, 0, 16);
      }
      if constexpr (j == 2 && g == 1) { const int lx = olane(); sdst = lx < 2 ? (unsigned)(STAB + (m0 + tn * 8 + wave * 2 + lx) * 16) : 0x80000000u; asm volatile("" : "+v"(sdst)); }
      if constexpr (j == KN_S1P && g >= 0 && g < 4) { stat(std::integral_constant<int, 2 * g>{}); stat(std::integral_constant<int, 2 * g + 1>{}); }
      if constexpr (j == KN_S1P && (g == 9 || g == 10)) stat(std::integral_constant<int, g - 1>{});
      if constexpr (j == KN_S1P + 1 && g >= 0 && g < 8) stat(std::integral_constant<int, 10 + g>{});
      if constexpr (j == 5 && g == 1) { const int se = wave * 64 + olane(), sr = m0 - 16 + se;
        ssrc = se < 144 ? (unsigned)(STAB + (sr < 0 ? se : sr) * 16) : (wave == 3 ? (unsigned)(VTAB - 3072 + se * 16) : 0x80000000u); sld = lds0 + QRING + STL + se * 16; asm volatile("" : "+v"(ssrc), "+v"(sld)); }
      if constexpr (j == 5 && g == 5) { const int se = wave * 64 + olane();
        smu = lds0 + (se < 144 ? QMU + se * 4 : QTCH); srs = lds0 + (se < 144 ? QRS + (se & 1) * 288 + (se >> 1) * 4 : QTCH); asm volatile("" : "+v"(smu), "+v"(srs)); }
      if constexpr (j == 6 && g == 1) { const int lx = olane(); osrc = (unsigned)((wave * 8 + (lx >> 3)) * D_ + ((xcd & 1) * 8 + (idx & 7)) * 64 + ((lx & 7) ^ ((lx >> 3) & 6)) * 8) * 2u; asm volatile("" : "+v"(osrc)); }
      if constexpr (j == 6 && g == 5) { const int lx = olane(); gv = lx < 56 ? (unsigned)(((bid & 1) * 512 + (wave >> 1) * 128 + (bid >> 1)) * (DF_ * 2) + lx * 128) : 0x80000000u; asm volatile("" : "+v"(gv)); }
      if constexpr (j == 7 && g == 1) { const int lx = olane();
        fa0 = lds0 + QCS + ((lx & 15) == 0 ? wave * 64 + lx : 1024 + lx * 16); fa1 = lds0 + QCS + (lx < 16 ? 512 + wave * 64 : 1024) + lx * 4; asm volatile("" : "+v"(fa0), "+v"(fa1)); }
      if constexpr (j == 7 && g == 5) { fa2 = lds0 + (wave >> 1) * 1024 + (wave & 1) * 8 + l16;
        const int p2 = (xcd >> 1) * 2 + (idx >> 4), mq = (xcd & 1) * 16 + (idx & 15);
        mko = rbH(p2 * 16 + (mq >> 1), xcd >> 2) + omH((xcd >> 2) * 16384) + (mq & 1) * 3072; asm volatile("" : "+v"(fa2), "+s"(mko)); }
      if constexpr (j == KN_S1C && g == 10) fetch();
      if constexpr (j == KN_S1C + 3 && g == -1) ent = __builtin_bit_cast(u32x4, ds_b128<0>(sld));
      if constexpr (j == KN_S1C + 3 && g == 5) {
        static_assert(SchedQ::cnt(j) <= SchedQ::tot(j - 1) - SchedQ::tot(KN_S1C) && KN_S1C >= 8, "table");
        asm volatile("" : "+v"(ent));
        auto stale = [&]() { return __builtin_amdgcn_ballot_w64(wave == 3 ? ((ent[0] ^ tag) | (ent[1] ^ tag) | (ent[2] ^ tag) | (ent[3] ^ tag)) > 1u : (int)ssrc >= 0 && (ent[2] != tag || ent[3] != ~tag)) != 0; };
        if (__builtin_expect(stale(), 0)) {
          unsigned polls = 0;
          do {
            fetch();
            wait_vm<0>();
            ent = __builtin_bit_cast(u32x4, ds_b128<0>(sld));
            asm volatile("s_waitcnt lgkmcnt(0)" : "+v"(ent) :: "memory");
          } while (stale() && ++polls < 4000000u);
        }
      }
      if constexpr (j == KN_S1C + 3 && g == 8) {
        ds_w32(smu, (int)ent[0]); ds_w32(srs, (int)ent[1]);
        if (wave == 3) {
          int pc = 0;
#pragma unroll
          for (int i = 0; i < 4; ++i) pc += __builtin_popcountll(__builtin_amdgcn_ballot_w64(ent[i] & 1));
          tag = (tag & ~1u) | (pc > 128 ? 1u : 0u);
          ds_w32(lds0 + QFL + 8, (int)tag);
        }
      }
      if constexpr (j == KN_S1C + 4 && g >= 0 && g % 3 == 2) {
        if (wave == 3) {
          __builtin_amdgcn_raw_buffer_store_b128((u32x4){~0u, ~0u, ~0u, ~0u}, __builtin_amdgcn_make_buffer_rsrc((void*)a.st, 0, 0x7fffffff, 0x00020000), l16 + g / 3 * 1024,
                                                 mko ^ ((KN_NUMA & 1) ? ((SIDE ^ (xcd >> 2)) & 1) << 13 : 0), 16);
          asm volatile("s_nop 1" ::: "memory");
        }
      }
      if constexpr (j == NT - 2 && g == -1) { const int lx = olane(); hm4 = __builtin_bit_cast(f32x4, ds_b128<0>(lds0 + QMU + (lx & 48))); hm1 = ds_r32(lds0 + QMU + (lx & 15) * 4); htg = ds_r32(lds0 + QFL + 8); }
      if constexpr (j == NT - 2 && g == 5) { asm volatile("" : "+v"(hm4), "+v"(hm1), "+v"(htg)); tag = __builtin_amdgcn_readfirstlane(htg); }
      if constexpr (j == NT - 3 && g == 8 && (KN_CT & 2)) { if (wave == 2) code_touch(lane * 32 + idx); }
    };
    static_assert(KN_S1C + 4 <= NT - 4 && SchedQ::cnt(NT - 1) <= SchedQ::tot(NT - 2) - SchedQ::tot(KN_S1C + 4) && KN_H0 == 0, "marks");
    unsigned tsq = 0;
    using SQ = SchedQ;
    static_for<0, NT>([&](auto jc) {
      constexpr int j = decltype(jc)::value, NX = (j + 2 < NT ? XI : 0) + SQ::ns(j), NW = SQ::wh(j) - SQ::wh(j - 1), NL = NX + NW + SQ::oh(j) - SQ::oh(j - 1);
      wait_vm<SQ::cnt(j)>();
      lds_barrier();
      bf16x8 xh[2]; s16x4 lo[2][3], hi[2][3];
      auto rd = [&](auto rc) { constexpr int r = decltype(rc)::value, q = r < 4 ? r : r - 2, n = q >> 2, h = (q >> 1) & 1, o = (j & 1) * QWU + h * 4096 + n * 8192;
        if constexpr (r == 4 || r == 5) xh[r - 4] = ds_b128<0>(r == 5 ? xh1[j % 3] : xh0[j % 3]);
        else if constexpr (q & 1) hi[h][n] = ds_tr<o + 512>(wb[(j >> 1) & 1]); else lo[h][n] = ds_tr<o>(wb[(j >> 1) & 1]); };
      auto mm = [&](auto ic) { constexpr int n = decltype(ic)::value;
        asm volatile("s_waitcnt lgkmcnt(%0)" :: "n"(n == 0 ? 6 : (n == 1 ? 4 : 0)) : "memory");
        asm volatile("" : "+v"(lo[0][n]), "+v"(hi[0][n]), "+v"(lo[1][n]), "+v"(hi[1][n]));
        if constexpr (n == 1) asm volatile("" : "+v"(xh[0]), "+v"(xh[1]));
#pragma unroll
        for (int h = 0; h < 2; ++h) {
          if constexpr (n == 0) acq = __builtin_amdgcn_mfma_f32_16x16x32_bf16(cat(lo[h][0], hi[h][0]), __builtin_bit_cast(bf16x8, one1), acq, 0, 0, 0);
          if constexpr (n == 1) ack = __builtin_amdgcn_mfma_f32_16x16x32_bf16(cat(lo[h][1], hi[h][1]), xh[h], ack, 0, 0, 0);
          if constexpr (n == 2) acv = __builtin_amdgcn_mfma_f32_16x16x32_bf16(xh[h], cat(lo[h][2], hi[h][2]), acv, 0, 0, 0);
        } };
      chore(jc, std::integral_constant<int, -1>{});
      constexpr int H0 = j < NT - 1 ? KN_H0 : 0, NG = NL > 10 + H0 ? NL : 10 + H0;
      if constexpr (KN_HS > 0) asm volatile("s_nop %0" :: "n"(KN_HS - 1));
      static_for<0, NG>([&](auto gc) {
        constexpr int g = decltype(gc)::value, h = g - H0;
        __builtin_amdgcn_sched_barrier(0);
        if constexpr (g < NX - SQ::ns(j)) ldX(gc, std::integral_constant<int, j + 2>{});
        else if constexpr (g < NX) {
          constexpr int q = 2 * j + g - XI;
          asm volatile("s_nop 4\n\tglobal_load_dwordx4 %0, %1, %2 offset:%3" : "=&v"(sv[q]) : "v"(l16), "s"(srows), "n"(q * 1024) : "memory");
        }
        else if constexpr (g < NX + NW) ldW(std::integral_constant<int, SQ::wh(j - 1) + g - NX>{});
        else if constexpr (g < NL) ldO(std::integral_constant<int, SQ::oh(j - 1) + g - NX - NW>{});
        else if constexpr (h < 9) asm volatile("s_nop %0" :: "n"(KN_HN));
        if constexpr (j <= 12 && g == KN_TG) { if (__builtin_expect((evm >> j) & 1, 0)) { touch((const void*)tb, tv, to); to += ts; } }
        if constexpr (g == (KN_PG < 0 ? NG - 1 : KN_PG) && j <= KN_PT && j >= KN_PT - 3 * KN_PS && (KN_PS == 0 || (KN_PT - j) % (KN_PS ? KN_PS : 1) == 0)) {
          if (KN_PS == 0 || wave == 3 - (KN_PT - j) / (KN_PS ? KN_PS : 1)) touch(wave & 1 ? a.CW1 : a.CW0, gv, 0);
        }
        __builtin_amdgcn_sched_barrier(0);
        if constexpr (h >= 0 && h < 7) { rd(std::integral_constant<int, 2 * h>{}); rd(std::integral_constant<int, 2 * h + 1>{}); }
        if constexpr (h == 4 || h == 7 || h == 9) mm(std::integral_constant<int, h == 4 ? 0 : (h == 7 ? 1 : 2)>{});
        __builtin_amdgcn_sched_barrier(0);
        chore(jc, gc);
      });
      __builtin_amdgcn_sched_barrier(0);
    });
    {
      f32x4 ck;
#pragma unroll
      for (int e = 0; e < 4; ++e) ck[e] = dpp(ack[e], std::integral_constant<int, 0x150>{});
      const auto c1 = __builtin_amdgcn_permlane16_swap(__float_as_uint(acv[0]), __float_as_uint(acv[0]), false, false);
      const auto c2 = __builtin_amdgcn_permlane32_swap(c1[0], c1[0], false, false);
      const float nm = __int_as_float(hm1), nc = __uint_as_float(c2[0]);
      asm volatile("ds_write_b128 %0, %1\n\tds_write_b128 %0, %2 offset:256\n\tds_write_b32 %3, %4" :: "v"(fa0), "v"(acq), "v"(ck), "v"(fa1), "v"(nc) : "memory");
      const u32x2 kk = {pk_bf16(fma1(nm, ck[0], ack[0]), fma1(nm, ck[1], ack[1])), pk_bf16(fma1(nm, ck[2], ack[2]), fma1(nm, ck[3], ack[3]))};
      const u32x2 vv = {pk_bf16(fma1(nc, hm4[0], acv[0]), fma1(nc, hm4[1], acv[1])), pk_bf16(fma1(nc, hm4[2], acv[2]), fma1(nc, hm4[3], acv[3]))};
      asm volatile("ds_write_b64 %0, %1 offset:%3\n\tds_write_b64 %0, %2 offset:%4" :: "v"(fa2), "v"(kk), "v"(vv), "n"(QKH), "n"(QVH) : "memory");
    }
    lds_barrier();
    if (tid == 192) set_flag(a.sync + FHM((xcd >> 1) * 2 + (idx >> 4)) + (xcd & 1) * 16 + (idx & 15), tag);
    static_for<SQ::oh(NT - 1), 2 * KN_QO4>(ldO);
    wait_vm<2 * (KN_QO4 - 2)>();
    const int sd = SIDE;
    unsigned fo = tid == 0 ? (unsigned)(FLO(tm) + tn * 32) * 4u : 0x80000000u, ft = tag;
    const auto rF = __builtin_amdgcn_make_buffer_rsrc((void*)a.sync, 0, 147456, 0x00020000);
    asm volatile("" : "+v"(fo), "+v"(ft) : "s"(rF));
    raw_barrier();
    if (wave == 0) __builtin_amdgcn_raw_buffer_store_b32(ft, rF, fo, 0, 16);
    static_for<2 * KN_QO4, 32>(ldO);
  } else {
    const int wm = (wave >> 1) & 1, hd = wave & 1;
    if (wave == 4) {
      if (lane < 4) { *(unsigned*)(smem + QRING + lane * 4) = 0x3f803f80u; if (lane < 2) *(unsigned*)(smem + QFL + lane * 4) = 0u; }
      if (KN_FW) __builtin_amdgcn_raw_ptr_buffer_load_lds(__builtin_amdgcn_make_buffer_rsrc((void*)a.sync, 0, 147456, 0x00020000), (lds_void_ptr)(size_t)(lds0 + QTCH), 4, lane * 32768 + bid * 128, 0, 0, 16);
    }
    if (wave == 6) { hset(); if (hj1 < 0 && hj1 > -100) touch(a.W0, htv, (hj1 + KN_HQ) * (64 * LDW * 2));
      if (KN_G0 && bid >= KN_G0 * 48) touch(a.W0, (bid * 64 + lane) * 128, 0); }
    if ((KN_CT & 1) && wave == 7) code_touch(lane * 32 + idx);
    {
      const int id = bid * 2 + wm;
      const char* pb = (const char*)a.sync + PRB + (id >> 3) * 16384 + hd * 8192 + (id & 7) * (KN_PN == 8 ? 128 : 256);
      unsigned long long p0, p1; unsigned t;
#define SL4(a, b) "s_load_dword %1, %3, 0x" a "00\n\ts_load_dword %1, %3, 0x" b "00\n\ts_load_dword %1, %3, 0x1" a "00\n\ts_load_dword %1, %3, 0x1" b "00\n\t"
      if (KN_PN == 8) asm volatile("s_sleep %5\n\ts_memtime %0\n\ts_waitcnt lgkmcnt(0)\n\t" SL4("0", "8") SL4("4", "c") "s_waitcnt lgkmcnt(%4)\n\ts_memtime %2\n\ts_waitcnt lgkmcnt(0)" : "=&s"(p0), "=&s"(t), "=&s"(p1) : "s"(pb), "n"(KN_PQ), "n"(KN_PZ));
      else asm volatile("s_sleep %5\n\ts_memtime %0\n\ts_waitcnt lgkmcnt(0)\n\t" SL4("0", "8") "s_waitcnt lgkmcnt(%4)\n\ts_memtime %2\n\ts_waitcnt lgkmcnt(0)" : "=&s"(p0), "=&s"(t), "=&s"(p1) : "s"(pb), "n"(KN_PQ), "n"(KN_PZ));
      ds_w32(lds0 + QFL + 16 + (wave - 4) * 4, (int)((unsigned)p1 - (unsigned)p0));
    }
    f32x2 bias[2], bias0[2];
    {
      const int srow = m0 + wm * 64 + fr;
      const unsigned mk = 0x5555u & ((2u << (srow < 15 ? srow : 15)) - 1u), sft = (fr & 14) - fq * 4 + 14;
      const unsigned b0 = ~(0x5555u >> sft), b1 = ~(0x55550000u >> sft), c0 = ~(mk >> sft), c1 = ~((mk << 16) >> sft);
#pragma unroll
      for (int j = 0; j < 4; ++j) {
        bias[j >> 1][j & 1] = __uint_as_float((unsigned)__builtin_amdgcn_sbfe((int)(j < 2 ? b0 : b1), 2 - 2 * (j & 1), 1) & 0xff61b1e6u);
        bias0[j >> 1][j & 1] = __uint_as_float((unsigned)__builtin_amdgcn_sbfe((int)(j < 2 ? c0 : c1), 2 - 2 * (j & 1), 1) & 0xff61b1e6u);
      }
    }
    unsigned pl = (fr & 1) ? 0x01000c0cu : 0x0c0c0100u, ph = (fr & 1) ? 0x03020c0cu : 0x0c0c0302u;
    unsigned ta = lds0 + QRING + STL + (16 + wm * 64 + fr) * 16, tm4 = lds0 + QMU + (16 + wm * 64 + fq * 4) * 4, trk = lds0 + QRS + (fr & 1) * 288 + (wm * 32 + fq * 2) * 4;
    unsigned tcs = lds0 + hd * 128 + fq * 16, tcv = lds0 + QCS + 512 + hd * 128 + fr * 4, tex = lds0 + hd * 1024 + lane * 16, tfl = lds0 + QFL + hd * 4, odst = rbH((m0 >> 4) + wm * 4, xcd >> 2) + omH(tn * 2048) + hd * 1024 + lane * 16;
    asm volatile("" : "+v"(bias[0]), "+v"(bias[1]), "+v"(bias0[0]), "+v"(bias0[1]), "+v"(pl), "+v"(ph), "+v"(ta), "+v"(tm4), "+v"(trk), "+v"(tcs), "+v"(tcv), "+v"(tex), "+v"(tfl), "+v"(odst));
    f32x4 acc[6][4];
#pragma unroll
    for (int n = 0; n < 6; ++n)
#pragma unroll
      for (int m = 0; m < 4; ++m) acc[n][m] = z4;
    const unsigned xa[2] = {lds0 + (16 + wm * 64) * 128 + xo, lds0 + (16 + wm * 64) * 128 + (xo ^ 64)}, wa[2] = {wfrag(hd * 2), wfrag(hd * 2 + 1)};
    struct Ctx { unsigned x[2], w[2]; } c = {{xa[0], xa[1]}, {wa[0], wa[1]}};
    using SP = SchedP<6, KN_QS>;
    FragQ fa, fb;
    auto rd = [&](auto rc) {
      constexpr int r = decltype(rc)::value;
      if constexpr (r >= 24) { constexpr int m = (r - 24) >> 1; if constexpr (r & 1) fb.x[m] = ds_b128<m * 2048>(c.x[1]); else fa.x[m] = ds_b128<m * 2048>(c.x[0]); }
      else { constexpr int n = r >> 2, o = ((r >> 1) & 1) * 4096 + (n >> 1) * 8192 + (r & 1) * 512; FragQ& f = (r & 2) ? fb : fa;
        if constexpr (r & 1) f.hi[n] = ds_tr<o>(c.w[n & 1]); else f.lo[n] = ds_tr<o>(c.w[n & 1]); }
    };
    auto pin = [&](auto pc) {
      static_for<0, 32>([&](auto rc) { constexpr int r = decltype(rc)::value;
        if constexpr (SP::need(r) == decltype(pc)::value) {
          FragQ& f = (r >= 24 ? r & 1 : r & 2) ? fb : fa;
          if constexpr (r >= 24) { bf16x8& q = f.x[(r - 24) >> 1]; asm volatile("" : "+v"(q)); } else { s16x4& q = (r & 1) ? f.hi[r >> 2] : f.lo[r >> 2]; asm volatile("" : "+v"(q)); } } });
    };
    int sx = QXT, sw = QWU;
    auto pairs = [&](auto PBc, auto PEc, auto RDc, auto&& gap) {
      constexpr bool RD = decltype(RDc)::value;
      static_for<decltype(PBc)::value, decltype(PEc)::value>([&](auto pc) {
        constexpr int p = decltype(pc)::value, A = SP::pa(p), n = SP::pn(A), m = SP::pm(A);
        if constexpr (SP::cnt(p) >= 0) { asm volatile("s_waitcnt lgkmcnt(%0)" :: "n"(SP::cnt(p)) : "memory"); pin(pc); }
        static_for<2 * p, 2 * p + 2>([&](auto gc) {
          constexpr int g = decltype(gc)::value;
          FragQ& u = ((A ^ g) & 1) ? fb : fa;
          if constexpr (n < 4) acc[n][m] = __builtin_amdgcn_mfma_f32_16x16x32_bf16(cat(u.lo[n], u.hi[n]), u.x[m], acc[n][m], 0, 0, 0);
          else acc[n][m] = __builtin_amdgcn_mfma_f32_16x16x32_bf16(u.x[m], cat(u.lo[n], u.hi[n]), acc[n][m], 0, 0, 0);
          __builtin_amdgcn_sched_barrier(0);
          gap(gc);
          if constexpr (RD) {
            static_for<0, 32>([&](auto rc) { if constexpr (SP::gap(decltype(rc)::value) == g) rd(rc); });
            if constexpr (g == 13) { sx = sx + QXT; sw = sw + QWU; asm volatile("" : "+s"(sx), "+s"(sw)); }
            if constexpr (g == 17) { sx = sx == 3 * QXT ? 0 : sx; asm volatile("" : "+s"(sx)); }
            if constexpr (g == 21) { sw = sw == 4 * QWU ? 0 : sw; asm volatile("" : "+s"(sw)); }
            if constexpr (g == 44) { c.x[0] = xa[0] + sx; c.x[1] = xa[1] + sx; asm volatile("" : "+v"(c.x[0]), "+v"(c.x[1])); }
            if constexpr (g == 45) { c.w[0] = wa[0] + sw; c.w[1] = wa[1] + sw; asm volatile("" : "+v"(c.w[0]), "+v"(c.w[1])); }
          }
          __builtin_amdgcn_sched_barrier(0);
        });
      });
      if constexpr (RD) { asm volatile("s_waitcnt lgkmcnt(0)" ::: "memory"); pin(std::integral_constant<int, 24>{}); }
    };
    static_assert(SP::gap(23) <= 44, "addresses");
    using I12 = std::integral_constant<int, 12>; using I24 = std::integral_constant<int, 24>;
    lds_barrier();
    static_for<0, 24>([&](auto gc) { static_for<0, 32>([&](auto rc) { if constexpr (SP::gap(decltype(rc)::value) == decltype(gc)::value) rd(rc); }); });
    STAMP(1);
    pairs(I12{}, I24{}, T{}, [&](auto) {});
#pragma unroll 1
    for (int j = 1; j < NT; ++j) pairs(I0{}, I24{}, T{}, [&](auto pc) { if constexpr (decltype(pc)::value == 1) raw_barrier(); });
#define ES(i)
    ES(0);
    if (KN_QP) __builtin_amdgcn_s_setprio(KN_QP);
    f32x2 st[4], rk[5], cv, fs[4][2]; f32x4 m4[4], cs[4]; int tg;
    pairs(I0{}, I12{}, F{}, [&](auto ic) { constexpr int I = decltype(ic)::value;
      if constexpr (I < 4) st[I] = ds_b64<I * 256>(ta);
      if constexpr (I == 4) tg = ds_r32(lds0 + QFL + 8);
      if constexpr (I < 5) rk[I] = ds_b64<I * 32>(trk);
      if constexpr (I >= 5 && I < 9) m4[I - 5] = __builtin_bit_cast(f32x4, ds_b128<(I - 5) * 64>(tm4));
      if constexpr (I == 9) { asm volatile("s_waitcnt lgkmcnt(4)" : "+v"(st[0]), "+v"(st[1]), "+v"(st[2]), "+v"(st[3]), "+v"(rk[0]), "+v"(rk[1]), "+v"(rk[2]), "+v"(rk[3]), "+v"(rk[4]) :: "memory"); }
      if constexpr (I == 9 || I == 10) { constexpr int m = (I - 9) * 2; const float sc2 = 0.17677669529663687f * 1.4426950408889634f; st[m][1] *= sc2; st[m + 1][1] *= sc2; asm volatile("" : "+v"(st[m]), "+v"(st[m + 1])); }
      if constexpr (I >= 11 && I < 19) { constexpr int m = (I - 11) >> 1, h = (I - 11) & 1; fs[m][h] = rk[m + h] * st[m][1]; asm volatile("" : "+v"(fs[m][h])); }
      if constexpr (I == 19) { int& q = tg; asm volatile("" : "+v"(q)); tag = __builtin_amdgcn_readfirstlane(tg); if (KN_NUMA & 1) odst ^= ((SIDE ^ (xcd >> 2)) & 1) << 13; asm volatile("" : "+v"(odst)); }
      if constexpr (I == 4) ES(1);
      if constexpr (I == KN_QLB) { ES(2); raw_barrier(); ES(3); }
      if constexpr (I >= 14 && I < 18) { constexpr int q = (I - 14) ^ 2; cs[q] = __builtin_bit_cast(f32x4, ds_b128<QCS + (q >> 1) * 256 + (q & 1) * 64>(tcs)); }
      if constexpr (I == 18) cv = ds_r2(tcv); });
    static_assert(KN_QLB <= 14, "barrier");
    STAMP(2);
    ES(4);
    asm volatile("s_waitcnt lgkmcnt(0)" : "+v"(cs[0]), "+v"(cs[1]), "+v"(cs[2]), "+v"(cs[3]), "+v"(cv), "+v"(m4[0]), "+v"(m4[1]), "+v"(m4[2]), "+v"(m4[3]) :: "memory");
    u32x4 Q[4], Kf[5], V[5];
    auto nrm = [&](f32x4 x, f32x4 y, float sc) {
      const f32x2 p = (f32x2){y[0], y[1]} * sc + (f32x2){x[0], x[1]}, q = (f32x2){y[2], y[3]} * sc + (f32x2){x[2], x[3]};
      return (u32x2){pk_bf16(p[0], p[1]), pk_bf16(q[0], q[1])}; };
    auto c4 = [](u32x2 p, u32x2 q) { return (u32x4){p[0], p[1], q[0], q[1]}; };
    auto mkK = [&](int m) { Kf[m + 1] = c4(nrm(acc[2][m], cs[2], st[m][0]), nrm(acc[3][m], cs[3], st[m][0])); };
    auto mkQ = [&](int m) { Q[m] = c4(nrm(acc[0][m], cs[0], st[m][0]), nrm(acc[1][m], cs[1], st[m][0])); };
    auto mkV = [&](int m) { V[m + 1] = c4(nrm(acc[4][m], m4[m], cv[0]), nrm(acc[5][m], m4[m], cv[1])); };
    mkK(3); mkV(3);
    if (wm == 0) asm volatile("ds_write_b128 %0, %1 offset:%5\n\tds_write_b128 %0, %2 offset:%6\n\tds_write_b32 %3, %4" :: "v"(tex), "v"(Kf[4]), "v"(V[4]), "v"(tfl), "v"(pl), "n"(QKH + 2048), "n"(QVH + 2048) : "memory");
    __builtin_amdgcn_sched_barrier(0);
    f32x4 sc[4][2]; f32x2 lg[4][2]; float mx[4];
    auto qk = [&](int m, int h) { sc[m][h] = __builtin_amdgcn_mfma_f32_16x16x32_bf16(__builtin_bit_cast(bf16x8, Kf[m + h]), __builtin_bit_cast(bf16x8, Q[m]), z4, 0, 0, 0); };
    mkQ(3); mkK(2); qk(3, 1); qk(3, 0); mkQ(2); mkK(1); qk(2, 1); qk(2, 0); mkQ(1); mkK(0); qk(1, 1); qk(1, 0);
    __builtin_amdgcn_sched_barrier(0);
    int fl; bf16x8 k0, v0;
    tex += wm * 2048;
    auto front = [&]() { fl = ds_r32(tfl); k0 = ds_b128<QKH>(tex); v0 = ds_b128<QVH>(tex); };
    front();
    __builtin_amdgcn_sched_barrier(0);
    mkQ(0); qk(0, 1); mkV(2); mkV(1);
    __builtin_amdgcn_sched_barrier(0);
    asm volatile("s_waitcnt lgkmcnt(0)" : "+v"(fl), "+v"(k0), "+v"(v0), "+v"(V[3]), "+v"(V[2]) :: "memory");
    if (__builtin_expect(wm && __builtin_amdgcn_readfirstlane(fl) == 0, 0)) {
      unsigned polls = 0;
      do { front(); asm volatile("s_waitcnt lgkmcnt(0)" : "+v"(fl), "+v"(k0), "+v"(v0) :: "memory"); } while (__builtin_amdgcn_readfirstlane(fl) == 0 && ++polls < 40000000u);
    }
    Kf[0] = __builtin_bit_cast(u32x4, k0); V[0] = __builtin_bit_cast(u32x4, v0);
    qk(0, 0); mkV(0);
    STAMP(4);
    __builtin_amdgcn_sched_barrier(0); ES(5);
    __builtin_amdgcn_sched_barrier(0);
    const bool par = pl & 4;
#pragma unroll
    for (int m = 3; m >= 0; --m) {
#pragma unroll
      for (int h = 0; h < 2; ++h) {
        float e[4] = {sc[m][h][0], sc[m][h][1], sc[m][h][2], sc[m][h][3]};
        asm("" : "+v"(e[0]), "+v"(e[2]));
        lg[m][h] = (f32x2){par ? e[1] : e[0], par ? e[3] : e[2]} * fs[m][h] + (m == 0 ? bias0[h] : bias[h]);
      }
      mx[m] = fmaxf(fmaxf(lg[m][0][0], lg[m][0][1]), fmaxf(lg[m][1][0], lg[m][1][1]));
    }
    __builtin_amdgcn_sched_barrier(0);
    auto s16 = [](float x, float y) { auto p = __builtin_amdgcn_permlane16_swap(__float_as_uint(x), __float_as_uint(y), false, false); return (f32x2){__uint_as_float(p[0]), __uint_as_float(p[1])}; };
    auto s32 = [](float x, float y) { auto p = __builtin_amdgcn_permlane32_swap(__float_as_uint(x), __float_as_uint(y), false, false); return (f32x2){__uint_as_float(p[0]), __uint_as_float(p[1])}; };
    auto spread = [&](float r) { const f32x2 t = s32(r, r), u = s16(t[0], t[0]), w = s16(t[1], t[1]); mx[0] = u[0]; mx[1] = u[1]; mx[2] = w[0]; mx[3] = w[1]; };
    { const f32x2 p = s16(mx[0], mx[1]), q = s16(mx[2], mx[3]), r = s32(fmaxf(p[0], p[1]), fmaxf(q[0], q[1])); spread(fmaxf(r[0], r[1])); }
    __builtin_amdgcn_sched_barrier(0);
#pragma unroll
    for (int m = 3; m >= 0; --m) {
#pragma unroll
      for (int h = 0; h < 2; ++h) { const f32x2 d = lg[m][h] - mx[m]; lg[m][h] = (f32x2){__builtin_amdgcn_exp2f(d[0]), __builtin_amdgcn_exp2f(d[1])}; }
      const f32x2 t = lg[m][0] + lg[m][1];
      mx[m] = t[0] + t[1];
    }
    __builtin_amdgcn_sched_barrier(0);
    { const f32x2 p = s16(mx[0], mx[1]), q = s16(mx[2], mx[3]);
#pragma unroll
      for (int m = 3; m >= 0; --m) lg[m][0] = lg[m][0] * rk[m];
      const f32x2 r = s32(p[0] + p[1], q[0] + q[1]);
#pragma unroll
      for (int m = 3; m >= 0; --m) lg[m][1] = lg[m][1] * rk[m + 1];
      spread(__builtin_amdgcn_rcpf(r[0] + r[1])); }
    __builtin_amdgcn_sched_barrier(0);
    auto half = [](u32x4 v, int h) { return __builtin_bit_cast(s16x4, (u32x2){v[2 * h], v[2 * h + 1]}); };
#pragma unroll
    for (int m = 3; m >= 0; --m) {
      s16x4 pf[2];
#pragma unroll
      for (int h = 0; h < 2; ++h) { const f32x2 t = lg[m][h] * mx[m]; const unsigned u = pk_bf16(t[0], t[1]); pf[h] = __builtin_bit_cast(s16x4, (u32x2){__builtin_amdgcn_perm(0u, u, pl), __builtin_amdgcn_perm(0u, u, ph)}); }
#pragma unroll
      for (int f = 0; f < 2; ++f) sc[m][f] = __builtin_amdgcn_mfma_f32_16x16x16bf16_1k(half(V[m], f), pf[0], z4, 0, 0, 0);
#pragma unroll
      for (int f = 0; f < 2; ++f) sc[m][f] = __builtin_amdgcn_mfma_f32_16x16x16bf16_1k(half(V[m + 1], f), pf[1], sc[m][f], 0, 0, 0);
    }
    __builtin_amdgcn_sched_barrier(0);
    STAMP(10);
    ES(6);
    const auto rO = __builtin_amdgcn_make_buffer_rsrc((void*)a.O, 0, 0x7fffffff, 0x00020000);
    u32x4 ov[4];
#pragma unroll
    for (int m = 3; m >= 0; --m) {
      ov[m] = (u32x4){pk_bf16(sc[m][0][0], sc[m][0][1]), pk_bf16(sc[m][0][2], sc[m][0][3]), pk_bf16(sc[m][1][0], sc[m][1][1]), pk_bf16(sc[m][1][2], sc[m][1][3])};
      __builtin_amdgcn_raw_buffer_store_b128(ov[m], rO, odst, m * RSH, 16);
    }
    asm volatile("" :: "v"(ov[0]), "v"(ov[1]), "v"(ov[2]), "v"(ov[3]));
    if (KN_QP) __builtin_amdgcn_s_setprio(0);
    STAMP(7);
    ES(7);
    wait_vm<0>();
    STAMP(8);
    ES(8);
    raw_barrier();
    ES(9);
  }
  STAMP(3);
#endif
  return tag;
}

struct FragW { s16x4 lo[4], hi[4]; };
__device__ __forceinline__ void wo_phase(const GemmArgs& a, const GemmArgs& nx, unsigned tag) {
#if defined(__HIP_DEVICE_COMPILE__)
  extern __shared__ __attribute__((aligned(16))) char smem[];
  STAMP(0);
  const unsigned lds0 = (unsigned)(size_t)(lds_char_ptr)smem;
  const int tid = threadIdx.x, lane = tid & 63;
  const int wave = __builtin_amdgcn_readfirstlane(tid >> 6);
  const int bid = blockIdx.x, xcd = bid & 7, idx = bid >> 3;
  const int tm = (xcd >> 1) * 4 + (idx >> 3), tn = (xcd & 1) * 8 + (idx & 7);
  const int m0 = tm * 128, panel = m0 >> 8, rot = (xcd & 3) * 4, sd = SIDE;
  {
    constexpr int PAD = QTCH;
    const int g = wave >> 2, rw = wave & 3;
    const int fr = lane & 15, fq = lane >> 4, wq = (lane >> 2) & 3, wp = lane & 3;
    unsigned wa[4];
#pragma unroll
    for (int c = 0; c < 4; ++c) { const int row = 4 * fq + wq; wa[c] = lds0 + g * 8192 + row * 128 + (((c << 1) ^ (row & 6)) << 4) + wp * 8; }
    FragW f[3];
    auto rd = [&](auto ic, auto rc) {
      constexpr int i = decltype(ic)::value, r = decltype(rc)::value, P = wo_pos(i >> 1) + (i & 1) * 4096, B = P & ~32767, o = P & 32767;
      if constexpr (r & 1) f[i % 3].hi[r >> 1] = ds_tr<o + 2048>(wa[r >> 1] + B); else f[i % 3].lo[r >> 1] = ds_tr<o>(wa[r >> 1] + B);
    };
    auto pin = [&](FragW& q) {
#pragma unroll
      for (int n = 0; n < 4; ++n) asm volatile("" : "+v"(q.lo[n]), "+v"(q.hi[n]));
    };
    static_for<0, 16>([&](auto rc) { constexpr int r = decltype(rc)::value; rd(std::integral_constant<int, r / 8>{}, std::integral_constant<int, r % 8>{}); });
    if constexpr (KN_CT & 4) { if (wave == 2) {
      unsigned long long c0, c1; CODE_ADDR(c0, "k_all"); CODE_ADDR(c1, "k_zend");
      wait_vm<0>();
      __builtin_amdgcn_raw_ptr_buffer_load_lds(__builtin_amdgcn_make_buffer_rsrc((void*)c0, 0, c1 - c0 < 262144ull ? (unsigned)(c1 - c0) : 0u, 0x00020000), (lds_void_ptr)(size_t)(lds0 + PAD), 4, (unsigned)((lane * 32 + idx) * 128), 0, 0, 0); } }
    if (wave == 1) wait_flags(a.sync + FLO(tm), (lane & 15) * 128, tag, 0xffffull);
    if constexpr (KN_E & 2) {
      if (wave == 3) {
        wait_flags(a.sync, (FHM(lane >= 32 ? (xcd >> 2) * 4 + (idx >> 3) : panel) + (lane & 31)) * 4, tag, ~0ull);
      }
    }
    const auto rX = __builtin_amdgcn_make_buffer_rsrc((void*)a.X, 0, 0x7fffffff, 0x00020000);
    const int rb = (m0 >> 4) + rw * 2 + g;
    unsigned xs[2] = {(unsigned)(rbH(rb, sd) + lane * 16), (unsigned)(rbH(rb ^ 1, sd) + lane * 16)};
    const int ck = (fq & 1) * 2 + (fq >> 1);
    unsigned orow = (unsigned)((rb * 16 + fr) * (D_ * 2) + tn * 128 + ck * 16), ofrg = (unsigned)(rbH(rb, sd) + omH(tn * 2048) + ck * 256 + fr * 16);
    asm volatile("" : "+v"(xs[0]), "+v"(xs[1]), "+v"(orow), "+v"(ofrg));
    STAMP(9);
    if constexpr (!KN_B3) { if (!(KN_CT & 4) || wave != 2) wait_vm<0>(); }
    lds_barrier();
    STAMP(6);
    pin(f[0]); pin(f[1]);
    u32x4 xr[16][2], res[2];
#define KN_XD 4
    auto ldX = [&](int i, int m) {
      if (i < 16) xr[i][m] = __builtin_amdgcn_raw_buffer_load_b128(rX, xs[m] + (i & 1) * 1024, omH((i >> 1) * 2048) ^ omH(((g * 8) ^ rot) * 2048), XAUXW);
      else if (i == 16) res[m] = __builtin_amdgcn_raw_buffer_load_b128(__builtin_amdgcn_make_buffer_rsrc((void*)a.R, 0, 0x7fffffff, 0x00020000), orow + m * 64, 0, 0);
    };
#pragma unroll
    for (int i = 0; i < KN_XD; ++i) { ldX(i, 0); ldX(i, 1); __builtin_amdgcn_sched_barrier(0); }
    unsigned gsrc[2];
#pragma unroll
    for (int i = 0; i < 2; ++i) {
      const int j = wave + 8 * i, jj = j >= 7 ? j - 7 : j;
      const int b = jj * 1024 + lane * 16, q = b / 224, pos = (b % 224) >> 4;
      gsrc[i] = (unsigned)((8 * ((q & 15) >> 2) + (q & 3) + 4 * (q >> 4)) * DF_ + ((xcd & 3) * 8 + (idx & 7)) * 112 + pos * 8) * 2u;
      asm volatile("" : "+v"(gsrc[i]));
    }
    f32x4 acc[4][2];
#pragma unroll
    for (int n = 0; n < 4; ++n) { acc[n][0] = (f32x4){0.f, 0.f, 0.f, 0.f}; acc[n][1] = acc[n][0]; }
    __builtin_amdgcn_sched_barrier(0);
    static_for<0, 16>([&](auto ic) {
      constexpr int i = decltype(ic)::value;
      if constexpr (i >= 2) { asm volatile("s_waitcnt lgkmcnt(%0)" :: "n"(i < 15 ? 8 : 0) : "memory"); pin(f[i % 3]); }
      if constexpr (KN_B3 && i == (KN_WB & ~1) - 2) raw_barrier();
      static_for<0, 8>([&](auto jc) {
        constexpr int j = decltype(jc)::value, n = j >> 1, m = j & 1;
        const bf16x8 w = __builtin_shufflevector(f[i % 3].lo[n], f[i % 3].hi[n], 0, 1, 2, 3, 4, 5, 6, 7);
        acc[n][m] = __builtin_amdgcn_mfma_f32_16x16x32_bf16(w, __builtin_bit_cast(bf16x8, xr[i][m]), acc[n][m], 0, 0, 0);
        if constexpr (i + 2 < 16) rd(std::integral_constant<int, i + 2>{}, jc);
        if constexpr (j == 1 || j == 5) ldX(i + KN_XD, j >> 2);
        __builtin_amdgcn_sched_barrier(0);
      });
      if constexpr (i == 0) STAMP(1);
    });
    STAMP(2);
    auto slot = [&](int w) { return 32768 + w * 4096 + lane * 16; };
#pragma unroll
    for (int n = 0; n < 4; ++n) *(f32x4*)(smem + slot(wave) + n * 1024) = acc[n][1];
    asm volatile("" : "+v"(res[0]), "+v"(res[1]));
    lds_barrier();
    STAMP(4);
    f32x4 pa[4];
    static_for<0, 4>([&](auto nc) { constexpr int n = decltype(nc)::value; pa[n] = __builtin_bit_cast(f32x4, ds_b128<n * 1024>(lds0 + slot(wave ^ 4))); });
    if constexpr (KN_GW) {
      auto ldG = [&](int i, int u) { const int j = wave + 8 * i;
        __builtin_amdgcn_raw_ptr_buffer_load_lds(__builtin_amdgcn_make_buffer_rsrc((void*)(j >= 7 ? nx.W1 : nx.W0), 0, 0x7fffffff, 0x00020000),
                          (lds_void_ptr)(size_t)(lds0 + 98304 + (u ^ 2) * 14592 + (j >= 7 ? 7184 + (j - 7) * 1024 : j * 1024)), 16, gsrc[i], (u + (xcd >> 2) * 16) * 32 * DF_ * 2, 0, 0); };
      ldG(0, 0); ldG(0, 1);
      if (wave < 6) { ldG(1, 0); ldG(1, 1); }
    }
    asm volatile("s_waitcnt lgkmcnt(0)" : "+v"(pa[0]), "+v"(pa[1]), "+v"(pa[2]), "+v"(pa[3]) :: "memory");
    const auto rO = __builtin_amdgcn_make_buffer_rsrc((void*)a.O, 0, 0x7fffffff, 0x00020000);
    u32x4 o[2];
#pragma unroll
    for (int i = 0; i < 2; ++i) {
      const f32x4 u = acc[2 * i][0] + pa[2 * i], v = acc[2 * i + 1][0] + pa[2 * i + 1];
      const auto s0 = __builtin_amdgcn_permlane16_swap(pk_bf16(u[0], u[1]), pk_bf16(v[0], v[1]), false, false);
      const auto s1 = __builtin_amdgcn_permlane16_swap(pk_bf16(u[2], u[3]), pk_bf16(v[2], v[3]), false, false);
      const u32x4 q4 = {s0[0], s1[0], s0[1], s1[1]};
#pragma unroll
      for (int j = 0; j < 4; ++j) { const unsigned r = res[i][j], q = q4[j]; o[i][j] = pk_bf16(bflo(r) + bflo(q), bfhi(r) + bfhi(q)); }
      __builtin_amdgcn_raw_buffer_store_b128(o[i], rO, ofrg + i * 1024, 0, 16);
    }
    auto sum4 = [](float v) {
      auto p = __builtin_amdgcn_permlane16_swap(__float_as_uint(v), __float_as_uint(v), false, false); v = __uint_as_float(p[0]) + __uint_as_float(p[1]);
      auto q = __builtin_amdgcn_permlane32_swap(__float_as_uint(v), __float_as_uint(v), false, false); return __uint_as_float(q[0]) + __uint_as_float(q[1]); };
    f32x2 e[8], sm = {0.f, 0.f};
#pragma unroll
    for (int j = 0; j < 8; ++j) { const unsigned q = o[j >> 2][j & 3]; e[j] = (f32x2){bflo(q), bfhi(q)}; sm += e[j]; }
    const float mu = sum4(sm[0] + sm[1]) * (1.f / 64.f);
    f32x2 t2 = {0.f, 0.f};
#pragma unroll
    for (int j = 0; j < 8; ++j) { const f32x2 d = e[j] - mu; t2 += d * d; }
    const float q2 = sum4(t2[0] + t2[1]);
    __builtin_amdgcn_raw_buffer_store_b64((u32x2){__float_as_uint(mu), __float_as_uint(q2)}, __builtin_amdgcn_make_buffer_rsrc((void*)a.st, 0, 0x7fffffff, 0x00020000),
                                          lane < 16 ? (unsigned)((tn * S_ + rb * 16 + lane) * 8) : 0x80000000u, 0, 16);
    STAMP(7);
    STAMP(8);
    asm volatile("s_waitcnt vmcnt(0) lgkmcnt(0)" ::: "memory");
    raw_barrier();
    if (tid == 0) set_flag(a.sync + FHF(panel) + ((tm & 1) * 16 + tn) * 32, tag);
    STAMP(3);
  }
#endif
}

struct FragE { bf16x8 x[4]; s16x4 lo[4], hi[4]; };
template <int MODE, int HTD, int TA, int TB, int TL = 0>
__device__ __forceinline__ void dn_phase(const GemmArgs& a, unsigned tag) {
#if defined(__HIP_DEVICE_COMPILE__)
  constexpr int SX = 5, SW = 4, WB = SX * 16384, PAD = WB + SW * 16384, V = KN_V;
  static_assert(TA == 2 && TB == 2, "schedule");
  extern __shared__ __attribute__((aligned(16))) char smem[];
  if constexpr (MODE != 1) STAMP(0);
  const unsigned lds0 = (unsigned)(size_t)(lds_char_ptr)smem;
  const int tid = threadIdx.x;
  int lane = tid & 63; asm volatile("" : "+v"(lane));
  const int wave = __builtin_amdgcn_readfirstlane(tid >> 6);
  const int bid = blockIdx.x, xcd = bid & 7, idx = bid >> 3;
  const int tm = (xcd >> 1) * 4 + (idx >> 3), tn = (idx & 7) * 2 + (xcd & 1), A = (xcd & 1) ? 24 - KN_D : 24 + KN_D;
  const int m0 = tm * 128, panel = m0 >> 8, sd = SIDE;
  unsigned* pcnt = a.sync + FAE((xcd >> 2) * 4 + (idx >> 3)) + ((xcd & 3) * 8 + (idx & 7)) * 32;
  const unsigned orow = (unsigned)((m0 + wave * 16 + (lane >> 3)) * (D_ * 2) + tn * 128 + (lane & 7) * 16);
  const unsigned ofrg = (unsigned)(rbH((m0 >> 4) + wave, sd) + omH(tn * 2048) + (lane & 7) * 256 + (lane >> 3) * 16);
  u32x4 res[2];
  auto res_loads = [&]() {
    const auto rR = __builtin_amdgcn_make_buffer_rsrc((void*)a.R, 0, 0x7fffffff, 0x00020000);
#pragma unroll
    for (int i = 0; i < 2; ++i) res[i] = __builtin_amdgcn_raw_buffer_load_b128(rR, ofrg, i * 128, 16);
  };
  constexpr int STG = 0;
  if (MODE == 1 || wave < 4) {
    const int lw = wave & 3;
    const int kf = (xcd & 3) * 8;
    auto ktile = [&](int t) { const int l = (xcd & 2) * 6 + (t < A ? t - 16 + (xcd & 1) * (8 + KN_D) : t - A + 16); return t < 16 ? (kf ^ ((t & 8) * 2)) + (t & 7) : (l < 24 ? l + 32 : l + 8); };
    auto wrow = [&](int kt, int j) { const int p = kt * 8 + j, b = p - 256, v1 = p + 3 * (p >> 2), v2 = b + 4 * ((b * 21846) >> 16) + 4, m = b >> 31; return ((v1 & m) | (v2 & ~m)) * 8; };
    const auto rW = __builtin_amdgcn_make_buffer_rsrc((void*)a.W0, 0, 0x7fffffff, 0x00020000);
    const u32x4 qX = mk_rsrc(a.X), qW = mk_rsrc((const char*)a.W0 - 4096);
    const unsigned xv0 = (unsigned)(rbA((m0 >> 4) + lw * 2, sd) + lane * 16), xv1 = xv0 + (RSA - 2048);
    unsigned wv[4];
    { const unsigned w0 = (unsigned)((lane >> 3) * D_ + tn * 64 + ((lane & 7) ^ wswz<128>(lane >> 3)) * 8) * 2u;
#pragma unroll
      for (int i = 0; i < 4; ++i) wv[i] = (w0 ^ (i * 64)) + (4096 - i * 1024); }
    const unsigned mX = lds0 + lw * 4096, mW = mX + WB;
    auto ldXo = [&](int so, int slot) { ld_x4(mX + slot * 16384, qX, xv0, xv1, so); };
    int tbx = 0, tb0 = 0, tb1 = 0;
    auto tables = [&]() { const int kt = ktile(lane < A + 8 ? lane : 0); tbx = omA(kt * 2048); tb0 = wrow(kt, 2 * lw) * (D_ * 2); tb1 = wrow(kt, 2 * lw + 1) * (D_ * 2); asm volatile("" : "+v"(tbx), "+v"(tb0), "+v"(tb1)); };
    int o0, o1, ox;
    auto offs = [&](int t) { o0 = __builtin_amdgcn_readlane(tb0, t + 4); o1 = __builtin_amdgcn_readlane(tb1, t + 4); ox = __builtin_amdgcn_readlane(tbx, t + 4); asm volatile("s_nop 1" : "+s"(o0), "+s"(o1), "+s"(ox)); };
    const unsigned* fcnt = a.sync + FAE(panel);
    const unsigned fof = (lane & 31) * 128 + (lane >> 5) * 4;
    auto ldF = [&]() { __builtin_amdgcn_raw_ptr_buffer_load_lds(__builtin_amdgcn_make_buffer_rsrc((void*)fcnt, 0, 4096, 0x00020000), (lds_void_ptr)(size_t)(lds0 + PAD + 256), 4, fof, 0, 0, 16); };
    int ht; unsigned htv;
    auto hset = [&]() {
      int idh = idx; asm volatile("" : "+s"(idh));
      const int slc = (3 - ((unsigned)(size_t)a.W0 >> 7)) & 7, pr = idh & 3;
      const bool hlp = pr != (slc >> 1) && lw < 3;
      const int htau = KN_WS + lw * 12 + (idh >> 3) * 3 + pr - (pr > (slc >> 1) ? 1 : 0);
      ht = (hlp && htau < (((slc ^ xcd) & 1) ? A : A + 8)) ? htau - HTD : 1000;
      htv = (unsigned)((wrow(ktile(htau < A + 8 ? htau : 0), lane >> 3) + (lane & 7)) * (D_ * 2) + ((idh & 4) * 2 + slc) * 128);
    };
    auto help = [&]() { __builtin_amdgcn_raw_ptr_buffer_load_lds(rW, (lds_void_ptr)(size_t)(lds0 + FPAD - 256), 4, htv, 0, 0, 0); };
    auto ldW0 = [&](int i) { const int so = ((kf + i) * 112 + (lw >> 1) * 56 + (lw & 1) * 16) * (D_ * 2); ld_w4<-1>(mW + i * 16384, qW, wv, so, so + 8 * D_ * 2); };
    if constexpr (MODE == 1) {
      if constexpr (TL == 1) __builtin_amdgcn_raw_ptr_buffer_load_lds(__builtin_amdgcn_make_buffer_rsrc((void*)(a.sync + FAM(panel)), 0, 256, 0x00020000), (lds_void_ptr)(size_t)(lds0 + FPAD), 4, lane * 4, 0, 0, 16);
      if constexpr (TL == 2) { if (lw == 0) { hset(); if (ht < -1) help(); } }
      if constexpr (TL == 0 || TL == 3) { ldW0(TL ? 2 : 0); ldW0(TL ? 3 : 1); }
      return;
    }
#define TS0
#define TS1(T)
    wait_vm<3>();
    lds_inc(lds0 + ACNT);
    STAMPW(4, 0);
    const bool early = !lds_missing(lds0 + FPAD, tag, ((1ull << (4 * V)) - 1) << ((xcd & 1) * 32));
    lds_wait8(lds0 + ACNT);
    STAMPW(9, 0);
    if (tid == 192) set_flag(pcnt, tag);
    asm volatile("" : "+v"(lane) :: "memory");
    hset();
    tables();
    asm volatile("" :: "s"(ht), "v"(htv));
    lds_wait8(lds0 + ACNT + 4, 4);
    STAMPW(5, 0);
    ldXo(omA(kf * 2048), 0); ldXo(omA(kf * 2048) + 2048, 1); ldW0(2);
    static_assert(KN_WS == 2 && V >= 5 && V <= 8, "first tiles");
    unsigned sx = mX + 2 * 16384, sw = 3 * 16384;
    auto next = [&]() { sw = (sw + 16384) & 65535; sx = sx + 16384; sx = sx >= mX + SX * 16384 ? mX : sx; asm volatile("" : "+s"(sw), "+s"(sx)); };
    { unsigned sc = mX + lane * 16;
      const int te = lw == 0 ? V - 4 : 1000;
      if (ht < -1) ht = -1;
      ox = omA(kf * 2048) + 4096;
      wait_vm<8>();
#pragma unroll 1
      for (int t = -1; t < V - 1; ++t) {
        const int oy = ox;
        offs(t);
        wait_vm<12>();
        if (__builtin_expect(ld_x4c(sx, qX, xv0, xv1, oy, sc) != 0 || !early, 0)) {
          for (int tries = early ? 0 : KN_TRY;; ++tries) {
            if (tries >= KN_TRY) wait_flags(fcnt, fof, tag, LO32);
            int so = __builtin_amdgcn_readlane(tbx, t + 1), sm = __builtin_amdgcn_readfirstlane(sc); asm volatile("s_nop 3" : "+s"(so), "+s"(sm));
            ld_x4(sm, qX, xv0, xv1, so);
            wait_vm<0>();
            if (tries >= KN_TRY || !marked<4, 1024>(sc)) break;
          }
        }
        if (t == -1) STAMPW(6, 0);
        if (__builtin_expect(t == te, 0)) { wait_vm<12>(); if (lds_missing(lds0 + PAD + 256, tag, LO32)) wait_flags(fcnt, fof, tag, LO32); }
        ld_w4<63>(mW + sw, qW, wv, o0, o1);
        TS0;
        next();
        if (__builtin_expect(t == te - 2, 0)) { ldF(); asm volatile("" ::: "memory"); }
        if (__builtin_expect(t == ht, 0)) help();
        sc = t == 3 ? sc - 4 * 16384 : sc + 16384;
        TS1(t + 1);
      }
    }
    ld_x4(sx, qX, xv0, xv1, ox);
    sx = sx + 16384; sx = sx >= mX + SX * 16384 ? mX : sx;
    const int tf = lw == 0 ? 9 : 1000;
    offs(V - 1);
    auto round = [&](int t, auto P1c, auto WAITc) {
      constexpr bool P1 = decltype(P1c)::value; constexpr int WAIT = decltype(WAITc)::value;
      if constexpr (P1) { if (__builtin_expect(t == tf + 3, 0)) { wait_vm<WAIT>(); if (lds_missing(lds0 + PAD + 256, tag, HI32)) wait_flags(fcnt, fof, tag, HI32); } }
      if constexpr (P1) ld_w4<WAIT>(mW + sw, qW, wv, o0, o1); else ld_w2<WAIT>(mW + sw, qW, wv, o0, o1);
      TS0;
      __builtin_amdgcn_sched_barrier(0);
      if (__builtin_expect(t == ht, 0)) help();
      if constexpr (P1) { if (__builtin_expect(t == tf, 0)) ldF(); }
      const int oy = ox; const unsigned sy = sx;
      offs(t + 1);
      next();
      TS1(t + 1);
      __builtin_amdgcn_sched_barrier(0);
      ld_x4(sy, qX, xv0, xv1, oy);
    };
    int t = V - 1;
#pragma unroll 1
    for (; t < A - 4; ++t) round(t, std::true_type{}, std::integral_constant<int, 16>{});
    round(t++, std::false_type{}, std::integral_constant<int, 16>{});
    round(t++, std::false_type{}, std::integral_constant<int, 14>{});
#pragma unroll 1
    for (; t < A + 4; ++t) round(t, std::false_type{}, std::integral_constant<int, 12>{});
    wait_vm<12>(); raw_barrier(); TS0; TS1(t + 1);
    res_loads();
    wait_vm<8>(); raw_barrier(); TS0; TS1(t + 2);
    wait_vm<2>(); raw_barrier(); TS0; TS1(t + 3);
    raw_barrier(); TS0; TS1(t + 4);
    asm volatile("" : "+v"(res[0]), "+v"(res[1]));
  } else {
    if constexpr (MODE == 1) return;
    const int fr = lane & 15, fq = lane >> 4, wq = (lane >> 2) & 3, wp = lane & 3, cw = wave & 3, wm = cw >> 1, wn = cw & 1;
#define KN_DP 0
    if (KN_DP) __builtin_amdgcn_s_setprio(KN_DP);
    f32x4 acc[4][4];
#pragma unroll
    for (int n = 0; n < 4; ++n)
#pragma unroll
      for (int m = 0; m < 4; ++m) acc[n][m] = (f32x4){0.f, 0.f, 0.f, 0.f};
    const unsigned xa = lds0 + wm * 8192 + lane * 16;
    unsigned wa[2];
#pragma unroll
    for (int c = 0; c < 2; ++c) { const int row = 8 * fq + wq; wa[c] = lds0 + WB + (row >> 4) * 4096 + (row & 15) * 128 + ((((wn * 2 + c) << 1) ^ wswz<128>(row)) << 4) + wp * 8; }
    struct Ctx { unsigned x, w[2]; };
    auto mk = [&](unsigned xo, unsigned wo) { Ctx c; c.x = xa + xo; c.w[0] = wa[0] + wo; c.w[1] = wa[1] + wo; return c; };
    auto rd = [&](FragE& f, const Ctx& c, auto KSc, auto kc) {
      constexpr int KS = decltype(KSc)::value, k = decltype(kc)::value;
      if constexpr (k < 4) f.x[k] = ds_b128<KS * 1024 + k * 2048>(c.x);
      else { constexpr int n = k - 4, o = KS * 8192 + (n >> 1) * 2048; f.lo[n] = ds_tr<o>(c.w[n & 1]); f.hi[n] = ds_tr<o + 512>(c.w[n & 1]); }
    };
    auto pin = [&](FragE& f, auto Nc) {
#pragma unroll
      for (int m = 0; m < 4; ++m) asm volatile("" : "+v"(f.x[m]));
#pragma unroll
      for (int n = 0; n < decltype(Nc)::value; ++n) asm volatile("" : "+v"(f.lo[n]), "+v"(f.hi[n]));
    };
    using I0 = std::integral_constant<int, 0>; using I1 = std::integral_constant<int, 1>; using I2 = std::integral_constant<int, 2>; using I4 = std::integral_constant<int, 4>;
    FragE fa, fb, fc, fd;
    const auto rS = __builtin_amdgcn_make_buffer_rsrc((void*)a.slab, 0, 0x7fffffff, 0x00020000);
    unsigned sla = rbH(tm * 8 + (tn >> 1), sd) + omH((tn & 1) * 16384 + cw * 4096) + lane * 16, slb = sla ^ omH(16384);
    asm volatile("" : "+v"(sla), "+v"(slb));
    raw_barrier();
    { FragE& f0 = (KN_D & 1) ? fc : fa; FragE& f1 = (KN_D & 1) ? fd : fb;
      const Ctx c = mk(0, 0); static_for<0, 8>([&](auto kc) { rd(f0, c, I0{}, kc); }); asm volatile("s_waitcnt lgkmcnt(4)" ::: "memory"); static_for<0, 8>([&](auto kc) { rd(f1, c, I1{}, kc); });
      asm volatile("s_waitcnt lgkmcnt(0)" ::: "memory");
      pin(f0, I4{}); pin(f1, I4{}); }
    STAMP(1);
    static_assert((SW & (SW - 1)) == 0, "W ring");
    int sx = 16384, sw = 16384;
    Ctx cn = mk(sx, sw);
#define KN_SP4 26
#define KN_IL 1
#define KN_IB 1
    auto rd1 = [&](FragE& f, const Ctx& c, auto KSc, auto rc) {
      constexpr int KS = decltype(KSc)::value, r = decltype(rc)::value;
      if constexpr (r < 4) f.x[r] = ds_b128<KS * 1024 + r * 2048>(c.x);
      else { constexpr int n = (r - 4) >> 1, o = KS * 8192 + (n >> 1) * 2048; if constexpr (r & 1) f.hi[n] = ds_tr<o + 512>(c.w[n & 1]); else f.lo[n] = ds_tr<o>(c.w[n & 1]); }
    };
    auto send = [&](int m) {
      __builtin_amdgcn_raw_buffer_store_b128((u32x4){pk_bf16(acc[2][m][0], acc[2][m][1]), pk_bf16(acc[2][m][2], acc[2][m][3]), pk_bf16(acc[3][m][0], acc[3][m][1]), pk_bf16(acc[3][m][2], acc[3][m][3])},
                                             rS, slb + m * 1024, 0, 16); };
    auto tile = [&](FragE& c0, FragE& c1, FragE& n0, FragE& n1, auto NCc, auto NNc, auto&& post) {
      constexpr int NC = decltype(NCc)::value, NN = decltype(NNc)::value;
      constexpr bool PF = KN_PF && NC == 4 && NN == 2;
      constexpr int R2 = 8 + 4 * NN, NP = 4 * NC;
      const Ctx c = cn;
      static_for<0, 2 * NP>([&](auto ic) {
        constexpr int I = decltype(ic)::value, P = I >> 1, Q = PF && P > 8 ? P - 8 : P;
        constexpr int n = PF && P >= 1 && P <= 8 ? 2 + ((P >> 1) & 1) : Q / 4, m = PF && P >= 1 && P <= 8 ? (P - 1) >> 1 : ((n & 1) ? 3 - Q % 4 : Q % 4);
        constexpr int SP = NC == 4 ? (NN == 4 ? KN_SP4 : 20) : 12;
        constexpr int before = I < 1 ? 0 : ((I - 1) * R2 / SP < R2 ? (I - 1) * R2 / SP : R2), after = I * R2 / SP < R2 ? I * R2 / SP : R2;
        FragE& u = ((P ^ I) & 1) ? c1 : c0;
        acc[n][m] = __builtin_amdgcn_mfma_f32_16x16x32_bf16(__builtin_shufflevector(u.lo[n], u.hi[n], 0, 1, 2, 3, 4, 5, 6, 7), u.x[m], acc[n][m], 0, 0, 0);
        __builtin_amdgcn_sched_barrier(0);
        if constexpr (I == 1) { asm volatile("s_waitcnt lgkmcnt(0)" ::: "memory"); pin(c0, NCc); pin(c1, NCc); raw_barrier(); post(); }
        static_for<before, after>([&](auto rc) { constexpr int r = decltype(rc)::value;
          constexpr int h = r < 6 ? r / 3 : (r < 12 ? r & 1 : (r >> 1) & 1), q = r < 6 ? (r % 3 == 2 ? 0 : 4 + r % 3) : (r < 12 ? (r - 4) >> 1 : 4 + ((r - 8) >> 2) * 2 + (r & 1));
          if constexpr (h) rd1(n1, c, I1{}, std::integral_constant<int, q>{}); else rd1(n0, c, I0{}, std::integral_constant<int, q>{}); });
        if constexpr (PF && I >= 9 && I <= 21 && (I - 9) % 4 == 0) send((I - 9) / 4);
        if constexpr (I == 2 * NP - 4) { sw = (sw + 16384) & (SW * 16384 - 1); asm volatile("" : "+s"(sw)); }
        if constexpr (I == 2 * NP - 3) { cn.w[0] = wa[0] + sw; cn.w[1] = wa[1] + sw; asm volatile("" : "+v"(cn.w[0]), "+v"(cn.w[1])); }
        if constexpr (I == 2 * NP - 5) { sx = sx + 16384; asm volatile("" : "+s"(sx)); }
        if constexpr (I == 2 * NP - 2) { sx = sx == SX * 16384 ? 0 : sx; asm volatile("" : "+s"(sx)); }
        if constexpr (I == 2 * NP - 1) { cn.x = xa + sx; asm volatile("" : "+v"(cn.x)); }
        __builtin_amdgcn_sched_barrier(0);
      });
      asm volatile("s_waitcnt lgkmcnt(%0)" :: "n"(R2 - 6 < 15 ? R2 - 6 : 15) : "memory");
      asm volatile("" : "+v"(n0.x[0]), "+v"(n0.lo[0]), "+v"(n0.hi[0]), "+v"(n1.x[0]), "+v"(n1.lo[0]), "+v"(n1.hi[0]));
    };
    auto nop = [&]() {};
    if constexpr (KN_D & 1) tile(fc, fd, fa, fb, I4{}, I4{}, nop);
#pragma unroll 1
    for (int t = 0; t < A - 3; t += 2) {
      tile(fa, fb, fc, fd, I4{}, I4{}, [&]() { if (t == 2) { if (tid == 256) set_flag(pcnt + 1, tag); } });
      tile(fc, fd, fa, fb, I4{}, I4{}, nop);
      if (t == 0) wait_vm<0>();
    }
    tile(fa, fb, fc, fd, I4{}, I4{}, nop); tile(fc, fd, fa, fb, I4{}, I2{}, nop);
    STAMP(10);
    if (!KN_PF) { send(0); send(1); send(2); send(3); }
    auto pairs = [&](int n) {
#pragma unroll 1
      for (int t = 0; t < n; t += 2) { tile(fa, fb, fc, fd, I2{}, I2{}, nop); tile(fc, fd, fa, fb, I2{}, I2{}, nop); } };
    const unsigned* flag = a.sync + FPR(bid);
    u32x4 pp[4];
    auto ldP = [&]() {
#pragma unroll
      for (int i = 0; i < 4; ++i) pp[i] = __builtin_amdgcn_raw_buffer_load_b128(rS, sla + i * 1024, 0, 16);
    };
    {
    pairs(TA);
    STAMPV(11);
    wait_vm<0>();
    STAMPV(12);
    tile(fa, fb, fc, fd, I2{}, I2{}, [&]() { if (tid == 256) set_flag(a.sync + FPR(bid ^ 1), tag); });
    tile(fc, fd, fa, fb, I2{}, I2{}, nop);
    if constexpr (TB > TA) pairs(TB - TA);
    unsigned fv = 1u;
    if (wave == 4) fv = __hip_atomic_load((gu32)flag, __ATOMIC_RELAXED, __HIP_MEMORY_SCOPE_AGENT);
    tile(fa, fb, fc, fd, I2{}, I2{}, nop);
    tile(fc, fd, fa, fb, I2{}, I2{}, nop);
    if (wave == 4) {
      if (__builtin_amdgcn_readfirstlane(fv) != tag) wait_flags(flag, 0, tag, 1ull);
    }
    STAMPV(4);
    tile(fa, fb, fc, fd, I2{}, I2{}, [&]() {
#pragma unroll
      for (int i = 0; i < 4; ++i) pp[i] = __builtin_amdgcn_raw_buffer_load_b128(rS, sla + i * 1024, 0, 16);
      res_loads();
    });
    tile(fc, fd, fa, fb, I2{}, I2{}, nop);
    }
    asm volatile("s_waitcnt lgkmcnt(0)" ::: "memory");
    pin(fa, I2{}); pin(fb, I2{});
    STAMP(2);
#pragma unroll
    for (int m = 0; m < 4; ++m)
#pragma unroll
      for (int n = 0; n < 2; ++n) {
        const unsigned q0 = pp[m][n * 2], q1 = pp[m][n * 2 + 1];
        const f32x4 v = acc[n][m] + (f32x4){bflo(q0), bfhi(q0), bflo(q1), bfhi(q1)};
        *(u32x2*)(smem + STG + (wm * 64 + m * 16 + fr) * 128 + ((((wn * 2 + n) * 2 + (fq >> 1)) ^ (fr & 7)) << 4) + (fq & 1) * 8) = (u32x2){pk_bf16(v[0], v[1]), pk_bf16(v[2], v[3])};
      }
    STAMPV(5);
    asm volatile("" : "+v"(res[0]), "+v"(res[1]));
  }
  lds_barrier();
  {
    const auto rO = __builtin_amdgcn_make_buffer_rsrc((void*)a.O, 0, 0x7fffffff, 0x00020000);
    u32x4 o[2];
    int lz = lane; asm volatile("" : "+v"(lz));
#pragma unroll
    for (int i = 0; i < 2; ++i) {
      const u32x4 v = *(const u32x4*)(smem + STG + (wave * 16 + i * 8 + (lz >> 3)) * 128 + (((lz & 7) ^ (lz >> 3)) << 4));
#pragma unroll
      for (int j = 0; j < 4; ++j) { const unsigned r = res[i][j], q = v[j]; o[i][j] = pk_bf16(bflo(r) + bflo(q), bfhi(r) + bfhi(q)); }
    }
    asm volatile("" : "+v"(o[0]), "+v"(o[1]));
#pragma unroll
#define KN_OAUX 16
    for (int i = 0; i < 2; ++i) __builtin_amdgcn_raw_buffer_store_b128(o[i], rO, orow, i * 8 * D_ * 2, KN_OAUX);
    if (bid == 0 && tid == 0) set_flag(a.sync + FEP, tag);
  }
  STAMP(7);
  STAMP(8);
  STAMP(3);
#endif
}

struct Ptrs { const bf16_t *x, *wqkv, *wo, *wg, *wu, *wd; bf16_t* out; char* ws; };
#define DBG(i)
#define WS_O ((bf16_t*)(p.ws + (8u << 20)))
#define WS_SL (KN_DM == 1 ? (float*)(p.ws + (12u << 20)) : (float*)WS_O)
#define WS_H ((bf16_t*)(p.ws + (16u << 20)))
#define WS_ST ((float*)(p.ws + (24u << 20)))
#define WS_CS ((float*)(p.ws + (25u << 20)))
#define WS_SY ((unsigned*)(p.ws + (26u << 20)))
#define WS_A ((bf16_t*)(p.ws + (28u << 20)))
#if defined(__HIP_DEVICE_COMPILE__) && !defined(KN_NOWARM)
#define ARG_WARM(v) do { asm volatile("s_load_dword %0, %1, 0x0" : "=s"(v) : "s"(__builtin_amdgcn_kernarg_segment_ptr())); __builtin_amdgcn_sched_barrier(0); } while (0)
#else
#define ARG_WARM(v)
#endif
struct Args4 { GemmArgs q, b, c, d; };
extern "C" __global__ __launch_bounds__(512, 1) void k_all(Ptrs p) {
  unsigned warm = 0;
  ARG_WARM(warm);
  p.ws += -(unsigned)(size_t)p.ws & 0x1fffffu;
  const Args4 a{GemmArgs{p.x, p.wqkv, nullptr, nullptr, WS_O, (float*)WS_H, p.wg, p.wu, WS_CS, p.wd, p.wo, WS_SY, (float*)WS_A, warm DBG(0)},
    GemmArgs{WS_O, p.wo, nullptr, p.x, WS_H, WS_ST, nullptr, nullptr, nullptr, p.wd, nullptr, WS_SY, nullptr, 0u DBG(1)},
    GemmArgs{WS_H, p.wg, p.wu, p.out, WS_A, WS_ST, nullptr, nullptr, WS_CS, p.wd, nullptr, WS_SY, WS_SL, 0u DBG(2)},
    GemmArgs{WS_A, p.wd, nullptr, WS_H, p.out, nullptr, nullptr, nullptr, nullptr, nullptr, nullptr, WS_SY, WS_SL, 0u DBG(3)}};
#define KN_WDX 4
#define KN_WHT 8
#define KN_DDX 4
#define KN_DDW 8
#define KN_DPW 9
#define KN_DHT 14
#define KN_WPW 5
  const unsigned tag = k1_phase(a.q);
  wo_phase(a.b, a.c, tag);
#define KN_A 24
#define KN_EX 4
#define KN_EW 4
#define KN_EH 10
#define KN_TA 2
#define KN_TB 2
  auto pre = [&](auto tc) { dn_phase<1, KN_EH, KN_TA, KN_TB, decltype(tc)::value>(a.d, tag); };
  gemm_phase<EPI_GLU, 256, 28, 4, 2, 1024, 3584, 8, 32, 4, 8, 32, 3, 4, 1>(a.c, tag, pre);
  dn_phase<2, KN_EH, KN_TA, KN_TB>(a.d, tag);
}

extern "C" __global__ void k_zend() {}

extern "C" void kernel_launch(void* const* d_in, const int* in_sizes, int n_in, void* d_out, int out_size, void* d_ws,
                              size_t ws_size, hipStream_t stream) {
  const Ptrs p{(const bf16_t*)d_in[0], (const bf16_t*)d_in[1], (const bf16_t*)d_in[2], (const bf16_t*)d_in[4], (const bf16_t*)d_in[5], (const bf16_t*)d_in[6], (bf16_t*)d_out, (char*)d_ws};
  constexpr int L_all = SNAP + 64 + 512;
  static_assert(SNAP >= 3 * 256 * 128 + 4 * 14592 + 4096 + 1024 + 256 + 512 + 128 + 64 && L_all <= 163840, "LDS");
  (void)hipFuncSetAttribute((const void*)k_all, hipFuncAttributeMaxDynamicSharedMemorySize, L_all);
  k_all<<<256, 512, L_all, stream>>>(p);
}
```

```cpp
#include <hip/hip_runtime.h>
#include <hip/hip_bf16.h>
#include <stdint.h>
#include <type_traits>

typedef unsigned short bf16_t;
typedef short bf16x8 __attribute__((ext_vector_type(8)));
typedef short s16x4 __attribute__((ext_vector_type(4)));
typedef float f32x4 __attribute__((ext_vector_type(4)));
typedef float f32x2 __attribute__((ext_vector_type(2)));
typedef unsigned u32x2 __attribute__((ext_vector_type(2)));
typedef unsigned u32x4 __attribute__((ext_vector_type(4)));
typedef __bf16 bf16x2_t __attribute__((ext_vector_type(2)));
typedef __attribute__((address_space(3))) void* lds_void_ptr;
typedef __attribute__((address_space(3))) char* lds_char_ptr;

constexpr int S_ = 2048, D_ = 1024, DF_ = 3584;
#define KN_HTD 11
#define KN_CT 7
#define KN_HQ 5
#define KN_HK 2
#define KN_KT 4
#define KN_SPLIT 1
#define KN_PO 0
#define KN_PH 0
#define KN_PA 0
#define KN_NUMA 3
constexpr int RSH = (KN_NUMA & 1) ? 65536 : 32768, RSA = (KN_NUMA & 2) ? 262144 : 114688;
__device__ __forceinline__ int rbH(int rb, int sd) { return (KN_NUMA & 1) ? (rb & 63) * RSH + (((sd ^ (rb >> 5)) & 1) << 13) : rb * RSH; }
__device__ __forceinline__ int rbA(int rb, int sd) { return (KN_NUMA & 2) ? (rb & 63) * RSA + (((sd ^ (rb >> 3)) & 1) << 13) : rb * RSA; }
__device__ __forceinline__ constexpr int omH(int o) { return (KN_NUMA & 1) ? o + (o & ~8191) : o; }
__device__ __forceinline__ constexpr int omA(int o) { return (KN_NUMA & 2) ? o + (o & ~8191) : o; }

__device__ __forceinline__ float bflo(unsigned v) { return __uint_as_float(v << 16); }
__device__ __forceinline__ float bfhi(unsigned v) { return __uint_as_float(v & 0xffff0000u); }
__device__ __forceinline__ unsigned pk_bf16(float lo, float hi) {
  f32x2 v = {lo, hi};
  bf16x2_t b = __builtin_convertvector(v, bf16x2_t);
  return __builtin_bit_cast(unsigned, b);
}
__device__ __forceinline__ float rbf(float v) { return bflo(pk_bf16(v, v)); }

template <int STRIDE> __device__ __forceinline__ int wswz(int row) {
  if constexpr (STRIDE % 256 == 0) return ((row & 3) | (((row >> 3) & 1) << 2)) << 1;
  else if constexpr (STRIDE % 256 == 128) return (((row >> 1) & 1) | (((row >> 3) & 1) << 1)) << 1;
  else return ((row >> 3) & 1) << 1;
}
template <int STRIDE> constexpr int wcls() { return STRIDE % 256 == 0 ? 8 : (STRIDE % 256 == 128 ? 4 : 2); }

template <int OFF> __device__ __forceinline__ s16x4 ds_tr(unsigned addr) {
  s16x4 r; asm volatile("ds_read_b64_tr_b16 %0, %1 offset:%2" : "=v"(r) : "v"(addr), "n"(OFF)); return r;
}
template <int OFF> __device__ __forceinline__ bf16x8 ds_b128(unsigned addr) {
  bf16x8 r; asm volatile("ds_read_b128 %0, %1 offset:%2" : "=v"(r) : "v"(addr), "n"(OFF)); return r;
}
template <int OFF> __device__ __forceinline__ f32x2 ds_b64(unsigned addr) {
  f32x2 r; asm volatile("ds_read_b64 %0, %1 offset:%2" : "=v"(r) : "v"(addr), "n"(OFF)); return r;
}
__device__ __forceinline__ float sum8(float v) {
  v += __int_as_float(__builtin_amdgcn_update_dpp(0, __float_as_int(v), 0x128, 0xf, 0xf, false));
  auto a = __builtin_amdgcn_permlane16_swap(__float_as_uint(v), __float_as_uint(v), false, false);
  v = __uint_as_float(a[0]) + __uint_as_float(a[1]);
  auto b = __builtin_amdgcn_permlane32_swap(__float_as_uint(v), __float_as_uint(v), false, false);
  return __uint_as_float(b[0]) + __uint_as_float(b[1]);
}
__device__ __forceinline__ float sum8l(float v) {
  v += __int_as_float(__builtin_amdgcn_update_dpp(0, __float_as_int(v), 0xb1, 0xf, 0xf, false));
  v += __int_as_float(__builtin_amdgcn_update_dpp(0, __float_as_int(v), 0x4e, 0xf, 0xf, false));
  return v + __int_as_float(__builtin_amdgcn_update_dpp(0, __float_as_int(v), 0x141, 0xf, 0xf, false));
}
__device__ __forceinline__ int ds_r32(unsigned addr) { int r; asm volatile("ds_read_b32 %0, %1" : "=v"(r) : "v"(addr)); return r; }
__device__ __forceinline__ void ds_w32(unsigned addr, int v) { asm volatile("ds_write_b32 %0, %1" :: "v"(addr), "v"(v) : "memory"); }
template <int N> __device__ __forceinline__ void wait_vm() { asm volatile("s_waitcnt vmcnt(%0)" ::"n"(N) : "memory"); }
__device__ __forceinline__ void raw_barrier() { asm volatile("" ::: "memory"); __builtin_amdgcn_s_barrier(); asm volatile("" ::: "memory"); }
__device__ __forceinline__ void loop_barrier() { raw_barrier(); }
__device__ __forceinline__ void lds_barrier() { asm volatile("s_waitcnt lgkmcnt(0)" ::: "memory"); __builtin_amdgcn_s_barrier(); asm volatile("" ::: "memory"); }

__device__ __forceinline__ u32x4 mk_rsrc(const void* p) { const unsigned long long v = (unsigned long long)p; return (u32x4){(unsigned)v, (unsigned)(v >> 32) & 0xffffu, 0x7fffffffu, 0x00020000u}; }
#define LD1K(v, so, off, sc) "buffer_load_dwordx4 " v ", %1, " so " offen offset:" off sc " lds\n\t"
template <int WAIT> __device__ __forceinline__ void ld_w4(unsigned m, u32x4 r, const unsigned (&v)[4], unsigned s0, unsigned s1) {
  if constexpr (WAIT >= 0) asm volatile("s_mov_b32 m0, %0\n\ts_waitcnt vmcnt(%8)\n\ts_barrier\n\t" LD1K("%2", "%6", "0", "") LD1K("%3", "%7", "1024", "") LD1K("%4", "%6", "2048", "") LD1K("%5", "%7", "3072", "")
                                        :: "s"(m), "s"(r), "v"(v[0]), "v"(v[1]), "v"(v[2]), "v"(v[3]), "s"(s0), "s"(s1), "n"(WAIT) : "memory", "m0");
  else asm volatile("s_mov_b32 m0, %0\n\ts_nop 0\n\t" LD1K("%2", "%6", "0", "") LD1K("%3", "%7", "1024", "") LD1K("%4", "%6", "2048", "") LD1K("%5", "%7", "3072", "")
                    :: "s"(m), "s"(r), "v"(v[0]), "v"(v[1]), "v"(v[2]), "v"(v[3]), "s"(s0), "s"(s1) : "memory", "m0");
}
template <int WAIT> __device__ __forceinline__ void ld_w2(unsigned m, u32x4 r, const unsigned (&v)[4], unsigned s0, unsigned s1) {
  if constexpr (WAIT >= 0) asm volatile("s_mov_b32 m0, %0\n\ts_waitcnt vmcnt(%6)\n\ts_barrier\n\t" LD1K("%2", "%4", "0", "") LD1K("%3", "%5", "1024", "")
                                        :: "s"(m), "s"(r), "v"(v[0]), "v"(v[1]), "s"(s0), "s"(s1), "n"(WAIT) : "memory", "m0");
  else asm volatile("s_mov_b32 m0, %0\n\ts_nop 0\n\t" LD1K("%2", "%4", "0", "") LD1K("%3", "%5", "1024", "") :: "s"(m), "s"(r), "v"(v[0]), "v"(v[1]), "s"(s0), "s"(s1) : "memory", "m0");
}
__device__ __forceinline__ void ld_x4(unsigned m, u32x4 r, unsigned v0, unsigned v1, unsigned so) {
  asm volatile("s_mov_b32 m0, %0\n\ts_nop 0\n\t" LD1K("%2", "%4", "0", " sc1") LD1K("%2", "%4", "1024", " sc1") LD1K("%3", "%4", "2048", " sc1") LD1K("%3", "%4", "3072", " sc1")
               :: "s"(m), "s"(r), "v"(v0), "v"(v1), "s"(so) : "memory", "m0");
}
__device__ __forceinline__ unsigned long long ld_x4c(unsigned m, u32x4 r, unsigned v0, unsigned v1, unsigned so, unsigned ca) {
  u32x4 a, b, c, d; unsigned long long f;
  asm volatile("ds_read_b128 %1, %10\n\tds_read_b128 %2, %10 offset:1024\n\tds_read_b128 %3, %10 offset:2048\n\tds_read_b128 %4, %10 offset:3072\n\ts_mov_b32 m0, %5\n\ts_nop 0\n\t"
               "buffer_load_dwordx4 %7, %6, %9 offen sc1 lds\n\tbuffer_load_dwordx4 %7, %6, %9 offen offset:1024 sc1 lds\n\ts_waitcnt lgkmcnt(0)"
               : "=s"(f), "=&v"(a), "=&v"(b), "=&v"(c), "=&v"(d) : "s"(m), "s"(r), "v"(v0), "v"(v1), "s"(so), "v"(ca) : "memory", "m0");
  unsigned x = max(max(a[0], a[1]), a[2]), y = max(max(b[0], b[1]), b[2]); x = max(max(x, a[3]), c[0]); y = max(max(y, b[3]), d[0]);
  asm volatile("s_mov_b32 m0, %2\n\ts_nop 0\n\tbuffer_load_dwordx4 %4, %3, %5 offen offset:2048 sc1 lds" : "+v"(x), "+v"(y) : "s"(m), "s"(r), "v"(v1), "s"(so) : "memory", "m0");
  x = max(max(x, c[1]), c[2]); y = max(max(y, d[1]), d[2]); x = max(max(x, c[3]), d[3]);
  f = __builtin_amdgcn_ballot_w64(max(x, y) == 0xffffffffu);
  asm volatile("s_mov_b32 m0, %1\n\ts_nop 0\n\tbuffer_load_dwordx4 %3, %2, %4 offen offset:3072 sc1 lds" : "+s"(f) : "s"(m), "s"(r), "v"(v1), "s"(so) : "memory", "m0");
  return f;
}
template <int LO, int SO, bool SUB, bool SC = false> __device__ __forceinline__ void ld_ca(unsigned mb, u32x4 r, unsigned v, unsigned sd) {
  unsigned t;
  if constexpr (SUB) asm volatile("s_add_i32 m0, %1, %4\n\ts_sub_i32 %0, %5, %6\n\tbuffer_load_dwordx4 %3, %2, %0 offen lds" : "=&s"(t) : "s"(mb), "s"(r), "v"(v), "n"(LO), "n"(SO), "s"(sd) : "memory", "m0", "scc");
  else if constexpr (SC) asm volatile("s_add_i32 m0, %1, %4\n\ts_add_i32 %0, %6, %5\n\tbuffer_load_dwordx4 %3, %2, %0 offen sc1 lds" : "=&s"(t) : "s"(mb), "s"(r), "v"(v), "n"(LO), "n"(SO), "s"(sd) : "memory", "m0", "scc");
  else asm volatile("s_add_i32 m0, %1, %4\n\ts_add_i32 %0, %6, %5\n\tbuffer_load_dwordx4 %3, %2, %0 offen lds" : "=&s"(t) : "s"(mb), "s"(r), "v"(v), "n"(LO), "n"(SO), "s"(sd) : "memory", "m0", "scc");
}
template <int LO, int SO> __device__ __forceinline__ void ld_cx(unsigned mb, u32x4 r, unsigned v, unsigned sx) {
  unsigned t;
  asm volatile("s_add_i32 m0, %1, %4\n\ts_xor_b32 %0, %6, %5\n\tbuffer_load_dwordx4 %3, %2, %0 offen lds" : "=&s"(t) : "s"(mb), "s"(r), "v"(v), "n"(LO), "n"(SO), "s"(sx) : "memory", "m0", "scc");
}
template <int I, int N, class F> __device__ __forceinline__ void static_for(F&& f) {
  if constexpr (I < N) { f(std::integral_constant<int, I>{}); static_for<I + 1, N>(f); }
}
#define CODE_ADDR(v, sym) asm volatile("s_getpc_b64 s[40:41]\n\ts_add_u32 s40, s40, " sym "@rel32@lo+4\n\ts_addc_u32 s41, s41, " sym "@rel32@hi+12\n\ts_mov_b64 %0, s[40:41]" : "=s"(v) :: "s40", "s41", "scc")
enum { EPI_QKV = 0, EPI_WO = 1, EPI_GLU = 2, EPI_DN = 3 };
#define MERGED 1
constexpr int SNAP = 162752;
#define KN_P 1
#define KN_S1 1
#define KN_S1C 8
#define KN_S1P 3
constexpr int STAB = 786432, STL = 3584;
#define FHM(p) ((p) * 256)
#define FRC(p) ((p) * 256 + 64)
#define FAM(p) ((p) * 256 + 128)
#define FCS(t) (2048 + (t) * 32)
#define FLN(k, i) ((1 + 2 * (k) + (((i) >> 6) & 1)) * 4096 + ((~sd & 1) << 11) + ((i) & 63) * 32)
#define FLO(t) FLN(0, (t) * 16)
#define FHF(p) FLN(1, (p) * 32)
#define FAE(p) FLN(2, (p) * 32)
#define FPR(b) FLN(3, ((b) & 3) * 32 + ((b) >> 3))
#define SIDE (((xcd >> 2) ^ tag) & 1)
constexpr int VTAB = STAB + 32768, PRB = 1048576;
constexpr int FEP = 3072;
constexpr unsigned long long LO32 = 0xffffffffull, HI32 = LO32 << 32;
#define KN_E 6
#define KN_MK 2
#define KN_GW 1
#define KN_V 5
#define KN_D 2
#define KN_PF 0
#define KN_TRY 6
#define KN_DM 0
#define KN_LX 0
#define KN_TP 5
typedef __attribute__((address_space(1))) unsigned* gu32;
__device__ __forceinline__ void set_flag(unsigned* p, unsigned tag) { __hip_atomic_store((gu32)p, tag, __ATOMIC_RELAXED, __HIP_MEMORY_SCOPE_AGENT); }
__device__ __forceinline__ bool missing(unsigned v, unsigned tag, unsigned long long mask) { return (__builtin_amdgcn_ballot_w64(v != tag) & mask) != 0; }
__device__ __forceinline__ void wait_flags(const unsigned* p, unsigned off, unsigned tag, unsigned long long mask) {
  unsigned v, polls = 0;
  do { asm volatile("s_nop 4\n\tglobal_load_dword %0, %1, %2 sc1\n\ts_waitcnt vmcnt(0)" : "=v"(v) : "v"(off), "s"(p) : "memory"); } while (missing(v, tag, mask) && ++polls < 4000000u);
}
__device__ __forceinline__ unsigned lane4() { unsigned l = __builtin_amdgcn_mbcnt_hi(~0u, __builtin_amdgcn_mbcnt_lo(~0u, 0u)) * 4u; asm volatile("" : "+v"(l)); return l; }
__device__ __forceinline__ bool lds_missing(unsigned addr, unsigned tag, unsigned long long mask) {
  int v = ds_r32(addr + lane4());
  asm volatile("s_waitcnt lgkmcnt(0)" : "+v"(v) :: "memory");
  return missing((unsigned)v, tag, mask);
}
constexpr int ACNT = SNAP - 60;
__device__ __forceinline__ void lds_inc(unsigned addr) { if (__builtin_amdgcn_mbcnt_hi(~0u, __builtin_amdgcn_mbcnt_lo(~0u, 0u)) == 0) asm volatile("ds_add_u32 %0, %1" :: "v"(addr), "v"(1) : "memory"); }
constexpr int FPAD = SNAP - 704;
__device__ __forceinline__ void lds_wait8(unsigned addr, int n = 8) {
  int dv; unsigned polls = 0;
  do { dv = ds_r32(addr); asm volatile("s_waitcnt lgkmcnt(0)" : "+v"(dv) :: "memory"); } while (__builtin_amdgcn_readfirstlane(dv) < n && ++polls < 40000000u);
}
template <int N> __device__ __forceinline__ bool marked_in(bf16x8 (&v)[N]) {
  asm volatile("s_waitcnt lgkmcnt(0)" ::: "memory");
  unsigned m = 0;
#pragma unroll
  for (int i = 0; i < N; ++i) {
    asm volatile("" : "+v"(v[i]));
    const u32x4 u = __builtin_bit_cast(u32x4, v[i]);
    m = max(max(m, u[0]), u[1]); m = max(max(m, u[2]), u[3]);
  }
  return __builtin_amdgcn_ballot_w64(m == 0xffffffffu) != 0;
}
template <int N, int STR = 4096> __device__ __forceinline__ bool marked(unsigned addr) {
  bf16x8 v[N];
  static_for<0, N>([&](auto ic) { constexpr int i = decltype(ic)::value; v[i] = ds_b128<i * STR>(addr); });
  return marked_in<N>(v);
}

struct GemmArgs {
  const bf16_t* X;
  const bf16_t* W0;
  const bf16_t* W1;
  const bf16_t* R;
  bf16_t* O;
  float* st;
  const bf16_t* CW0;
  const bf16_t* CW1;
  float* cs;
  const bf16_t* PF0;
  const bf16_t* PF1;
  unsigned* sync;
  float* slab;
  unsigned warm;
};
#define STAMP(i)
#define STAMPW(i, w)

#define STAMPV(i) STAMP(i)
#define XAUXD 16
#define XAUXW 16
#define XAUX 16
template <int MF, int NF> struct Frags { bf16x8 x[MF]; s16x4 lo[NF], hi[NF]; };

#define KN_WL 2
#define KN_WA KN_WL
#define KN_WB 2
#define KN_B3 0
constexpr int WOB = 55296;
constexpr int wo_pos(int s) { return s < 6 ? WOB + s * 16384 : (s - 6) * 16384; }
#define KN_GP 15
#define KN_GX 1
#define KN_GL 0
#define KN_GR 24
#define KN_GE 26
template <int NF, int GR> struct SchedP {
  static constexpr int NP = 4 * NF, NR = NP + 8;
  static constexpr int pa(int p) { return (p + NP - 12) % NP; }
  static constexpr int pn(int a) { return a < 8 ? (a ^ (a >> 1)) & 1 : (a < NP - 8 ? a / 4 : NF - 2 + ((a ^ (a >> 1)) & 1)); }
  static constexpr int pm(int a) { return a < 8 ? a >> 1 : (a < NP - 8 ? ((a & 4) ? a & 3 : 3 - (a & 3)) : (a - NP + 8) >> 1); }
  static constexpr int gap(int r) { return r < 8 ? 2 + r : (r < 12 ? 12 + 4 * (r - 8) : (r < NP ? 26 + (r - 12) * GR / (NP - 12) : 11 + 2 * ((r - NP) & ~1) - (r & 1))); }
  static constexpr int need(int r) { return r < 8 ? 12 : (r < NP ? (12 + (r & ~3) < NP ? 12 + (r & ~3) : NP) : 12 + ((r - NP) & ~1)); }
  static constexpr int cnt(int p) {
    int g = -1, q = -1;
    for (int r = 0; r < NR; ++r) if (need(r) == p && (gap(r) > g || (gap(r) == g && r > q))) { g = gap(r); q = r; }
    if (g < 0) return -1;
    int c = 0;
    for (int r = 0; r < NR; ++r) if (gap(r) < 2 * p && (gap(r) > g || (gap(r) == g && r > q))) ++c;
    return c < 15 ? c : 15;
  }
  static constexpr bool last(int a) { for (int b = a + 1; b < NP; ++b) if (pn(b) == pn(a)) return false; return true; }
};
using SchedG = SchedP<7, KN_GR>;
struct NoHook { template <class A> __device__ __forceinline__ void operator()(A) const {} };
template <int EPI, int BM, int NCH, int WM, int WN, int K, int LDW, int TM, int TN, int XA, int XB, int KU, int SX, int SW, int SPK, class HK = NoHook>
__device__ __forceinline__ void gemm_phase(const GemmArgs& a, unsigned tag, HK hook = HK{}) {
#if defined(__HIP_DEVICE_COMPILE__)
  constexpr bool SEAM_IN = EPI == EPI_GLU || EPI == EPI_DN, SEAM_OUT = EPI != EPI_DN;
  constexpr bool HALO = EPI == EPI_QKV;
  constexpr bool FX = EPI != EPI_QKV;
  constexpr int HR = HALO ? 16 : 0, XROWS = BM + HR;
  constexpr bool GLU = EPI == EPI_GLU;
  constexpr int STRIDE = GLU ? 224 : (HALO ? 128 : NCH * 16);
  constexpr int SEGB = GLU ? 16 : 32;
  constexpr int UPOFF = KU * 224 + 16;
  constexpr int XT = XROWS * 128, WU = GLU ? (UPOFF + KU * 224 + 255) / 256 * 256 : KU * NCH * 16;
  constexpr int XBASE = 0, WBASE = SX * XT, RING = WBASE + SW * WU;
  constexpr int MF = BM / 16 / WM, NF = NCH / 2 / WN;
  constexpr int MFX = MF + (HALO ? 1 : 0), NFX = NF;
  constexpr int NT = K / SPK / 64, NU = K / SPK / KU;
  constexpr int C = GLU ? 2 : (HALO ? 1 : (wcls<STRIDE>() < NF ? wcls<STRIDE>() : NF));
  constexpr int NXI = (XROWS / 8) * KU / 64;
  constexpr int NWI = KU * NCH * 16 / 1024;
#define KN_NLW 4
  constexpr int NLW = KN_NLW;
  constexpr int XI = (NXI + NLW - 1) / NLW, XIMIN = NXI / NLW, WI = 7;
  extern __shared__ __attribute__((aligned(16))) char smem[];
  STAMP(0);
  const unsigned lds0 = (unsigned)(size_t)(lds_char_ptr)smem;
  const int tid = threadIdx.x, lane = tid & 63;
  const int wave = __builtin_amdgcn_readfirstlane(tid >> 6);
  const int wm = wave / WN, wn = wave % WN;
  const int bid = blockIdx.x, xcd = bid & 7, kh = SPK > 1 ? ((bid >> 3) % SPK) : 0, idx = (bid >> 3) / SPK;
  constexpr int XN = TN / XB;
#define KN_ROT 0
  const int tm = (xcd / XN) * XA + idx / XB, tn = (xcd % XN) * XB + (idx + (EPI == EPI_DN ? KN_ROT : 0)) % XB;
  const int m0 = tm * BM, sd = SIDE;
  const int krot = (tm >> 2) * 8;
  const int fr = lane & 15, fq = lane >> 4;
  const int wq = (lane >> 2) & 3, wp = lane & 3;

  const int panel = m0 >> 8;
  constexpr int TCH = RING + (EPI == EPI_QKV ? 2304 : (EPI == EPI_GLU ? 5120 : 0));
  constexpr int DONE = TCH + 896;
  auto done_inc = [&]() { if (lane == 0) asm volatile("ds_add_u32 %0, %1" :: "v"(lds0 + DONE), "v"(1) : "memory"); };
  auto touch = [&](const void* base, unsigned voff, unsigned soff, unsigned lim = 0x7fffffffu) {
    __builtin_amdgcn_raw_ptr_buffer_load_lds(__builtin_amdgcn_make_buffer_rsrc((void*)base, 0, lim, 0x00020000), (lds_void_ptr)(size_t)(lds0 + TCH), 4, voff, soff, 0, 0);
  };
  const auto rO = __builtin_amdgcn_make_buffer_rsrc((void*)a.O, 0, 0x7fffffff, 0x00020000);

  unsigned xsrc[XI], wsrc[WI];
  if constexpr (HALO) {
    const int w4 = wave & 3, lr = lane >> 3;
    const unsigned xv = (unsigned)(lr * (K * 2) + (((lane & 7) ^ ((w4 * 4 + (lr >> 1)) & 7)) << 4));
    const int r0 = m0 - HR + w4 * 8;
#pragma unroll
    for (int i = 0; i < XI; ++i) xsrc[i] = xv + (unsigned)((i == 0 ? (r0 < 0 ? 0 : r0) : r0 + 32 * (i < 4 ? i : 4)) * (K * 2));
    const unsigned wv = (unsigned)((w4 * 8 + lr) * (LDW * 2) + tn * 128 + (((lane & 7) ^ wswz<128>(w4 * 8 + lr)) << 4));
#pragma unroll
    for (int i = 0; i < WI; ++i) wsrc[i] = wv + (unsigned)((i & 1) * 32 * (LDW * 2) + (i >> 1) * 2048);
  } else {
#pragma unroll
  for (int i = 0; i < XI; ++i) {
    int j = (wave % NLW) + NLW * i; if (j >= NXI) j = wave % NLW;
    const int r = 8 * j + (lane >> 3), c = (lane & 7) ^ ((r >> 1) & 7);
    int gr = m0 - HR + r; if (gr < 0) gr = 0;
    xsrc[i] = FX ? (unsigned)(rbH(m0 >> 4, sd) + (j >> 1) * RSH + (j & 1) * 1024 + lane * 16) : (unsigned)(gr * K + kh * (K / SPK) + c * 8) * 2u;
  }
#pragma unroll
  for (int i = 0; i < WI; ++i) {
    const int b = i * 1024 + lane * 16, p = b / 224, pos = (b % 224) >> 4;
    const int row = 8 * ((p & 15) >> 2) + (p & 3) + 4 * (p >> 4);
    wsrc[i] = (unsigned)(row * LDW + tn * 112 + pos * 8) * 2u;
  }
  }
  auto ldX = [&](int i, int tile, int part, int slot, bool ok = true) {
    const auto rX = __builtin_amdgcn_make_buffer_rsrc((void*)a.X, 0, ok ? 0x7fffffff : 0, 0x00020000);
    if constexpr (GLU) tile = (tile + krot) & 15;
    const int j = wave + NLW * i;
    if (NXI % NLW != 0 && j >= NXI) return;
    __builtin_amdgcn_raw_ptr_buffer_load_lds(rX, (lds_void_ptr)(size_t)(lds0 + XBASE + slot * XT + (part * NXI + j) * 1024), 16, xsrc[i],
                                             omH(tile * 2048) + part * (NXI / 2) * RSH, 0, SEAM_IN ? XAUX : 0);
  };
#define KN_WNT 0
  constexpr int WAUX = ((KN_WNT >> EPI) & 1) ? 2 : 0;
  auto ldW = [&](int i, int tile, int slot) {
    __builtin_amdgcn_raw_ptr_buffer_load_lds(__builtin_amdgcn_make_buffer_rsrc((void*)((wave & 1) ? a.W1 : a.W0), 0, 0x7fffffff, 0x00020000),
                                             (lds_void_ptr)(size_t)(lds0 + WBASE + (slot + (wave >> 1)) * WU + (wave & 1) * UPOFF + i * 1024), 16, wsrc[i],
                                             (((tile + krot) & 15) * 2 + (wave >> 1)) * KU * LDW * 2, 0, WAUX);
  };
  auto issueX = [&](int tile, int part, int slot) {
    if (wave >= NLW) return;
#pragma unroll
    for (int i = 0; i < XI; ++i) ldX(i, tile, part, slot);
  };
  auto issueW = [&](int unit, int slot) {
    if (wave >= NLW) return;
#pragma unroll
    for (int i = 0; i < WI; ++i) ldW(i, unit, slot);
  };

  f32x4 acc[NF][MF];
#pragma unroll
  for (int n = 0; n < NF; ++n)
#pragma unroll
    for (int m = 0; m < MF; ++m) acc[n][m] = (f32x4){0.f, 0.f, 0.f, 0.f};
  f32x4 acch = {0.f, 0.f, 0.f, 0.f};
  f32x4 accs = {0.f, 0.f, 0.f, 0.f};
  f32x4 gram = {0.f, 0.f, 0.f, 0.f}, rsum = {0.f, 0.f, 0.f, 0.f}, gramh = {0.f, 0.f, 0.f, 0.f};
  constexpr int ONES = RING;

  unsigned xa0, xa1, xh0 = 0, xh1 = 0;
  { const unsigned o = fr * 128 + ((fq ^ ((fr >> 1) & 7)) << 4), mo = (HR + wm * MF * 16) * 128;
    xa0 = lds0 + XBASE + mo + o; xa1 = lds0 + XBASE + mo + (o ^ 64); xh0 = lds0 + XBASE + o; xh1 = lds0 + XBASE + (o ^ 64);
    if constexpr (FX) { xa0 = lds0 + XBASE + mo + lane * 16; xa1 = xa0 + 1024; } }
  unsigned wa[C], wha = 0;
#pragma unroll
  for (int c = 0; c < C; ++c) {
    const int row = 8 * fq + wq, seg = HALO ? wn : wn * NF + c;
    if constexpr (GLU) wa[c] = lds0 + WBASE + (4 * fq + wq) * 224 + wn * 112 + (c == 0 ? wp * 8 : 96 + (wp & 1) * 8 + (wp >> 1) * UPOFF);
    else wa[c] = lds0 + WBASE + row * STRIDE + (((seg << 1) ^ wswz<STRIDE>(row)) << 4) + wp * 8;
  }

  using I0 = std::integral_constant<int, 0>; using I1 = std::integral_constant<int, 1>;
  using T = std::true_type; using F = std::false_type;
  typedef Frags<MFX, NFX> FR;
  struct RdCtx { unsigned xa, xh, wb[C], wh; };
  auto mkctx = [&](bool ks1, unsigned xoffs, unsigned woffs) {
    RdCtx c; c.xa = (ks1 ? xa1 : xa0) + xoffs; c.xh = fr == 0 ? lds0 + ONES : (ks1 ? xh1 : xh0) + xoffs; c.wh = wha + woffs;
#pragma unroll
    for (int i = 0; i < C; ++i) c.wb[i] = wa[i] + woffs;
    return c;
  };
  auto rd_item = [&](FR& f, const RdCtx& c, auto KSc, auto kc) {
    constexpr int KS = decltype(KSc)::value, k = decltype(kc)::value;
    if constexpr (k < MF) f.x[k] = ds_b128<k * 2048>(c.xa);
    else if constexpr (k < MFX) f.x[k] = ds_b128<0>(c.xh);
    else if constexpr (GLU) {
      constexpr int n = k - MFX, cc = n == 6, o = n == 6 ? 0 : (n >> 1) * 32 + (n & 1) * UPOFF;
      f.lo[n] = ds_tr<o>(c.wb[cc]); f.hi[n] = ds_tr<o + 16 * 224>(c.wb[cc]);
    } else if constexpr (k < MFX + NF) {
      constexpr int n = k - MFX, cc = n % C, o = (HALO ? n * 8192 : (n / C) * C * SEGB) + KS * 32 * STRIDE;
      f.lo[n] = ds_tr<o>(c.wb[cc]); f.hi[n] = ds_tr<o + 4 * STRIDE>(c.wb[cc]);
    }
  };
  auto wait_frags = [&](FR& f) {
    asm volatile("s_waitcnt lgkmcnt(0)" ::: "memory");
#pragma unroll
    for (int m = 0; m < MFX; ++m) asm volatile("" : "+v"(f.x[m]));
#pragma unroll
    for (int n = 0; n < NFX; ++n) asm volatile("" : "+v"(f.lo[n]), "+v"(f.hi[n]));
  };
#define KN_DPP 3
#define KN_FG 1
#define KN_HOLD 0
#define KN_HG 6
#define KN_FB 1
#define KN_FL 22
#define KN_FL2 20
#define KN_FE 25
  FR fa, fb;
  auto rdg = [&](const RdCtx& c, auto rc) {
    constexpr int r = decltype(rc)::value;
    if constexpr (r >= 28) { constexpr int m = (r - 28) >> 1; if constexpr (r & 1) fb.x[m] = ds_b128<m * 2048 + 1024>(c.xa); else fa.x[m] = ds_b128<m * 2048>(c.xa); }
    else {
      constexpr int n = r >> 2, cc = n == 6, o = (n == 6 ? 0 : (n >> 1) * 32 + (n & 1) * UPOFF) + ((r >> 1) & 1) * WU + (r & 1) * 16 * 224;
      FR& f = (r & 2) ? fb : fa;
      if constexpr (r & 1) f.hi[n] = ds_tr<o>(c.wb[cc]); else f.lo[n] = ds_tr<o>(c.wb[cc]);
    }
  };
  auto ping = [&](auto pc) {
    static_for<0, 36>([&](auto rc) { constexpr int r = decltype(rc)::value;
      if constexpr (SchedG::need(r) == decltype(pc)::value) {
        FR& f = (r >= 28 ? r & 1 : r & 2) ? fb : fa;
        if constexpr (r >= 28) { bf16x8& q = f.x[(r - 28) >> 1]; asm volatile("" : "+v"(q)); } else { s16x4& q = (r & 1) ? f.hi[r >> 2] : f.lo[r >> 2]; asm volatile("" : "+v"(q)); } } });
  };
  auto pairs = [&](auto PBc, auto PEc, auto RDc, auto CSc, const RdCtx& c, auto&& gapf) {
    constexpr bool RD = decltype(RDc)::value; constexpr int CS = decltype(CSc)::value;
    static_for<decltype(PBc)::value, decltype(PEc)::value>([&](auto pc) {
      constexpr int p = decltype(pc)::value, a = (p + 16) % 28, n = SchedG::pn(a), m = SchedG::pm(a);
      if constexpr (SchedG::cnt(p) >= 0) { asm volatile("s_waitcnt lgkmcnt(%0)" :: "n"(SchedG::cnt(p)) : "memory"); ping(pc); }
      static_for<0, 2>([&](auto hc) {
        constexpr int g = 2 * p + decltype(hc)::value;
        FR& u = ((a ^ g) & 1) ? fb : fa;
        acc[n][m] = __builtin_amdgcn_mfma_f32_16x16x32_bf16(__builtin_shufflevector(u.lo[n], u.hi[n], 0, 1, 2, 3, 4, 5, 6, 7), u.x[m], acc[n][m], 0, 0, 0);
        __builtin_amdgcn_sched_barrier(0);
        if constexpr (CS >= 0 && SchedG::last(a) && (g & 1)) {
          const unsigned sv = fr == n ? 0x3f803f80u : 0u;
          const u32x4 s4 = {sv, sv, sv, sv};
          FR& w = CS ? fb : fa;
          accs = __builtin_amdgcn_mfma_f32_16x16x32_bf16(__builtin_shufflevector(w.lo[n], w.hi[n], 0, 1, 2, 3, 4, 5, 6, 7), __builtin_bit_cast(bf16x8, s4), accs, 0, 0, 0);
          __builtin_amdgcn_sched_barrier(0);
        }
        if constexpr (RD) static_for<0, 36>([&](auto rc) { if constexpr (SchedG::gap(decltype(rc)::value) == g) rdg(c, rc); });
        if constexpr (g & 1) gapf(pc);
        __builtin_amdgcn_sched_barrier(0);
      });
    });
    if constexpr (RD) { asm volatile("s_waitcnt lgkmcnt(0)" ::: "memory"); ping(std::integral_constant<int, 28>{}); }
  };

  f32x4* tb_row = (f32x4*)(smem + RING);
  float* tb_cs = (float*)(tb_row + 256);
  constexpr int RTAB = 524288;
  auto glu_stats_issue = [&]() {
    if constexpr (EPI == EPI_GLU) {
      const auto rS = __builtin_amdgcn_make_buffer_rsrc((void*)a.st, 0, 0x7fffffff, 0x00020000);
      if (wave == 7) __builtin_amdgcn_raw_ptr_buffer_load_lds(rS, (lds_void_ptr)(size_t)(lds0 + RING + 3072), 16, ((lane >> 2) * S_ + m0 + tn * 8 + (lane & 3) * 2) * 8, 0, 0, 16);
    }
  };
  auto glu_stats = [&]() {
    if constexpr (EPI == EPI_GLU) {
      int lx = lane; asm volatile("" : "+v"(lx));
      const unsigned sa = lds0 + RING + 3072 + lx * 8;
      f32x2 p0 = ds_b64<0>(sa), p1 = ds_b64<512>(sa);
      asm volatile("s_waitcnt lgkmcnt(0)" : "+v"(p0), "+v"(p1) :: "memory");
      float s0 = p0[0] + p1[0]; asm volatile("" : "+v"(s0), "+v"(p0));
      const float mu = sum8(s0) * (1.f / 16.f);
      const float v = sum8((p0[1] + p1[1]) + 64.f * ((p0[0] - mu) * (p0[0] - mu) + (p1[0] - mu) * (p1[0] - mu))) * (1.f / 1024.f) + 1e-5f;
      float rs = __builtin_amdgcn_rsqf(v), k2 = -2.3022082f * rs; asm volatile("" : "+v"(k2), "+v"(rs));
      const f32x4 rv = {mu, -0.10294324f * rs * rs * rs, k2, v};
      __builtin_amdgcn_raw_buffer_store_b128(__builtin_bit_cast(u32x4, rv), __builtin_amdgcn_make_buffer_rsrc((void*)a.cs, 0, 0x7fffffff, 0x00020000),
                                             lx < 8 ? (unsigned)(RTAB + (m0 + tn * 8 + lx) * 16) : 0x80000000u, 0, 16);
    }
  };
  auto epilogue = [&](auto EAc) __attribute__((always_inline)) {
  STAMP(2);

  if constexpr (EPI == EPI_GLU) {
    constexpr bool EARLY = decltype(EAc)::value;
    static_assert(DONE + 4 == ACNT && TCH + 256 == FPAD, "LDS");
#define KN_EP 1
#define KN_HE 0
#define KN_W1 0
#define KN_W2 0
#define KN_WS (KN_W2 ? 4 : 2)
    if constexpr (!EARLY) {
      wait_vm<0>();
      if (KN_HE) hook(std::integral_constant<int, 2>{});
      lds_wait8(lds0 + DONE);
      if (KN_EP) __builtin_amdgcn_s_setprio(2);
      STAMPW(9, 4);
      if (KN_W1 == 0) hook(I0{});
      if (KN_W2 == 4) hook(std::integral_constant<int, 3>{});
      STAMPW(10, 4);
    } else { if (KN_EP) __builtin_amdgcn_s_setprio(3); hook(I1{}); }
    asm volatile("" ::: "memory");
    f32x4 cg[4], cu[4], rw[MF], rw6[2];
    int lz = lane; asm volatile("" : "+v"(lz));
    const int fr = lz & 15, fq = lz >> 4;
#pragma unroll
    for (int p = 0; p < 4; ++p) {
      const int o = wn * 56 + p * 16 + (p == 3 ? (fq & 1) : fq) * 4;
      cg[p] = *(const f32x4*)(tb_cs + o); cu[p] = *(const f32x4*)(tb_cs + 112 + o);
    }
#pragma unroll
    for (int m = 0; m < MF; ++m) rw[m] = tb_row[(wm * MF + m) * 16 + fr];
#pragma unroll
    for (int q = 0; q < 2; ++q) rw6[q] = tb_row[(wm * MF + 2 * q + (fq >> 1)) * 16 + fr];
    const int gz = rbA((m0 >> 4) + wm * 4, sd) + fr * 16, g = tn * 2 + wn, g1 = omA(g * 1024);
    const int go0 = gz + (fq & 1) * RSA + (fq >> 1) * 256, go6 = gz + ((fq & 1) * 2 + (fq >> 1)) * RSA;
    unsigned hv[MF][NF];
    auto put = [&](unsigned a0, unsigned a1, unsigned b0, unsigned b1, int vo, int so) {
      const auto s0 = __builtin_amdgcn_permlane16_swap(a0, b0, false, false), s1 = __builtin_amdgcn_permlane16_swap(a1, b1, false, false);
      __builtin_amdgcn_raw_buffer_store_b128((u32x4){s0[0], s1[0], s0[1], s1[1]}, rO, vo, so, 16);
      __builtin_amdgcn_sched_barrier(0);
      asm volatile("s_nop 1" ::: "memory");
      __builtin_amdgcn_sched_barrier(0);
    };
#define STAGE(body) _Pragma("unroll") for (int i = 0; i < 2 * NN; ++i) { const int m = MB + i / NN, n = N0 + i % NN; const f32x4 k = n == 6 ? rw6[m >> 1] : rw[m]; body; } __builtin_amdgcn_sched_barrier(0)
    auto grp = [&](auto MBc, auto N0c, auto NNc, auto&& mid) {
      constexpr int MB = decltype(MBc)::value, N0 = decltype(N0c)::value, NN = decltype(NNc)::value;
      f32x2 G[2 * NN], U[2 * NN], P[2 * NN];
      __builtin_amdgcn_sched_barrier(0);
      STAGE({ const int e = (n == 6 ? m & 1 : n & 1) * 2; const f32x4 x = n == 6 ? acc[6][m & 2] : acc[n & 6][m]; G[i] = ((f32x2){x[e], x[e + 1]}) - k[0] * ((f32x2){cg[n >> 1][e], cg[n >> 1][e + 1]}); });
      STAGE(P[i] = G[i] * G[i]);
      STAGE(P[i] = P[i] * k[1] + k[2]);
      STAGE(P[i] = G[i] * P[i]);
      STAGE({ const int e = (n == 6 ? m & 1 : n & 1) * 2; const f32x4 x = n == 6 ? acc[6][m | 1] : acc[n | 1][m]; U[i] = ((f32x2){x[e], x[e + 1]}) - k[0] * ((f32x2){cu[n >> 1][e], cu[n >> 1][e + 1]}); });
      STAGE(P[i] = ((f32x2){__builtin_amdgcn_exp2f(P[i][0]), __builtin_amdgcn_exp2f(P[i][1])}));
      mid();
      __builtin_amdgcn_sched_barrier(0);
      STAGE(P[i] = P[i] * k[3] + k[3]);
      STAGE(P[i] = ((f32x2){__builtin_amdgcn_rcpf(P[i][0]), __builtin_amdgcn_rcpf(P[i][1])}));
      STAGE(U[i] = G[i] * U[i]);
      STAGE(U[i] = U[i] * P[i]);
      STAGE({ hv[m][n] = pk_bf16(U[i][0], U[i][1]); asm volatile("" : "+v"(hv[m][n])); });
    };
#undef STAGE
    using I2 = std::integral_constant<int, 2>; using I3 = std::integral_constant<int, 3>; using I4 = std::integral_constant<int, 4>;
    grp(I0{}, I0{}, I4{}, [&]() { if (EARLY && KN_W1 == 2) { lds_wait8(lds0 + DONE); hook(I0{}); asm volatile("" ::: "memory"); } });
    if (EARLY && KN_W1 == 1) { lds_wait8(lds0 + DONE); hook(I0{}); asm volatile("" ::: "memory"); }
    if (!EARLY && KN_W2 == 3) { hook(I3{}); asm volatile("" ::: "memory"); }
    grp(I2{}, I0{}, I4{}, [&]() {
#pragma unroll
      for (int p = 0; p < 2; ++p) put(hv[0][2 * p], hv[0][2 * p + 1], hv[1][2 * p], hv[1][2 * p + 1], go0 + p * 512, g1); });
#pragma unroll
    for (int p = 0; p < 2; ++p) put(hv[2][2 * p], hv[2][2 * p + 1], hv[3][2 * p], hv[3][2 * p + 1], go0 + p * 512, g1 + 2 * RSA);
    asm volatile("" ::: "memory");
    __builtin_amdgcn_sched_barrier(0);
    STAMP(4);
    STAMPW(11, 4);
    if (KN_EP) __builtin_amdgcn_s_setprio(EARLY ? 1 : 0);
#define KN_AW 1
    auto arrived = [&](int w) { if (!EARLY && w == KN_AW) { wait_vm<0>(); lds_inc(lds0 + ACNT); STAMPW(6, 4); } };
    arrived(0);
    if (EARLY && KN_W2 == 1) { if (KN_W1 == 0) lds_wait8(lds0 + DONE); hook(I3{}); asm volatile("" ::: "memory"); }
#pragma unroll
    for (int q = 0; q < 2; ++q)
#pragma unroll
      for (int e = 0; e < 4; ++e) {
        const float x0 = acc[6][2 * q][e], x1 = acc[6][2 * q + 1][e];
        auto sw = __builtin_amdgcn_permlane32_swap(__float_as_uint(x0), __float_as_uint(x1), false, false);
        acc[6][2 * q][e] = __uint_as_float(sw[0]); acc[6][2 * q + 1][e] = __uint_as_float(sw[1]);
      }
    const int sb = omA((256 + g * 3) * 256), go4 = go0 + (fq >> 1) * (omA((257 + g * 3) * 256) - sb - 256);
    grp(I0{}, I4{}, I3{}, [&]() { arrived(1); });
    arrived(2);
    if (EARLY && KN_W2 == 2) { if (KN_W1 == 0) lds_wait8(lds0 + DONE); hook(I3{}); asm volatile("" ::: "memory"); }
    grp(I2{}, I4{}, I3{}, [&]() { arrived(3); put(hv[0][4], hv[0][5], hv[1][4], hv[1][5], go4, sb); });
    put(hv[2][4], hv[2][5], hv[3][4], hv[3][5], go4, sb + 2 * RSA);
    put(hv[0][6], hv[1][6], hv[2][6], hv[3][6], go6, omA((258 + g * 3) * 256));
    asm volatile("" ::: "memory");
    if constexpr (!EARLY) lds_inc(lds0 + ACNT + 4);
  }
  };
  using IM1 = std::integral_constant<int, -1>;

  if constexpr (KU == 32) {
    static_assert(SX == 3 && SW == 4 && NXI % NLW == 0, "ring");
    constexpr int WR = KN_GW ? 2 : 0;
    constexpr bool EG = (KN_E & 2) != 0;
    static_assert(KN_GW == 1, "first W tile");
    if (wave == 5) { ds_w32(lds0 + DONE, 0); ds_w32(lds0 + DONE + 4, 0); ds_w32(lds0 + DONE + 8, 0); }
    static_assert(EG, "early start");
    auto check_tile = [&](int t) {
      int lx = lane; asm volatile("" : "+v"(lx));
      const unsigned ad = lds0 + XBASE + t * XT + wave * 1024 + lx * 16;
      if (__builtin_expect(marked<8>(ad), 0)) {
        for (int tries = 0;; ++tries) {
          if (tries >= KN_TRY) wait_flags(a.sync + FHF(panel), (lane4() & 127) * 32, tag, LO32);
#pragma unroll 1
          for (int u = t; u < t + 1 + (t == 0 && KN_GP >= 8); ++u) { issueX(u, 0, u); issueX(u, 1, u); }
          if (t == 0 && KN_GP >= 8) wait_vm<2 * XI>(); else wait_vm<0>();
          if (tries >= KN_TRY || !marked<8>(ad)) break;
        }
      }
    };
    auto first = [&](auto ic) { constexpr int i = decltype(ic)::value; if constexpr (i < 8) ldX(i % XI, 1, i / XI, 1); else if constexpr (i < 15) ldW(i - 8, 1, 2 ^ WR); else ldX((i - 15) % XI, 2, (i - 15) / XI, 2); };
    auto run = [&](auto LDRc, auto STc) __attribute__((always_inline)) {
      constexpr bool LDR = decltype(LDRc)::value;
      if constexpr (LDR) {
        if (!KN_GX) { issueX(0, 0, 0); issueX(0, 1, 0); }
        static_for<0, KN_GP>(first);
        wait_vm<KN_GP>();
        STAMPW(5, 0);
        if constexpr (EG) check_tile(0);
      } else if constexpr (!EG) glu_stats_issue();
      lds_barrier();
      if constexpr (EG && !LDR) { if (wave == 4) __builtin_amdgcn_raw_ptr_buffer_load_lds(__builtin_amdgcn_make_buffer_rsrc((void*)(a.sync + FHF(panel)), 0, 4096, 0x00020000), (lds_void_ptr)(size_t)(lds0 + TCH + 256), 4, lane * 128, 0, 0, 16); }
      using I12 = std::integral_constant<int, 12>; using I28 = std::integral_constant<int, 28>;
      { const RdCtx c = mkctx(false, 0, WR * WU);
        static_for<0, 24>([&](auto gc) { static_for<0, 36>([&](auto rc) { if constexpr (SchedG::gap(decltype(rc)::value) == decltype(gc)::value) rdg(c, rc); }); });
        STAMP(1);
        pairs(I12{}, I28{}, T{}, IM1{}, c, [&](auto pc) { if constexpr (LDR) static_for<KN_GP, 23>([&](auto ic) { if constexpr (12 + (decltype(ic)::value - KN_GP) * 15 / (23 - KN_GP) == decltype(pc)::value) first(ic); }); }); }
      int xs = 1;
#define KN_CTJ (KN_SPLIT ? 11 : 12)
      unsigned long long tb = (unsigned long long)a.PF0; unsigned tv = 0, tl = 0x7fffffffu, ts = KN_SPLIT ? 1835008u : 1048576u, tmask = 0; int tj = 1, tsh = 1;
      if constexpr (!LDR) {
        unsigned long long c0, c1; CODE_ADDR(c0, "k_all"); CODE_ADDR(c1, "k_zend");
#define KN_WJ 1
#define KN_WE 8
#define KN_DJ 6
        tj = KN_DJ;
        if (wave == 4) { tb = c0; tl = c1 - c0 < 262144ull ? (unsigned)(c1 - c0) : 0u; ts = 0; tmask = 1u << KN_CTJ; }
        if (wave == 5) tmask = 0x55u << (KN_DJ + (idx & 1));
        if (wave == 6) { tb = (unsigned long long)((idx & 16) ? a.W1 : a.W0); ts = 64 * LDW * 2; tmask = ((1u << (KN_WE - KN_KT)) - 1u) << KN_WJ; tj = KN_WJ - krot - KN_KT; tsh = 0; }
        const int l4 = lane / 14;
        tv = wave == 4 ? (unsigned)((lane * 32 + idx) * 128)
           : (lane >= 56 ? 0x80000000u : (wave == 5 ? (unsigned)(bid * 7168 + lane * 128) : (unsigned)(((idx & 15) * 4 + l4) * (LDW * 2) + (xcd & 3) * 1792 + (lane - l4 * 14) * 128)));
      }
      const int js = 2 * (tm & 3) + 1;
      const auto rC = __builtin_amdgcn_make_buffer_rsrc((void*)a.cs, 0, 0x7fffffff, 0x00020000);
      unsigned* ccnt = a.sync + FCS(tn);
#define KN_JR 10
#define KN_JS ((KN_E & 2) ? 2 : 1)
      const int jf = wave == 4 ? NT - 4 : (wave == 7 ? KN_JR : 64);
      const unsigned* fcnt = wave == 4 ? ccnt : a.sync + FRC(panel);
      const bool mrk = (KN_E & 4) && KN_MK == 0 && (idx >> 4) == ((xcd >> 1) & 1);
      const int jm = (idx >> 4) == ((xcd >> 1) & 1) && (idx & 7) < KN_V ? 3 : 64;
      static_assert(KN_MK == 2 && (KN_E & 4) && !KN_DM, "marks");
      int mo = rbA((m0 >> 4) + wave * 4, sd) + omA(tn * 2048);
      const unsigned evm = tmask | (8u << js) | (mrk ? 0xbfcu : 0u) | (wave == 4 ? 3u << (NT - 4) : 0u) | (wave == 7 ? ((5u << KN_JS) | 2u | (11u << KN_JR)) : 0u);
      constexpr int CFL = TCH + 256, CPB = XBASE + ((NT - 3) % 3) * XT;
      auto iter = [&](int j, auto LWc, auto LXc, auto WAITc, auto CSc) {
        constexpr bool LW = decltype(LWc)::value && LDR, LX = decltype(LXc)::value && LDR, LP = LW && !LX;
        if constexpr (LDR) {
          wait_vm<decltype(WAITc)::value>();
          if constexpr (EG && LX) { if (__builtin_expect(j <= 2, 0)) check_tile(j); }
          if constexpr ((KN_E & 4) && KN_MK == 2 && LX) {
            if ((unsigned)(j - jm) < 8u) {
              int lx = lane; asm volatile("" : "+v"(lx));
              unsigned on = 0xffffffffu; asm volatile("" : "+v"(on));
              __builtin_amdgcn_raw_buffer_store_b128((u32x4){on, on, on, on}, rO, lx * 16, mo, 16);
              mo += (j & 1) ? 1024 : RSA - 1024;
            }
            if (j == jm + 9) { if (lane == 0) set_flag(a.sync + FAM(panel) + (tn & 15) * 4 + wave, tag); }
          }
        }
        if constexpr (!LDR) {
          if (__builtin_expect((evm >> j) & 1, 0)) {
            if (j == js + 3) {
              if ((tmask >> (j - 1)) & 1) wait_vm<1>(); else wait_vm<0>();
              if (lane == 0) set_flag(ccnt + tm * 4 + wave - 4, tag);
            }
            if (EG && j == 1) glu_stats_issue();
            if (j == KN_JS && wave == 7) { wait_vm<0>(); glu_stats(); }
            if (j == KN_JS + 2 && wave == 7) {
              if (j == js + 2) wait_vm<1>(); else wait_vm<0>();
              if (lane == 0) set_flag((unsigned*)fcnt + tn, tag);
            }
            {
              int lx = lane; asm volatile("" : "+v"(lx));
              if (j == jf) __builtin_amdgcn_raw_ptr_buffer_load_lds(__builtin_amdgcn_make_buffer_rsrc((void*)fcnt, 0, 128, 0x00020000), (lds_void_ptr)(size_t)(lds0 + CFL + (wave & 1) * 256), 4, lx * 4, 0, 0, 16);
              if (j == jf + 1) {
                wait_vm<0>();
                if (lds_missing(lds0 + CFL + (wave & 1) * 256, tag, LO32)) wait_flags(fcnt, lx * 4, tag, LO32);
                if (wave == 7) {
#pragma unroll
                  for (int i = 0; i < 4; ++i) __builtin_amdgcn_raw_ptr_buffer_load_lds(rC, (lds_void_ptr)(size_t)(lds0 + RING + i * 1024), 16, lx * 16, RTAB + m0 * 16 + i * 1024, 0, 16);
                }
              }
              if (j == jf + 3) wait_vm<0>();
            }
            if ((tmask >> j) & 1) touch((const void*)tb, tv, (((j - tj) >> tsh) & 15) * ts, tl);
          }
          __builtin_amdgcn_sched_barrier(0);
        }
        int xp = xs + 2; if (xp >= 3) xp -= 3;
        const int ws = ((j & 1) * 2) ^ WR;
        auto lda = [&](int i) { ldW(i, j + 1, ws ^ 2); };
        auto ldb = [&](int i) {
            if constexpr (LX) ldX(i % XI, j + 2, i / XI, xp);
            else __builtin_amdgcn_raw_ptr_buffer_load_lds(rC, (lds_void_ptr)(size_t)(lds0 + CPB + wave * 8192 + i * 1024), 16, lane * 16, (tn * 2 + wn) * 7168 + i * 1024, 0, 16); };
        using NLA = std::integral_constant<int, LW ? WI : 0>; using NLB = std::integral_constant<int, LX ? 2 * XI : (LP ? 7 : 0)>;
        { const RdCtx c = mkctx(false, xs * XT, ws * WU);
          pairs(I0{}, I28{}, T{}, CSc, c, [&](auto pc) { constexpr int p = decltype(pc)::value;
            if constexpr (LDR && p == 0) loop_barrier();
            if constexpr (!LDR && p == KN_GE) { asm volatile("s_waitcnt lgkmcnt(0)" ::: "memory"); if (decltype(WAITc)::value == 0 || j + 1 < NT) loop_barrier(); }
            static_for<0, NLA::value>([&](auto ic) { constexpr int i = decltype(ic)::value; if constexpr (i * 13 / 7 == p) lda(i); });
            static_for<0, NLB::value>([&](auto ic) { constexpr int i = decltype(ic)::value; if constexpr (14 + i * 13 / 8 == p) ldb(i); }); }); }
        xs = xs + 1; if (xs >= 3) xs = 0;
      };
      using WX_ = std::integral_constant<int, 2 * XI>;
      bf16x8 pv[8];
      if constexpr (LDR) {
        for (int j = 1; j < NT - 2; ++j) iter(j, T{}, T{}, WX_{}, IM1{});
#define KN_PRI 0
        if (KN_PRI == 2) __builtin_amdgcn_s_setprio(3);
        iter(NT - 2, T{}, F{}, WX_{}, IM1{});
        if (KN_PRI == 1) __builtin_amdgcn_s_setprio(3);
        iter(NT - 1, F{}, F{}, std::integral_constant<int, 7>{}, IM1{});
        wait_vm<0>();
        int lc = lane; asm volatile("" : "+v"(lc));
        const unsigned ca = lds0 + CPB + wave * 8192 + (lc >> 4) * 1792 + ((lc & 15) < 14 ? lc & 15 : 0) * 16;
        static_for<0, 8>([&](auto ic) { constexpr int i = decltype(ic)::value; pv[i] = ds_b128<i * 224>(ca); });
      } else {
        int j = 1;
        if constexpr (EG) {
          if (wave == 4) {
            wait_vm<0>();
            if (lds_missing(lds0 + TCH + 256, tag, LO32)) wait_flags(a.sync + FHF(panel), (lane4() & 127) * 32, tag, LO32);
          }
        }
        loop_barrier();
#pragma unroll 1
        for (; j < js; ++j) iter(j, F{}, F{}, I0{}, IM1{});
        const int ja = wm == 2 ? js + 2 : js;
#pragma unroll 1
        for (; j < ja; ++j) iter(j, F{}, F{}, I0{}, I0{});
#pragma unroll 1
        for (; j < js + 2; ++j) iter(j, F{}, F{}, I0{}, I1{});
        { int lx = lane; asm volatile("" : "+v"(lx));
          __builtin_amdgcn_raw_buffer_store_b128(__builtin_bit_cast(u32x4, accs), rC, (lx & 15) < NF ? (unsigned)((tn * 4 + wn * 2) * 3584 + (tm * 2 + (wm & 1)) * 224
                  + ((lx & 15) < 6 ? (lx & 1) * 3584 + ((lx & 15) >> 1) * 64 + (lx >> 4) * 16 : (lx >> 5) * 3584 + 192 + (lx & 16))) : 0x80000000u, 0, 16); }
#pragma unroll 1
        for (; j < NT; ++j) iter(j, F{}, F{}, I1{}, IM1{});
        done_inc();
      }
      { const RdCtx c = mkctx(false, 0, 0); pairs(I0{}, I12{}, F{}, IM1{}, c, [&](auto pc) {
          if constexpr (LDR && decltype(pc)::value == 4) {
            asm volatile("s_waitcnt lgkmcnt(0)" ::: "memory");
#pragma unroll
            for (int k = 0; k < 8; ++k) { bf16x8& q = pv[k]; asm volatile("" : "+v"(q)); }
            f32x4 cv = ((__builtin_bit_cast(f32x4, pv[0]) + __builtin_bit_cast(f32x4, pv[1])) + (__builtin_bit_cast(f32x4, pv[2]) + __builtin_bit_cast(f32x4, pv[3])))
                     + ((__builtin_bit_cast(f32x4, pv[4]) + __builtin_bit_cast(f32x4, pv[5])) + (__builtin_bit_cast(f32x4, pv[6]) + __builtin_bit_cast(f32x4, pv[7])));
#pragma unroll
            for (int k = 0; k < 4; ++k) { const float t = cv[k]; auto sw = __builtin_amdgcn_permlane16_swap(__float_as_uint(t), __float_as_uint(t), false, false); cv[k] = __uint_as_float(sw[0]) + __uint_as_float(sw[1]); }
            if ((lane & 31) < 14) *(f32x4*)(tb_cs + (lane >> 5) * 112 + wn * 56 + fr * 4) = cv;
            done_inc();
          } }); }
#define KN_PRE 0
      if constexpr ((LDR || (KN_HOLD & 2)) && KN_PRE == 0) __builtin_amdgcn_s_setprio(0);
      epilogue(LDRc);
      if constexpr (LDR && KN_PRE == 1) __builtin_amdgcn_s_setprio(0);
    };
    if (wave < NLW) run(T{}, IM1{}); else run(F{}, IM1{});
  }
  STAMP(7);
  STAMP(8);
  if constexpr (GLU) STAMPW(12, 4);
  STAMP(3);
#endif
}

#define KN_PT 13
#define KN_QR 0
#define KN_PS 0
#define KN_PG -1
#define KN_FW 1
#define KN_GQ 6
#define KN_G0 4
#define KN_OC 12
#define KN_TG 4
#define KN_PQ 1
#define KN_PZ 0
#define KN_PN 4
#define KN_H0 0
#define KN_HS 0
#define KN_HN 7
#define KN_X2 0
#define KN_QO1 3
#define KN_QO2 3
#define KN_QO3 3
#define KN_QO4 16
#define KN_QP 2
#define KN_QH 0
#define KN_QS 18
#define KN_QLB 13

constexpr int QXT = 144 * 128, QWU = 24576, QWB = 3 * QXT, QRING = QWB + 4 * QWU;
constexpr int QMU = QRING + 64, QRS = QMU + 576, QFL = QRS + 576, QTCH = QRING + 2304;
constexpr int QCS = 2 * QXT, QKH = QCS + 2560, QVH = QKH + 4096;
#define KN_QWC 8
struct SchedQ {
  static constexpr int ns(int j) { return j < 2 ? 2 : 0; }
  static constexpr int nx(int j) { return (j + 2 < 16 ? 4 : 0) + ns(j); }
  static constexpr int wh(int j) { if (j < 0) return 12; int v = wh(j - 1) + KN_QWC - ns(j); if (v > 6 * (j + 4)) v = 6 * (j + 4); return v > 96 ? 96 : v; }
  static constexpr int oh(int j) { return j < 13 ? 0 : 2 * (j == 13 ? KN_QO1 : (j == 14 ? KN_QO2 : KN_QO3)); }
  static constexpr int tot(int j) { return j < 0 ? 20 : tot(j - 1) + nx(j) + wh(j) - wh(j - 1) + oh(j) - oh(j - 1); }
  static constexpr int cnt(int j) {
    int pos = j < 2 ? 10 * (j + 1) : tot(j - 3) + 4;
    for (int i = 0; i < j; ++i) if (wh(i - 1) < 6 * (j + 1) && wh(i) >= 6 * (j + 1)) { const int q = tot(i - 1) + nx(i) + 6 * (j + 1) - wh(i - 1); if (q > pos) pos = q; }
    return tot(j - 1) - pos;
  }
};
static_assert(SchedQ::cnt(0) == 10 && SchedQ::cnt(8) == 16 && KN_QO1 <= 3 && KN_QO2 <= 6 && KN_QO3 <= 9 && KN_QWC >= 8, "schedule");
static_assert(SchedQ::cnt(KN_S1P) <= SchedQ::tot(KN_S1P - 1) - SchedQ::tot(0) - 6 && KN_S1C > KN_S1P + 2, "statistics");
struct FragQ { bf16x8 x[4]; s16x4 lo[6], hi[6]; };
__device__ __forceinline__ f32x2 ds_r2(unsigned addr) { f32x2 r; asm volatile("ds_read2_b32 %0, %1 offset1:16" : "=v"(r) : "v"(addr)); return r; }
__device__ __forceinline__ float fma1(float a, float b, float c) { float r; asm("v_fma_f32 %0, %1, %2, %3" : "=v"(r) : "v"(a), "v"(b), "v"(c)); return r; }
__device__ __forceinline__ unsigned k1_phase(const GemmArgs& a) {
  unsigned tag = 0;
#if defined(__HIP_DEVICE_COMPILE__)
  constexpr int K = D_, LDW = 3 * D_, NT = 16, XI = 5, XIMIN = 4, WI = 6;
  extern __shared__ __attribute__((aligned(16))) char smem[];
  STAMP(0);
  const unsigned lds0 = (unsigned)(size_t)(lds_char_ptr)smem;
  const int tid = threadIdx.x, lane = tid & 63;
  const int wave = __builtin_amdgcn_readfirstlane(tid >> 6);
  const int bid = blockIdx.x, xcd = bid & 7, idx = bid >> 3;
  const int tm = (xcd >> 2) * 8 + (idx >> 2), tn = (xcd & 3) * 4 + (idx & 3), m0 = tm * 128;
  const int fr = lane & 15, fq = lane >> 4, wq = (lane >> 2) & 3, wp = lane & 3;
  using I0 = std::integral_constant<int, 0>; using I1 = std::integral_constant<int, 1>; using I2 = std::integral_constant<int, 2>; using I3 = std::integral_constant<int, 3>;
  using T = std::true_type; using F = std::false_type;
  auto touch = [&](const void* base, unsigned voff, unsigned soff, unsigned lim = 0x7fffffffu) {
    __builtin_amdgcn_raw_ptr_buffer_load_lds(__builtin_amdgcn_make_buffer_rsrc((void*)base, 0, lim, 0x00020000), (lds_void_ptr)(size_t)(lds0 + QTCH), 4, voff, soff, 0, 0);
  };
  auto code_touch = [&](int line) {
    unsigned long long c0, c1; CODE_ADDR(c0, "k_all"); CODE_ADDR(c1, "k_zend");
    const unsigned len = c1 - c0 < 262144ull ? (unsigned)(c1 - c0) : 0u;
    touch((const void*)c0, (unsigned)(line * 128), 0, len);
  };
  int hj1, hj2; unsigned htv;
  auto hset = [&]() {
    const int slc = (3 - ((unsigned)(size_t)a.W0 >> 7) - (xcd & 3) * 4) & 7;
    const int hid = (idx >> 2) * 3 + (idx & 3) - ((idx & 3) > slc ? 1 : 0);
    const bool hlp = slc < 4 && (idx & 3) != slc;
    hj1 = hlp ? hid / 3 - KN_HQ : -100; hj2 = hlp ? hid / 3 + 8 - KN_HQ : -100;
    htv = (unsigned)(lane * (LDW * 2) + (hid % 3) * 2048 + ((xcd & 3) * 4 + slc) * 128);
  };
  const unsigned xo = fr * 128 + ((fq ^ ((fr >> 1) & 7)) << 4);
  auto wfrag = [&](int seg) { const int row = 8 * fq + wq; return lds0 + QWB + row * 128 + (((seg << 1) ^ wswz<128>(row)) << 4) + wp * 8; };
  const f32x4 z4 = {0.f, 0.f, 0.f, 0.f};
  auto cat = [](s16x4 lo, s16x4 hi) { return __builtin_shufflevector(lo, hi, 0, 1, 2, 3, 4, 5, 6, 7); };
  unsigned xsrc[XI], wsrc[WI];
  { const int lr = lane >> 3, w4 = wave & 3;
    const unsigned xv = (unsigned)(lr * (K * 2) + (((lane & 7) ^ ((w4 * 4 + (lr >> 1)) & 7)) << 4));
    const int r0 = m0 - 16 + w4 * 8;
#pragma unroll
    for (int i = 0; i < XI; ++i) xsrc[i] = xv + (unsigned)((i == 0 ? (r0 < 0 ? 0 : r0) : r0 + 32 * i) * (K * 2));
    const unsigned wv = (unsigned)((w4 * 8 + lr) * (LDW * 2) + tn * 128 + (((lane & 7) ^ wswz<128>(w4 * 8 + lr)) << 4));
#pragma unroll
    for (int i = 0; i < WI; ++i) wsrc[i] = wv + (unsigned)((i & 1) * 32 * (LDW * 2) + (i >> 1) * 2048); }
  unsigned mb = lds0 + wave * 1024, rx = KN_QR ? ((tm ^ tn) & 1) * 1024 : 0, rw = rx * (64 * LDW * 2 / 128);
  asm volatile("s_waitcnt lgkmcnt(0)" : "+s"(mb), "+s"(rx), "+s"(rw) : "s"(a.warm), "v"(wsrc[0]), "v"(wsrc[1]), "v"(wsrc[2]), "v"(wsrc[3]), "v"(wsrc[4]), "v"(wsrc[5]), "v"(xsrc[0]), "v"(xsrc[1]), "v"(xsrc[2]), "v"(xsrc[3]), "v"(xsrc[4]));
  __builtin_amdgcn_sched_barrier(0);
  const u32x4 qX = mk_rsrc(a.X), qW = mk_rsrc(a.W0);
  auto ldX = [&](auto ic, auto tc) { constexpr int i = decltype(ic)::value, t = decltype(tc)::value; if (i < 4 || wave < 2) ld_cx<t % 3 * QXT + i * 4096, t * 128>(mb, qX, xsrc[i], rx); };
  auto ldW = [&](auto qc) { constexpr int i = decltype(qc)::value % WI, t = decltype(qc)::value / WI; ld_ca<QWB + (t & 3) * QWU + i * 4096, t * (64 * LDW * 2), (t >= 8)>(mb, qW, wsrc[i], rw); };
  if (__builtin_expect(wave < 4, 1)) {
    static_for<0, WI>(ldW);
    static_for<0, XI>([&](auto ic) { ldX(ic, I0{}); });
  }
  __builtin_amdgcn_sched_barrier(0);
  if (__builtin_expect(wave < 4, 1)) {
    unsigned seed;
    asm volatile("s_load_dword %0, %1, %2" : "=s"(seed) : "s"(a.sync), "n"(FEP * 4));
    auto sb0 = []() { __builtin_amdgcn_sched_barrier(0); };
    static_for<WI, WI + 2>(ldW); sb0();
    unsigned xh0[3], xh1[3], wb[2], l16 = lane * 16;
#pragma unroll
    for (int i = 0; i < 3; ++i) { xh0[i] = fr == 0 ? lds0 + QRING : lds0 + i * QXT + xo; xh1[i] = fr == 0 ? lds0 + QRING : lds0 + i * QXT + (xo ^ 64); asm volatile("" : "+v"(xh0[i]), "+v"(xh1[i])); }
    sb0(); static_for<WI + 2, WI + 4>(ldW); sb0();
    wb[0] = wfrag(wave); wb[1] = wb[0] + 2 * QWU;
    u32x4 one1; { const unsigned o = fr == 0 ? 0x3f803f80u : 0u; one1 = (u32x4){o, o, o, o}; }
    const char* srows = (const char*)a.X + (size_t)(m0 + tn * 8 + wave * 2) * (K * 2);
    asm volatile("" : "+v"(wb[0]), "+v"(wb[1]), "+v"(one1), "+v"(l16), "+s"(srows));
    sb0(); static_for<WI + 4, 2 * WI>(ldW); sb0();
    hset();
    asm volatile("" : "+s"(hj1), "+s"(hj2), "+v"(htv));
    sb0(); static_for<0, 2>([&](auto ic) { ldX(ic, I1{}); }); sb0();
    unsigned long long tb = (unsigned long long)((wave == 3 || wave == 0) ? a.PF1 : a.W0);
    unsigned tv, ts, to, evm;
    { const int t1 = (bid + 256) / 48 - KN_GQ, t2 = (bid + 512) / 48 - KN_GQ;
      const unsigned e0 = KN_OC ? 1u << KN_OC : 0u, e1 = KN_GQ ? (1u << (t1 > 0 ? t1 : 0)) | (1u << t2) : 0u, e2 = (hj1 >= 0 ? 1u << hj1 : 0u) | (hj2 > 0 ? 1u << hj2 : 0u), e3 = 2u << (idx % 12);
      evm = wave == 0 ? e0 : (wave == 1 ? e1 : (wave == 2 ? e2 : e3));
      ts = wave == 1 ? 2u << 20 : 8 * 64 * LDW * 2;
      to = wave == 1 ? 2u << 20 : (wave == 2 ? ((hj1 >= 0 ? hj1 : hj2) + KN_HQ) * (64 * LDW * 2) : 0); }
    asm volatile("" : "+s"(evm), "+s"(ts), "+s"(to), "+s"(tb));
    sb0(); static_for<2, 4>([&](auto ic) { ldX(ic, I1{}); }); sb0();
    tv = (wave & 1) ? (unsigned)((bid * 64 + lane) * 128)
       : (wave ? htv : (lane < 32 ? (unsigned)((idx * 32 + lane) * 2048 + (xcd & 1) * 1024 + ((3 - ((unsigned)(size_t)a.PF1 >> 7)) & 7) * 128) : 0x80000000u));
    asm volatile("" : "+v"(tv));
    sb0(); ldX(std::integral_constant<int, 4>{}, I1{}); sb0();
    asm volatile("s_waitcnt lgkmcnt(0)\n\ts_mul_i32 %0, %1, 0x9e3779b1\n\ts_add_u32 %0, %0, 0x7f4a7c15" : "=s"(tag) : "s"(seed) : "scc");
    unsigned osrc, gv, sdst, ssrc, sld, smu, srs, fa0, fa1, fa2, mko;
    auto ldO = [&](auto qc) { constexpr int i = decltype(qc)::value >> 1, h = decltype(qc)::value & 1;
      ld_cx<wo_pos(i >> 1) + (i & 1) * 8192 + h * 4096, (((i & 1) * 8 + (i >> 1)) * 2 + h) * (32 * D_ * 2)>(mb, mk_rsrc(a.PF1), osrc, (xcd & 3) * 8 * (32 * D_ * 2)); };
    u32x4 sv[4]; f32x4 s1s[2], s1q[2]; float sa[4], sb;
    auto dpp = [](float t, auto c) { return __int_as_float(__builtin_amdgcn_update_dpp(0, __float_as_int(t), decltype(c)::value, 0xf, 0xf, false)); };
    auto stat = [&](auto kc) {
      constexpr int k = decltype(kc)::value;
      if constexpr (k == 0) asm volatile("" : "+v"(sv[0]), "+v"(sv[1]), "+v"(sv[2]), "+v"(sv[3]));
      if constexpr (k < 8) {
        constexpr int r = (k >> 1) & 1, h = k >> 2;
        const u32x4 one4 = {0x3f803f80u, 0x3f803f80u, 0x3f803f80u, 0x3f803f80u};
        const bf16x8 x = __builtin_bit_cast(bf16x8, sv[2 * r + h]);
        if constexpr (k & 1) s1q[r] = __builtin_amdgcn_mfma_f32_16x16x32_bf16(x, x, h ? s1q[r] : z4, 0, 0, 0);
        else s1s[r] = __builtin_amdgcn_mfma_f32_16x16x32_bf16(__builtin_bit_cast(bf16x8, one4), x, h ? s1s[r] : z4, 0, 0, 0);
      }
      if constexpr (k == 8 || k == 9) {
        constexpr int r = k - 8;
        const f32x4 g = s1q[r];
        const float d = (lane & 2) ? ((lane & 1) ? g[3] : g[2]) : ((lane & 1) ? g[1] : g[0]);
        sa[r] = fq == 0 ? s1s[r][0] : 0.f;
        sa[2 + r] = fq == wq ? d : 0.f;
        asm volatile("" : "+v"(sa[r]), "+v"(sa[2 + r]));
      }
      if constexpr (k == 10 || k == 11) { constexpr int i = (k - 10) * 2; sa[i] = ((lane & 1) ? sa[i + 1] : sa[i]) + dpp((lane & 1) ? sa[i] : sa[i + 1], std::integral_constant<int, 0xb1>{}); asm volatile("" : "+v"(sa[i])); }
      if constexpr (k == 12) sb = ((lane & 2) ? sa[2] : sa[0]) + dpp((lane & 2) ? sa[0] : sa[2], std::integral_constant<int, 0x4e>{});
      if constexpr (k == 13) { sb += dpp(sb, std::integral_constant<int, 0x124>{}); sb += dpp(sb, std::integral_constant<int, 0x128>{}); }
      if constexpr (k == 14) { auto q = __builtin_amdgcn_permlane16_swap(__float_as_uint(sb), __float_as_uint(sb), false, false); sb = __uint_as_float(q[0]) + __uint_as_float(q[1]); }
      if constexpr (k == 15) { auto q = __builtin_amdgcn_permlane32_swap(__float_as_uint(sb), __float_as_uint(sb), false, false); sb = (__uint_as_float(q[0]) + __uint_as_float(q[1])) * (1.f / 1024.f); }
      if constexpr (k == 16) { const float var = dpp(sb, std::integral_constant<int, 0x4e>{}) - sb * sb; sa[0] = __builtin_amdgcn_rsqf((var > 0.f ? var : 0.f) + 1e-5f); asm volatile("" : "+v"(sa[0])); }
      if constexpr (k >= 12 && k < 16) asm volatile("" : "+v"(sb));
      if constexpr (k == 17) {
        __builtin_amdgcn_raw_buffer_store_b128((u32x4){__float_as_uint(-sb), __float_as_uint(sa[0]), tag, ~tag}, __builtin_amdgcn_make_buffer_rsrc((void*)a.cs, 0, 0x7fffffff, 0x00020000), sdst, 0, 16);
        asm volatile("s_nop 1" ::: "memory");
      }
    };
    f32x4 acq = z4, ack = z4, acv = z4;
    const auto rC = __builtin_amdgcn_make_buffer_rsrc((void*)a.cs, 0, 0x7fffffff, 0x00020000);
    auto fetch = [&]() { __builtin_amdgcn_raw_ptr_buffer_load_lds(rC, (lds_void_ptr)(size_t)(lds0 + QRING + STL + wave * 1024), 16, ssrc, 0, 0, 16); };
    u32x4 ent; f32x4 hm4; int hm1, htg;
    auto olane = [&]() { int lx = lane; asm volatile("" : "+v"(lx)); return lx; };
    auto chore = [&](auto jc, auto gc) {
      constexpr int j = decltype(jc)::value, g = decltype(gc)::value;
      if constexpr (j == 1 && g == -1) ent = __builtin_bit_cast(u32x4, ds_b128<0>(lds0 + QFL + 16));
      if constexpr (j == 1 && g == 5) {
        asm volatile("" : "+v"(ent));
        if (wave == 3) __builtin_amdgcn_raw_buffer_store_b32((tag & ~1u) | (((ent[0] + ent[2] > ent[1] + ent[3] ? 0u : 1u) ^ (xcd >> 2)) & 1u), rC, lane == 0 ? (unsigned)(VTAB + bid * 4) : 0x80000000u, 0, 16);
      }
      if constexpr (j == 2 && g == 1) { const int lx = olane(); sdst = lx < 2 ? (unsigned)(STAB + (m0 + tn * 8 + wave * 2 + lx) * 16) : 0x80000000u; asm volatile("" : "+v"(sdst)); }
      if constexpr (j == KN_S1P && g >= 0 && g < 4) { stat(std::integral_constant<int, 2 * g>{}); stat(std::integral_constant<int, 2 * g + 1>{}); }
      if constexpr (j == KN_S1P && (g == 9 || g == 10)) stat(std::integral_constant<int, g - 1>{});
      if constexpr (j == KN_S1P + 1 && g >= 0 && g < 8) stat(std::integral_constant<int, 10 + g>{});
      if constexpr (j == 5 && g == 1) { const int se = wave * 64 + olane(), sr = m0 - 16 + se;
        ssrc = se < 144 ? (unsigned)(STAB + (sr < 0 ? se : sr) * 16) : (wave == 3 ? (unsigned)(VTAB - 3072 + se * 16) : 0x80000000u); sld = lds0 + QRING + STL + se * 16; asm volatile("" : "+v"(ssrc), "+v"(sld)); }
      if constexpr (j == 5 && g == 5) { const int se = wave * 64 + olane();
        smu = lds0 + (se < 144 ? QMU + se * 4 : QTCH); srs = lds0 + (se < 144 ? QRS + (se & 1) * 288 + (se >> 1) * 4 : QTCH); asm volatile("" : "+v"(smu), "+v"(srs)); }
      if constexpr (j == 6 && g == 1) { const int lx = olane(); osrc = (unsigned)((wave * 8 + (lx >> 3)) * D_ + ((xcd & 1) * 8 + (idx & 7)) * 64 + ((lx & 7) ^ ((lx >> 3) & 6)) * 8) * 2u; asm volatile("" : "+v"(osrc)); }
      if constexpr (j == 6 && g == 5) { const int lx = olane(); gv = lx < 56 ? (unsigned)(((bid & 1) * 512 + (wave >> 1) * 128 + (bid >> 1)) * (DF_ * 2) + lx * 128) : 0x80000000u; asm volatile("" : "+v"(gv)); }
      if constexpr (j == 7 && g == 1) { const int lx = olane();
        fa0 = lds0 + QCS + ((lx & 15) == 0 ? wave * 64 + lx : 1024 + lx * 16); fa1 = lds0 + QCS + (lx < 16 ? 512 + wave * 64 : 1024) + lx * 4; asm volatile("" : "+v"(fa0), "+v"(fa1)); }
      if constexpr (j == 7 && g == 5) { fa2 = lds0 + (wave >> 1) * 1024 + (wave & 1) * 8 + l16;
        const int p2 = (xcd >> 1) * 2 + (idx >> 4), mq = (xcd & 1) * 16 + (idx & 15);
        mko = rbH(p2 * 16 + (mq >> 1), xcd >> 2) + omH((xcd >> 2) * 16384) + (mq & 1) * 3072; asm volatile("" : "+v"(fa2), "+s"(mko)); }
      if constexpr (j == KN_S1C && g == 10) fetch();
      if constexpr (j == KN_S1C + 3 && g == -1) ent = __builtin_bit_cast(u32x4, ds_b128<0>(sld));
      if constexpr (j == KN_S1C + 3 && g == 5) {
        static_assert(SchedQ::cnt(j) <= SchedQ::tot(j - 1) - SchedQ::tot(KN_S1C) && KN_S1C >= 8, "table");
        asm volatile("" : "+v"(ent));
        auto stale = [&]() { return __builtin_amdgcn_ballot_w64(wave == 3 ? ((ent[0] ^ tag) | (ent[1] ^ tag) | (ent[2] ^ tag) | (ent[3] ^ tag)) > 1u : (int)ssrc >= 0 && (ent[2] != tag || ent[3] != ~tag)) != 0; };
        if (__builtin_expect(stale(), 0)) {
          unsigned polls = 0;
          do {
            fetch();
            wait_vm<0>();
            ent = __builtin_bit_cast(u32x4, ds_b128<0>(sld));
            asm volatile("s_waitcnt lgkmcnt(0)" : "+v"(ent) :: "memory");
          } while (stale() && ++polls < 4000000u);
        }
      }
      if constexpr (j == KN_S1C + 3 && g == 8) {
        ds_w32(smu, (int)ent[0]); ds_w32(srs, (int)ent[1]);
        if (wave == 3) {
          int pc = 0;
#pragma unroll
          for (int i = 0; i < 4; ++i) pc += __builtin_popcountll(__builtin_amdgcn_ballot_w64(ent[i] & 1));
          tag = (tag & ~1u) | (pc > 128 ? 1u : 0u);
          ds_w32(lds0 + QFL + 8, (int)tag);
        }
      }
      if constexpr (j == KN_S1C + 4 && g >= 0 && g % 3 == 2) {
        if (wave == 3) {
          __builtin_amdgcn_raw_buffer_store_b128((u32x4){~0u, ~0u, ~0u, ~0u}, __builtin_amdgcn_make_buffer_rsrc((void*)a.st, 0, 0x7fffffff, 0x00020000), l16 + g / 3 * 1024,
                                                 mko ^ ((KN_NUMA & 1) ? ((SIDE ^ (xcd >> 2)) & 1) << 13 : 0), 16);
          asm volatile("s_nop 1" ::: "memory");
        }
      }
      if constexpr (j == NT - 2 && g == -1) { const int lx = olane(); hm4 = __builtin_bit_cast(f32x4, ds_b128<0>(lds0 + QMU + (lx & 48))); hm1 = ds_r32(lds0 + QMU + (lx & 15) * 4); htg = ds_r32(lds0 + QFL + 8); }
      if constexpr (j == NT - 2 && g == 5) { asm volatile("" : "+v"(hm4), "+v"(hm1), "+v"(htg)); tag = __builtin_amdgcn_readfirstlane(htg); }
      if constexpr (j == NT - 3 && g == 8 && (KN_CT & 2)) { if (wave == 2) code_touch(lane * 32 + idx); }
    };
    static_assert(KN_S1C + 4 <= NT - 4 && SchedQ::cnt(NT - 1) <= SchedQ::tot(NT - 2) - SchedQ::tot(KN_S1C + 4) && KN_H0 == 0, "marks");
    unsigned tsq = 0;
    using SQ = SchedQ;
    static_for<0, NT>([&](auto jc) {
      constexpr int j = decltype(jc)::value, NX = (j + 2 < NT ? XI : 0) + SQ::ns(j), NW = SQ::wh(j) - SQ::wh(j - 1), NL = NX + NW + SQ::oh(j) - SQ::oh(j - 1);
      wait_vm<SQ::cnt(j)>();
      lds_barrier();
      bf16x8 xh[2]; s16x4 lo[2][3], hi[2][3];
      auto rd = [&](auto rc) { constexpr int r = decltype(rc)::value, q = r < 4 ? r : r - 2, n = q >> 2, h = (q >> 1) & 1, o = (j & 1) * QWU + h * 4096 + n * 8192;
        if constexpr (r == 4 || r == 5) xh[r - 4] = ds_b128<0>(r == 5 ? xh1[j % 3] : xh0[j % 3]);
        else if constexpr (q & 1) hi[h][n] = ds_tr<o + 512>(wb[(j >> 1) & 1]); else lo[h][n] = ds_tr<o>(wb[(j >> 1) & 1]); };
      auto mm = [&](auto ic) { constexpr int n = decltype(ic)::value;
        asm volatile("s_waitcnt lgkmcnt(%0)" :: "n"(n == 0 ? 6 : (n == 1 ? 4 : 0)) : "memory");
        asm volatile("" : "+v"(lo[0][n]), "+v"(hi[0][n]), "+v"(lo[1][n]), "+v"(hi[1][n]));
        if constexpr (n == 1) asm volatile("" : "+v"(xh[0]), "+v"(xh[1]));
#pragma unroll
        for (int h = 0; h < 2; ++h) {
          if constexpr (n == 0) acq = __builtin_amdgcn_mfma_f32_16x16x32_bf16(cat(lo[h][0], hi[h][0]), __builtin_bit_cast(bf16x8, one1), acq, 0, 0, 0);
          if constexpr (n == 1) ack = __builtin_amdgcn_mfma_f32_16x16x32_bf16(cat(lo[h][1], hi[h][1]), xh[h], ack, 0, 0, 0);
          if constexpr (n == 2) acv = __builtin_amdgcn_mfma_f32_16x16x32_bf16(xh[h], cat(lo[h][2], hi[h][2]), acv, 0, 0, 0);
        } };
      chore(jc, std::integral_constant<int, -1>{});
      constexpr int H0 = j < NT - 1 ? KN_H0 : 0, NG = NL > 10 + H0 ? NL : 10 + H0;
      if constexpr (KN_HS > 0) asm volatile("s_nop %0" :: "n"(KN_HS - 1));
      static_for<0, NG>([&](auto gc) {
        constexpr int g = decltype(gc)::value, h = g - H0;
        __builtin_amdgcn_sched_barrier(0);
        if constexpr (g < NX - SQ::ns(j)) ldX(gc, std::integral_constant<int, j + 2>{});
        else if constexpr (g < NX) {
          constexpr int q = 2 * j + g - XI;
          asm volatile("s_nop 4\n\tglobal_load_dwordx4 %0, %1, %2 offset:%3" : "=&v"(sv[q]) : "v"(l16), "s"(srows), "n"(q * 1024) : "memory");
        }
        else if constexpr (g < NX + NW) ldW(std::integral_constant<int, SQ::wh(j - 1) + g - NX>{});
        else if constexpr (g < NL) ldO(std::integral_constant<int, SQ::oh(j - 1) + g - NX - NW>{});
        else if constexpr (h < 9) asm volatile("s_nop %0" :: "n"(KN_HN));
        if constexpr (j <= 12 && g == KN_TG) { if (__builtin_expect((evm >> j) & 1, 0)) { touch((const void*)tb, tv, to); to += ts; } }
        if constexpr (g == (KN_PG < 0 ? NG - 1 : KN_PG) && j <= KN_PT && j >= KN_PT - 3 * KN_PS && (KN_PS == 0 || (KN_PT - j) % (KN_PS ? KN_PS : 1) == 0)) {
          if (KN_PS == 0 || wave == 3 - (KN_PT - j) / (KN_PS ? KN_PS : 1)) touch(wave & 1 ? a.CW1 : a.CW0, gv, 0);
        }
        __builtin_amdgcn_sched_barrier(0);
        if constexpr (h >= 0 && h < 7) { rd(std::integral_constant<int, 2 * h>{}); rd(std::integral_constant<int, 2 * h + 1>{}); }
        if constexpr (h == 4 || h == 7 || h == 9) mm(std::integral_constant<int, h == 4 ? 0 : (h == 7 ? 1 : 2)>{});
        __builtin_amdgcn_sched_barrier(0);
        chore(jc, gc);
      });
      __builtin_amdgcn_sched_barrier(0);
    });
    {
      f32x4 ck;
#pragma unroll
      for (int e = 0; e < 4; ++e) ck[e] = dpp(ack[e], std::integral_constant<int, 0x150>{});
      const auto c1 = __builtin_amdgcn_permlane16_swap(__float_as_uint(acv[0]), __float_as_uint(acv[0]), false, false);
      const auto c2 = __builtin_amdgcn_permlane32_swap(c1[0], c1[0], false, false);
      const float nm = __int_as_float(hm1), nc = __uint_as_float(c2[0]);
      asm volatile("ds_write_b128 %0, %1\n\tds_write_b128 %0, %2 offset:256\n\tds_write_b32 %3, %4" :: "v"(fa0), "v"(acq), "v"(ck), "v"(fa1), "v"(nc) : "memory");
      const u32x2 kk = {pk_bf16(fma1(nm, ck[0], ack[0]), fma1(nm, ck[1], ack[1])), pk_bf16(fma1(nm, ck[2], ack[2]), fma1(nm, ck[3], ack[3]))};
      const u32x2 vv = {pk_bf16(fma1(nc, hm4[0], acv[0]), fma1(nc, hm4[1], acv[1])), pk_bf16(fma1(nc, hm4[2], acv[2]), fma1(nc, hm4[3], acv[3]))};
      asm volatile("ds_write_b64 %0, %1 offset:%3\n\tds_write_b64 %0, %2 offset:%4" :: "v"(fa2), "v"(kk), "v"(vv), "n"(QKH), "n"(QVH) : "memory");
    }
    lds_barrier();
    if (tid == 192) set_flag(a.sync + FHM((xcd >> 1) * 2 + (idx >> 4)) + (xcd & 1) * 16 + (idx & 15), tag);
    static_for<SQ::oh(NT - 1), 2 * KN_QO4>(ldO);
    wait_vm<2 * (KN_QO4 - 2)>();
    const int sd = SIDE;
    unsigned fo = tid == 0 ? (unsigned)(FLO(tm) + tn * 32) * 4u : 0x80000000u, ft = tag;
    const auto rF = __builtin_amdgcn_make_buffer_rsrc((void*)a.sync, 0, 147456, 0x00020000);
    asm volatile("" : "+v"(fo), "+v"(ft) : "s"(rF));
    raw_barrier();
    if (wave == 0) __builtin_amdgcn_raw_buffer_store_b32(ft, rF, fo, 0, 16);
    static_for<2 * KN_QO4, 32>(ldO);
  } else {
    const int wm = (wave >> 1) & 1, hd = wave & 1;
    if (wave == 4) {
      if (lane < 4) { *(unsigned*)(smem + QRING + lane * 4) = 0x3f803f80u; if (lane < 2) *(unsigned*)(smem + QFL + lane * 4) = 0u; }
      if (KN_FW) __builtin_amdgcn_raw_ptr_buffer_load_lds(__builtin_amdgcn_make_buffer_rsrc((void*)a.sync, 0, 147456, 0x00020000), (lds_void_ptr)(size_t)(lds0 + QTCH), 4, lane * 32768 + bid * 128, 0, 0, 16);
    }
    if (KN_X2 && wave == 5 && bid < 32 * KN_X2) touch(a.X, (unsigned)((bid * (64 / (KN_X2 ? KN_X2 : 1)) + lane / (KN_X2 ? KN_X2 : 1)) * (K * 2) + 256 + lane % (KN_X2 ? KN_X2 : 1) * 128), 0);
    if (wave == 6) { hset(); if (hj1 < 0 && hj1 > -100) touch(a.W0, htv, (hj1 + KN_HQ) * (64 * LDW * 2));
      if (KN_G0 && bid >= KN_G0 * 48) touch(a.W0, (bid * 64 + lane) * 128, 0); }
    if ((KN_CT & 1) && wave == 7) code_touch(lane * 32 + idx);
    {
      const int id = bid * 2 + wm;
      const char* pb = (const char*)a.sync + PRB + (id >> 3) * 16384 + hd * 8192 + (id & 7) * (KN_PN == 8 ? 128 : 256);
      unsigned long long p0, p1; unsigned t;
#define SL4(a, b) "s_load_dword %1, %3, 0x" a "00\n\ts_load_dword %1, %3, 0x" b "00\n\ts_load_dword %1, %3, 0x1" a "00\n\ts_load_dword %1, %3, 0x1" b "00\n\t"
      if (KN_PN == 8) asm volatile("s_sleep %5\n\ts_memtime %0\n\ts_waitcnt lgkmcnt(0)\n\t" SL4("0", "8") SL4("4", "c") "s_waitcnt lgkmcnt(%4)\n\ts_memtime %2\n\ts_waitcnt lgkmcnt(0)" : "=&s"(p0), "=&s"(t), "=&s"(p1) : "s"(pb), "n"(KN_PQ), "n"(KN_PZ));
      else asm volatile("s_sleep %5\n\ts_memtime %0\n\ts_waitcnt lgkmcnt(0)\n\t" SL4("0", "8") "s_waitcnt lgkmcnt(%4)\n\ts_memtime %2\n\ts_waitcnt lgkmcnt(0)" : "=&s"(p0), "=&s"(t), "=&s"(p1) : "s"(pb), "n"(KN_PQ), "n"(KN_PZ));
      ds_w32(lds0 + QFL + 16 + (wave - 4) * 4, (int)((unsigned)p1 - (unsigned)p0));
    }
    f32x2 bias[2], bias0[2];
    {
      const int srow = m0 + wm * 64 + fr;
      const unsigned mk = 0x5555u & ((2u << (srow < 15 ? srow : 15)) - 1u), sft = (fr & 14) - fq * 4 + 14;
      const unsigned b0 = ~(0x5555u >> sft), b1 = ~(0x55550000u >> sft), c0 = ~(mk >> sft), c1 = ~((mk << 16) >> sft);
#pragma unroll
      for (int j = 0; j < 4; ++j) {
        bias[j >> 1][j & 1] = __uint_as_float((unsigned)__builtin_amdgcn_sbfe((int)(j < 2 ? b0 : b1), 2 - 2 * (j & 1), 1) & 0xff61b1e6u);
        bias0[j >> 1][j & 1] = __uint_as_float((unsigned)__builtin_amdgcn_sbfe((int)(j < 2 ? c0 : c1), 2 - 2 * (j & 1), 1) & 0xff61b1e6u);
      }
    }
    unsigned pl = (fr & 1) ? 0x01000c0cu : 0x0c0c0100u, ph = (fr & 1) ? 0x03020c0cu : 0x0c0c0302u;
    unsigned ta = lds0 + QRING + STL + (16 + wm * 64 + fr) * 16, tm4 = lds0 + QMU + (16 + wm * 64 + fq * 4) * 4, trk = lds0 + QRS + (fr & 1) * 288 + (wm * 32 + fq * 2) * 4;
    unsigned tcs = lds0 + hd * 128 + fq * 16, tcv = lds0 + QCS + 512 + hd * 128 + fr * 4, tex = lds0 + hd * 1024 + lane * 16, tfl = lds0 + QFL + hd * 4, odst = rbH((m0 >> 4) + wm * 4, xcd >> 2) + omH(tn * 2048) + hd * 1024 + lane * 16;
    asm volatile("" : "+v"(bias[0]), "+v"(bias[1]), "+v"(bias0[0]), "+v"(bias0[1]), "+v"(pl), "+v"(ph), "+v"(ta), "+v"(tm4), "+v"(trk), "+v"(tcs), "+v"(tcv), "+v"(tex), "+v"(tfl), "+v"(odst));
    f32x4 acc[6][4];
#pragma unroll
    for (int n = 0; n < 6; ++n)
#pragma unroll
      for (int m = 0; m < 4; ++m) acc[n][m] = z4;
    const unsigned xa[2] = {lds0 + (16 + wm * 64) * 128 + xo, lds0 + (16 + wm * 64) * 128 + (xo ^ 64)}, wa[2] = {wfrag(hd * 2), wfrag(hd * 2 + 1)};
    struct Ctx { unsigned x[2], w[2]; } c = {{xa[0], xa[1]}, {wa[0], wa[1]}};
    using SP = SchedP<6, KN_QS>;
    FragQ fa, fb;
    auto rd = [&](auto rc) {
      constexpr int r = decltype(rc)::value;
      if constexpr (r >= 24) { constexpr int m = (r - 24) >> 1; if constexpr (r & 1) fb.x[m] = ds_b128<m * 2048>(c.x[1]); else fa.x[m] = ds_b128<m * 2048>(c.x[0]); }
      else { constexpr int n = r >> 2, o = ((r >> 1) & 1) * 4096 + (n >> 1) * 8192 + (r & 1) * 512; FragQ& f = (r & 2) ? fb : fa;
        if constexpr (r & 1) f.hi[n] = ds_tr<o>(c.w[n & 1]); else f.lo[n] = ds_tr<o>(c.w[n & 1]); }
    };
    auto pin = [&](auto pc) {
      static_for<0, 32>([&](auto rc) { constexpr int r = decltype(rc)::value;
        if constexpr (SP::need(r) == decltype(pc)::value) {
          FragQ& f = (r >= 24 ? r & 1 : r & 2) ? fb : fa;
          if constexpr (r >= 24) { bf16x8& q = f.x[(r - 24) >> 1]; asm volatile("" : "+v"(q)); } else { s16x4& q = (r & 1) ? f.hi[r >> 2] : f.lo[r >> 2]; asm volatile("" : "+v"(q)); } } });
    };
    int sx = QXT, sw = QWU;
    auto pairs = [&](auto PBc, auto PEc, auto RDc, auto&& gap) {
      constexpr bool RD = decltype(RDc)::value;
      static_for<decltype(PBc)::value, decltype(PEc)::value>([&](auto pc) {
        constexpr int p = decltype(pc)::value, A = SP::pa(p), n = SP::pn(A), m = SP::pm(A);
        if constexpr (SP::cnt(p) >= 0) { asm volatile("s_waitcnt lgkmcnt(%0)" :: "n"(SP::cnt(p)) : "memory"); pin(pc); }
        static_for<2 * p, 2 * p + 2>([&](auto gc) {
          constexpr int g = decltype(gc)::value;
          FragQ& u = ((A ^ g) & 1) ? fb : fa;
          if constexpr (n < 4) acc[n][m] = __builtin_amdgcn_mfma_f32_16x16x32_bf16(cat(u.lo[n], u.hi[n]), u.x[m], acc[n][m], 0, 0, 0);
          else acc[n][m] = __builtin_amdgcn_mfma_f32_16x16x32_bf16(u.x[m], cat(u.lo[n], u.hi[n]), acc[n][m], 0, 0, 0);
          __builtin_amdgcn_sched_barrier(0);
          gap(gc);
          if constexpr (RD) {
            static_for<0, 32>([&](auto rc) { if constexpr (SP::gap(decltype(rc)::value) == g) rd(rc); });
            if constexpr (g == 13) { sx = sx + QXT; sw = sw + QWU; asm volatile("" : "+s"(sx), "+s"(sw)); }
            if constexpr (g == 17) { sx = sx == 3 * QXT ? 0 : sx; asm volatile("" : "+s"(sx)); }
            if constexpr (g == 21) { sw = sw == 4 * QWU ? 0 : sw; asm volatile("" : "+s"(sw)); }
            if constexpr (g == 44) { c.x[0] = xa[0] + sx; c.x[1] = xa[1] + sx; asm volatile("" : "+v"(c.x[0]), "+v"(c.x[1])); }
            if constexpr (g == 45) { c.w[0] = wa[0] + sw; c.w[1] = wa[1] + sw; asm volatile("" : "+v"(c.w[0]), "+v"(c.w[1])); }
          }
          __builtin_amdgcn_sched_barrier(0);
        });
      });
      if constexpr (RD) { asm volatile("s_waitcnt lgkmcnt(0)" ::: "memory"); pin(std::integral_constant<int, 24>{}); }
    };
    static_assert(SP::gap(23) <= 44, "addresses");
    using I12 = std::integral_constant<int, 12>; using I24 = std::integral_constant<int, 24>;
    lds_barrier();
    static_for<0, 24>([&](auto gc) { static_for<0, 32>([&](auto rc) { if constexpr (SP::gap(decltype(rc)::value) == decltype(gc)::value) rd(rc); }); });
    STAMP(1);
    pairs(I12{}, I24{}, T{}, [&](auto) {});
#pragma unroll 1
    for (int j = 1; j < NT; ++j) pairs(I0{}, I24{}, T{}, [&](auto pc) { if constexpr (decltype(pc)::value == 1) raw_barrier(); });
#define ES(i)
    ES(0);
    if (KN_QP) __builtin_amdgcn_s_setprio(KN_QP);
    f32x2 st[4], rk[5], cv, fs[4][2]; f32x4 m4[4], cs[4]; int tg;
    pairs(I0{}, I12{}, F{}, [&](auto ic) { constexpr int I = decltype(ic)::value;
      if constexpr (I < 4) st[I] = ds_b64<I * 256>(ta);
      if constexpr (I == 4) tg = ds_r32(lds0 + QFL + 8);
      if constexpr (I < 5) rk[I] = ds_b64<I * 32>(trk);
      if constexpr (I >= 5 && I < 9) m4[I - 5] = __builtin_bit_cast(f32x4, ds_b128<(I - 5) * 64>(tm4));
      if constexpr (I == 9) { asm volatile("s_waitcnt lgkmcnt(4)" : "+v"(st[0]), "+v"(st[1]), "+v"(st[2]), "+v"(st[3]), "+v"(rk[0]), "+v"(rk[1]), "+v"(rk[2]), "+v"(rk[3]), "+v"(rk[4]) :: "memory"); }
      if constexpr (I == 9 || I == 10) { constexpr int m = (I - 9) * 2; const float sc2 = 0.17677669529663687f * 1.4426950408889634f; st[m][1] *= sc2; st[m + 1][1] *= sc2; asm volatile("" : "+v"(st[m]), "+v"(st[m + 1])); }
      if constexpr (I >= 11 && I < 19) { constexpr int m = (I - 11) >> 1, h = (I - 11) & 1; fs[m][h] = rk[m + h] * st[m][1]; asm volatile("" : "+v"(fs[m][h])); }
      if constexpr (I == 19) { int& q = tg; asm volatile("" : "+v"(q)); tag = __builtin_amdgcn_readfirstlane(tg); if (KN_NUMA & 1) odst ^= ((SIDE ^ (xcd >> 2)) & 1) << 13; asm volatile("" : "+v"(odst)); }
      if constexpr (I == 4) ES(1);
      if constexpr (I == KN_QLB) { ES(2); raw_barrier(); ES(3); }
      if constexpr (I >= 14 && I < 18) { constexpr int q = (I - 14) ^ 2; cs[q] = __builtin_bit_cast(f32x4, ds_b128<QCS + (q >> 1) * 256 + (q & 1) * 64>(tcs)); }
      if constexpr (I == 18) cv = ds_r2(tcv); });
    static_assert(KN_QLB <= 14, "barrier");
    STAMP(2);
    ES(4);
    asm volatile("s_waitcnt lgkmcnt(0)" : "+v"(cs[0]), "+v"(cs[1]), "+v"(cs[2]), "+v"(cs[3]), "+v"(cv), "+v"(m4[0]), "+v"(m4[1]), "+v"(m4[2]), "+v"(m4[3]) :: "memory");
    u32x4 Q[4], Kf[5], V[5];
    auto nrm = [&](f32x4 x, f32x4 y, float sc) {
      const f32x2 p = (f32x2){y[0], y[1]} * sc + (f32x2){x[0], x[1]}, q = (f32x2){y[2], y[3]} * sc + (f32x2){x[2], x[3]};
      return (u32x2){pk_bf16(p[0], p[1]), pk_bf16(q[0], q[1])}; };
    auto c4 = [](u32x2 p, u32x2 q) { return (u32x4){p[0], p[1], q[0], q[1]}; };
    auto mkK = [&](int m) { Kf[m + 1] = c4(nrm(acc[2][m], cs[2], st[m][0]), nrm(acc[3][m], cs[3], st[m][0])); };
    auto mkQ = [&](int m) { Q[m] = c4(nrm(acc[0][m], cs[0], st[m][0]), nrm(acc[1][m], cs[1], st[m][0])); };
    auto mkV = [&](int m) { V[m + 1] = c4(nrm(acc[4][m], m4[m], cv[0]), nrm(acc[5][m], m4[m], cv[1])); };
    mkK(3); mkV(3);
    if (wm == 0) asm volatile("ds_write_b128 %0, %1 offset:%5\n\tds_write_b128 %0, %2 offset:%6\n\tds_write_b32 %3, %4" :: "v"(tex), "v"(Kf[4]), "v"(V[4]), "v"(tfl), "v"(pl), "n"(QKH + 2048), "n"(QVH + 2048) : "memory");
    __builtin_amdgcn_sched_barrier(0);
    f32x4 sc[4][2]; f32x2 lg[4][2]; float mx[4];
    auto qk = [&](int m, int h) { sc[m][h] = __builtin_amdgcn_mfma_f32_16x16x32_bf16(__builtin_bit_cast(bf16x8, Kf[m + h]), __builtin_bit_cast(bf16x8, Q[m]), z4, 0, 0, 0); };
    mkQ(3); mkK(2); qk(3, 1); qk(3, 0); mkQ(2); mkK(1); qk(2, 1); qk(2, 0); mkQ(1); mkK(0); qk(1, 1); qk(1, 0);
    __builtin_amdgcn_sched_barrier(0);
    int fl; bf16x8 k0, v0;
    tex += wm * 2048;
    auto front = [&]() { fl = ds_r32(tfl); k0 = ds_b128<QKH>(tex); v0 = ds_b128<QVH>(tex); };
    front();
    __builtin_amdgcn_sched_barrier(0);
    mkQ(0); qk(0, 1); mkV(2); mkV(1);
    __builtin_amdgcn_sched_barrier(0);
    asm volatile("s_waitcnt lgkmcnt(0)" : "+v"(fl), "+v"(k0), "+v"(v0), "+v"(V[3]), "+v"(V[2]) :: "memory");
    if (__builtin_expect(wm && __builtin_amdgcn_readfirstlane(fl) == 0, 0)) {
      unsigned polls = 0;
      do { front(); asm volatile("s_waitcnt lgkmcnt(0)" : "+v"(fl), "+v"(k0), "+v"(v0) :: "memory"); } while (__builtin_amdgcn_readfirstlane(fl) == 0 && ++polls < 40000000u);
    }
    Kf[0] = __builtin_bit_cast(u32x4, k0); V[0] = __builtin_bit_cast(u32x4, v0);
    qk(0, 0); mkV(0);
    STAMP(4);
    __builtin_amdgcn_sched_barrier(0); ES(5);
    __builtin_amdgcn_sched_barrier(0);
    const bool par = pl & 4;
#pragma unroll
    for (int m = 3; m >= 0; --m) {
#pragma unroll
      for (int h = 0; h < 2; ++h) {
        float e[4] = {sc[m][h][0], sc[m][h][1], sc[m][h][2], sc[m][h][3]};
        asm("" : "+v"(e[0]), "+v"(e[2]));
        lg[m][h] = (f32x2){par ? e[1] : e[0], par ? e[3] : e[2]} * fs[m][h] + (m == 0 ? bias0[h] : bias[h]);
      }
      mx[m] = fmaxf(fmaxf(lg[m][0][0], lg[m][0][1]), fmaxf(lg[m][1][0], lg[m][1][1]));
    }
    __builtin_amdgcn_sched_barrier(0);
    auto s16 = [](float x, float y) { auto p = __builtin_amdgcn_permlane16_swap(__float_as_uint(x), __float_as_uint(y), false, false); return (f32x2){__uint_as_float(p[0]), __uint_as_float(p[1])}; };
    auto s32 = [](float x, float y) { auto p = __builtin_amdgcn_permlane32_swap(__float_as_uint(x), __float_as_uint(y), false, false); return (f32x2){__uint_as_float(p[0]), __uint_as_float(p[1])}; };
    auto spread = [&](float r) { const f32x2 t = s32(r, r), u = s16(t[0], t[0]), w = s16(t[1], t[1]); mx[0] = u[0]; mx[1] = u[1]; mx[2] = w[0]; mx[3] = w[1]; };
    { const f32x2 p = s16(mx[0], mx[1]), q = s16(mx[2], mx[3]), r = s32(fmaxf(p[0], p[1]), fmaxf(q[0], q[1])); spread(fmaxf(r[0], r[1])); }
    __builtin_amdgcn_sched_barrier(0);
#pragma unroll
    for (int m = 3; m >= 0; --m) {
#pragma unroll
      for (int h = 0; h < 2; ++h) { const f32x2 d = lg[m][h] - mx[m]; lg[m][h] = (f32x2){__builtin_amdgcn_exp2f(d[0]), __builtin_amdgcn_exp2f(d[1])}; }
      const f32x2 t = lg[m][0] + lg[m][1];
      mx[m] = t[0] + t[1];
    }
    __builtin_amdgcn_sched_barrier(0);
    { const f32x2 p = s16(mx[0], mx[1]), q = s16(mx[2], mx[3]);
#pragma unroll
      for (int m = 3; m >= 0; --m) lg[m][0] = lg[m][0] * rk[m];
      const f32x2 r = s32(p[0] + p[1], q[0] + q[1]);
#pragma unroll
      for (int m = 3; m >= 0; --m) lg[m][1] = lg[m][1] * rk[m + 1];
      spread(__builtin_amdgcn_rcpf(r[0] + r[1])); }
    __builtin_amdgcn_sched_barrier(0);
    auto half = [](u32x4 v, int h) { return __builtin_bit_cast(s16x4, (u32x2){v[2 * h], v[2 * h + 1]}); };
#pragma unroll
    for (int m = 3; m >= 0; --m) {
      s16x4 pf[2];
#pragma unroll
      for (int h = 0; h < 2; ++h) { const f32x2 t = lg[m][h] * mx[m]; const unsigned u = pk_bf16(t[0], t[1]); pf[h] = __builtin_bit_cast(s16x4, (u32x2){__builtin_amdgcn_perm(0u, u, pl), __builtin_amdgcn_perm(0u, u, ph)}); }
#pragma unroll
      for (int f = 0; f < 2; ++f) sc[m][f] = __builtin_amdgcn_mfma_f32_16x16x16bf16_1k(half(V[m], f), pf[0], z4, 0, 0, 0);
#pragma unroll
      for (int f = 0; f < 2; ++f) sc[m][f] = __builtin_amdgcn_mfma_f32_16x16x16bf16_1k(half(V[m + 1], f), pf[1], sc[m][f], 0, 0, 0);
    }
    __builtin_amdgcn_sched_barrier(0);
    STAMP(10);
    ES(6);
    const auto rO = __builtin_amdgcn_make_buffer_rsrc((void*)a.O, 0, 0x7fffffff, 0x00020000);
    u32x4 ov[4];
#pragma unroll
    for (int m = 3; m >= 0; --m) {
      ov[m] = (u32x4){pk_bf16(sc[m][0][0], sc[m][0][1]), pk_bf16(sc[m][0][2], sc[m][0][3]), pk_bf16(sc[m][1][0], sc[m][1][1]), pk_bf16(sc[m][1][2], sc[m][1][3])};
      __builtin_amdgcn_raw_buffer_store_b128(ov[m], rO, odst, m * RSH, 16);
    }
    asm volatile("" :: "v"(ov[0]), "v"(ov[1]), "v"(ov[2]), "v"(ov[3]));
    if (KN_QP) __builtin_amdgcn_s_setprio(0);
    STAMP(7);
    ES(7);
    wait_vm<0>();
    STAMP(8);
    ES(8);
    raw_barrier();
    ES(9);
  }
  STAMP(3);
#endif
  return tag;
}

struct FragW { s16x4 lo[4], hi[4]; };
__device__ __forceinline__ void wo_phase(const GemmArgs& a, const GemmArgs& nx, unsigned tag) {
#if defined(__HIP_DEVICE_COMPILE__)
  extern __shared__ __attribute__((aligned(16))) char smem[];
  STAMP(0);
  const unsigned lds0 = (unsigned)(size_t)(lds_char_ptr)smem;
  const int tid = threadIdx.x, lane = tid & 63;
  const int wave = __builtin_amdgcn_readfirstlane(tid >> 6);
  const int bid = blockIdx.x, xcd = bid & 7, idx = bid >> 3;
  const int tm = (xcd >> 1) * 4 + (idx >> 3), tn = (xcd & 1) * 8 + (idx & 7);
  const int m0 = tm * 128, panel = m0 >> 8, rot = (xcd & 3) * 4, sd = SIDE;
  {
    constexpr int PAD = QTCH;
    const int g = wave >> 2, rw = wave & 3;
    const int fr = lane & 15, fq = lane >> 4, wq = (lane >> 2) & 3, wp = lane & 3;
    unsigned wa[4];
#pragma unroll
    for (int c = 0; c < 4; ++c) { const int row = 4 * fq + wq; wa[c] = lds0 + g * 8192 + row * 128 + (((c << 1) ^ (row & 6)) << 4) + wp * 8; }
    FragW f[3];
    auto rd = [&](auto ic, auto rc) {
      constexpr int i = decltype(ic)::value, r = decltype(rc)::value, P = wo_pos(i >> 1) + (i & 1) * 4096, B = P & ~32767, o = P & 32767;
      if constexpr (r & 1) f[i % 3].hi[r >> 1] = ds_tr<o + 2048>(wa[r >> 1] + B); else f[i % 3].lo[r >> 1] = ds_tr<o>(wa[r >> 1] + B);
    };
    auto pin = [&](FragW& q) {
#pragma unroll
      for (int n = 0; n < 4; ++n) asm volatile("" : "+v"(q.lo[n]), "+v"(q.hi[n]));
    };
    static_for<0, 16>([&](auto rc) { constexpr int r = decltype(rc)::value; rd(std::integral_constant<int, r / 8>{}, std::integral_constant<int, r % 8>{}); });
    if constexpr (KN_CT & 4) { if (wave == 2) {
      unsigned long long c0, c1; CODE_ADDR(c0, "k_all"); CODE_ADDR(c1, "k_zend");
      wait_vm<0>();
      __builtin_amdgcn_raw_ptr_buffer_load_lds(__builtin_amdgcn_make_buffer_rsrc((void*)c0, 0, c1 - c0 < 262144ull ? (unsigned)(c1 - c0) : 0u, 0x00020000), (lds_void_ptr)(size_t)(lds0 + PAD), 4, (unsigned)((lane * 32 + idx) * 128), 0, 0, 0); } }
    if (wave == 1) wait_flags(a.sync + FLO(tm), (lane & 15) * 128, tag, 0xffffull);
    if constexpr (KN_E & 2) {
      if (wave == 3) {
        wait_flags(a.sync, (FHM(lane >= 32 ? (xcd >> 2) * 4 + (idx >> 3) : panel) + (lane & 31)) * 4, tag, ~0ull);
      }
    }
    const auto rX = __builtin_amdgcn_make_buffer_rsrc((void*)a.X, 0, 0x7fffffff, 0x00020000);
    const int rb = (m0 >> 4) + rw * 2 + g;
    unsigned xs[2] = {(unsigned)(rbH(rb, sd) + lane * 16), (unsigned)(rbH(rb ^ 1, sd) + lane * 16)};
    const int ck = (fq & 1) * 2 + (fq >> 1);
    unsigned orow = (unsigned)((rb * 16 + fr) * (D_ * 2) + tn * 128 + ck * 16), ofrg = (unsigned)(rbH(rb, sd) + omH(tn * 2048) + ck * 256 + fr * 16);
    asm volatile("" : "+v"(xs[0]), "+v"(xs[1]), "+v"(orow), "+v"(ofrg));
    STAMP(9);
    if constexpr (!KN_B3) { if (!(KN_CT & 4) || wave != 2) wait_vm<0>(); }
    lds_barrier();
    STAMP(6);
    pin(f[0]); pin(f[1]);
    u32x4 xr[16][2], res[2];
#define KN_XD 4
    auto ldX = [&](int i, int m) {
      if (i < 16) xr[i][m] = __builtin_amdgcn_raw_buffer_load_b128(rX, xs[m] + (i & 1) * 1024, omH((i >> 1) * 2048) ^ omH(((g * 8) ^ rot) * 2048), XAUXW);
      else if (i == 16) res[m] = __builtin_amdgcn_raw_buffer_load_b128(__builtin_amdgcn_make_buffer_rsrc((void*)a.R, 0, 0x7fffffff, 0x00020000), orow + m * 64, 0, 0);
    };
#pragma unroll
    for (int i = 0; i < KN_XD; ++i) { ldX(i, 0); ldX(i, 1); __builtin_amdgcn_sched_barrier(0); }
    unsigned gsrc[2];
#pragma unroll
    for (int i = 0; i < 2; ++i) {
      const int j = wave + 8 * i, jj = j >= 7 ? j - 7 : j;
      const int b = jj * 1024 + lane * 16, q = b / 224, pos = (b % 224) >> 4;
      gsrc[i] = (unsigned)((8 * ((q & 15) >> 2) + (q & 3) + 4 * (q >> 4)) * DF_ + ((xcd & 3) * 8 + (idx & 7)) * 112 + pos * 8) * 2u;
      asm volatile("" : "+v"(gsrc[i]));
    }
    f32x4 acc[4][2];
#pragma unroll
    for (int n = 0; n < 4; ++n) { acc[n][0] = (f32x4){0.f, 0.f, 0.f, 0.f}; acc[n][1] = acc[n][0]; }
    __builtin_amdgcn_sched_barrier(0);
    static_for<0, 16>([&](auto ic) {
      constexpr int i = decltype(ic)::value;
      if constexpr (i >= 2) { asm volatile("s_waitcnt lgkmcnt(%0)" :: "n"(i < 15 ? 8 : 0) : "memory"); pin(f[i % 3]); }
      if constexpr (KN_B3 && i == (KN_WB & ~1) - 2) raw_barrier();
      static_for<0, 8>([&](auto jc) {
        constexpr int j = decltype(jc)::value, n = j >> 1, m = j & 1;
        const bf16x8 w = __builtin_shufflevector(f[i % 3].lo[n], f[i % 3].hi[n], 0, 1, 2, 3, 4, 5, 6, 7);
        acc[n][m] = __builtin_amdgcn_mfma_f32_16x16x32_bf16(w, __builtin_bit_cast(bf16x8, xr[i][m]), acc[n][m], 0, 0, 0);
        if constexpr (i + 2 < 16) rd(std::integral_constant<int, i + 2>{}, jc);
        if constexpr (j == 1 || j == 5) ldX(i + KN_XD, j >> 2);
        __builtin_amdgcn_sched_barrier(0);
      });
      if constexpr (i == 0) STAMP(1);
    });
    STAMP(2);
    auto slot = [&](int w) { return 32768 + w * 4096 + lane * 16; };
#pragma unroll
    for (int n = 0; n < 4; ++n) *(f32x4*)(smem + slot(wave) + n * 1024) = acc[n][1];
    asm volatile("" : "+v"(res[0]), "+v"(res[1]));
    lds_barrier();
    STAMP(4);
    f32x4 pa[4];
    static_for<0, 4>([&](auto nc) { constexpr int n = decltype(nc)::value; pa[n] = __builtin_bit_cast(f32x4, ds_b128<n * 1024>(lds0 + slot(wave ^ 4))); });
    auto ldGs = [&]() {
      auto ldG = [&](int i, int u) { const int j = wave + 8 * i;
        __builtin_amdgcn_raw_ptr_buffer_load_lds(__builtin_amdgcn_make_buffer_rsrc((void*)(j >= 7 ? nx.W1 : nx.W0), 0, 0x7fffffff, 0x00020000),
                          (lds_void_ptr)(size_t)(lds0 + 98304 + (u ^ 2) * 14592 + (j >= 7 ? 7184 + (j - 7) * 1024 : j * 1024)), 16, gsrc[i], (u + (xcd >> 2) * 16) * 32 * DF_ * 2, 0, 0); };
      ldG(0, 0); ldG(0, 1);
      if (wave < 6) { ldG(1, 0); ldG(1, 1); }
    };
    if (!KN_GL) ldGs();
    asm volatile("s_waitcnt lgkmcnt(0)" : "+v"(pa[0]), "+v"(pa[1]), "+v"(pa[2]), "+v"(pa[3]) :: "memory");
    const auto rO = __builtin_amdgcn_make_buffer_rsrc((void*)a.O, 0, 0x7fffffff, 0x00020000);
    u32x4 o[2];
#pragma unroll
    for (int i = 0; i < 2; ++i) {
      const f32x4 u = acc[2 * i][0] + pa[2 * i], v = acc[2 * i + 1][0] + pa[2 * i + 1];
      const auto s0 = __builtin_amdgcn_permlane16_swap(pk_bf16(u[0], u[1]), pk_bf16(v[0], v[1]), false, false);
      const auto s1 = __builtin_amdgcn_permlane16_swap(pk_bf16(u[2], u[3]), pk_bf16(v[2], v[3]), false, false);
      const u32x4 q4 = {s0[0], s1[0], s0[1], s1[1]};
#pragma unroll
      for (int j = 0; j < 4; ++j) { const unsigned r = res[i][j], q = q4[j]; o[i][j] = pk_bf16(bflo(r) + bflo(q), bfhi(r) + bfhi(q)); }
      __builtin_amdgcn_raw_buffer_store_b128(o[i], rO, ofrg + i * 1024, 0, 16);
    }
    if (KN_GL) { __builtin_amdgcn_sched_barrier(0); asm volatile("s_nop 1" ::: "memory"); ldGs(); __builtin_amdgcn_sched_barrier(0); }
    auto sum4 = [](float v) {
      auto p = __builtin_amdgcn_permlane16_swap(__float_as_uint(v), __float_as_uint(v), false, false); v = __uint_as_float(p[0]) + __uint_as_float(p[1]);
      auto q = __builtin_amdgcn_permlane32_swap(__float_as_uint(v), __float_as_uint(v), false, false); return __uint_as_float(q[0]) + __uint_as_float(q[1]); };
    f32x2 e[8], sm = {0.f, 0.f};
#pragma unroll
    for (int j = 0; j < 8; ++j) { const unsigned q = o[j >> 2][j & 3]; e[j] = (f32x2){bflo(q), bfhi(q)}; sm += e[j]; }
    const float mu = sum4(sm[0] + sm[1]) * (1.f / 64.f);
    f32x2 t2 = {0.f, 0.f};
#pragma unroll
    for (int j = 0; j < 8; ++j) { const f32x2 d = e[j] - mu; t2 += d * d; }
    const float q2 = sum4(t2[0] + t2[1]);
    __builtin_amdgcn_raw_buffer_store_b64((u32x2){__float_as_uint(mu), __float_as_uint(q2)}, __builtin_amdgcn_make_buffer_rsrc((void*)a.st, 0, 0x7fffffff, 0x00020000),
                                          lane < 16 ? (unsigned)((tn * S_ + rb * 16 + lane) * 8) : 0x80000000u, 0, 16);
    STAMP(7);
    STAMP(8);
    unsigned fo = lane == 0 ? (unsigned)(FHF(panel) + ((tm & 1) * 16 + tn) * 32) * 4u : 0x80000000u, ft = tag;
    unsigned gx = rbH(((xcd >> 2) * 4 + (idx >> 3)) * 16, sd) + (rw >> 1) * RSH + (rw & 1) * 1024 + lane * 16, gm = lds0 + rw * 1024, gs = omH((xcd >> 2) * 16384);
    const auto rF = __builtin_amdgcn_make_buffer_rsrc((void*)a.sync, 0, 147456, 0x00020000);
    const u32x4 qH = mk_rsrc(a.O);
    asm volatile("s_waitcnt vmcnt(0) lgkmcnt(0)" : "+v"(fo), "+v"(ft), "+v"(gx), "+s"(gm), "+s"(gs) : "s"(rF), "s"(qH) : "memory");
    raw_barrier();
    if (wave == 4) __builtin_amdgcn_raw_buffer_store_b32(ft, rF, fo, 0, 16);
    if (KN_GX && wave < 4) {
      if (KN_GX > 1) __builtin_amdgcn_s_sleep(KN_GX - 1);
      static_for<0, 8>([&](auto ic) { constexpr int i = decltype(ic)::value; ld_ca<i * 4096, i * 2 * RSH, false, true>(gm, qH, gx, gs); });
    }
    STAMP(3);
  }
#endif
}

struct FragE { bf16x8 x[4]; s16x4 lo[4], hi[4]; };
template <int MODE, int HTD, int TA, int TB, int TL = 0>
__device__ __forceinline__ void dn_phase(const GemmArgs& a, unsigned tag) {
#if defined(__HIP_DEVICE_COMPILE__)
  constexpr int SX = 5, SW = 4, WB = SX * 16384, PAD = WB + SW * 16384, V = KN_V;
  static_assert(TA == 2 && TB == 2, "schedule");
  extern __shared__ __attribute__((aligned(16))) char smem[];
  if constexpr (MODE != 1) STAMP(0);
  const unsigned lds0 = (unsigned)(size_t)(lds_char_ptr)smem;
  const int tid = threadIdx.x;
  int lane = tid & 63; asm volatile("" : "+v"(lane));
  const int wave = __builtin_amdgcn_readfirstlane(tid >> 6);
  const int bid = blockIdx.x, xcd = bid & 7, idx = bid >> 3;
  const int tm = (xcd >> 1) * 4 + (idx >> 3), tn = (idx & 7) * 2 + (xcd & 1), A = (xcd & 1) ? 24 - KN_D : 24 + KN_D;
  const int m0 = tm * 128, panel = m0 >> 8, sd = SIDE;
  unsigned* pcnt = a.sync + FAE((xcd >> 2) * 4 + (idx >> 3)) + ((xcd & 3) * 8 + (idx & 7)) * 32;
  const unsigned orow = (unsigned)((m0 + wave * 16 + (lane >> 3)) * (D_ * 2) + tn * 128 + (lane & 7) * 16);
  const unsigned ofrg = (unsigned)(rbH((m0 >> 4) + wave, sd) + omH(tn * 2048) + (lane & 7) * 256 + (lane >> 3) * 16);
  u32x4 res[2];
  auto res_loads = [&]() {
    const auto rR = __builtin_amdgcn_make_buffer_rsrc((void*)a.R, 0, 0x7fffffff, 0x00020000);
#pragma unroll
    for (int i = 0; i < 2; ++i) res[i] = __builtin_amdgcn_raw_buffer_load_b128(rR, ofrg, i * 128, 16);
  };
  constexpr int STG = 0;
  if (MODE == 1 || wave < 4) {
    const int lw = wave & 3;
    const int kf = (xcd & 3) * 8;
    auto ktile = [&](int t) { const int l = (xcd & 2) * 6 + (t < A ? t - 16 + (xcd & 1) * (8 + KN_D) : t - A + 16); return t < 16 ? (kf ^ ((t & 8) * 2)) + (t & 7) : (l < 24 ? l + 32 : l + 8); };
    auto wrow = [&](int kt, int j) { const int p = kt * 8 + j, b = p - 256, v1 = p + 3 * (p >> 2), v2 = b + 4 * ((b * 21846) >> 16) + 4, m = b >> 31; return ((v1 & m) | (v2 & ~m)) * 8; };
    const auto rW = __builtin_amdgcn_make_buffer_rsrc((void*)a.W0, 0, 0x7fffffff, 0x00020000);
    const u32x4 qX = mk_rsrc(a.X), qW = mk_rsrc((const char*)a.W0 - 4096);
    const unsigned xv0 = (unsigned)(rbA((m0 >> 4) + lw * 2, sd) + lane * 16), xv1 = xv0 + (RSA - 2048);
    unsigned wv[4];
    { const unsigned w0 = (unsigned)((lane >> 3) * D_ + tn * 64 + ((lane & 7) ^ wswz<128>(lane >> 3)) * 8) * 2u;
#pragma unroll
      for (int i = 0; i < 4; ++i) wv[i] = (w0 ^ (i * 64)) + (4096 - i * 1024); }
    const unsigned mX = lds0 + lw * 4096, mW = mX + WB;
    auto ldXo = [&](int so, int slot) { ld_x4(mX + slot * 16384, qX, xv0, xv1, so); };
    int tbx = 0, tb0 = 0, tb1 = 0;
    auto tables = [&]() { const int kt = ktile(lane < A + 8 ? lane : 0); tbx = omA(kt * 2048); tb0 = wrow(kt, 2 * lw) * (D_ * 2); tb1 = wrow(kt, 2 * lw + 1) * (D_ * 2); asm volatile("" : "+v"(tbx), "+v"(tb0), "+v"(tb1)); };
    int o0, o1, ox;
    auto offs = [&](int t) { o0 = __builtin_amdgcn_readlane(tb0, t + 4); o1 = __builtin_amdgcn_readlane(tb1, t + 4); ox = __builtin_amdgcn_readlane(tbx, t + 4); asm volatile("s_nop 1" : "+s"(o0), "+s"(o1), "+s"(ox)); };
    const unsigned* fcnt = a.sync + FAE(panel);
    const unsigned fof = (lane & 31) * 128 + (lane >> 5) * 4;
    auto ldF = [&]() { __builtin_amdgcn_raw_ptr_buffer_load_lds(__builtin_amdgcn_make_buffer_rsrc((void*)fcnt, 0, 4096, 0x00020000), (lds_void_ptr)(size_t)(lds0 + PAD + 256), 4, fof, 0, 0, 16); };
    int ht; unsigned htv;
    auto hset = [&]() {
      int idh = idx; asm volatile("" : "+s"(idh));
      const int slc = (3 - ((unsigned)(size_t)a.W0 >> 7)) & 7, pr = idh & 3;
      const bool hlp = pr != (slc >> 1) && lw < 3;
      const int htau = KN_WS + lw * 12 + (idh >> 3) * 3 + pr - (pr > (slc >> 1) ? 1 : 0);
      ht = (hlp && htau < (((slc ^ xcd) & 1) ? A : A + 8)) ? htau - HTD : 1000;
      htv = (unsigned)((wrow(ktile(htau < A + 8 ? htau : 0), lane >> 3) + (lane & 7)) * (D_ * 2) + ((idh & 4) * 2 + slc) * 128);
    };
    auto help = [&]() { __builtin_amdgcn_raw_ptr_buffer_load_lds(rW, (lds_void_ptr)(size_t)(lds0 + FPAD - 256), 4, htv, 0, 0, 0); };
    auto ldW0 = [&](int i) { const int so = ((kf + i) * 112 + (lw >> 1) * 56 + (lw & 1) * 16) * (D_ * 2); ld_w4<-1>(mW + i * 16384, qW, wv, so, so + 8 * D_ * 2); };
    if constexpr (MODE == 1) {
      if constexpr (TL == 1) __builtin_amdgcn_raw_ptr_buffer_load_lds(__builtin_amdgcn_make_buffer_rsrc((void*)(a.sync + FAM(panel)), 0, 256, 0x00020000), (lds_void_ptr)(size_t)(lds0 + FPAD), 4, lane * 4, 0, 0, 16);
      if constexpr (TL == 2) { if (lw == 0) { hset(); if (ht < -1) help(); } }
      if constexpr (TL == 0 || TL == 3) { ldW0(TL ? 2 : 0); ldW0(TL ? 3 : 1); }
      return;
    }
#define TS0
#define TS1(T)
#define KN_XW 4
    wait_vm<3>();
    lds_inc(lds0 + ACNT);
    STAMPW(4, 0);
    if (KN_XW && KN_XW < 4) { hset(); tables(); asm volatile("" :: "s"(ht), "v"(htv)); }
    if (KN_XW == 3) ldW0(2);
    const bool early = !lds_missing(lds0 + FPAD, tag, ((1ull << (4 * V)) - 1) << ((xcd & 1) * 32));
    lds_wait8(lds0 + ACNT);
    STAMPW(9, 0);
    if (tid == 192) set_flag(pcnt, tag);
    asm volatile("" : "+v"(lane) :: "memory");
    if (!KN_XW) { hset(); tables(); asm volatile("" :: "s"(ht), "v"(htv)); }
    if (KN_XW <= 1 || KN_XW == 5) lds_wait8(lds0 + ACNT + 4, 4);
    STAMPW(5, 0);
    ldXo(omA(kf * 2048), 0); ldXo(omA(kf * 2048) + 2048, 1); if (KN_XW != 3) ldW0(2);
    if (KN_XW >= 4) { asm volatile("" : "+v"(lane) :: "memory"); hset(); tables(); asm volatile("" :: "s"(ht), "v"(htv)); }
    static_assert(KN_WS == 2 && V >= 5 && V <= 8, "first tiles");
    unsigned sx = mX + 2 * 16384, sw = 3 * 16384;
    auto next = [&]() { sw = (sw + 16384) & 65535; sx = sx + 16384; sx = sx >= mX + SX * 16384 ? mX : sx; asm volatile("" : "+s"(sw), "+s"(sx)); };
    { unsigned sc = mX + lane * 16;
      const int te = lw == 0 ? V - 4 : 1000;
      if (ht < -1) ht = -1;
      ox = omA(kf * 2048) + 4096;
      wait_vm<KN_XW == 3 ? 4 : 8>();
#pragma unroll 1
      for (int t = -1; t < V - 1; ++t) {
        const int oy = ox;
        offs(t);
        if (KN_XW == 3 && t == 0) wait_vm<8>(); else wait_vm<12>();
        if (__builtin_expect(ld_x4c(sx, qX, xv0, xv1, oy, sc) != 0 || !early, 0)) {
          for (int tries = early ? 0 : KN_TRY;; ++tries) {
            if (tries >= KN_TRY) wait_flags(fcnt, fof, tag, LO32);
            int so = __builtin_amdgcn_readlane(tbx, t + 1), sm = __builtin_amdgcn_readfirstlane(sc); asm volatile("s_nop 3" : "+s"(so), "+s"(sm));
            ld_x4(sm, qX, xv0, xv1, so);
            wait_vm<0>();
            if (tries >= KN_TRY || !marked<4, 1024>(sc)) break;
          }
        }
        if (t == -1) STAMPW(6, 0);
        if (__builtin_expect(t == te, 0)) { wait_vm<12>(); if (lds_missing(lds0 + PAD + 256, tag, LO32)) wait_flags(fcnt, fof, tag, LO32); }
        ld_w4<63>(mW + sw, qW, wv, o0, o1);
        TS0;
        next();
        if (__builtin_expect(t == te - 2, 0)) { ldF(); asm volatile("" ::: "memory"); }
        if (__builtin_expect(t == ht, 0)) help();
        sc = t == 3 ? sc - 4 * 16384 : sc + 16384;
        TS1(t + 1);
      }
    }
    ld_x4(sx, qX, xv0, xv1, ox);
    sx = sx + 16384; sx = sx >= mX + SX * 16384 ? mX : sx;
    const int tf = lw == 0 ? 9 : 1000;
    offs(V - 1);
    auto round = [&](int t, auto P1c, auto WAITc) {
      constexpr bool P1 = decltype(P1c)::value; constexpr int WAIT = decltype(WAITc)::value;
      if constexpr (P1) { if (__builtin_expect(t == tf + 3, 0)) { wait_vm<WAIT>(); if (lds_missing(lds0 + PAD + 256, tag, HI32)) wait_flags(fcnt, fof, tag, HI32); } }
      if constexpr (P1) ld_w4<WAIT>(mW + sw, qW, wv, o0, o1); else ld_w2<WAIT>(mW + sw, qW, wv, o0, o1);
      TS0;
      __builtin_amdgcn_sched_barrier(0);
      if (__builtin_expect(t == ht, 0)) help();
      if constexpr (P1) { if (__builtin_expect(t == tf, 0)) ldF(); }
      const int oy = ox; const unsigned sy = sx;
      offs(t + 1);
      next();
      TS1(t + 1);
      __builtin_amdgcn_sched_barrier(0);
      ld_x4(sy, qX, xv0, xv1, oy);
    };
    int t = V - 1;
#pragma unroll 1
    for (; t < A - 4; ++t) round(t, std::true_type{}, std::integral_constant<int, 16>{});
    round(t++, std::false_type{}, std::integral_constant<int, 16>{});
    round(t++, std::false_type{}, std::integral_constant<int, 14>{});
#pragma unroll 1
    for (; t < A + 4; ++t) round(t, std::false_type{}, std::integral_constant<int, 12>{});
    wait_vm<12>(); raw_barrier(); TS0; TS1(t + 1);
    res_loads();
    wait_vm<8>(); raw_barrier(); TS0; TS1(t + 2);
    wait_vm<2>(); raw_barrier(); TS0; TS1(t + 3);
    raw_barrier(); TS0; TS1(t + 4);
    asm volatile("" : "+v"(res[0]), "+v"(res[1]));
  } else {
    if constexpr (MODE == 1) return;
    const int fr = lane & 15, fq = lane >> 4, wq = (lane >> 2) & 3, wp = lane & 3, cw = wave & 3, wm = cw >> 1, wn = cw & 1;
#define KN_DP 0
    if (KN_DP) __builtin_amdgcn_s_setprio(KN_DP);
    f32x4 acc[4][4];
#pragma unroll
    for (int n = 0; n < 4; ++n)
#pragma unroll
      for (int m = 0; m < 4; ++m) acc[n][m] = (f32x4){0.f, 0.f, 0.f, 0.f};
    const unsigned xa = lds0 + wm * 8192 + lane * 16;
    unsigned wa[2];
#pragma unroll
    for (int c = 0; c < 2; ++c) { const int row = 8 * fq + wq; wa[c] = lds0 + WB + (row >> 4) * 4096 + (row & 15) * 128 + ((((wn * 2 + c) << 1) ^ wswz<128>(row)) << 4) + wp * 8; }
    struct Ctx { unsigned x, w[2]; };
    auto mk = [&](unsigned xo, unsigned wo) { Ctx c; c.x = xa + xo; c.w[0] = wa[0] + wo; c.w[1] = wa[1] + wo; return c; };
    auto rd = [&](FragE& f, const Ctx& c, auto KSc, auto kc) {
      constexpr int KS = decltype(KSc)::value, k = decltype(kc)::value;
      if constexpr (k < 4) f.x[k] = ds_b128<KS * 1024 + k * 2048>(c.x);
      else { constexpr int n = k - 4, o = KS * 8192 + (n >> 1) * 2048; f.lo[n] = ds_tr<o>(c.w[n & 1]); f.hi[n] = ds_tr<o + 512>(c.w[n & 1]); }
    };
    auto pin = [&](FragE& f, auto Nc) {
#pragma unroll
      for (int m = 0; m < 4; ++m) asm volatile("" : "+v"(f.x[m]));
#pragma unroll
      for (int n = 0; n < decltype(Nc)::value; ++n) asm volatile("" : "+v"(f.lo[n]), "+v"(f.hi[n]));
    };
    using I0 = std::integral_constant<int, 0>; using I1 = std::integral_constant<int, 1>; using I2 = std::integral_constant<int, 2>; using I4 = std::integral_constant<int, 4>;
    FragE fa, fb, fc, fd;
    const auto rS = __builtin_amdgcn_make_buffer_rsrc((void*)a.slab, 0, 0x7fffffff, 0x00020000);
    unsigned sla = rbH(tm * 8 + (tn >> 1), sd) + omH((tn & 1) * 16384 + cw * 4096) + lane * 16, slb = sla ^ omH(16384);
    asm volatile("" : "+v"(sla), "+v"(slb));
    raw_barrier();
    { FragE& f0 = (KN_D & 1) ? fc : fa; FragE& f1 = (KN_D & 1) ? fd : fb;
      const Ctx c = mk(0, 0); static_for<0, 8>([&](auto kc) { rd(f0, c, I0{}, kc); }); asm volatile("s_waitcnt lgkmcnt(4)" ::: "memory"); static_for<0, 8>([&](auto kc) { rd(f1, c, I1{}, kc); });
      asm volatile("s_waitcnt lgkmcnt(0)" ::: "memory");
      pin(f0, I4{}); pin(f1, I4{}); }
    STAMP(1);
    static_assert((SW & (SW - 1)) == 0, "W ring");
    int sx = 16384, sw = 16384;
    Ctx cn = mk(sx, sw);
#define KN_SP4 26
#define KN_IL 1
#define KN_IB 1
    auto rd1 = [&](FragE& f, const Ctx& c, auto KSc, auto rc) {
      constexpr int KS = decltype(KSc)::value, r = decltype(rc)::value;
      if constexpr (r < 4) f.x[r] = ds_b128<KS * 1024 + r * 2048>(c.x);
      else { constexpr int n = (r - 4) >> 1, o = KS * 8192 + (n >> 1) * 2048; if constexpr (r & 1) f.hi[n] = ds_tr<o + 512>(c.w[n & 1]); else f.lo[n] = ds_tr<o>(c.w[n & 1]); }
    };
    auto send = [&](int m) {
      __builtin_amdgcn_raw_buffer_store_b128((u32x4){pk_bf16(acc[2][m][0], acc[2][m][1]), pk_bf16(acc[2][m][2], acc[2][m][3]), pk_bf16(acc[3][m][0], acc[3][m][1]), pk_bf16(acc[3][m][2], acc[3][m][3])},
                                             rS, slb + m * 1024, 0, 16); };
    auto tile = [&](FragE& c0, FragE& c1, FragE& n0, FragE& n1, auto NCc, auto NNc, auto&& post) {
      constexpr int NC = decltype(NCc)::value, NN = decltype(NNc)::value;
      constexpr bool PF = KN_PF && NC == 4 && NN == 2;
      constexpr int R2 = 8 + 4 * NN, NP = 4 * NC;
      const Ctx c = cn;
      static_for<0, 2 * NP>([&](auto ic) {
        constexpr int I = decltype(ic)::value, P = I >> 1, Q = PF && P > 8 ? P - 8 : P;
        constexpr int n = PF && P >= 1 && P <= 8 ? 2 + ((P >> 1) & 1) : Q / 4, m = PF && P >= 1 && P <= 8 ? (P - 1) >> 1 : ((n & 1) ? 3 - Q % 4 : Q % 4);
        constexpr int SP = NC == 4 ? (NN == 4 ? KN_SP4 : 20) : 12;
        constexpr int before = I < 1 ? 0 : ((I - 1) * R2 / SP < R2 ? (I - 1) * R2 / SP : R2), after = I * R2 / SP < R2 ? I * R2 / SP : R2;
        FragE& u = ((P ^ I) & 1) ? c1 : c0;
        acc[n][m] = __builtin_amdgcn_mfma_f32_16x16x32_bf16(__builtin_shufflevector(u.lo[n], u.hi[n], 0, 1, 2, 3, 4, 5, 6, 7), u.x[m], acc[n][m], 0, 0, 0);
        __builtin_amdgcn_sched_barrier(0);
        if constexpr (I == 1) { asm volatile("s_waitcnt lgkmcnt(0)" ::: "memory"); pin(c0, NCc); pin(c1, NCc); raw_barrier(); post(); }
        static_for<before, after>([&](auto rc) { constexpr int r = decltype(rc)::value;
          constexpr int h = r < 6 ? r / 3 : (r < 12 ? r & 1 : (r >> 1) & 1), q = r < 6 ? (r % 3 == 2 ? 0 : 4 + r % 3) : (r < 12 ? (r - 4) >> 1 : 4 + ((r - 8) >> 2) * 2 + (r & 1));
          if constexpr (h) rd1(n1, c, I1{}, std::integral_constant<int, q>{}); else rd1(n0, c, I0{}, std::integral_constant<int, q>{}); });
        if constexpr (PF && I >= 9 && I <= 21 && (I - 9) % 4 == 0) send((I - 9) / 4);
        if constexpr (I == 2 * NP - 4) { sw = (sw + 16384) & (SW * 16384 - 1); asm volatile("" : "+s"(sw)); }
        if constexpr (I == 2 * NP - 3) { cn.w[0] = wa[0] + sw; cn.w[1] = wa[1] + sw; asm volatile("" : "+v"(cn.w[0]), "+v"(cn.w[1])); }
        if constexpr (I == 2 * NP - 5) { sx = sx + 16384; asm volatile("" : "+s"(sx)); }
        if constexpr (I == 2 * NP - 2) { sx = sx == SX * 16384 ? 0 : sx; asm volatile("" : "+s"(sx)); }
        if constexpr (I == 2 * NP - 1) { cn.x = xa + sx; asm volatile("" : "+v"(cn.x)); }
        __builtin_amdgcn_sched_barrier(0);
      });
      asm volatile("s_waitcnt lgkmcnt(%0)" :: "n"(R2 - 6 < 15 ? R2 - 6 : 15) : "memory");
      asm volatile("" : "+v"(n0.x[0]), "+v"(n0.lo[0]), "+v"(n0.hi[0]), "+v"(n1.x[0]), "+v"(n1.lo[0]), "+v"(n1.hi[0]));
    };
    auto nop = [&]() {};
    if constexpr (KN_D & 1) tile(fc, fd, fa, fb, I4{}, I4{}, nop);
#pragma unroll 1
    for (int t = 0; t < A - 3; t += 2) {
      tile(fa, fb, fc, fd, I4{}, I4{}, [&]() { if (t == 2) { if (tid == 256) set_flag(pcnt + 1, tag); } });
      tile(fc, fd, fa, fb, I4{}, I4{}, nop);
      if (t == 0) wait_vm<0>();
    }
    tile(fa, fb, fc, fd, I4{}, I4{}, nop); tile(fc, fd, fa, fb, I4{}, I2{}, nop);
    STAMP(10);
    if (!KN_PF) { send(0); send(1); send(2); send(3); }
    auto pairs = [&](int n) {
#pragma unroll 1
      for (int t = 0; t < n; t += 2) { tile(fa, fb, fc, fd, I2{}, I2{}, nop); tile(fc, fd, fa, fb, I2{}, I2{}, nop); } };
    const unsigned* flag = a.sync + FPR(bid);
    u32x4 pp[4];
    auto ldP = [&]() {
#pragma unroll
      for (int i = 0; i < 4; ++i) pp[i] = __builtin_amdgcn_raw_buffer_load_b128(rS, sla + i * 1024, 0, 16);
    };
    {
    pairs(TA);
    STAMPV(11);
    wait_vm<0>();
    STAMPV(12);
    tile(fa, fb, fc, fd, I2{}, I2{}, [&]() { if (tid == 256) set_flag(a.sync + FPR(bid ^ 1), tag); });
    tile(fc, fd, fa, fb, I2{}, I2{}, nop);
    if constexpr (TB > TA) pairs(TB - TA);
    unsigned fv = 1u;
    if (wave == 4) fv = __hip_atomic_load((gu32)flag, __ATOMIC_RELAXED, __HIP_MEMORY_SCOPE_AGENT);
    tile(fa, fb, fc, fd, I2{}, I2{}, nop);
    tile(fc, fd, fa, fb, I2{}, I2{}, nop);
    if (wave == 4) {
      if (__builtin_amdgcn_readfirstlane(fv) != tag) wait_flags(flag, 0, tag, 1ull);
    }
    STAMPV(4);
    tile(fa, fb, fc, fd, I2{}, I2{}, [&]() {
#pragma unroll
      for (int i = 0; i < 4; ++i) pp[i] = __builtin_amdgcn_raw_buffer_load_b128(rS, sla + i * 1024, 0, 16);
      res_loads();
    });
    tile(fc, fd, fa, fb, I2{}, I2{}, nop);
    }
    asm volatile("s_waitcnt lgkmcnt(0)" ::: "memory");
    pin(fa, I2{}); pin(fb, I2{});
    STAMP(2);
#pragma unroll
    for (int m = 0; m < 4; ++m)
#pragma unroll
      for (int n = 0; n < 2; ++n) {
        const unsigned q0 = pp[m][n * 2], q1 = pp[m][n * 2 + 1];
        const f32x4 v = acc[n][m] + (f32x4){bflo(q0), bfhi(q0), bflo(q1), bfhi(q1)};
        *(u32x2*)(smem + STG + (wm * 64 + m * 16 + fr) * 128 + ((((wn * 2 + n) * 2 + (fq >> 1)) ^ (fr & 7)) << 4) + (fq & 1) * 8) = (u32x2){pk_bf16(v[0], v[1]), pk_bf16(v[2], v[3])};
      }
    STAMPV(5);
    asm volatile("" : "+v"(res[0]), "+v"(res[1]));
  }
  lds_barrier();
  {
    const auto rO = __builtin_amdgcn_make_buffer_rsrc((void*)a.O, 0, 0x7fffffff, 0x00020000);
    u32x4 o[2];
    int lz = lane; asm volatile("" : "+v"(lz));
#pragma unroll
    for (int i = 0; i < 2; ++i) {
      const u32x4 v = *(const u32x4*)(smem + STG + (wave * 16 + i * 8 + (lz >> 3)) * 128 + (((lz & 7) ^ (lz >> 3)) << 4));
#pragma unroll
      for (int j = 0; j < 4; ++j) { const unsigned r = res[i][j], q = v[j]; o[i][j] = pk_bf16(bflo(r) + bflo(q), bfhi(r) + bfhi(q)); }
    }
    asm volatile("" : "+v"(o[0]), "+v"(o[1]));
#pragma unroll
#define KN_OAUX 16
    for (int i = 0; i < 2; ++i) __builtin_amdgcn_raw_buffer_store_b128(o[i], rO, orow, i * 8 * D_ * 2, KN_OAUX);
    __builtin_amdgcn_sched_barrier(0);
    asm volatile("s_nop 1" ::: "memory");
    __builtin_amdgcn_sched_barrier(0);
    if (bid == 0 && tid == 0) set_flag(a.sync + FEP, tag);
  }
  STAMP(7);
  STAMP(8);
  STAMP(3);
#endif
}

struct Ptrs { const bf16_t *x, *wqkv, *wo, *wg, *wu, *wd; bf16_t* out; char* ws; };
#define DBG(i)
#define WS_O ((bf16_t*)(p.ws + (8u << 20)))
#define WS_SL (KN_DM == 1 ? (float*)(p.ws + (12u << 20)) : (float*)WS_O)
#define WS_H ((bf16_t*)(p.ws + (16u << 20)))
#define WS_ST ((float*)(p.ws + (24u << 20)))
#define WS_CS ((float*)(p.ws + (25u << 20)))
#define WS_SY ((unsigned*)(p.ws + (26u << 20)))
#define WS_A ((bf16_t*)(p.ws + (28u << 20)))
#if defined(__HIP_DEVICE_COMPILE__) && !defined(KN_NOWARM)
#define ARG_WARM(v) do { asm volatile("s_load_dword %0, %1, 0x0" : "=s"(v) : "s"(__builtin_amdgcn_kernarg_segment_ptr())); __builtin_amdgcn_sched_barrier(0); } while (0)
#else
#define ARG_WARM(v)
#endif
struct Args4 { GemmArgs q, b, c, d; };
extern "C" __global__ __launch_bounds__(512, 1) void k_all(Ptrs p) {
  unsigned warm = 0;
  ARG_WARM(warm);
  p.ws += -(unsigned)(size_t)p.ws & 0x1fffffu;
  const Args4 a{GemmArgs{p.x, p.wqkv, nullptr, nullptr, WS_O, (float*)WS_H, p.wg, p.wu, WS_CS, p.wd, p.wo, WS_SY, (float*)WS_A, warm DBG(0)},
    GemmArgs{WS_O, p.wo, nullptr, p.x, WS_H, WS_ST, nullptr, nullptr, nullptr, p.wd, nullptr, WS_SY, nullptr, 0u DBG(1)},
    GemmArgs{WS_H, p.wg, p.wu, p.out, WS_A, WS_ST, nullptr, nullptr, WS_CS, p.wd, nullptr, WS_SY, WS_SL, 0u DBG(2)},
    GemmArgs{WS_A, p.wd, nullptr, WS_H, p.out, nullptr, nullptr, nullptr, nullptr, nullptr, nullptr, WS_SY, WS_SL, 0u DBG(3)}};
#define KN_WDX 4
#define KN_WHT 8
#define KN_DDX 4
#define KN_DDW 8
#define KN_DPW 9
#define KN_DHT 14
#define KN_WPW 5
  const unsigned tag = k1_phase(a.q);
  wo_phase(a.b, a.c, tag);
#define KN_A 24
#define KN_EX 4
#define KN_EW 4
#define KN_EH 10
#define KN_TA 2
#define KN_TB 2
  auto pre = [&](auto tc) { dn_phase<1, KN_EH, KN_TA, KN_TB, decltype(tc)::value>(a.d, tag); };
  gemm_phase<EPI_GLU, 256, 28, 4, 2, 1024, 3584, 8, 32, 4, 8, 32, 3, 4, 1>(a.c, tag, pre);
  dn_phase<2, KN_EH, KN_TA, KN_TB>(a.d, tag);
#if defined(DIAG) && defined(__HIP_DEVICE_COMPILE__)
  lds_barrier();
  if (threadIdx.x < 64 && threadIdx.x != 12) { int v = ds_r32(SNAP + 320 + threadIdx.x * 4); asm volatile("s_waitcnt lgkmcnt(0)" : "+v"(v)); a.q.dbg[(threadIdx.x >> 4) * 8192 + blockIdx.x * 16 + (threadIdx.x & 15)] = (unsigned)v; }
#endif
}

extern "C" __global__ void k_zend() {}

extern "C" void kernel_launch(void* const* d_in, const int* in_sizes, int n_in, void* d_out, int out_size, void* d_ws,
                              size_t ws_size, hipStream_t stream) {
  const Ptrs p{(const bf16_t*)d_in[0], (const bf16_t*)d_in[1], (const bf16_t*)d_in[2], (const bf16_t*)d_in[4], (const bf16_t*)d_in[5], (const bf16_t*)d_in[6], (bf16_t*)d_out, (char*)d_ws};
  constexpr int L_all = SNAP + 64 + 512;
  static_assert(SNAP >= 3 * 256 * 128 + 4 * 14592 + 4096 + 1024 + 256 + 512 + 128 + 64 && L_all <= 163840, "LDS");
  (void)hipFuncSetAttribute((const void*)k_all, hipFuncAttributeMaxDynamicSharedMemorySize, L_all);
  k_all<<<256, 512, L_all, stream>>>(p);
}
```

```cpp
#include <hip/hip_runtime.h>
#include <hip/hip_bf16.h>
#include <stdint.h>
#include <type_traits>

typedef unsigned short bf16_t;
typedef short bf16x8 __attribute__((ext_vector_type(8)));
typedef short s16x4 __attribute__((ext_vector_type(4)));
typedef float f32x4 __attribute__((ext_vector_type(4)));
typedef float f32x2 __attribute__((ext_vector_type(2)));
typedef unsigned u32x2 __attribute__((ext_vector_type(2)));
typedef unsigned u32x4 __attribute__((ext_vector_type(4)));
typedef __bf16 bf16x2_t __attribute__((ext_vector_type(2)));
typedef __attribute__((address_space(3))) void* lds_void_ptr;
typedef __attribute__((address_space(3))) char* lds_char_ptr;

constexpr int S_ = 2048, D_ = 1024, DF_ = 3584;
#define KN_CT 7
#define KN_HQ 5
#define KN_KT 4
#define KN_RSA 262144
#define KN_NUMA 3
constexpr int RSH = (KN_NUMA & 1) ? 65536 : 32768, RSA = (KN_NUMA & 2) ? KN_RSA : 114688;
__device__ __forceinline__ int rbH(int rb, int sd) { return (KN_NUMA & 1) ? (rb & 63) * RSH + (((sd ^ (rb >> 5)) & 1) << 13) : rb * RSH; }
__device__ __forceinline__ int rbA(int rb, int sd) { return (KN_NUMA & 2) ? ((rb >> 3) & 7) * 2097152 + (rb & 7) * RSA + (((sd ^ (rb >> 3)) & 1) << 13) : rb * RSA; }
__device__ __forceinline__ constexpr int omH(int o) { return (KN_NUMA & 1) ? o + (o & ~8191) : o; }
__device__ __forceinline__ constexpr int omA(int o) { return (KN_NUMA & 2) ? o + (o & ~8191) : o; }

__device__ __forceinline__ float bflo(unsigned v) { return __uint_as_float(v << 16); }
__device__ __forceinline__ float bfhi(unsigned v) { return __uint_as_float(v & 0xffff0000u); }
__device__ __forceinline__ unsigned pk_bf16(float lo, float hi) {
  f32x2 v = {lo, hi};
  bf16x2_t b = __builtin_convertvector(v, bf16x2_t);
  return __builtin_bit_cast(unsigned, b);
}
__device__ __forceinline__ float rbf(float v) { return bflo(pk_bf16(v, v)); }

template <int STRIDE> __device__ __forceinline__ int wswz(int row) {
  if constexpr (STRIDE % 256 == 0) return ((row & 3) | (((row >> 3) & 1) << 2)) << 1;
  else if constexpr (STRIDE % 256 == 128) return (((row >> 1) & 1) | (((row >> 3) & 1) << 1)) << 1;
  else return ((row >> 3) & 1) << 1;
}

template <int OFF> __device__ __forceinline__ s16x4 ds_tr(unsigned addr) {
  s16x4 r; asm volatile("ds_read_b64_tr_b16 %0, %1 offset:%2" : "=v"(r) : "v"(addr), "n"(OFF)); return r;
}
template <int OFF> __device__ __forceinline__ bf16x8 ds_b128(unsigned addr) {
  bf16x8 r; asm volatile("ds_read_b128 %0, %1 offset:%2" : "=v"(r) : "v"(addr), "n"(OFF)); return r;
}
template <int OFF> __device__ __forceinline__ f32x2 ds_b64(unsigned addr) {
  f32x2 r; asm volatile("ds_read_b64 %0, %1 offset:%2" : "=v"(r) : "v"(addr), "n"(OFF)); return r;
}
__device__ __forceinline__ float sum8(float v) {
  v += __int_as_float(__builtin_amdgcn_update_dpp(0, __float_as_int(v), 0x128, 0xf, 0xf, false));
  auto a = __builtin_amdgcn_permlane16_swap(__float_as_uint(v), __float_as_uint(v), false, false);
  v = __uint_as_float(a[0]) + __uint_as_float(a[1]);
  auto b = __builtin_amdgcn_permlane32_swap(__float_as_uint(v), __float_as_uint(v), false, false);
  return __uint_as_float(b[0]) + __uint_as_float(b[1]);
}
__device__ __forceinline__ float sum8l(float v) {
  v += __int_as_float(__builtin_amdgcn_update_dpp(0, __float_as_int(v), 0xb1, 0xf, 0xf, false));
  v += __int_as_float(__builtin_amdgcn_update_dpp(0, __float_as_int(v), 0x4e, 0xf, 0xf, false));
  return v + __int_as_float(__builtin_amdgcn_update_dpp(0, __float_as_int(v), 0x141, 0xf, 0xf, false));
}
__device__ __forceinline__ int ds_r32(unsigned addr) { int r; asm volatile("ds_read_b32 %0, %1" : "=v"(r) : "v"(addr)); return r; }
__device__ __forceinline__ void ds_w32(unsigned addr, int v) { asm volatile("ds_write_b32 %0, %1" :: "v"(addr), "v"(v) : "memory"); }
template <int N> __device__ __forceinline__ void wait_vm() { asm volatile("s_waitcnt vmcnt(%0)" ::"n"(N) : "memory"); }
__device__ __forceinline__ void raw_barrier() { asm volatile("" ::: "memory"); __builtin_amdgcn_s_barrier(); asm volatile("" ::: "memory"); }
__device__ __forceinline__ void loop_barrier() { raw_barrier(); }
__device__ __forceinline__ void lds_barrier() { asm volatile("s_waitcnt lgkmcnt(0)" ::: "memory"); __builtin_amdgcn_s_barrier(); asm volatile("" ::: "memory"); }

__device__ __forceinline__ u32x4 mk_rsrc(const void* p) { const unsigned long long v = (unsigned long long)p; return (u32x4){(unsigned)v, (unsigned)(v >> 32) & 0xffffu, 0x7fffffffu, 0x00020000u}; }
#define LD1K(v, so, off, sc) "buffer_load_dwordx4 " v ", %1, " so " offen offset:" off sc " lds\n\t"
template <int WAIT> __device__ __forceinline__ void ld_w4(unsigned m, u32x4 r, const unsigned (&v)[4], unsigned s0, unsigned s1) {
  if constexpr (WAIT >= 0) asm volatile("s_mov_b32 m0, %0\n\ts_waitcnt vmcnt(%8)\n\ts_barrier\n\t" LD1K("%2", "%6", "0", "") LD1K("%3", "%7", "1024", "") LD1K("%4", "%6", "2048", "") LD1K("%5", "%7", "3072", "")
                                        :: "s"(m), "s"(r), "v"(v[0]), "v"(v[1]), "v"(v[2]), "v"(v[3]), "s"(s0), "s"(s1), "n"(WAIT) : "memory", "m0");
  else asm volatile("s_mov_b32 m0, %0\n\ts_nop 0\n\t" LD1K("%2", "%6", "0", "") LD1K("%3", "%7", "1024", "") LD1K("%4", "%6", "2048", "") LD1K("%5", "%7", "3072", "")
                    :: "s"(m), "s"(r), "v"(v[0]), "v"(v[1]), "v"(v[2]), "v"(v[3]), "s"(s0), "s"(s1) : "memory", "m0");
}
template <int WAIT> __device__ __forceinline__ void ld_w2(unsigned m, u32x4 r, const unsigned (&v)[4], unsigned s0, unsigned s1) {
  if constexpr (WAIT >= 0) asm volatile("s_mov_b32 m0, %0\n\ts_waitcnt vmcnt(%6)\n\ts_barrier\n\t" LD1K("%2", "%4", "0", "") LD1K("%3", "%5", "1024", "")
                                        :: "s"(m), "s"(r), "v"(v[0]), "v"(v[1]), "s"(s0), "s"(s1), "n"(WAIT) : "memory", "m0");
  else asm volatile("s_mov_b32 m0, %0\n\ts_nop 0\n\t" LD1K("%2", "%4", "0", "") LD1K("%3", "%5", "1024", "") :: "s"(m), "s"(r), "v"(v[0]), "v"(v[1]), "s"(s0), "s"(s1) : "memory", "m0");
}
__device__ __forceinline__ void ld_x4(unsigned m, u32x4 r, unsigned v0, unsigned v1, unsigned so) {
  asm volatile("s_mov_b32 m0, %0\n\ts_nop 0\n\t" LD1K("%2", "%4", "0", " sc1") LD1K("%2", "%4", "1024", " sc1") LD1K("%3", "%4", "2048", " sc1") LD1K("%3", "%4", "3072", " sc1")
               :: "s"(m), "s"(r), "v"(v0), "v"(v1), "s"(so) : "memory", "m0");
}
__device__ __forceinline__ unsigned long long ld_x4c(unsigned m, u32x4 r, unsigned v0, unsigned v1, unsigned so, unsigned ca) {
  u32x4 a, b, c, d; unsigned long long f;
  asm volatile("ds_read_b128 %1, %10\n\tds_read_b128 %2, %10 offset:1024\n\tds_read_b128 %3, %10 offset:2048\n\tds_read_b128 %4, %10 offset:3072\n\ts_mov_b32 m0, %5\n\ts_nop 0\n\t"
               "buffer_load_dwordx4 %7, %6, %9 offen sc1 lds\n\tbuffer_load_dwordx4 %7, %6, %9 offen offset:1024 sc1 lds\n\ts_waitcnt lgkmcnt(0)"
               : "=s"(f), "=&v"(a), "=&v"(b), "=&v"(c), "=&v"(d) : "s"(m), "s"(r), "v"(v0), "v"(v1), "s"(so), "v"(ca) : "memory", "m0");
  unsigned x = max(max(a[0], a[1]), a[2]), y = max(max(b[0], b[1]), b[2]); x = max(max(x, a[3]), c[0]); y = max(max(y, b[3]), d[0]);
  asm volatile("s_mov_b32 m0, %2\n\ts_nop 0\n\tbuffer_load_dwordx4 %4, %3, %5 offen offset:2048 sc1 lds" : "+v"(x), "+v"(y) : "s"(m), "s"(r), "v"(v1), "s"(so) : "memory", "m0");
  x = max(max(x, c[1]), c[2]); y = max(max(y, d[1]), d[2]); x = max(max(x, c[3]), d[3]);
  f = __builtin_amdgcn_ballot_w64(max(x, y) == 0xffffffffu);
  asm volatile("s_mov_b32 m0, %1\n\ts_nop 0\n\tbuffer_load_dwordx4 %3, %2, %4 offen offset:3072 sc1 lds" : "+s"(f) : "s"(m), "s"(r), "v"(v1), "s"(so) : "memory", "m0");
  return f;
}
template <int LO, int SO, bool SUB, bool SC = false> __device__ __forceinline__ void ld_ca(unsigned mb, u32x4 r, unsigned v, unsigned sd) {
  unsigned t;
  if constexpr (SUB) asm volatile("s_add_i32 m0, %1, %4\n\ts_sub_i32 %0, %5, %6\n\tbuffer_load_dwordx4 %3, %2, %0 offen lds" : "=&s"(t) : "s"(mb), "s"(r), "v"(v), "n"(LO), "n"(SO), "s"(sd) : "memory", "m0", "scc");
  else if constexpr (SC) asm volatile("s_add_i32 m0, %1, %4\n\ts_add_i32 %0, %6, %5\n\tbuffer_load_dwordx4 %3, %2, %0 offen sc1 lds" : "=&s"(t) : "s"(mb), "s"(r), "v"(v), "n"(LO), "n"(SO), "s"(sd) : "memory", "m0", "scc");
  else asm volatile("s_add_i32 m0, %1, %4\n\ts_add_i32 %0, %6, %5\n\tbuffer_load_dwordx4 %3, %2, %0 offen lds" : "=&s"(t) : "s"(mb), "s"(r), "v"(v), "n"(LO), "n"(SO), "s"(sd) : "memory", "m0", "scc");
}
template <int LO, int SO> __device__ __forceinline__ void ld_cx(unsigned mb, u32x4 r, unsigned v, unsigned sx) {
  unsigned t;
  asm volatile("s_add_i32 m0, %1, %4\n\ts_xor_b32 %0, %6, %5\n\tbuffer_load_dwordx4 %3, %2, %0 offen lds" : "=&s"(t) : "s"(mb), "s"(r), "v"(v), "n"(LO), "n"(SO), "s"(sx) : "memory", "m0", "scc");
}
template <int I, int N, class F> __device__ __forceinline__ void static_for(F&& f) {
  if constexpr (I < N) { f(std::integral_constant<int, I>{}); static_for<I + 1, N>(f); }
}
#define CODE_ADDR(v, sym) asm volatile("s_getpc_b64 s[40:41]\n\ts_add_u32 s40, s40, " sym "@rel32@lo+4\n\ts_addc_u32 s41, s41, " sym "@rel32@hi+12\n\ts_mov_b64 %0, s[40:41]" : "=s"(v) :: "s40", "s41", "scc")
#define MERGED 1
constexpr int SNAP = 162752;
#define KN_S1C 8
#define KN_S1P 3
constexpr int STAB = 786432, STL = 3584;
#define FHM(p) ((p) * 256)
#define FRC(p) ((p) * 256 + 64)
#define FAM(p) ((p) * 256 + 128)
#define FCS(t) (2048 + (t) * 32)
#define FLN(k, i) ((1 + 2 * (k) + (((i) >> 6) & 1)) * 4096 + ((~sd & 1) << 11) + ((i) & 63) * 32)
#define FLO(t) FLN(0, (t) * 16)
#define FHF(p) FLN(1, (p) * 32)
#define FAE(p) FLN(2, (p) * 32)
#define FPR(b) FLN(3, ((b) & 3) * 32 + ((b) >> 3))
#define SIDE (((xcd >> 2) ^ tag) & 1)
constexpr int VTAB = STAB + 32768, PRB = 1048576;
constexpr int FEP = 3072;
constexpr unsigned long long LO32 = 0xffffffffull, HI32 = LO32 << 32;
#define KN_V 5
#define KN_D 2
#define KN_PF 0
#define KN_TRY 6
typedef __attribute__((address_space(1))) unsigned* gu32;
__device__ __forceinline__ void set_flag(unsigned* p, unsigned tag) { __hip_atomic_store((gu32)p, tag, __ATOMIC_RELAXED, __HIP_MEMORY_SCOPE_AGENT); }
__device__ __forceinline__ bool missing(unsigned v, unsigned tag, unsigned long long mask) { return (__builtin_amdgcn_ballot_w64(v != tag) & mask) != 0; }
__device__ __forceinline__ void wait_flags(const unsigned* p, unsigned off, unsigned tag, unsigned long long mask) {
  unsigned v, polls = 0;
  do { asm volatile("s_nop 4\n\tglobal_load_dword %0, %1, %2 sc1\n\ts_waitcnt vmcnt(0)" : "=v"(v) : "v"(off), "s"(p) : "memory"); } while (missing(v, tag, mask) && ++polls < 4000000u);
}
__device__ __forceinline__ unsigned lane4() { unsigned l = __builtin_amdgcn_mbcnt_hi(~0u, __builtin_amdgcn_mbcnt_lo(~0u, 0u)) * 4u; asm volatile("" : "+v"(l)); return l; }
__device__ __forceinline__ bool lds_missing(unsigned addr, unsigned tag, unsigned long long mask) {
  int v = ds_r32(addr + lane4());
  asm volatile("s_waitcnt lgkmcnt(0)" : "+v"(v) :: "memory");
  return missing((unsigned)v, tag, mask);
}
constexpr int ACNT = SNAP - 60;
__device__ __forceinline__ void lds_inc(unsigned addr) { if (__builtin_amdgcn_mbcnt_hi(~0u, __builtin_amdgcn_mbcnt_lo(~0u, 0u)) == 0) asm volatile("ds_add_u32 %0, %1" :: "v"(addr), "v"(1) : "memory"); }
constexpr int FPAD = SNAP - 704;
__device__ __forceinline__ void lds_wait8(unsigned addr, int n = 8) {
  int dv; unsigned polls = 0;
  do { dv = ds_r32(addr); asm volatile("s_waitcnt lgkmcnt(0)" : "+v"(dv) :: "memory"); } while (__builtin_amdgcn_readfirstlane(dv) < n && ++polls < 40000000u);
}
template <int N> __device__ __forceinline__ bool marked_in(bf16x8 (&v)[N]) {
  asm volatile("s_waitcnt lgkmcnt(0)" ::: "memory");
  unsigned m = 0;
#pragma unroll
  for (int i = 0; i < N; ++i) {
    asm volatile("" : "+v"(v[i]));
    const u32x4 u = __builtin_bit_cast(u32x4, v[i]);
    m = max(max(m, u[0]), u[1]); m = max(max(m, u[2]), u[3]);
  }
  return __builtin_amdgcn_ballot_w64(m == 0xffffffffu) != 0;
}
template <int N, int STR = 4096> __device__ __forceinline__ bool marked(unsigned addr) {
  bf16x8 v[N];
  static_for<0, N>([&](auto ic) { constexpr int i = decltype(ic)::value; v[i] = ds_b128<i * STR>(addr); });
  return marked_in<N>(v);
}

struct GemmArgs {
  const bf16_t* X;
  const bf16_t* W0;
  const bf16_t* W1;
  const bf16_t* R;
  bf16_t* O;
  float* st;
  const bf16_t* CW0;
  const bf16_t* CW1;
  float* cs;
  const bf16_t* PF0;
  const bf16_t* PF1;
  unsigned* sync;
  float* slab;
  unsigned warm;
};
#define STAMP(i)
#define STAMPW(i, w)

#define STAMPV(i) STAMP(i)
template <int MF, int NF> struct Frags { bf16x8 x[MF]; s16x4 lo[NF], hi[NF]; };

constexpr int WOB = 55296;
constexpr int wo_pos(int s) { return s < 6 ? WOB + s * 16384 : (s - 6) * 16384; }
#define KN_GP 15
#define KN_GX 1
#define KN_GL 0
#define KN_EW 1
#define KN_GR 24
#define KN_GE 26
template <int NF, int GR> struct SchedP {
  static constexpr int NP = 4 * NF, NR = NP + 8;
  static constexpr int pa(int p) { return (p + NP - 12) % NP; }
  static constexpr int pn(int a) { return a < 8 ? (a ^ (a >> 1)) & 1 : (a < NP - 8 ? a / 4 : NF - 2 + ((a ^ (a >> 1)) & 1)); }
  static constexpr int pm(int a) { return a < 8 ? a >> 1 : (a < NP - 8 ? ((a & 4) ? a & 3 : 3 - (a & 3)) : (a - NP + 8) >> 1); }
  static constexpr int gap(int r) { return r < 8 ? 2 + r : (r < 12 ? 12 + 4 * (r - 8) : (r < NP ? 26 + (r - 12) * GR / (NP - 12) : 11 + 2 * ((r - NP) & ~1) - (r & 1))); }
  static constexpr int need(int r) { return r < 8 ? 12 : (r < NP ? (12 + (r & ~3) < NP ? 12 + (r & ~3) : NP) : 12 + ((r - NP) & ~1)); }
  static constexpr int cnt(int p, int e = 0) {
    int g = -1, q = -1;
    for (int r = 0; r < NR; ++r) if (need(r) == p && (gap(r) > g || (gap(r) == g && r > q))) { g = gap(r); q = r; }
    if (g < 0) return -1;
    int c = 0;
    for (int r = 0; r < NR; ++r) if (gap(r) < 2 * p - e - (e > 0) && (gap(r) > g || (gap(r) == g && r > q))) ++c;
    return c < 15 ? c : 15;
  }
  static constexpr bool last(int a) { for (int b = a + 1; b < NP; ++b) if (pn(b) == pn(a)) return false; return true; }
};
using SchedG = SchedP<7, KN_GR>;
template <class HK>
__device__ __forceinline__ void glu_phase(const GemmArgs& a, unsigned tag, HK hook) {
#if defined(__HIP_DEVICE_COMPILE__)
  constexpr int LDW = DF_, KU = 32, NT = 16, MF = 4, NF = 7, NLW = 4, XI = 4, WI = 7;
  constexpr int UPOFF = KU * 224 + 16;
  constexpr int XT = 256 * 128, WU = (UPOFF + KU * 224 + 255) / 256 * 256;
  constexpr int XBASE = 0, WBASE = 3 * XT, RING = WBASE + 4 * WU;
  extern __shared__ __attribute__((aligned(16))) char smem[];
  STAMP(0);
  const unsigned lds0 = (unsigned)(size_t)(lds_char_ptr)smem;
  const int tid = threadIdx.x, lane = tid & 63;
  const int wave = __builtin_amdgcn_readfirstlane(tid >> 6);
  const int wm = wave >> 1, wn = wave & 1;
  const int bid = blockIdx.x, xcd = bid & 7, idx = bid >> 3;
  const int tm = (xcd >> 2) * 4 + (idx >> 3), tn = (xcd & 3) * 8 + (idx & 7);
  const int m0 = tm * 256, sd = SIDE;
  const int krot = (tm >> 2) * 8;
  const int fr = lane & 15, fq = lane >> 4;
  const int wq = (lane >> 2) & 3, wp = lane & 3;

  const int panel = m0 >> 8;
  constexpr int TCH = RING + 5120;
  constexpr int DONE = TCH + 896;
  auto done_inc = [&]() { if (lane == 0) asm volatile("ds_add_u32 %0, %1" :: "v"(lds0 + DONE), "v"(1) : "memory"); };
  auto touch = [&](const void* base, unsigned voff, unsigned soff, unsigned lim = 0x7fffffffu) {
    __builtin_amdgcn_raw_ptr_buffer_load_lds(__builtin_amdgcn_make_buffer_rsrc((void*)base, 0, lim, 0x00020000), (lds_void_ptr)(size_t)(lds0 + TCH), 4, voff, soff, 0, 0);
  };
  const auto rO = __builtin_amdgcn_make_buffer_rsrc((void*)a.O, 0, 0x7fffffff, 0x00020000);

  unsigned xsrc[XI], wsrc[WI];
#pragma unroll
  for (int i = 0; i < XI; ++i) { const int j = (wave & 3) + NLW * i; xsrc[i] = (unsigned)(rbH(m0 >> 4, sd) + (j >> 1) * RSH + (j & 1) * 1024 + lane * 16); }
#pragma unroll
  for (int i = 0; i < WI; ++i) {
    const int b = i * 1024 + lane * 16, p = b / 224, pos = (b % 224) >> 4;
    const int row = 8 * ((p & 15) >> 2) + (p & 3) + 4 * (p >> 4);
    wsrc[i] = (unsigned)(row * LDW + tn * 112 + pos * 8) * 2u;
  }
  auto ldX = [&](int i, int tile, int part, int slot) {
    __builtin_amdgcn_raw_ptr_buffer_load_lds(__builtin_amdgcn_make_buffer_rsrc((void*)a.X, 0, 0x7fffffff, 0x00020000), (lds_void_ptr)(size_t)(lds0 + XBASE + slot * XT + (part * 16 + wave + NLW * i) * 1024), 16, xsrc[i],
                                             omH(((tile + krot) & 15) * 2048) + part * 8 * RSH, 0, 16);
  };
  auto ldW = [&](int i, int tile, int slot) {
    __builtin_amdgcn_raw_ptr_buffer_load_lds(__builtin_amdgcn_make_buffer_rsrc((void*)((wave & 1) ? a.W1 : a.W0), 0, 0x7fffffff, 0x00020000),
                                             (lds_void_ptr)(size_t)(lds0 + WBASE + (slot + (wave >> 1)) * WU + (wave & 1) * UPOFF + i * 1024), 16, wsrc[i],
                                             (((tile + krot) & 15) * 2 + (wave >> 1)) * KU * LDW * 2, 0, 0);
  };
  auto issueX = [&](int tile, int part, int slot) {
    if (wave >= NLW) return;
#pragma unroll
    for (int i = 0; i < XI; ++i) ldX(i, tile, part, slot);
  };

  f32x4 acc[NF][MF];
#pragma unroll
  for (int n = 0; n < NF; ++n)
#pragma unroll
    for (int m = 0; m < MF; ++m) acc[n][m] = (f32x4){0.f, 0.f, 0.f, 0.f};
  f32x4 accs = {0.f, 0.f, 0.f, 0.f};

  const unsigned xa0 = lds0 + XBASE + wm * MF * 2048 + lane * 16;
  unsigned wa[2];
#pragma unroll
  for (int c = 0; c < 2; ++c) wa[c] = lds0 + WBASE + (4 * fq + wq) * 224 + wn * 112 + (c == 0 ? wp * 8 : 96 + (wp & 1) * 8 + (wp >> 1) * UPOFF);

  using I0 = std::integral_constant<int, 0>; using I1 = std::integral_constant<int, 1>;
  using T = std::true_type; using F = std::false_type;
  typedef Frags<MF, NF> FR;
  struct RdCtx { unsigned xa, wb[2]; };
  auto mkctx = [&](unsigned xoffs, unsigned woffs) { return RdCtx{xa0 + xoffs, {wa[0] + woffs, wa[1] + woffs}}; };
  FR fa, fb;
  auto rdg = [&](const RdCtx& c, auto rc) {
    constexpr int r = decltype(rc)::value;
    if constexpr (r >= 28) { constexpr int m = (r - 28) >> 1; if constexpr (r & 1) fb.x[m] = ds_b128<m * 2048 + 1024>(c.xa); else fa.x[m] = ds_b128<m * 2048>(c.xa); }
    else {
      constexpr int n = r >> 2, cc = n == 6, o = (n == 6 ? 0 : (n >> 1) * 32 + (n & 1) * UPOFF) + ((r >> 1) & 1) * WU + (r & 1) * 16 * 224;
      FR& f = (r & 2) ? fb : fa;
      if constexpr (r & 1) f.hi[n] = ds_tr<o>(c.wb[cc]); else f.lo[n] = ds_tr<o>(c.wb[cc]);
    }
  };
  auto ping = [&](auto pc) {
    static_for<0, 36>([&](auto rc) { constexpr int r = decltype(rc)::value;
      if constexpr (SchedG::need(r) == decltype(pc)::value) {
        FR& f = (r >= 28 ? r & 1 : r & 2) ? fb : fa;
        if constexpr (r >= 28) { bf16x8& q = f.x[(r - 28) >> 1]; asm volatile("" : "+v"(q)); } else { s16x4& q = (r & 1) ? f.hi[r >> 2] : f.lo[r >> 2]; asm volatile("" : "+v"(q)); } } });
  };
  auto pairs = [&](auto PBc, auto PEc, auto RDc, auto CSc, const RdCtx& c, auto&& gapf) {
    constexpr bool RD = decltype(RDc)::value; constexpr int CS = decltype(CSc)::value;
    static_for<decltype(PBc)::value, decltype(PEc)::value>([&](auto pc) {
      constexpr int p = decltype(pc)::value, a = (p + 16) % 28, n = SchedG::pn(a), m = SchedG::pm(a);
      if constexpr (SchedG::cnt(p) >= 0) { asm volatile("s_waitcnt lgkmcnt(%0)" :: "n"(SchedG::cnt(p)) : "memory"); ping(pc); }
      static_for<0, 2>([&](auto hc) {
        constexpr int g = 2 * p + decltype(hc)::value;
        FR& u = ((a ^ g) & 1) ? fb : fa;
        acc[n][m] = __builtin_amdgcn_mfma_f32_16x16x32_bf16(__builtin_shufflevector(u.lo[n], u.hi[n], 0, 1, 2, 3, 4, 5, 6, 7), u.x[m], acc[n][m], 0, 0, 0);
        __builtin_amdgcn_sched_barrier(0);
        if constexpr (CS >= 0 && SchedG::last(a) && (g & 1)) {
          const unsigned sv = fr == n ? 0x3f803f80u : 0u;
          const u32x4 s4 = {sv, sv, sv, sv};
          FR& w = CS ? fb : fa;
          accs = __builtin_amdgcn_mfma_f32_16x16x32_bf16(__builtin_shufflevector(w.lo[n], w.hi[n], 0, 1, 2, 3, 4, 5, 6, 7), __builtin_bit_cast(bf16x8, s4), accs, 0, 0, 0);
          __builtin_amdgcn_sched_barrier(0);
        }
        if constexpr (RD) static_for<0, 36>([&](auto rc) { if constexpr (SchedG::gap(decltype(rc)::value) == g) rdg(c, rc); });
        if constexpr (g & 1) gapf(pc);
        __builtin_amdgcn_sched_barrier(0);
      });
    });
    if constexpr (RD) { asm volatile("s_waitcnt lgkmcnt(0)" ::: "memory"); ping(std::integral_constant<int, 28>{}); }
  };

  f32x4* tb_row = (f32x4*)(smem + RING);
  float* tb_cs = (float*)(tb_row + 256);
  constexpr int RTAB = 524288;
  auto glu_stats_issue = [&]() {
    {
      const auto rS = __builtin_amdgcn_make_buffer_rsrc((void*)a.st, 0, 0x7fffffff, 0x00020000);
      if (wave == 7) __builtin_amdgcn_raw_ptr_buffer_load_lds(rS, (lds_void_ptr)(size_t)(lds0 + RING + 3072), 16, ((lane >> 2) * S_ + m0 + tn * 8 + (lane & 3) * 2) * 8, 0, 0, 16);
    }
  };
  auto glu_stats = [&]() {
    {
      int lx = lane; asm volatile("" : "+v"(lx));
      const unsigned sa = lds0 + RING + 3072 + lx * 8;
      f32x2 p0 = ds_b64<0>(sa), p1 = ds_b64<512>(sa);
      asm volatile("s_waitcnt lgkmcnt(0)" : "+v"(p0), "+v"(p1) :: "memory");
      float s0 = p0[0] + p1[0]; asm volatile("" : "+v"(s0), "+v"(p0));
      const float mu = sum8(s0) * (1.f / 16.f);
      const float v = sum8((p0[1] + p1[1]) + 64.f * ((p0[0] - mu) * (p0[0] - mu) + (p1[0] - mu) * (p1[0] - mu))) * (1.f / 1024.f) + 1e-5f;
      float rs = __builtin_amdgcn_rsqf(v), k2 = -2.3022082f * rs; asm volatile("" : "+v"(k2), "+v"(rs));
      const f32x4 rv = {mu, -0.10294324f * rs * rs * rs, k2, v};
      __builtin_amdgcn_raw_buffer_store_b128(__builtin_bit_cast(u32x4, rv), __builtin_amdgcn_make_buffer_rsrc((void*)a.cs, 0, 0x7fffffff, 0x00020000),
                                             lx < 8 ? (unsigned)(RTAB + (m0 + tn * 8 + lx) * 16) : 0x80000000u, 0, 16);
    }
  };
  auto epilogue = [&](auto EAc) __attribute__((always_inline)) {
  STAMP(2);

  {
    constexpr bool EARLY = decltype(EAc)::value;
    static_assert(DONE + 4 == ACNT && TCH + 256 == FPAD, "LDS");
#define KN_EP 1
    if constexpr (!EARLY) {
      wait_vm<0>();
      lds_wait8(lds0 + DONE);
      if (KN_EP) __builtin_amdgcn_s_setprio(2);
      STAMPW(9, 4);
      hook(I0{});
      STAMPW(10, 4);
    } else { if (KN_EP) __builtin_amdgcn_s_setprio(3); hook(I1{}); }
    asm volatile("" ::: "memory");
    f32x4 cg[4], cu[4], rw[MF], rw6[2];
    int lz = lane; asm volatile("" : "+v"(lz));
    const int fr = lz & 15, fq = lz >> 4;
#pragma unroll
    for (int p = 0; p < 4; ++p) {
      const int o = wn * 56 + p * 16 + (p == 3 ? (fq & 1) : fq) * 4;
      cg[p] = *(const f32x4*)(tb_cs + o); cu[p] = *(const f32x4*)(tb_cs + 112 + o);
    }
#pragma unroll
    for (int m = 0; m < MF; ++m) rw[m] = tb_row[(wm * MF + m) * 16 + fr];
#pragma unroll
    for (int q = 0; q < 2; ++q) rw6[q] = tb_row[(wm * MF + 2 * q + (fq >> 1)) * 16 + fr];
    const int gz = rbA((m0 >> 4) + wm * 4, sd) + fr * 16, g = tn * 2 + wn, g1 = omA(g * 1024);
    const int go0 = gz + (fq & 1) * RSA + (fq >> 1) * 256, go6 = gz + ((fq & 1) * 2 + (fq >> 1)) * RSA;
    unsigned hv[MF][NF];
    auto put = [&](unsigned a0, unsigned a1, unsigned b0, unsigned b1, int vo, int so) {
      const auto s0 = __builtin_amdgcn_permlane16_swap(a0, b0, false, false), s1 = __builtin_amdgcn_permlane16_swap(a1, b1, false, false);
      __builtin_amdgcn_raw_buffer_store_b128((u32x4){s0[0], s1[0], s0[1], s1[1]}, rO, vo, so, 16);
      __builtin_amdgcn_sched_barrier(0);
      asm volatile("s_nop 1" ::: "memory");
      __builtin_amdgcn_sched_barrier(0);
    };
#define STAGE(body) _Pragma("unroll") for (int i = 0; i < 2 * NN; ++i) { const int m = MB + i / NN, n = N0 + i % NN; const f32x4 k = n == 6 ? rw6[m >> 1] : rw[m]; body; } __builtin_amdgcn_sched_barrier(0)
    auto grp = [&](auto MBc, auto N0c, auto NNc, auto&& mid) {
      constexpr int MB = decltype(MBc)::value, N0 = decltype(N0c)::value, NN = decltype(NNc)::value;
      f32x2 G[2 * NN], U[2 * NN], P[2 * NN];
      __builtin_amdgcn_sched_barrier(0);
      STAGE({ const int e = (n == 6 ? m & 1 : n & 1) * 2; const f32x4 x = n == 6 ? acc[6][m & 2] : acc[n & 6][m]; G[i] = ((f32x2){x[e], x[e + 1]}) - k[0] * ((f32x2){cg[n >> 1][e], cg[n >> 1][e + 1]}); });
      STAGE(P[i] = G[i] * G[i]);
      STAGE(P[i] = P[i] * k[1] + k[2]);
      STAGE(P[i] = G[i] * P[i]);
      STAGE({ const int e = (n == 6 ? m & 1 : n & 1) * 2; const f32x4 x = n == 6 ? acc[6][m | 1] : acc[n | 1][m]; U[i] = ((f32x2){x[e], x[e + 1]}) - k[0] * ((f32x2){cu[n >> 1][e], cu[n >> 1][e + 1]}); });
      STAGE(P[i] = ((f32x2){__builtin_amdgcn_exp2f(P[i][0]), __builtin_amdgcn_exp2f(P[i][1])}));
      mid();
      __builtin_amdgcn_sched_barrier(0);
      STAGE(P[i] = P[i] * k[3] + k[3]);
      STAGE(P[i] = ((f32x2){__builtin_amdgcn_rcpf(P[i][0]), __builtin_amdgcn_rcpf(P[i][1])}));
      STAGE(U[i] = G[i] * U[i]);
      STAGE(U[i] = U[i] * P[i]);
      STAGE({ hv[m][n] = pk_bf16(U[i][0], U[i][1]); asm volatile("" : "+v"(hv[m][n])); });
    };
#undef STAGE
    using I2 = std::integral_constant<int, 2>; using I3 = std::integral_constant<int, 3>; using I4 = std::integral_constant<int, 4>;
    grp(I0{}, I0{}, I4{}, [&]() {});
    grp(I2{}, I0{}, I4{}, [&]() {
#pragma unroll
      for (int p = 0; p < 2; ++p) put(hv[0][2 * p], hv[0][2 * p + 1], hv[1][2 * p], hv[1][2 * p + 1], go0 + p * 512, g1); });
#pragma unroll
    for (int p = 0; p < 2; ++p) put(hv[2][2 * p], hv[2][2 * p + 1], hv[3][2 * p], hv[3][2 * p + 1], go0 + p * 512, g1 + 2 * RSA);
    asm volatile("" ::: "memory");
    __builtin_amdgcn_sched_barrier(0);
    STAMP(4);
    STAMPW(11, 4);
    if (KN_EP) __builtin_amdgcn_s_setprio(EARLY ? 1 : 0);
#define KN_AW 1
    auto arrived = [&](int w) { if (!EARLY && w == KN_AW) { wait_vm<0>(); lds_inc(lds0 + ACNT); STAMPW(6, 4); } };
    arrived(0);
#pragma unroll
    for (int q = 0; q < 2; ++q)
#pragma unroll
      for (int e = 0; e < 4; ++e) {
        const float x0 = acc[6][2 * q][e], x1 = acc[6][2 * q + 1][e];
        auto sw = __builtin_amdgcn_permlane32_swap(__float_as_uint(x0), __float_as_uint(x1), false, false);
        acc[6][2 * q][e] = __uint_as_float(sw[0]); acc[6][2 * q + 1][e] = __uint_as_float(sw[1]);
      }
    const int sb = omA((256 + g * 3) * 256), go4 = go0 + (fq >> 1) * (omA((257 + g * 3) * 256) - sb - 256);
    grp(I0{}, I4{}, I3{}, [&]() { arrived(1); });
    arrived(2);
    grp(I2{}, I4{}, I3{}, [&]() { arrived(3); put(hv[0][4], hv[0][5], hv[1][4], hv[1][5], go4, sb); });
    put(hv[2][4], hv[2][5], hv[3][4], hv[3][5], go4, sb + 2 * RSA);
    put(hv[0][6], hv[1][6], hv[2][6], hv[3][6], go6, omA((258 + g * 3) * 256));
    asm volatile("" ::: "memory");
    if constexpr (!EARLY) lds_inc(lds0 + ACNT + 4);
  }
  };
  using IM1 = std::integral_constant<int, -1>;

  {
    constexpr int WR = 2;
    if (wave == 5) { ds_w32(lds0 + DONE, 0); ds_w32(lds0 + DONE + 4, 0); ds_w32(lds0 + DONE + 8, 0); }
    auto check_tile = [&](int t) {
      int lx = lane; asm volatile("" : "+v"(lx));
      const unsigned ad = lds0 + XBASE + t * XT + wave * 1024 + lx * 16;
      if (__builtin_expect(marked<8>(ad), 0)) {
        for (int tries = 0;; ++tries) {
          if (tries >= KN_TRY) wait_flags(a.sync + FHF(panel), (lane4() & 127) * 32, tag, LO32);
#pragma unroll 1
          for (int u = t; u < t + 1 + (t == 0 && KN_GP >= 8); ++u) { issueX(u, 0, u); issueX(u, 1, u); }
          if (t == 0 && KN_GP >= 8) wait_vm<2 * XI>(); else wait_vm<0>();
          if (tries >= KN_TRY || !marked<8>(ad)) break;
        }
      }
    };
    auto first = [&](auto ic) { constexpr int i = decltype(ic)::value; if constexpr (i < 8) ldX(i % XI, 1, i / XI, 1); else if constexpr (i < 15) ldW(i - 8, 1, 2 ^ WR); else ldX((i - 15) % XI, 2, (i - 15) / XI, 2); };
    auto run = [&](auto LDRc) __attribute__((always_inline)) {
      constexpr bool LDR = decltype(LDRc)::value;
      if constexpr (LDR) {
        if (!KN_GX) { issueX(0, 0, 0); issueX(0, 1, 0); }
        static_for<0, KN_GP>(first);
        wait_vm<KN_GP>();
        STAMPW(5, 0);
        check_tile(0);
      }
      lds_barrier();
      if constexpr (!LDR) { if (wave == 4) __builtin_amdgcn_raw_ptr_buffer_load_lds(__builtin_amdgcn_make_buffer_rsrc((void*)(a.sync + FHF(panel)), 0, 4096, 0x00020000), (lds_void_ptr)(size_t)(lds0 + TCH + 256), 4, lane * 128, 0, 0, 16); }
      using I12 = std::integral_constant<int, 12>; using I28 = std::integral_constant<int, 28>;
      { const RdCtx c = mkctx(0, WR * WU);
        static_for<0, 24>([&](auto gc) { static_for<0, 36>([&](auto rc) { if constexpr (SchedG::gap(decltype(rc)::value) == decltype(gc)::value) rdg(c, rc); }); });
        STAMP(1);
        pairs(I12{}, I28{}, T{}, IM1{}, c, [&](auto pc) { if constexpr (LDR) static_for<KN_GP, 23>([&](auto ic) { if constexpr (12 + (decltype(ic)::value - KN_GP) * 15 / (23 - KN_GP) == decltype(pc)::value) first(ic); }); }); }
      int xs = 1;
#define KN_CTJ 11
      unsigned long long tb = (unsigned long long)a.PF0; unsigned tv = 0, tl = 0x7fffffffu, ts = 1835008u, tmask = 0; int tj = 1, tsh = 1;
      if constexpr (!LDR) {
        unsigned long long c0, c1; CODE_ADDR(c0, "k_all"); CODE_ADDR(c1, "k_zend");
#define KN_WJ 1
#define KN_WE 8
#define KN_DJ 6
        tj = KN_DJ;
        if (wave == 4) { tb = c0; tl = c1 - c0 < 262144ull ? (unsigned)(c1 - c0) : 0u; ts = 0; tmask = 1u << KN_CTJ; }
        if (wave == 5) tmask = 0x55u << (KN_DJ + (idx & 1));
        if (wave == 6) { tb = (unsigned long long)((idx & 16) ? a.W1 : a.W0); ts = 64 * LDW * 2; tmask = ((1u << (KN_WE - KN_KT)) - 1u) << KN_WJ; tj = KN_WJ - krot - KN_KT; tsh = 0; }
        const int l4 = lane / 14;
        tv = wave == 4 ? (unsigned)((lane * 32 + idx) * 128)
           : (lane >= 56 ? 0x80000000u : (wave == 5 ? (unsigned)(bid * 7168 + lane * 128) : (unsigned)(((idx & 15) * 4 + l4) * (LDW * 2) + (xcd & 3) * 1792 + (lane - l4 * 14) * 128)));
      }
      const int js = 2 * (tm & 3) + 1;
      const auto rC = __builtin_amdgcn_make_buffer_rsrc((void*)a.cs, 0, 0x7fffffff, 0x00020000);
      unsigned* ccnt = a.sync + FCS(tn);
#define KN_JR 10
#define KN_JS 2
      const int jf = wave == 4 ? NT - 4 : (wave == 7 ? KN_JR : 64);
      const unsigned* fcnt = wave == 4 ? ccnt : a.sync + FRC(panel);
      const int jm = (idx >> 4) == ((xcd >> 1) & 1) && (idx & 7) < KN_V ? 3 : 64;
      int mo = rbA((m0 >> 4) + wave * 4, sd) + omA(tn * 2048);
      const unsigned evm = tmask | (8u << js) | (wave == 4 ? 3u << (NT - 4) : 0u) | (wave == 7 ? ((5u << KN_JS) | 2u | (11u << KN_JR)) : 0u);
      constexpr int CFL = TCH + 256, CPB = XBASE + ((NT - 3) % 3) * XT;
      auto iter = [&](int j, auto LWc, auto LXc, auto WAITc, auto CSc) {
        constexpr bool LW = decltype(LWc)::value && LDR, LX = decltype(LXc)::value && LDR, LP = LW && !LX;
        if constexpr (LDR) {
          wait_vm<decltype(WAITc)::value>();
          if constexpr (LX) { if (__builtin_expect(j <= 2, 0)) check_tile(j); }
          if constexpr (LX) {
            if ((unsigned)(j - jm) < 8u) {
              int lx = lane; asm volatile("" : "+v"(lx));
              unsigned on = 0xffffffffu; asm volatile("" : "+v"(on));
              __builtin_amdgcn_raw_buffer_store_b128((u32x4){on, on, on, on}, rO, lx * 16, mo, 16);
              mo += (j & 1) ? 1024 : RSA - 1024;
            }
            if (j == jm + 9) { if (lane == 0) set_flag(a.sync + FAM(panel) + (tn & 15) * 4 + wave, tag); }
          }
        }
        if constexpr (!LDR) {
          if (__builtin_expect((evm >> j) & 1, 0)) {
            if (j == js + 3) {
              if ((tmask >> (j - 1)) & 1) wait_vm<1>(); else wait_vm<0>();
              if (lane == 0) set_flag(ccnt + tm * 4 + wave - 4, tag);
            }
            if (j == 1) glu_stats_issue();
            if (j == KN_JS && wave == 7) { wait_vm<0>(); glu_stats(); }
            if (j == KN_JS + 2 && wave == 7) {
              if (j == js + 2) wait_vm<1>(); else wait_vm<0>();
              if (lane == 0) set_flag((unsigned*)fcnt + tn, tag);
            }
            {
              int lx = lane; asm volatile("" : "+v"(lx));
              if (j == jf) __builtin_amdgcn_raw_ptr_buffer_load_lds(__builtin_amdgcn_make_buffer_rsrc((void*)fcnt, 0, 128, 0x00020000), (lds_void_ptr)(size_t)(lds0 + CFL + (wave & 1) * 256), 4, lx * 4, 0, 0, 16);
              if (j == jf + 1) {
                wait_vm<0>();
                if (lds_missing(lds0 + CFL + (wave & 1) * 256, tag, LO32)) wait_flags(fcnt, lx * 4, tag, LO32);
                if (wave == 7) {
#pragma unroll
                  for (int i = 0; i < 4; ++i) __builtin_amdgcn_raw_ptr_buffer_load_lds(rC, (lds_void_ptr)(size_t)(lds0 + RING + i * 1024), 16, lx * 16, RTAB + m0 * 16 + i * 1024, 0, 16);
                }
              }
              if (j == jf + 3) wait_vm<0>();
            }
            if ((tmask >> j) & 1) touch((const void*)tb, tv, (((j - tj) >> tsh) & 15) * ts, tl);
          }
          __builtin_amdgcn_sched_barrier(0);
        }
        int xp = xs + 2; if (xp >= 3) xp -= 3;
        const int ws = ((j & 1) * 2) ^ WR;
        auto lda = [&](int i) { ldW(i, j + 1, ws ^ 2); };
        auto ldb = [&](int i) {
            if constexpr (LX) ldX(i % XI, j + 2, i / XI, xp);
            else __builtin_amdgcn_raw_ptr_buffer_load_lds(rC, (lds_void_ptr)(size_t)(lds0 + CPB + wave * 8192 + i * 1024), 16, lane * 16, (tn * 2 + wn) * 7168 + i * 1024, 0, 16); };
        using NLA = std::integral_constant<int, LW ? WI : 0>; using NLB = std::integral_constant<int, LX ? 2 * XI : (LP ? 7 : 0)>;
        { const RdCtx c = mkctx(xs * XT, ws * WU);
          pairs(I0{}, I28{}, T{}, CSc, c, [&](auto pc) { constexpr int p = decltype(pc)::value;
            if constexpr (LDR && p == 0) loop_barrier();
            if constexpr (!LDR && p == KN_GE) { asm volatile("s_waitcnt lgkmcnt(0)" ::: "memory"); if (decltype(WAITc)::value == 0 || j + 1 < NT) loop_barrier(); }
            static_for<0, NLA::value>([&](auto ic) { constexpr int i = decltype(ic)::value; if constexpr (i * 13 / 7 == p) lda(i); });
            static_for<0, NLB::value>([&](auto ic) { constexpr int i = decltype(ic)::value; if constexpr (14 + i * 13 / 8 == p) ldb(i); }); }); }
        xs = xs + 1; if (xs >= 3) xs = 0;
      };
      using WX_ = std::integral_constant<int, 2 * XI>;
      bf16x8 pv[8];
      if constexpr (LDR) {
        for (int j = 1; j < NT - 2; ++j) iter(j, T{}, T{}, WX_{}, IM1{});
#define KN_PRI 0
        if (KN_PRI == 2) __builtin_amdgcn_s_setprio(3);
        iter(NT - 2, T{}, F{}, WX_{}, IM1{});
        if (KN_PRI == 1) __builtin_amdgcn_s_setprio(3);
        iter(NT - 1, F{}, F{}, std::integral_constant<int, 7>{}, IM1{});
        wait_vm<0>();
        int lc = lane; asm volatile("" : "+v"(lc));
        const unsigned ca = lds0 + CPB + wave * 8192 + (lc >> 4) * 1792 + ((lc & 15) < 14 ? lc & 15 : 0) * 16;
        static_for<0, 8>([&](auto ic) { constexpr int i = decltype(ic)::value; pv[i] = ds_b128<i * 224>(ca); });
      } else {
        int j = 1;
        {
          if (wave == 4) {
            wait_vm<0>();
            if (lds_missing(lds0 + TCH + 256, tag, LO32)) wait_flags(a.sync + FHF(panel), (lane4() & 127) * 32, tag, LO32);
          }
        }
        loop_barrier();
#pragma unroll 1
        for (; j < js; ++j) iter(j, F{}, F{}, I0{}, IM1{});
        const int ja = wm == 2 ? js + 2 : js;
#pragma unroll 1
        for (; j < ja; ++j) iter(j, F{}, F{}, I0{}, I0{});
#pragma unroll 1
        for (; j < js + 2; ++j) iter(j, F{}, F{}, I0{}, I1{});
        { int lx = lane; asm volatile("" : "+v"(lx));
          __builtin_amdgcn_raw_buffer_store_b128(__builtin_bit_cast(u32x4, accs), rC, (lx & 15) < NF ? (unsigned)((tn * 4 + wn * 2) * 3584 + (tm * 2 + (wm & 1)) * 224
                  + ((lx & 15) < 6 ? (lx & 1) * 3584 + ((lx & 15) >> 1) * 64 + (lx >> 4) * 16 : (lx >> 5) * 3584 + 192 + (lx & 16))) : 0x80000000u, 0, 16); }
#pragma unroll 1
        for (; j < NT; ++j) iter(j, F{}, F{}, I1{}, IM1{});
        done_inc();
      }
      { const RdCtx c = mkctx(0, 0); pairs(I0{}, I12{}, F{}, IM1{}, c, [&](auto pc) {
          if constexpr (LDR && decltype(pc)::value == 4) {
            asm volatile("s_waitcnt lgkmcnt(0)" ::: "memory");
#pragma unroll
            for (int k = 0; k < 8; ++k) { bf16x8& q = pv[k]; asm volatile("" : "+v"(q)); }
            f32x4 cv = ((__builtin_bit_cast(f32x4, pv[0]) + __builtin_bit_cast(f32x4, pv[1])) + (__builtin_bit_cast(f32x4, pv[2]) + __builtin_bit_cast(f32x4, pv[3])))
                     + ((__builtin_bit_cast(f32x4, pv[4]) + __builtin_bit_cast(f32x4, pv[5])) + (__builtin_bit_cast(f32x4, pv[6]) + __builtin_bit_cast(f32x4, pv[7])));
#pragma unroll
            for (int k = 0; k < 4; ++k) { const float t = cv[k]; auto sw = __builtin_amdgcn_permlane16_swap(__float_as_uint(t), __float_as_uint(t), false, false); cv[k] = __uint_as_float(sw[0]) + __uint_as_float(sw[1]); }
            if ((lane & 31) < 14) *(f32x4*)(tb_cs + (lane >> 5) * 112 + wn * 56 + fr * 4) = cv;
            done_inc();
          } }); }
      if constexpr (LDR) __builtin_amdgcn_s_setprio(0);
      epilogue(LDRc);
    };
    if (wave < NLW) run(T{}); else run(F{});
  }
  STAMP(7);
  STAMP(8);
  STAMPW(12, 4);
  STAMP(3);
#endif
}

#define KN_PT 13
#define KN_QR 0
#define KN_PS 0
#define KN_PG -1
#define KN_FW 1
#define KN_GQ 6
#define KN_G0 4
#define KN_OC 12
#define KN_TG 4
#define KN_PQ 1
#define KN_PZ 0
#define KN_PN 4
#define KN_H0 0
#define KN_HS 0
#define KN_HN 7
#define KN_X2 0
#define KN_MP 0
#define KN_HD 0
#define KN_HM 7
#define KN_HT 1
#define KN_QO1 3
#define KN_QO2 3
#define KN_QO3 3
#define KN_QO4 16
#define KN_QP 2
#define KN_QS 18
#define KN_QLB 13

constexpr int QXT = 144 * 128, QWU = 24576, QWB = 3 * QXT, QRING = QWB + 4 * QWU;
constexpr int QMU = QRING + 64, QRS = QMU + 576, QFL = QRS + 576, QTCH = QRING + 2304;
constexpr int QCS = 2 * QXT, QKH = QCS + 2560, QVH = QKH + 4096;
#define KN_QWC 8
struct SchedQ {
  static constexpr int ns(int j) { return j < 2 ? 2 : 0; }
  static constexpr int nx(int j) { return (j + 2 < 16 ? 4 : 0) + ns(j); }
  static constexpr int wh(int j) { if (j < 0) return 12; int v = wh(j - 1) + KN_QWC - ns(j); if (v > 6 * (j + 4)) v = 6 * (j + 4); return v > 96 ? 96 : v; }
  static constexpr int oh(int j) { return j < 13 ? 0 : 2 * (j == 13 ? KN_QO1 : (j == 14 ? KN_QO2 : KN_QO3)); }
  static constexpr int tot(int j) { return j < 0 ? 20 : tot(j - 1) + nx(j) + wh(j) - wh(j - 1) + oh(j) - oh(j - 1); }
  static constexpr int cnt(int j) {
    int pos = j < 2 ? 10 * (j + 1) : tot(j - 3) + 4;
    for (int i = 0; i < j; ++i) if (wh(i - 1) < 6 * (j + 1) && wh(i) >= 6 * (j + 1)) { const int q = tot(i - 1) + nx(i) + 6 * (j + 1) - wh(i - 1); if (q > pos) pos = q; }
    return tot(j - 1) - pos;
  }
};
static_assert(SchedQ::cnt(0) == 10 && SchedQ::cnt(8) == 16 && KN_QO1 <= 3 && KN_QO2 <= 6 && KN_QO3 <= 9 && KN_QWC >= 8, "schedule");
static_assert(SchedQ::cnt(KN_S1P) <= SchedQ::tot(KN_S1P - 1) - SchedQ::tot(0) - 6 && KN_S1C > KN_S1P + 2, "statistics");
struct FragQ { bf16x8 x[4]; s16x4 lo[6], hi[6]; };
__device__ __forceinline__ f32x2 ds_r2(unsigned addr) { f32x2 r; asm volatile("ds_read2_b32 %0, %1 offset1:16" : "=v"(r) : "v"(addr)); return r; }
__device__ __forceinline__ float fma1(float a, float b, float c) { float r; asm("v_fma_f32 %0, %1, %2, %3" : "=v"(r) : "v"(a), "v"(b), "v"(c)); return r; }
__device__ __forceinline__ unsigned k1_phase(const GemmArgs& a) {
  unsigned tag = 0;
#if defined(__HIP_DEVICE_COMPILE__)
  constexpr int K = D_, LDW = 3 * D_, NT = 16, XI = 5, XIMIN = 4, WI = 6;
  extern __shared__ __attribute__((aligned(16))) char smem[];
  STAMP(0);
  const unsigned lds0 = (unsigned)(size_t)(lds_char_ptr)smem;
  const int tid = threadIdx.x, lane = tid & 63;
  const int wave = __builtin_amdgcn_readfirstlane(tid >> 6);
  const int bid = blockIdx.x, xcd = bid & 7, idx = bid >> 3;
  const int tm = (xcd >> 2) * 8 + (idx >> 2), tn = (xcd & 3) * 4 + (idx & 3), m0 = tm * 128;
  const int fr = lane & 15, fq = lane >> 4, wq = (lane >> 2) & 3, wp = lane & 3;
  using I0 = std::integral_constant<int, 0>; using I1 = std::integral_constant<int, 1>; using I2 = std::integral_constant<int, 2>; using I3 = std::integral_constant<int, 3>;
  using T = std::true_type; using F = std::false_type;
  auto touch = [&](const void* base, unsigned voff, unsigned soff, unsigned lim = 0x7fffffffu) {
    __builtin_amdgcn_raw_ptr_buffer_load_lds(__builtin_amdgcn_make_buffer_rsrc((void*)base, 0, lim, 0x00020000), (lds_void_ptr)(size_t)(lds0 + QTCH), 4, voff, soff, 0, 0);
  };
  auto code_touch = [&](int line) {
    unsigned long long c0, c1; CODE_ADDR(c0, "k_all"); CODE_ADDR(c1, "k_zend");
    const unsigned len = c1 - c0 < 262144ull ? (unsigned)(c1 - c0) : 0u;
    touch((const void*)c0, (unsigned)(line * 128), 0, len);
  };
  int hj1, hj2; unsigned htv;
  auto hset = [&]() {
    const int slc = (3 - ((unsigned)(size_t)a.W0 >> 7) - (xcd & 3) * 4) & 7;
    const int hid = (idx >> 2) * 3 + (idx & 3) - ((idx & 3) > slc ? 1 : 0);
    const bool hlp = slc < 4 && (idx & 3) != slc;
    hj1 = hlp ? hid / 3 - KN_HQ : -100; hj2 = hlp ? hid / 3 + 8 - KN_HQ : -100;
    htv = (unsigned)(lane * (LDW * 2) + (hid % 3) * 2048 + ((xcd & 3) * 4 + slc) * 128);
  };
  const unsigned xo = fr * 128 + ((fq ^ ((fr >> 1) & 7)) << 4);
  auto wfrag = [&](int seg) { const int row = 8 * fq + wq; return lds0 + QWB + row * 128 + (((seg << 1) ^ wswz<128>(row)) << 4) + wp * 8; };
  const f32x4 z4 = {0.f, 0.f, 0.f, 0.f};
  auto cat = [](s16x4 lo, s16x4 hi) { return __builtin_shufflevector(lo, hi, 0, 1, 2, 3, 4, 5, 6, 7); };
  unsigned xsrc[XI], wsrc[WI];
  { const int lr = lane >> 3, w4 = wave & 3;
    const unsigned xv = (unsigned)(lr * (K * 2) + (((lane & 7) ^ ((w4 * 4 + (lr >> 1)) & 7)) << 4));
    const int r0 = m0 - 16 + w4 * 8;
#pragma unroll
    for (int i = 0; i < XI; ++i) xsrc[i] = xv + (unsigned)((i == 0 ? (r0 < 0 ? 0 : r0) : r0 + 32 * i) * (K * 2));
    const unsigned wv = (unsigned)((w4 * 8 + lr) * (LDW * 2) + tn * 128 + (((lane & 7) ^ wswz<128>(w4 * 8 + lr)) << 4));
#pragma unroll
    for (int i = 0; i < WI; ++i) wsrc[i] = wv + (unsigned)((i & 1) * 32 * (LDW * 2) + (i >> 1) * 2048); }
  unsigned mb = lds0 + wave * 1024, rx = KN_QR ? ((tm ^ tn) & 1) * 1024 : 0, rw = rx * (64 * LDW * 2 / 128);
  asm volatile("s_waitcnt lgkmcnt(0)" : "+s"(mb), "+s"(rx), "+s"(rw) : "s"(a.warm), "v"(wsrc[0]), "v"(wsrc[1]), "v"(wsrc[2]), "v"(wsrc[3]), "v"(wsrc[4]), "v"(wsrc[5]), "v"(xsrc[0]), "v"(xsrc[1]), "v"(xsrc[2]), "v"(xsrc[3]), "v"(xsrc[4]));
  __builtin_amdgcn_sched_barrier(0);
  const u32x4 qX = mk_rsrc(a.X), qW = mk_rsrc(a.W0);
  auto ldX = [&](auto ic, auto tc) { constexpr int i = decltype(ic)::value, t = decltype(tc)::value; if (i < 4 || wave < 2) ld_cx<t % 3 * QXT + i * 4096, t * 128>(mb, qX, xsrc[i], rx); };
  auto ldW = [&](auto qc) { constexpr int i = decltype(qc)::value % WI, t = decltype(qc)::value / WI; ld_ca<QWB + (t & 3) * QWU + i * 4096, t * (64 * LDW * 2), (t >= 8)>(mb, qW, wsrc[i], rw); };
  if (__builtin_expect(wave < 4, 1)) {
    static_for<0, WI>(ldW);
    static_for<0, XI>([&](auto ic) { ldX(ic, I0{}); });
  }
  __builtin_amdgcn_sched_barrier(0);
  if (__builtin_expect(wave < 4, 1)) {
    unsigned seed;
    asm volatile("s_load_dword %0, %1, %2" : "=s"(seed) : "s"(a.sync), "n"(FEP * 4));
    auto sb0 = []() { __builtin_amdgcn_sched_barrier(0); };
    static_for<WI, WI + 2>(ldW); sb0();
    unsigned xh0[3], xh1[3], wb[2], l16 = lane * 16;
#pragma unroll
    for (int i = 0; i < 3; ++i) { xh0[i] = fr == 0 ? lds0 + QRING : lds0 + i * QXT + xo; xh1[i] = fr == 0 ? lds0 + QRING : lds0 + i * QXT + (xo ^ 64); asm volatile("" : "+v"(xh0[i]), "+v"(xh1[i])); }
    sb0(); static_for<WI + 2, WI + 4>(ldW); sb0();
    wb[0] = wfrag(wave); wb[1] = wb[0] + 2 * QWU;
    u32x4 one1; { const unsigned o = fr == 0 ? 0x3f803f80u : 0u; one1 = (u32x4){o, o, o, o}; }
    const char* srows = (const char*)a.X + (size_t)(m0 + tn * 8 + wave * 2) * (K * 2);
    asm volatile("" : "+v"(wb[0]), "+v"(wb[1]), "+v"(one1), "+v"(l16), "+s"(srows));
    sb0(); static_for<WI + 4, 2 * WI>(ldW); sb0();
    hset();
    asm volatile("" : "+s"(hj1), "+s"(hj2), "+v"(htv));
    sb0(); static_for<0, 2>([&](auto ic) { ldX(ic, I1{}); }); sb0();
    unsigned long long tb = (unsigned long long)((wave == 3 || wave == 0) ? a.PF1 : a.W0);
    unsigned tv, ts, to, evm;
    { const int t1 = (bid + 256) / 48 - KN_GQ, t2 = (bid + 512) / 48 - KN_GQ;
      const unsigned e0 = KN_OC ? 1u << KN_OC : 0u, e1 = KN_GQ ? (1u << (t1 > 0 ? t1 : 0)) | (1u << t2) : 0u, e2 = (hj1 >= 0 ? 1u << hj1 : 0u) | (hj2 > 0 ? 1u << hj2 : 0u), e3 = 2u << (idx % 12);
      evm = wave == 0 ? e0 : (wave == 1 ? e1 : (wave == 2 ? e2 : e3));
      ts = wave == 1 ? 2u << 20 : 8 * 64 * LDW * 2;
      to = wave == 1 ? 2u << 20 : (wave == 2 ? ((hj1 >= 0 ? hj1 : hj2) + KN_HQ) * (64 * LDW * 2) : 0); }
    asm volatile("" : "+s"(evm), "+s"(ts), "+s"(to), "+s"(tb));
    sb0(); static_for<2, 4>([&](auto ic) { ldX(ic, I1{}); }); sb0();
    tv = (wave & 1) ? (unsigned)((bid * 64 + lane) * 128)
       : (wave ? htv : (lane < 32 ? (unsigned)((idx * 32 + lane) * 2048 + (xcd & 1) * 1024 + ((3 - ((unsigned)(size_t)a.PF1 >> 7)) & 7) * 128) : 0x80000000u));
    asm volatile("" : "+v"(tv));
    sb0(); ldX(std::integral_constant<int, 4>{}, I1{}); sb0();
    asm volatile("s_waitcnt lgkmcnt(0)\n\ts_mul_i32 %0, %1, 0x9e3779b1\n\ts_add_u32 %0, %0, 0x7f4a7c15" : "=s"(tag) : "s"(seed) : "scc");
    unsigned osrc, gv, sdst, ssrc, sld, smu, srs, fa0, fa1, fa2, mko;
    auto ldO = [&](auto qc) { constexpr int i = decltype(qc)::value >> 1, h = decltype(qc)::value & 1;
      ld_cx<wo_pos(i >> 1) + (i & 1) * 8192 + h * 4096, (((i & 1) * 8 + (i >> 1)) * 2 + h) * (32 * D_ * 2)>(mb, mk_rsrc(a.PF1), osrc, (xcd & 3) * 8 * (32 * D_ * 2)); };
    u32x4 sv[4]; f32x4 s1s[2], s1q[2]; float sa[4], sb;
    auto dpp = [](float t, auto c) { return __int_as_float(__builtin_amdgcn_update_dpp(0, __float_as_int(t), decltype(c)::value, 0xf, 0xf, false)); };
    auto stat = [&](auto kc) {
      constexpr int k = decltype(kc)::value;
      if constexpr (k == 0) asm volatile("" : "+v"(sv[0]), "+v"(sv[1]), "+v"(sv[2]), "+v"(sv[3]));
      if constexpr (k < 8) {
        constexpr int r = (k >> 1) & 1, h = k >> 2;
        const u32x4 one4 = {0x3f803f80u, 0x3f803f80u, 0x3f803f80u, 0x3f803f80u};
        const bf16x8 x = __builtin_bit_cast(bf16x8, sv[2 * r + h]);
        if constexpr (k & 1) s1q[r] = __builtin_amdgcn_mfma_f32_16x16x32_bf16(x, x, h ? s1q[r] : z4, 0, 0, 0);
        else s1s[r] = __builtin_amdgcn_mfma_f32_16x16x32_bf16(__builtin_bit_cast(bf16x8, one4), x, h ? s1s[r] : z4, 0, 0, 0);
      }
      if constexpr (k == 8 || k == 9) {
        constexpr int r = k - 8;
        const f32x4 g = s1q[r];
        const float d = (lane & 2) ? ((lane & 1) ? g[3] : g[2]) : ((lane & 1) ? g[1] : g[0]);
        sa[r] = fq == 0 ? s1s[r][0] : 0.f;
        sa[2 + r] = fq == wq ? d : 0.f;
        asm volatile("" : "+v"(sa[r]), "+v"(sa[2 + r]));
      }
      if constexpr (k == 10 || k == 11) { constexpr int i = (k - 10) * 2; sa[i] = ((lane & 1) ? sa[i + 1] : sa[i]) + dpp((lane & 1) ? sa[i] : sa[i + 1], std::integral_constant<int, 0xb1>{}); asm volatile("" : "+v"(sa[i])); }
      if constexpr (k == 12) sb = ((lane & 2) ? sa[2] : sa[0]) + dpp((lane & 2) ? sa[0] : sa[2], std::integral_constant<int, 0x4e>{});
      if constexpr (k == 13) { sb += dpp(sb, std::integral_constant<int, 0x124>{}); sb += dpp(sb, std::integral_constant<int, 0x128>{}); }
      if constexpr (k == 14) { auto q = __builtin_amdgcn_permlane16_swap(__float_as_uint(sb), __float_as_uint(sb), false, false); sb = __uint_as_float(q[0]) + __uint_as_float(q[1]); }
      if constexpr (k == 15) { auto q = __builtin_amdgcn_permlane32_swap(__float_as_uint(sb), __float_as_uint(sb), false, false); sb = (__uint_as_float(q[0]) + __uint_as_float(q[1])) * (1.f / 1024.f); }
      if constexpr (k == 16) { const float var = dpp(sb, std::integral_constant<int, 0x4e>{}) - sb * sb; sa[0] = __builtin_amdgcn_rsqf((var > 0.f ? var : 0.f) + 1e-5f); asm volatile("" : "+v"(sa[0])); }
      if constexpr (k >= 12 && k < 16) asm volatile("" : "+v"(sb));
      if constexpr (k == 17) {
        __builtin_amdgcn_raw_buffer_store_b128((u32x4){__float_as_uint(-sb), __float_as_uint(sa[0]), tag, ~tag}, __builtin_amdgcn_make_buffer_rsrc((void*)a.cs, 0, 0x7fffffff, 0x00020000), sdst, 0, 16);
        asm volatile("s_nop 1" ::: "memory");
      }
    };
    f32x4 acq = z4, ack = z4, acv = z4;
    const auto rC = __builtin_amdgcn_make_buffer_rsrc((void*)a.cs, 0, 0x7fffffff, 0x00020000);
    auto fetch = [&]() { __builtin_amdgcn_raw_ptr_buffer_load_lds(rC, (lds_void_ptr)(size_t)(lds0 + QRING + STL + wave * 1024), 16, ssrc, 0, 0, 16); };
    u32x4 ent; f32x4 hm4; int hm1, htg;
    auto olane = [&]() { int lx = lane; asm volatile("" : "+v"(lx)); return lx; };
    auto chore = [&](auto jc, auto gc) {
      constexpr int j = decltype(jc)::value, g = decltype(gc)::value;
      if constexpr (j == 1 && g == -1) ent = __builtin_bit_cast(u32x4, ds_b128<0>(lds0 + QFL + 16));
      if constexpr (j == 1 && g == 5) {
        asm volatile("" : "+v"(ent));
        if (wave == 3) __builtin_amdgcn_raw_buffer_store_b32((tag & ~1u) | (((ent[0] + ent[2] > ent[1] + ent[3] ? 0u : 1u) ^ (xcd >> 2)) & 1u), rC, lane == 0 ? (unsigned)(VTAB + bid * 4) : 0x80000000u, 0, 16);
      }
      if constexpr (j == 2 && g == 1) { const int lx = olane(); sdst = lx < 2 ? (unsigned)(STAB + (m0 + tn * 8 + wave * 2 + lx) * 16) : 0x80000000u; asm volatile("" : "+v"(sdst)); }
      if constexpr (j == KN_S1P && g >= 0 && g < 4) {
        if (KN_MP) { __builtin_amdgcn_s_setprio(3); __builtin_amdgcn_sched_barrier(0); }
        stat(std::integral_constant<int, 2 * g>{}); stat(std::integral_constant<int, 2 * g + 1>{});
        if (KN_MP) { __builtin_amdgcn_sched_barrier(0); __builtin_amdgcn_s_setprio(0); } }
      if constexpr (j == KN_S1P && (g == 9 || g == 10)) stat(std::integral_constant<int, g - 1>{});
      if constexpr (j == KN_S1P + 1 && g >= 0 && g < 8) stat(std::integral_constant<int, 10 + g>{});
      if constexpr (j == 5 && g == 1) { const int se = wave * 64 + olane(), sr = m0 - 16 + se;
        ssrc = se < 144 ? (unsigned)(STAB + (sr < 0 ? se : sr) * 16) : (wave == 3 ? (unsigned)(VTAB - 3072 + se * 16) : 0x80000000u); sld = lds0 + QRING + STL + se * 16; asm volatile("" : "+v"(ssrc), "+v"(sld)); }
      if constexpr (j == 5 && g == 5) { const int se = wave * 64 + olane();
        smu = lds0 + (se < 144 ? QMU + se * 4 : QTCH); srs = lds0 + (se < 144 ? QRS + (se & 1) * 288 + (se >> 1) * 4 : QTCH); asm volatile("" : "+v"(smu), "+v"(srs)); }
      if constexpr (j == 6 && g == 1) { const int lx = olane(); osrc = (unsigned)((wave * 8 + (lx >> 3)) * D_ + ((xcd & 1) * 8 + (idx & 7)) * 64 + ((lx & 7) ^ ((lx >> 3) & 6)) * 8) * 2u; asm volatile("" : "+v"(osrc)); }
      if constexpr (j == 6 && g == 5) { const int lx = olane(); gv = lx < 56 ? (unsigned)(((bid & 1) * 512 + (wave >> 1) * 128 + (bid >> 1)) * (DF_ * 2) + lx * 128) : 0x80000000u; asm volatile("" : "+v"(gv)); }
      if constexpr (j == 7 && g == 1) { const int lx = olane();
        fa0 = lds0 + QCS + ((lx & 15) == 0 ? wave * 64 + lx : 1024 + lx * 16); fa1 = lds0 + QCS + (lx < 16 ? 512 + wave * 64 : 1024) + lx * 4; asm volatile("" : "+v"(fa0), "+v"(fa1)); }
      if constexpr (j == 7 && g == 5) { fa2 = lds0 + (wave >> 1) * 1024 + (wave & 1) * 8 + l16;
        const int p2 = (xcd >> 1) * 2 + (idx >> 4), mq = (xcd & 1) * 16 + (idx & 15);
        mko = rbH(p2 * 16 + (mq >> 1), xcd >> 2) + omH((xcd >> 2) * 16384) + (mq & 1) * 3072; asm volatile("" : "+v"(fa2), "+s"(mko)); }
      if constexpr (j == KN_S1C && g == 10) fetch();
      if constexpr (j == KN_S1C + 3 && g == -1) ent = __builtin_bit_cast(u32x4, ds_b128<0>(sld));
      if constexpr (j == KN_S1C + 3 && g == 5) {
        static_assert(SchedQ::cnt(j) <= SchedQ::tot(j - 1) - SchedQ::tot(KN_S1C) && KN_S1C >= 8, "table");
        asm volatile("" : "+v"(ent));
        auto stale = [&]() { return __builtin_amdgcn_ballot_w64(wave == 3 ? ((ent[0] ^ tag) | (ent[1] ^ tag) | (ent[2] ^ tag) | (ent[3] ^ tag)) > 1u : (int)ssrc >= 0 && (ent[2] != tag || ent[3] != ~tag)) != 0; };
        if (__builtin_expect(stale(), 0)) {
          unsigned polls = 0;
          do {
            fetch();
            wait_vm<0>();
            ent = __builtin_bit_cast(u32x4, ds_b128<0>(sld));
            asm volatile("s_waitcnt lgkmcnt(0)" : "+v"(ent) :: "memory");
          } while (stale() && ++polls < 4000000u);
        }
      }
      if constexpr (j == KN_S1C + 3 && g == 8) {
        ds_w32(smu, (int)ent[0]); ds_w32(srs, (int)ent[1]);
        if (wave == 3) {
          int pc = 0;
#pragma unroll
          for (int i = 0; i < 4; ++i) pc += __builtin_popcountll(__builtin_amdgcn_ballot_w64(ent[i] & 1));
          tag = (tag & ~1u) | (pc > 128 ? 1u : 0u);
          ds_w32(lds0 + QFL + 8, (int)tag);
        }
      }
      if constexpr (j == KN_S1C + 4 && g >= 0 && g % 3 == 2) {
        if (wave == 3) {
          __builtin_amdgcn_raw_buffer_store_b128((u32x4){~0u, ~0u, ~0u, ~0u}, __builtin_amdgcn_make_buffer_rsrc((void*)a.st, 0, 0x7fffffff, 0x00020000), l16 + g / 3 * 1024,
                                                 mko ^ ((KN_NUMA & 1) ? ((SIDE ^ (xcd >> 2)) & 1) << 13 : 0), 16);
          asm volatile("s_nop 1" ::: "memory");
        }
      }
      if constexpr (j == NT - 2 && g == -1) { const int lx = olane(); hm4 = __builtin_bit_cast(f32x4, ds_b128<0>(lds0 + QMU + (lx & 48))); hm1 = ds_r32(lds0 + QMU + (lx & 15) * 4); htg = ds_r32(lds0 + QFL + 8); }
      if constexpr (j == NT - 2 && g == 5) { asm volatile("" : "+v"(hm4), "+v"(hm1), "+v"(htg)); tag = __builtin_amdgcn_readfirstlane(htg); }
      if constexpr (j == NT - 3 && g == 8 && (KN_CT & 2)) { if (wave == 2) code_touch(lane * 32 + idx); }
    };
    static_assert(KN_S1C + 4 <= NT - 4 && SchedQ::cnt(NT - 1) <= SchedQ::tot(NT - 2) - SchedQ::tot(KN_S1C + 4) && KN_H0 == 0, "marks");
    struct FragH { bf16x8 x[2]; s16x4 lo[2][3], hi[2][3]; } hf[2];
    auto mm = [&](FragH& f, auto ic) { constexpr int n = decltype(ic)::value;
      asm volatile("" : "+v"(f.lo[0][n]), "+v"(f.hi[0][n]), "+v"(f.lo[1][n]), "+v"(f.hi[1][n]));
      if constexpr (n == 1) asm volatile("" : "+v"(f.x[0]), "+v"(f.x[1]));
#pragma unroll
      for (int h = 0; h < 2; ++h) {
        if constexpr (n == 0) acq = __builtin_amdgcn_mfma_f32_16x16x32_bf16(cat(f.lo[h][0], f.hi[h][0]), __builtin_bit_cast(bf16x8, one1), acq, 0, 0, 0);
        if constexpr (n == 1) ack = __builtin_amdgcn_mfma_f32_16x16x32_bf16(cat(f.lo[h][1], f.hi[h][1]), f.x[h], ack, 0, 0, 0);
        if constexpr (n == 2) acv = __builtin_amdgcn_mfma_f32_16x16x32_bf16(f.x[h], cat(f.lo[h][2], f.hi[h][2]), acv, 0, 0, 0);
      } };
    unsigned tsq = 0;
    using SQ = SchedQ;
    static_for<0, NT>([&](auto jc) {
      constexpr int j = decltype(jc)::value, NX = (j + 2 < NT ? XI : 0) + SQ::ns(j), NW = SQ::wh(j) - SQ::wh(j - 1), NL = NX + NW + SQ::oh(j) - SQ::oh(j - 1);
      wait_vm<SQ::cnt(j)>();
      lds_barrier();
      auto rd = [&](auto rc) { constexpr int r = decltype(rc)::value, q = r < 4 ? r : r - 2, n = q >> 2, h = (q >> 1) & 1, o = (j & 1) * QWU + h * 4096 + n * 8192;
        FragH& f = hf[j & 1];
        if constexpr (r == 4 || r == 5) f.x[r - 4] = ds_b128<0>(r == 5 ? xh1[j % 3] : xh0[j % 3]);
        else if constexpr (q & 1) f.hi[h][n] = ds_tr<o + 512>(wb[(j >> 1) & 1]); else f.lo[h][n] = ds_tr<o>(wb[(j >> 1) & 1]); };
      chore(jc, std::integral_constant<int, -1>{});
      constexpr int H0 = j < NT - 1 ? KN_H0 : 0, NG = NL > 10 + H0 ? NL : 10 + H0;
      if constexpr (KN_HS > 0) asm volatile("s_nop %0" :: "n"(KN_HS - 1));
      static_for<0, NG>([&](auto gc) {
        constexpr int g = decltype(gc)::value, h = g - H0;
        __builtin_amdgcn_sched_barrier(0);
        if constexpr (g < NX - SQ::ns(j)) ldX(gc, std::integral_constant<int, j + 2>{});
        else if constexpr (g < NX) {
          constexpr int q = 2 * j + g - XI;
          asm volatile("s_nop 4\n\tglobal_load_dwordx4 %0, %1, %2 offset:%3" : "=&v"(sv[q]) : "v"(l16), "s"(srows), "n"(q * 1024) : "memory");
        }
        else if constexpr (g < NX + NW) ldW(std::integral_constant<int, SQ::wh(j - 1) + g - NX>{});
        else if constexpr (g < NL) ldO(std::integral_constant<int, SQ::oh(j - 1) + g - NX - NW>{});
        else if constexpr (h < 9) asm volatile("s_nop %0" :: "n"(KN_HN));
        if constexpr (j <= 12 && g == KN_TG) { if (__builtin_expect((evm >> j) & 1, 0)) { touch((const void*)tb, tv, to); to += ts; } }
        if constexpr (g == (KN_PG < 0 ? NG - 1 : KN_PG) && j <= KN_PT && j >= KN_PT - 3 * KN_PS && (KN_PS == 0 || (KN_PT - j) % (KN_PS ? KN_PS : 1) == 0)) {
          if (KN_PS == 0 || wave == 3 - (KN_PT - j) / (KN_PS ? KN_PS : 1)) touch(wave & 1 ? a.CW1 : a.CW0, gv, 0);
        }
        __builtin_amdgcn_sched_barrier(0);
        if constexpr (h >= 0 && h < 7) { rd(std::integral_constant<int, 2 * h>{}); rd(std::integral_constant<int, 2 * h + 1>{}); }
        if constexpr (j >= 1 && j < KN_HD + 1 && g >= KN_HM && g < KN_HM + 3 * KN_HT && (g - KN_HM) % KN_HT == 0) mm(hf[~j & 1], std::integral_constant<int, (g - KN_HM) / KN_HT>{});
        if constexpr (j >= KN_HD && (h == 4 || h == 7 || h == 9)) { asm volatile("s_waitcnt lgkmcnt(%0)" :: "n"(h == 4 ? 6 : (h == 7 ? 4 : 0)) : "memory"); mm(hf[j & 1], std::integral_constant<int, h == 4 ? 0 : (h == 7 ? 1 : 2)>{}); }
        if constexpr (g == 5 && (j == 1 || j == KN_S1C + 3 || j == NT - 2)) asm volatile("s_waitcnt lgkmcnt(12)" ::: "memory");
        __builtin_amdgcn_sched_barrier(0);
        chore(jc, gc);
      });
      __builtin_amdgcn_sched_barrier(0);
    });
    {
      f32x4 ck;
#pragma unroll
      for (int e = 0; e < 4; ++e) ck[e] = dpp(ack[e], std::integral_constant<int, 0x150>{});
      const auto c1 = __builtin_amdgcn_permlane16_swap(__float_as_uint(acv[0]), __float_as_uint(acv[0]), false, false);
      const auto c2 = __builtin_amdgcn_permlane32_swap(c1[0], c1[0], false, false);
      const float nm = __int_as_float(hm1), nc = __uint_as_float(c2[0]);
      asm volatile("ds_write_b128 %0, %1\n\tds_write_b128 %0, %2 offset:256\n\tds_write_b32 %3, %4" :: "v"(fa0), "v"(acq), "v"(ck), "v"(fa1), "v"(nc) : "memory");
      const u32x2 kk = {pk_bf16(fma1(nm, ck[0], ack[0]), fma1(nm, ck[1], ack[1])), pk_bf16(fma1(nm, ck[2], ack[2]), fma1(nm, ck[3], ack[3]))};
      const u32x2 vv = {pk_bf16(fma1(nc, hm4[0], acv[0]), fma1(nc, hm4[1], acv[1])), pk_bf16(fma1(nc, hm4[2], acv[2]), fma1(nc, hm4[3], acv[3]))};
      asm volatile("ds_write_b64 %0, %1 offset:%3\n\tds_write_b64 %0, %2 offset:%4" :: "v"(fa2), "v"(kk), "v"(vv), "n"(QKH), "n"(QVH) : "memory");
    }
    lds_barrier();
    if (tid == 192) set_flag(a.sync + FHM((xcd >> 1) * 2 + (idx >> 4)) + (xcd & 1) * 16 + (idx & 15), tag);
    static_for<SQ::oh(NT - 1), 2 * KN_QO4>(ldO);
    wait_vm<2 * (KN_QO4 - 2)>();
    const int sd = SIDE;
    unsigned fo = tid == 0 ? (unsigned)(FLO(tm) + tn * 32) * 4u : 0x80000000u, ft = tag;
    const auto rF = __builtin_amdgcn_make_buffer_rsrc((void*)a.sync, 0, 147456, 0x00020000);
    asm volatile("" : "+v"(fo), "+v"(ft) : "s"(rF));
    raw_barrier();
    if (wave == 0) __builtin_amdgcn_raw_buffer_store_b32(ft, rF, fo, 0, 16);
    static_for<2 * KN_QO4, 32>(ldO);
  } else {
    const int wm = (wave >> 1) & 1, hd = wave & 1;
    if (wave == 4) {
      if (lane < 4) { *(unsigned*)(smem + QRING + lane * 4) = 0x3f803f80u; if (lane < 2) *(unsigned*)(smem + QFL + lane * 4) = 0u; }
      if (KN_FW) __builtin_amdgcn_raw_ptr_buffer_load_lds(__builtin_amdgcn_make_buffer_rsrc((void*)a.sync, 0, 147456, 0x00020000), (lds_void_ptr)(size_t)(lds0 + QTCH), 4, lane * 32768 + bid * 128, 0, 0, 16);
    }
    if (KN_X2 && wave == 5 && bid < 32 * KN_X2) touch(a.X, (unsigned)((bid * (64 / (KN_X2 ? KN_X2 : 1)) + lane / (KN_X2 ? KN_X2 : 1)) * (K * 2) + 256 + lane % (KN_X2 ? KN_X2 : 1) * 128), 0);
    if (wave == 6) { hset(); if (hj1 < 0 && hj1 > -100) touch(a.W0, htv, (hj1 + KN_HQ) * (64 * LDW * 2));
      if (KN_G0 && bid >= KN_G0 * 48) touch(a.W0, (bid * 64 + lane) * 128, 0); }
    if ((KN_CT & 1) && wave == 7) code_touch(lane * 32 + idx);
    {
      const int id = bid * 2 + wm;
      const char* pb = (const char*)a.sync + PRB + (id >> 3) * 16384 + hd * 8192 + (id & 7) * (KN_PN == 8 ? 128 : 256);
      unsigned long long p0, p1; unsigned t;
#define SL4(a, b) "s_load_dword %1, %3, 0x" a "00\n\ts_load_dword %1, %3, 0x" b "00\n\ts_load_dword %1, %3, 0x1" a "00\n\ts_load_dword %1, %3, 0x1" b "00\n\t"
      if (KN_PN == 8) asm volatile("s_sleep %5\n\ts_memtime %0\n\ts_waitcnt lgkmcnt(0)\n\t" SL4("0", "8") SL4("4", "c") "s_waitcnt lgkmcnt(%4)\n\ts_memtime %2\n\ts_waitcnt lgkmcnt(0)" : "=&s"(p0), "=&s"(t), "=&s"(p1) : "s"(pb), "n"(KN_PQ), "n"(KN_PZ));
      else asm volatile("s_sleep %5\n\ts_memtime %0\n\ts_waitcnt lgkmcnt(0)\n\t" SL4("0", "8") "s_waitcnt lgkmcnt(%4)\n\ts_memtime %2\n\ts_waitcnt lgkmcnt(0)" : "=&s"(p0), "=&s"(t), "=&s"(p1) : "s"(pb), "n"(KN_PQ), "n"(KN_PZ));
      ds_w32(lds0 + QFL + 16 + (wave - 4) * 4, (int)((unsigned)p1 - (unsigned)p0));
    }
    f32x2 bias[2], bias0[2];
    {
      const int srow = m0 + wm * 64 + fr;
      const unsigned mk = 0x5555u & ((2u << (srow < 15 ? srow : 15)) - 1u), sft = (fr & 14) - fq * 4 + 14;
      const unsigned b0 = ~(0x5555u >> sft), b1 = ~(0x55550000u >> sft), c0 = ~(mk >> sft), c1 = ~((mk << 16) >> sft);
#pragma unroll
      for (int j = 0; j < 4; ++j) {
        bias[j >> 1][j & 1] = __uint_as_float((unsigned)__builtin_amdgcn_sbfe((int)(j < 2 ? b0 : b1), 2 - 2 * (j & 1), 1) & 0xff61b1e6u);
        bias0[j >> 1][j & 1] = __uint_as_float((unsigned)__builtin_amdgcn_sbfe((int)(j < 2 ? c0 : c1), 2 - 2 * (j & 1), 1) & 0xff61b1e6u);
      }
    }
    unsigned pl = (fr & 1) ? 0x01000c0cu : 0x0c0c0100u, ph = (fr & 1) ? 0x03020c0cu : 0x0c0c0302u;
    unsigned ta = lds0 + QRING + STL + (16 + wm * 64 + fr) * 16, tm4 = lds0 + QMU + (16 + wm * 64 + fq * 4) * 4, trk = lds0 + QRS + (fr & 1) * 288 + (wm * 32 + fq * 2) * 4;
    unsigned tcs = lds0 + hd * 128 + fq * 16, tcv = lds0 + QCS + 512 + hd * 128 + fr * 4, tex = lds0 + hd * 1024 + lane * 16, tfl = lds0 + QFL + hd * 4, odst = rbH((m0 >> 4) + wm * 4, xcd >> 2) + omH(tn * 2048) + hd * 1024 + lane * 16;
    asm volatile("" : "+v"(bias[0]), "+v"(bias[1]), "+v"(bias0[0]), "+v"(bias0[1]), "+v"(pl), "+v"(ph), "+v"(ta), "+v"(tm4), "+v"(trk), "+v"(tcs), "+v"(tcv), "+v"(tex), "+v"(tfl), "+v"(odst));
    if (KN_MP) __builtin_amdgcn_s_setprio(1);
    f32x4 acc[6][4];
#pragma unroll
    for (int n = 0; n < 6; ++n)
#pragma unroll
      for (int m = 0; m < 4; ++m) acc[n][m] = z4;
    const unsigned xa[2] = {lds0 + (16 + wm * 64) * 128 + xo, lds0 + (16 + wm * 64) * 128 + (xo ^ 64)}, wa[2] = {wfrag(hd * 2), wfrag(hd * 2 + 1)};
    struct Ctx { unsigned x[2], w[2]; } c = {{xa[0], xa[1]}, {wa[0], wa[1]}};
    using SP = SchedP<6, KN_QS>;
    FragQ fa, fb;
    auto rd = [&](auto rc) {
      constexpr int r = decltype(rc)::value;
      if constexpr (r >= 24) { constexpr int m = (r - 24) >> 1; if constexpr (r & 1) fb.x[m] = ds_b128<m * 2048>(c.x[1]); else fa.x[m] = ds_b128<m * 2048>(c.x[0]); }
      else { constexpr int n = r >> 2, o = ((r >> 1) & 1) * 4096 + (n >> 1) * 8192 + (r & 1) * 512; FragQ& f = (r & 2) ? fb : fa;
        if constexpr (r & 1) f.hi[n] = ds_tr<o>(c.w[n & 1]); else f.lo[n] = ds_tr<o>(c.w[n & 1]); }
    };
    auto pin = [&](auto pc) {
      static_for<0, 32>([&](auto rc) { constexpr int r = decltype(rc)::value;
        if constexpr (SP::need(r) == decltype(pc)::value) {
          FragQ& f = (r >= 24 ? r & 1 : r & 2) ? fb : fa;
          if constexpr (r >= 24) { bf16x8& q = f.x[(r - 24) >> 1]; asm volatile("" : "+v"(q)); } else { s16x4& q = (r & 1) ? f.hi[r >> 2] : f.lo[r >> 2]; asm volatile("" : "+v"(q)); } } });
    };
    int sx = QXT, sw = QWU;
    auto pairs = [&](auto PBc, auto PEc, auto RDc, auto&& gap) {
      constexpr bool RD = decltype(RDc)::value;
      static_for<decltype(PBc)::value, decltype(PEc)::value>([&](auto pc) {
        constexpr int p = decltype(pc)::value, A = SP::pa(p), n = SP::pn(A), m = SP::pm(A);
        if constexpr (SP::cnt(p) >= 0 && (!KN_EW || p == decltype(PBc)::value)) { asm volatile("s_waitcnt lgkmcnt(%0)" :: "n"(SP::cnt(p)) : "memory"); pin(pc); }
        static_for<2 * p, 2 * p + 2>([&](auto gc) {
          constexpr int g = decltype(gc)::value;
          FragQ& u = ((A ^ g) & 1) ? fb : fa;
          if constexpr (n < 4) acc[n][m] = __builtin_amdgcn_mfma_f32_16x16x32_bf16(cat(u.lo[n], u.hi[n]), u.x[m], acc[n][m], 0, 0, 0);
          else acc[n][m] = __builtin_amdgcn_mfma_f32_16x16x32_bf16(u.x[m], cat(u.lo[n], u.hi[n]), acc[n][m], 0, 0, 0);
          __builtin_amdgcn_sched_barrier(0);
          if constexpr (KN_EW && RD && !(g & 1) && SP::cnt(p + 1, 1) >= 0) { asm volatile("s_waitcnt lgkmcnt(%0)" :: "n"(SP::cnt(p + 1, 1)) : "memory"); pin(std::integral_constant<int, p + 1>{}); }
          gap(gc);
          if constexpr (RD) {
            static_for<0, 32>([&](auto rc) { if constexpr (SP::gap(decltype(rc)::value) == g) rd(rc); });
            if constexpr (g == 13) { sx = sx + QXT; sw = sw + QWU; asm volatile("" : "+s"(sx), "+s"(sw)); }
            if constexpr (g == 17) { sx = sx == 3 * QXT ? 0 : sx; asm volatile("" : "+s"(sx)); }
            if constexpr (g == 21) { sw = sw == 4 * QWU ? 0 : sw; asm volatile("" : "+s"(sw)); }
            if constexpr (g == 44) { c.x[0] = xa[0] + sx; c.x[1] = xa[1] + sx; asm volatile("" : "+v"(c.x[0]), "+v"(c.x[1])); }
            if constexpr (g == 45) { c.w[0] = wa[0] + sw; c.w[1] = wa[1] + sw; asm volatile("" : "+v"(c.w[0]), "+v"(c.w[1])); }
          }
          __builtin_amdgcn_sched_barrier(0);
        });
      });
      if constexpr (RD && !KN_EW) { asm volatile("s_waitcnt lgkmcnt(0)" ::: "memory"); pin(std::integral_constant<int, 24>{}); }
      static_assert(SP::cnt(24, 1) == 0 && SP::gap(23) < 46, "all reads");
    };
    static_assert(SP::gap(23) <= 44, "addresses");
    using I12 = std::integral_constant<int, 12>; using I24 = std::integral_constant<int, 24>;
    lds_barrier();
    static_for<0, 24>([&](auto gc) { static_for<0, 32>([&](auto rc) { if constexpr (SP::gap(decltype(rc)::value) == decltype(gc)::value) rd(rc); }); });
    STAMP(1);
    pairs(I12{}, I24{}, T{}, [&](auto) {});
#pragma unroll 1
    for (int j = 1; j < NT; ++j) pairs(I0{}, I24{}, T{}, [&](auto pc) { if constexpr (decltype(pc)::value == 1) raw_barrier(); });
#define ES(i)
    ES(0);
    if (KN_QP) __builtin_amdgcn_s_setprio(KN_QP);
    f32x2 st[4], rk[5], cv, fs[4][2]; f32x4 m4[4], cs[4]; int tg;
    pairs(I0{}, I12{}, F{}, [&](auto ic) { constexpr int I = decltype(ic)::value;
      if constexpr (I < 4) st[I] = ds_b64<I * 256>(ta);
      if constexpr (I == 4) tg = ds_r32(lds0 + QFL + 8);
      if constexpr (I < 5) rk[I] = ds_b64<I * 32>(trk);
      if constexpr (I >= 5 && I < 9) m4[I - 5] = __builtin_bit_cast(f32x4, ds_b128<(I - 5) * 64>(tm4));
      if constexpr (I == 9) { asm volatile("s_waitcnt lgkmcnt(4)" : "+v"(st[0]), "+v"(st[1]), "+v"(st[2]), "+v"(st[3]), "+v"(rk[0]), "+v"(rk[1]), "+v"(rk[2]), "+v"(rk[3]), "+v"(rk[4]) :: "memory"); }
      if constexpr (I == 9 || I == 10) { constexpr int m = (I - 9) * 2; const float sc2 = 0.17677669529663687f * 1.4426950408889634f; st[m][1] *= sc2; st[m + 1][1] *= sc2; asm volatile("" : "+v"(st[m]), "+v"(st[m + 1])); }
      if constexpr (I >= 11 && I < 19) { constexpr int m = (I - 11) >> 1, h = (I - 11) & 1; fs[m][h] = rk[m + h] * st[m][1]; asm volatile("" : "+v"(fs[m][h])); }
      if constexpr (I == 19) { int& q = tg; asm volatile("" : "+v"(q)); tag = __builtin_amdgcn_readfirstlane(tg); if (KN_NUMA & 1) odst ^= ((SIDE ^ (xcd >> 2)) & 1) << 13; asm volatile("" : "+v"(odst)); }
      if constexpr (I == 4) ES(1);
      if constexpr (I == KN_QLB) { ES(2); raw_barrier(); ES(3); }
      if constexpr (I >= 14 && I < 18) { constexpr int q = (I - 14) ^ 2; cs[q] = __builtin_bit_cast(f32x4, ds_b128<QCS + (q >> 1) * 256 + (q & 1) * 64>(tcs)); }
      if constexpr (I == 18) cv = ds_r2(tcv); });
    static_assert(KN_QLB <= 14, "barrier");
    STAMP(2);
    ES(4);
    asm volatile("s_waitcnt lgkmcnt(0)" : "+v"(cs[0]), "+v"(cs[1]), "+v"(cs[2]), "+v"(cs[3]), "+v"(cv), "+v"(m4[0]), "+v"(m4[1]), "+v"(m4[2]), "+v"(m4[3]) :: "memory");
    u32x4 Q[4], Kf[5], V[5];
    auto nrm = [&](f32x4 x, f32x4 y, float sc) {
      const f32x2 p = (f32x2){y[0], y[1]} * sc + (f32x2){x[0], x[1]}, q = (f32x2){y[2], y[3]} * sc + (f32x2){x[2], x[3]};
      return (u32x2){pk_bf16(p[0], p[1]), pk_bf16(q[0], q[1])}; };
    auto c4 = [](u32x2 p, u32x2 q) { return (u32x4){p[0], p[1], q[0], q[1]}; };
    auto mkK = [&](int m) { Kf[m + 1] = c4(nrm(acc[2][m], cs[2], st[m][0]), nrm(acc[3][m], cs[3], st[m][0])); };
    auto mkQ = [&](int m) { Q[m] = c4(nrm(acc[0][m], cs[0], st[m][0]), nrm(acc[1][m], cs[1], st[m][0])); };
    auto mkV = [&](int m) { V[m + 1] = c4(nrm(acc[4][m], m4[m], cv[0]), nrm(acc[5][m], m4[m], cv[1])); };
    mkK(3); mkV(3);
    if (wm == 0) asm volatile("ds_write_b128 %0, %1 offset:%5\n\tds_write_b128 %0, %2 offset:%6\n\tds_write_b32 %3, %4" :: "v"(tex), "v"(Kf[4]), "v"(V[4]), "v"(tfl), "v"(pl), "n"(QKH + 2048), "n"(QVH + 2048) : "memory");
    __builtin_amdgcn_sched_barrier(0);
    f32x4 sc[4][2]; f32x2 lg[4][2]; float mx[4];
    auto qk = [&](int m, int h) { sc[m][h] = __builtin_amdgcn_mfma_f32_16x16x32_bf16(__builtin_bit_cast(bf16x8, Kf[m + h]), __builtin_bit_cast(bf16x8, Q[m]), z4, 0, 0, 0); };
    mkQ(3); mkK(2); qk(3, 1); qk(3, 0); mkQ(2); mkK(1); qk(2, 1); qk(2, 0); mkQ(1); mkK(0); qk(1, 1); qk(1, 0);
    __builtin_amdgcn_sched_barrier(0);
    int fl; bf16x8 k0, v0;
    tex += wm * 2048;
    auto front = [&]() { fl = ds_r32(tfl); k0 = ds_b128<QKH>(tex); v0 = ds_b128<QVH>(tex); };
    front();
    __builtin_amdgcn_sched_barrier(0);
    mkQ(0); qk(0, 1); mkV(2); mkV(1);
    __builtin_amdgcn_sched_barrier(0);
    asm volatile("s_waitcnt lgkmcnt(0)" : "+v"(fl), "+v"(k0), "+v"(v0), "+v"(V[3]), "+v"(V[2]) :: "memory");
    if (__builtin_expect(wm && __builtin_amdgcn_readfirstlane(fl) == 0, 0)) {
      unsigned polls = 0;
      do { front(); asm volatile("s_waitcnt lgkmcnt(0)" : "+v"(fl), "+v"(k0), "+v"(v0) :: "memory"); } while (__builtin_amdgcn_readfirstlane(fl) == 0 && ++polls < 40000000u);
    }
    Kf[0] = __builtin_bit_cast(u32x4, k0); V[0] = __builtin_bit_cast(u32x4, v0);
    qk(0, 0); mkV(0);
    STAMP(4);
    __builtin_amdgcn_sched_barrier(0); ES(5);
    __builtin_amdgcn_sched_barrier(0);
    const bool par = pl & 4;
#pragma unroll
    for (int m = 3; m >= 0; --m) {
#pragma unroll
      for (int h = 0; h < 2; ++h) {
        float e[4] = {sc[m][h][0], sc[m][h][1], sc[m][h][2], sc[m][h][3]};
        asm("" : "+v"(e[0]), "+v"(e[2]));
        lg[m][h] = (f32x2){par ? e[1] : e[0], par ? e[3] : e[2]} * fs[m][h] + (m == 0 ? bias0[h] : bias[h]);
      }
      mx[m] = fmaxf(fmaxf(lg[m][0][0], lg[m][0][1]), fmaxf(lg[m][1][0], lg[m][1][1]));
    }
    __builtin_amdgcn_sched_barrier(0);
    auto s16 = [](float x, float y) { auto p = __builtin_amdgcn_permlane16_swap(__float_as_uint(x), __float_as_uint(y), false, false); return (f32x2){__uint_as_float(p[0]), __uint_as_float(p[1])}; };
    auto s32 = [](float x, float y) { auto p = __builtin_amdgcn_permlane32_swap(__float_as_uint(x), __float_as_uint(y), false, false); return (f32x2){__uint_as_float(p[0]), __uint_as_float(p[1])}; };
    auto spread = [&](float r) { const f32x2 t = s32(r, r), u = s16(t[0], t[0]), w = s16(t[1], t[1]); mx[0] = u[0]; mx[1] = u[1]; mx[2] = w[0]; mx[3] = w[1]; };
    { const f32x2 p = s16(mx[0], mx[1]), q = s16(mx[2], mx[3]), r = s32(fmaxf(p[0], p[1]), fmaxf(q[0], q[1])); spread(fmaxf(r[0], r[1])); }
    __builtin_amdgcn_sched_barrier(0);
#pragma unroll
    for (int m = 3; m >= 0; --m) {
#pragma unroll
      for (int h = 0; h < 2; ++h) { const f32x2 d = lg[m][h] - mx[m]; lg[m][h] = (f32x2){__builtin_amdgcn_exp2f(d[0]), __builtin_amdgcn_exp2f(d[1])}; }
      const f32x2 t = lg[m][0] + lg[m][1];
      mx[m] = t[0] + t[1];
    }
    __builtin_amdgcn_sched_barrier(0);
    { const f32x2 p = s16(mx[0], mx[1]), q = s16(mx[2], mx[3]);
#pragma unroll
      for (int m = 3; m >= 0; --m) lg[m][0] = lg[m][0] * rk[m];
      const f32x2 r = s32(p[0] + p[1], q[0] + q[1]);
#pragma unroll
      for (int m = 3; m >= 0; --m) lg[m][1] = lg[m][1] * rk[m + 1];
      spread(__builtin_amdgcn_rcpf(r[0] + r[1])); }
    __builtin_amdgcn_sched_barrier(0);
    auto half = [](u32x4 v, int h) { return __builtin_bit_cast(s16x4, (u32x2){v[2 * h], v[2 * h + 1]}); };
#pragma unroll
    for (int m = 3; m >= 0; --m) {
      s16x4 pf[2];
#pragma unroll
      for (int h = 0; h < 2; ++h) { const f32x2 t = lg[m][h] * mx[m]; const unsigned u = pk_bf16(t[0], t[1]); pf[h] = __builtin_bit_cast(s16x4, (u32x2){__builtin_amdgcn_perm(0u, u, pl), __builtin_amdgcn_perm(0u, u, ph)}); }
#pragma unroll
      for (int f = 0; f < 2; ++f) sc[m][f] = __builtin_amdgcn_mfma_f32_16x16x16bf16_1k(half(V[m], f), pf[0], z4, 0, 0, 0);
#pragma unroll
      for (int f = 0; f < 2; ++f) sc[m][f] = __builtin_amdgcn_mfma_f32_16x16x16bf16_1k(half(V[m + 1], f), pf[1], sc[m][f], 0, 0, 0);
    }
    __builtin_amdgcn_sched_barrier(0);
    STAMP(10);
    ES(6);
    const auto rO = __builtin_amdgcn_make_buffer_rsrc((void*)a.O, 0, 0x7fffffff, 0x00020000);
    u32x4 ov[4];
#pragma unroll
    for (int m = 3; m >= 0; --m) {
      ov[m] = (u32x4){pk_bf16(sc[m][0][0], sc[m][0][1]), pk_bf16(sc[m][0][2], sc[m][0][3]), pk_bf16(sc[m][1][0], sc[m][1][1]), pk_bf16(sc[m][1][2], sc[m][1][3])};
      __builtin_amdgcn_raw_buffer_store_b128(ov[m], rO, odst, m * RSH, 16);
    }
    asm volatile("" :: "v"(ov[0]), "v"(ov[1]), "v"(ov[2]), "v"(ov[3]));
    if (KN_QP) __builtin_amdgcn_s_setprio(0);
    STAMP(7);
    ES(7);
    wait_vm<0>();
    STAMP(8);
    ES(8);
    raw_barrier();
    ES(9);
  }
  STAMP(3);
#endif
  return tag;
}

struct FragW { s16x4 lo[4], hi[4]; };
__device__ __forceinline__ void wo_phase(const GemmArgs& a, const GemmArgs& nx, unsigned tag) {
#if defined(__HIP_DEVICE_COMPILE__)
  extern __shared__ __attribute__((aligned(16))) char smem[];
  STAMP(0);
  const unsigned lds0 = (unsigned)(size_t)(lds_char_ptr)smem;
  const int tid = threadIdx.x, lane = tid & 63;
  const int wave = __builtin_amdgcn_readfirstlane(tid >> 6);
  const int bid = blockIdx.x, xcd = bid & 7, idx = bid >> 3;
  const int tm = (xcd >> 1) * 4 + (idx >> 3), tn = (xcd & 1) * 8 + (idx & 7);
  const int m0 = tm * 128, panel = m0 >> 8, rot = (xcd & 3) * 4, sd = SIDE;
  {
    constexpr int PAD = QTCH;
    const int g = wave >> 2, rw = wave & 3;
    const int fr = lane & 15, fq = lane >> 4, wq = (lane >> 2) & 3, wp = lane & 3;
    unsigned wa[4];
#pragma unroll
    for (int c = 0; c < 4; ++c) { const int row = 4 * fq + wq; wa[c] = lds0 + g * 8192 + row * 128 + (((c << 1) ^ (row & 6)) << 4) + wp * 8; }
    FragW f[3];
    auto rd = [&](auto ic, auto rc) {
      constexpr int i = decltype(ic)::value, r = decltype(rc)::value, P = wo_pos(i >> 1) + (i & 1) * 4096, B = P & ~32767, o = P & 32767;
      if constexpr (r & 1) f[i % 3].hi[r >> 1] = ds_tr<o + 2048>(wa[r >> 1] + B); else f[i % 3].lo[r >> 1] = ds_tr<o>(wa[r >> 1] + B);
    };
    auto pin = [&](FragW& q) {
#pragma unroll
      for (int n = 0; n < 4; ++n) asm volatile("" : "+v"(q.lo[n]), "+v"(q.hi[n]));
    };
    static_for<0, 16>([&](auto rc) { constexpr int r = decltype(rc)::value; rd(std::integral_constant<int, r / 8>{}, std::integral_constant<int, r % 8>{}); });
    if constexpr (KN_CT & 4) { if (wave == 2) {
      unsigned long long c0, c1; CODE_ADDR(c0, "k_all"); CODE_ADDR(c1, "k_zend");
      wait_vm<0>();
      __builtin_amdgcn_raw_ptr_buffer_load_lds(__builtin_amdgcn_make_buffer_rsrc((void*)c0, 0, c1 - c0 < 262144ull ? (unsigned)(c1 - c0) : 0u, 0x00020000), (lds_void_ptr)(size_t)(lds0 + PAD), 4, (unsigned)((lane * 32 + idx) * 128), 0, 0, 0); } }
    if (wave == 1) wait_flags(a.sync + FLO(tm), (lane & 15) * 128, tag, 0xffffull);
    {
      if (wave == 3) {
        wait_flags(a.sync, (FHM(lane >= 32 ? (xcd >> 2) * 4 + (idx >> 3) : panel) + (lane & 31)) * 4, tag, ~0ull);
      }
    }
    const auto rX = __builtin_amdgcn_make_buffer_rsrc((void*)a.X, 0, 0x7fffffff, 0x00020000);
    const int rb = (m0 >> 4) + rw * 2 + g;
    unsigned xs[2] = {(unsigned)(rbH(rb, sd) + lane * 16), (unsigned)(rbH(rb ^ 1, sd) + lane * 16)};
    const int ck = (fq & 1) * 2 + (fq >> 1);
    unsigned orow = (unsigned)((rb * 16 + fr) * (D_ * 2) + tn * 128 + ck * 16), ofrg = (unsigned)(rbH(rb, sd) + omH(tn * 2048) + ck * 256 + fr * 16);
    asm volatile("" : "+v"(xs[0]), "+v"(xs[1]), "+v"(orow), "+v"(ofrg));
    STAMP(9);
    if (!(KN_CT & 4) || wave != 2) wait_vm<0>();
    lds_barrier();
    STAMP(6);
    pin(f[0]); pin(f[1]);
    u32x4 xr[16][2], res[2];
#define KN_XD 4
    auto ldX = [&](int i, int m) {
      if (i < 16) xr[i][m] = __builtin_amdgcn_raw_buffer_load_b128(rX, xs[m] + (i & 1) * 1024, omH((i >> 1) * 2048) ^ omH(((g * 8) ^ rot) * 2048), 16);
      else if (i == 16) res[m] = __builtin_amdgcn_raw_buffer_load_b128(__builtin_amdgcn_make_buffer_rsrc((void*)a.R, 0, 0x7fffffff, 0x00020000), orow + m * 64, 0, 0);
    };
#pragma unroll
    for (int i = 0; i < KN_XD; ++i) { ldX(i, 0); ldX(i, 1); __builtin_amdgcn_sched_barrier(0); }
    unsigned gsrc[2];
#pragma unroll
    for (int i = 0; i < 2; ++i) {
      const int j = wave + 8 * i, jj = j >= 7 ? j - 7 : j;
      const int b = jj * 1024 + lane * 16, q = b / 224, pos = (b % 224) >> 4;
      gsrc[i] = (unsigned)((8 * ((q & 15) >> 2) + (q & 3) + 4 * (q >> 4)) * DF_ + ((xcd & 3) * 8 + (idx & 7)) * 112 + pos * 8) * 2u;
      asm volatile("" : "+v"(gsrc[i]));
    }
    f32x4 acc[4][2];
#pragma unroll
    for (int n = 0; n < 4; ++n) { acc[n][0] = (f32x4){0.f, 0.f, 0.f, 0.f}; acc[n][1] = acc[n][0]; }
    __builtin_amdgcn_sched_barrier(0);
    static_for<0, 16>([&](auto ic) {
      constexpr int i = decltype(ic)::value;
      if constexpr (i >= 2) { asm volatile("s_waitcnt lgkmcnt(%0)" :: "n"(i < 15 ? 8 : 0) : "memory"); pin(f[i % 3]); }
      static_for<0, 8>([&](auto jc) {
        constexpr int j = decltype(jc)::value, n = j >> 1, m = j & 1;
        const bf16x8 w = __builtin_shufflevector(f[i % 3].lo[n], f[i % 3].hi[n], 0, 1, 2, 3, 4, 5, 6, 7);
        acc[n][m] = __builtin_amdgcn_mfma_f32_16x16x32_bf16(w, __builtin_bit_cast(bf16x8, xr[i][m]), acc[n][m], 0, 0, 0);
        if constexpr (i + 2 < 16) rd(std::integral_constant<int, i + 2>{}, jc);
        if constexpr (j == 1 || j == 5) ldX(i + KN_XD, j >> 2);
        __builtin_amdgcn_sched_barrier(0);
      });
      if constexpr (i == 0) STAMP(1);
    });
    STAMP(2);
    auto slot = [&](int w) { return 32768 + w * 4096 + lane * 16; };
#pragma unroll
    for (int n = 0; n < 4; ++n) *(f32x4*)(smem + slot(wave) + n * 1024) = acc[n][1];
    asm volatile("" : "+v"(res[0]), "+v"(res[1]));
    lds_barrier();
    STAMP(4);
    f32x4 pa[4];
    static_for<0, 4>([&](auto nc) { constexpr int n = decltype(nc)::value; pa[n] = __builtin_bit_cast(f32x4, ds_b128<n * 1024>(lds0 + slot(wave ^ 4))); });
    auto ldGs = [&]() {
      auto ldG = [&](int i, int u) { const int j = wave + 8 * i;
        __builtin_amdgcn_raw_ptr_buffer_load_lds(__builtin_amdgcn_make_buffer_rsrc((void*)(j >= 7 ? nx.W1 : nx.W0), 0, 0x7fffffff, 0x00020000),
                          (lds_void_ptr)(size_t)(lds0 + 98304 + (u ^ 2) * 14592 + (j >= 7 ? 7184 + (j - 7) * 1024 : j * 1024)), 16, gsrc[i], (u + (xcd >> 2) * 16) * 32 * DF_ * 2, 0, 0); };
      ldG(0, 0); ldG(0, 1);
      if (wave < 6) { ldG(1, 0); ldG(1, 1); }
    };
    if (!KN_GL) ldGs();
    asm volatile("s_waitcnt lgkmcnt(0)" : "+v"(pa[0]), "+v"(pa[1]), "+v"(pa[2]), "+v"(pa[3]) :: "memory");
    const auto rO = __builtin_amdgcn_make_buffer_rsrc((void*)a.O, 0, 0x7fffffff, 0x00020000);
    u32x4 o[2];
#pragma unroll
    for (int i = 0; i < 2; ++i) {
      const f32x4 u = acc[2 * i][0] + pa[2 * i], v = acc[2 * i + 1][0] + pa[2 * i + 1];
      const auto s0 = __builtin_amdgcn_permlane16_swap(pk_bf16(u[0], u[1]), pk_bf16(v[0], v[1]), false, false);
      const auto s1 = __builtin_amdgcn_permlane16_swap(pk_bf16(u[2], u[3]), pk_bf16(v[2], v[3]), false, false);
      const u32x4 q4 = {s0[0], s1[0], s0[1], s1[1]};
#pragma unroll
      for (int j = 0; j < 4; ++j) { const unsigned r = res[i][j], q = q4[j]; o[i][j] = pk_bf16(bflo(r) + bflo(q), bfhi(r) + bfhi(q)); }
      __builtin_amdgcn_raw_buffer_store_b128(o[i], rO, ofrg + i * 1024, 0, 16);
    }
    if (KN_GL) { __builtin_amdgcn_sched_barrier(0); asm volatile("s_nop 1" ::: "memory"); ldGs(); __builtin_amdgcn_sched_barrier(0); }
    auto sum4 = [](float v) {
      auto p = __builtin_amdgcn_permlane16_swap(__float_as_uint(v), __float_as_uint(v), false, false); v = __uint_as_float(p[0]) + __uint_as_float(p[1]);
      auto q = __builtin_amdgcn_permlane32_swap(__float_as_uint(v), __float_as_uint(v), false, false); return __uint_as_float(q[0]) + __uint_as_float(q[1]); };
    f32x2 e[8], sm = {0.f, 0.f};
#pragma unroll
    for (int j = 0; j < 8; ++j) { const unsigned q = o[j >> 2][j & 3]; e[j] = (f32x2){bflo(q), bfhi(q)}; sm += e[j]; }
    const float mu = sum4(sm[0] + sm[1]) * (1.f / 64.f);
    f32x2 t2 = {0.f, 0.f};
#pragma unroll
    for (int j = 0; j < 8; ++j) { const f32x2 d = e[j] - mu; t2 += d * d; }
    const float q2 = sum4(t2[0] + t2[1]);
    __builtin_amdgcn_raw_buffer_store_b64((u32x2){__float_as_uint(mu), __float_as_uint(q2)}, __builtin_amdgcn_make_buffer_rsrc((void*)a.st, 0, 0x7fffffff, 0x00020000),
                                          lane < 16 ? (unsigned)((tn * S_ + rb * 16 + lane) * 8) : 0x80000000u, 0, 16);
    STAMP(7);
    STAMP(8);
    unsigned fo = lane == 0 ? (unsigned)(FHF(panel) + ((tm & 1) * 16 + tn) * 32) * 4u : 0x80000000u, ft = tag;
    unsigned gx = rbH(((xcd >> 2) * 4 + (idx >> 3)) * 16, sd) + (rw >> 1) * RSH + (rw & 1) * 1024 + lane * 16, gm = lds0 + rw * 1024, gs = omH((xcd >> 2) * 16384);
    const auto rF = __builtin_amdgcn_make_buffer_rsrc((void*)a.sync, 0, 147456, 0x00020000);
    const u32x4 qH = mk_rsrc(a.O);
    asm volatile("s_waitcnt vmcnt(0) lgkmcnt(0)" : "+v"(fo), "+v"(ft), "+v"(gx), "+s"(gm), "+s"(gs) : "s"(rF), "s"(qH) : "memory");
    raw_barrier();
    if (wave == 4) __builtin_amdgcn_raw_buffer_store_b32(ft, rF, fo, 0, 16);
    if (KN_GX && wave < 4) {
      if (KN_GX > 1) __builtin_amdgcn_s_sleep(KN_GX - 1);
      static_for<0, 8>([&](auto ic) { constexpr int i = decltype(ic)::value; ld_ca<i * 4096, i * 2 * RSH, false, true>(gm, qH, gx, gs); });
    }
    STAMP(3);
  }
#endif
}

struct FragE { bf16x8 x[4]; s16x4 lo[4], hi[4]; };
template <int MODE, int HTD, int TA, int TB, int TL = 0>
__device__ __forceinline__ void dn_phase(const GemmArgs& a, unsigned tag) {
#if defined(__HIP_DEVICE_COMPILE__)
  constexpr int SX = 5, SW = 4, WB = SX * 16384, PAD = WB + SW * 16384, V = KN_V;
  static_assert(TA == 2 && TB == 2, "schedule");
  extern __shared__ __attribute__((aligned(16))) char smem[];
  if constexpr (MODE != 1) STAMP(0);
  const unsigned lds0 = (unsigned)(size_t)(lds_char_ptr)smem;
  const int tid = threadIdx.x;
  int lane = tid & 63; asm volatile("" : "+v"(lane));
  const int wave = __builtin_amdgcn_readfirstlane(tid >> 6);
  const int bid = blockIdx.x, xcd = bid & 7, idx = bid >> 3;
  const int tm = (xcd >> 1) * 4 + (idx >> 3), tn = (idx & 7) * 2 + (xcd & 1), A = (xcd & 1) ? 24 - KN_D : 24 + KN_D;
  const int m0 = tm * 128, panel = m0 >> 8, sd = SIDE;
  unsigned* pcnt = a.sync + FAE((xcd >> 2) * 4 + (idx >> 3)) + ((xcd & 3) * 8 + (idx & 7)) * 32;
  const unsigned orow = (unsigned)((m0 + wave * 16 + (lane >> 3)) * (D_ * 2) + tn * 128 + (lane & 7) * 16);
  const unsigned ofrg = (unsigned)(rbH((m0 >> 4) + wave, sd) + omH(tn * 2048) + (lane & 7) * 256 + (lane >> 3) * 16);
  u32x4 res[2];
  auto res_loads = [&]() {
    const auto rR = __builtin_amdgcn_make_buffer_rsrc((void*)a.R, 0, 0x7fffffff, 0x00020000);
#pragma unroll
    for (int i = 0; i < 2; ++i) res[i] = __builtin_amdgcn_raw_buffer_load_b128(rR, ofrg, i * 128, 16);
  };
  constexpr int STG = 0;
  if (MODE == 1 || wave < 4) {
    const int lw = wave & 3;
    const int kf = (xcd & 3) * 8;
    auto ktile = [&](int t) { const int l = (xcd & 2) * 6 + (t < A ? t - 16 + (xcd & 1) * (8 + KN_D) : t - A + 16); return t < 16 ? (kf ^ ((t & 8) * 2)) + (t & 7) : (l < 24 ? l + 32 : l + 8); };
    auto wrow = [&](int kt, int j) { const int p = kt * 8 + j, b = p - 256, v1 = p + 3 * (p >> 2), v2 = b + 4 * ((b * 21846) >> 16) + 4, m = b >> 31; return ((v1 & m) | (v2 & ~m)) * 8; };
    const auto rW = __builtin_amdgcn_make_buffer_rsrc((void*)a.W0, 0, 0x7fffffff, 0x00020000);
    const u32x4 qX = mk_rsrc(a.X), qW = mk_rsrc((const char*)a.W0 - 4096);
    const unsigned xv0 = (unsigned)(rbA((m0 >> 4) + lw * 2, sd) + lane * 16), xv1 = xv0 + (RSA - 2048);
    unsigned wv[4];
    { const unsigned w0 = (unsigned)((lane >> 3) * D_ + tn * 64 + ((lane & 7) ^ wswz<128>(lane >> 3)) * 8) * 2u;
#pragma unroll
      for (int i = 0; i < 4; ++i) wv[i] = (w0 ^ (i * 64)) + (4096 - i * 1024); }
    const unsigned mX = lds0 + lw * 4096, mW = mX + WB;
    auto ldXo = [&](int so, int slot) { ld_x4(mX + slot * 16384, qX, xv0, xv1, so); };
    int tbx = 0, tb0 = 0, tb1 = 0;
    auto tables = [&]() { const int kt = ktile(lane < A + 8 ? lane : 0); tbx = omA(kt * 2048); tb0 = wrow(kt, 2 * lw) * (D_ * 2); tb1 = wrow(kt, 2 * lw + 1) * (D_ * 2); asm volatile("" : "+v"(tbx), "+v"(tb0), "+v"(tb1)); };
    int o0, o1, ox;
    auto offs = [&](int t) { o0 = __builtin_amdgcn_readlane(tb0, t + 4); o1 = __builtin_amdgcn_readlane(tb1, t + 4); ox = __builtin_amdgcn_readlane(tbx, t + 4); asm volatile("s_nop 1" : "+s"(o0), "+s"(o1), "+s"(ox)); };
    const unsigned* fcnt = a.sync + FAE(panel);
    const unsigned fof = (lane & 31) * 128 + (lane >> 5) * 4;
    auto ldF = [&]() { __builtin_amdgcn_raw_ptr_buffer_load_lds(__builtin_amdgcn_make_buffer_rsrc((void*)fcnt, 0, 4096, 0x00020000), (lds_void_ptr)(size_t)(lds0 + PAD + 256), 4, fof, 0, 0, 16); };
    int ht; unsigned htv;
    auto hset = [&]() {
      int idh = idx; asm volatile("" : "+s"(idh));
      const int slc = (3 - ((unsigned)(size_t)a.W0 >> 7)) & 7, pr = idh & 3;
      const bool hlp = pr != (slc >> 1) && lw < 3;
      const int htau = 2 + lw * 12 + (idh >> 3) * 3 + pr - (pr > (slc >> 1) ? 1 : 0);
      ht = (hlp && htau < (((slc ^ xcd) & 1) ? A : A + 8)) ? htau - HTD : 1000;
      htv = (unsigned)((wrow(ktile(htau < A + 8 ? htau : 0), lane >> 3) + (lane & 7)) * (D_ * 2) + ((idh & 4) * 2 + slc) * 128);
    };
    auto help = [&]() { __builtin_amdgcn_raw_ptr_buffer_load_lds(rW, (lds_void_ptr)(size_t)(lds0 + FPAD - 256), 4, htv, 0, 0, 0); };
    auto ldW0 = [&](int i) { const int so = ((kf + i) * 112 + (lw >> 1) * 56 + (lw & 1) * 16) * (D_ * 2); ld_w4<-1>(mW + i * 16384, qW, wv, so, so + 8 * D_ * 2); };
    if constexpr (MODE == 1) {
      if constexpr (TL == 1) __builtin_amdgcn_raw_ptr_buffer_load_lds(__builtin_amdgcn_make_buffer_rsrc((void*)(a.sync + FAM(panel)), 0, 256, 0x00020000), (lds_void_ptr)(size_t)(lds0 + FPAD), 4, lane * 4, 0, 0, 16);
      if constexpr (TL == 0) { ldW0(0); ldW0(1); }
      return;
    }
#define TS0
#define TS1(T)
#define KN_XW 4
    wait_vm<3>();
    lds_inc(lds0 + ACNT);
    STAMPW(4, 0);
    if (KN_XW && KN_XW < 4) { hset(); tables(); asm volatile("" :: "s"(ht), "v"(htv)); }
    if (KN_XW == 3) ldW0(2);
    const bool early = !lds_missing(lds0 + FPAD, tag, ((1ull << (4 * V)) - 1) << ((xcd & 1) * 32));
    lds_wait8(lds0 + ACNT);
    STAMPW(9, 0);
    if (tid == 192) set_flag(pcnt, tag);
    asm volatile("" : "+v"(lane) :: "memory");
    if (!KN_XW) { hset(); tables(); asm volatile("" :: "s"(ht), "v"(htv)); }
    if (KN_XW <= 1 || KN_XW == 5) lds_wait8(lds0 + ACNT + 4, 4);
    STAMPW(5, 0);
    ldXo(omA(kf * 2048), 0); ldXo(omA(kf * 2048) + 2048, 1); if (KN_XW != 3) ldW0(2);
    if (KN_XW >= 4) { asm volatile("" : "+v"(lane) :: "memory"); hset(); tables(); asm volatile("" :: "s"(ht), "v"(htv)); }
    static_assert(V >= 5 && V <= 8, "first tiles");
    unsigned sx = mX + 2 * 16384, sw = 3 * 16384;
    auto next = [&]() { sw = (sw + 16384) & 65535; sx = sx + 16384; sx = sx >= mX + SX * 16384 ? mX : sx; asm volatile("" : "+s"(sw), "+s"(sx)); };
    { unsigned sc = mX + lane * 16;
      const int te = lw == 0 ? V - 4 : 1000;
      if (ht < -1) ht = -1;
      ox = omA(kf * 2048) + 4096;
      wait_vm<KN_XW == 3 ? 4 : 8>();
#pragma unroll 1
      for (int t = -1; t < V - 1; ++t) {
        const int oy = ox;
        offs(t);
        if (KN_XW == 3 && t == 0) wait_vm<8>(); else wait_vm<12>();
        if (__builtin_expect(ld_x4c(sx, qX, xv0, xv1, oy, sc) != 0 || !early, 0)) {
          for (int tries = early ? 0 : KN_TRY;; ++tries) {
            if (tries >= KN_TRY) wait_flags(fcnt, fof, tag, LO32);
            int so = __builtin_amdgcn_readlane(tbx, t + 1), sm = __builtin_amdgcn_readfirstlane(sc); asm volatile("s_nop 3" : "+s"(so), "+s"(sm));
            ld_x4(sm, qX, xv0, xv1, so);
            wait_vm<0>();
            if (tries >= KN_TRY || !marked<4, 1024>(sc)) break;
          }
        }
        if (t == -1) STAMPW(6, 0);
        if (__builtin_expect(t == te, 0)) { wait_vm<12>(); if (lds_missing(lds0 + PAD + 256, tag, LO32)) wait_flags(fcnt, fof, tag, LO32); }
        ld_w4<63>(mW + sw, qW, wv, o0, o1);
        TS0;
        next();
        if (__builtin_expect(t == te - 2, 0)) { ldF(); asm volatile("" ::: "memory"); }
        if (__builtin_expect(t == ht, 0)) help();
        sc = t == 3 ? sc - 4 * 16384 : sc + 16384;
        TS1(t + 1);
      }
    }
    ld_x4(sx, qX, xv0, xv1, ox);
    sx = sx + 16384; sx = sx >= mX + SX * 16384 ? mX : sx;
    const int tf = lw == 0 ? 9 : 1000;
    offs(V - 1);
    auto round = [&](int t, auto P1c, auto WAITc) {
      constexpr bool P1 = decltype(P1c)::value; constexpr int WAIT = decltype(WAITc)::value;
      if constexpr (P1) { if (__builtin_expect(t == tf + 3, 0)) { wait_vm<WAIT>(); if (lds_missing(lds0 + PAD + 256, tag, HI32)) wait_flags(fcnt, fof, tag, HI32); } }
      if constexpr (P1) ld_w4<WAIT>(mW + sw, qW, wv, o0, o1); else ld_w2<WAIT>(mW + sw, qW, wv, o0, o1);
      TS0;
      __builtin_amdgcn_sched_barrier(0);
      if (__builtin_expect(t == ht, 0)) help();
      if constexpr (P1) { if (__builtin_expect(t == tf, 0)) ldF(); }
      const int oy = ox; const unsigned sy = sx;
      offs(t + 1);
      next();
      TS1(t + 1);
      __builtin_amdgcn_sched_barrier(0);
      ld_x4(sy, qX, xv0, xv1, oy);
    };
    int t = V - 1;
#pragma unroll 1
    for (; t < A - 4; ++t) round(t, std::true_type{}, std::integral_constant<int, 16>{});
    round(t++, std::false_type{}, std::integral_constant<int, 16>{});
    round(t++, std::false_type{}, std::integral_constant<int, 14>{});
#pragma unroll 1
    for (; t < A + 4; ++t) round(t, std::false_type{}, std::integral_constant<int, 12>{});
    wait_vm<12>(); raw_barrier(); TS0; TS1(t + 1);
    res_loads();
    wait_vm<8>(); raw_barrier(); TS0; TS1(t + 2);
    wait_vm<2>(); raw_barrier(); TS0; TS1(t + 3);
    raw_barrier(); TS0; TS1(t + 4);
    asm volatile("" : "+v"(res[0]), "+v"(res[1]));
  } else {
    if constexpr (MODE == 1) return;
    const int fr = lane & 15, fq = lane >> 4, wq = (lane >> 2) & 3, wp = lane & 3, cw = wave & 3, wm = cw >> 1, wn = cw & 1;
#define KN_DP 0
    if (KN_DP) __builtin_amdgcn_s_setprio(KN_DP);
    f32x4 acc[4][4];
#pragma unroll
    for (int n = 0; n < 4; ++n)
#pragma unroll
      for (int m = 0; m < 4; ++m) acc[n][m] = (f32x4){0.f, 0.f, 0.f, 0.f};
    const unsigned xa = lds0 + wm * 8192 + lane * 16;
    unsigned wa[2];
#pragma unroll
    for (int c = 0; c < 2; ++c) { const int row = 8 * fq + wq; wa[c] = lds0 + WB + (row >> 4) * 4096 + (row & 15) * 128 + ((((wn * 2 + c) << 1) ^ wswz<128>(row)) << 4) + wp * 8; }
    struct Ctx { unsigned x, w[2]; };
    auto mk = [&](unsigned xo, unsigned wo) { Ctx c; c.x = xa + xo; c.w[0] = wa[0] + wo; c.w[1] = wa[1] + wo; return c; };
    auto rd = [&](FragE& f, const Ctx& c, auto KSc, auto kc) {
      constexpr int KS = decltype(KSc)::value, k = decltype(kc)::value;
      if constexpr (k < 4) f.x[k] = ds_b128<KS * 1024 + k * 2048>(c.x);
      else { constexpr int n = k - 4, o = KS * 8192 + (n >> 1) * 2048; f.lo[n] = ds_tr<o>(c.w[n & 1]); f.hi[n] = ds_tr<o + 512>(c.w[n & 1]); }
    };
    auto pin = [&](FragE& f, auto Nc) {
#pragma unroll
      for (int m = 0; m < 4; ++m) asm volatile("" : "+v"(f.x[m]));
#pragma unroll
      for (int n = 0; n < decltype(Nc)::value; ++n) asm volatile("" : "+v"(f.lo[n]), "+v"(f.hi[n]));
    };
    using I0 = std::integral_constant<int, 0>; using I1 = std::integral_constant<int, 1>; using I2 = std::integral_constant<int, 2>; using I4 = std::integral_constant<int, 4>;
    FragE fa, fb, fc, fd;
    const auto rS = __builtin_amdgcn_make_buffer_rsrc((void*)a.slab, 0, 0x7fffffff, 0x00020000);
    unsigned sla = rbH(tm * 8 + (tn >> 1), sd) + omH((tn & 1) * 16384 + cw * 4096) + lane * 16, slb = sla ^ omH(16384);
    asm volatile("" : "+v"(sla), "+v"(slb));
    raw_barrier();
    { FragE& f0 = (KN_D & 1) ? fc : fa; FragE& f1 = (KN_D & 1) ? fd : fb;
      const Ctx c = mk(0, 0); static_for<0, 8>([&](auto kc) { rd(f0, c, I0{}, kc); }); asm volatile("s_waitcnt lgkmcnt(4)" ::: "memory"); static_for<0, 8>([&](auto kc) { rd(f1, c, I1{}, kc); });
      asm volatile("s_waitcnt lgkmcnt(0)" ::: "memory");
      pin(f0, I4{}); pin(f1, I4{}); }
    STAMP(1);
    static_assert((SW & (SW - 1)) == 0, "W ring");
    int sx = 16384, sw = 16384;
    Ctx cn = mk(sx, sw);
#define KN_SP4 26
    auto rd1 = [&](FragE& f, const Ctx& c, auto KSc, auto rc) {
      constexpr int KS = decltype(KSc)::value, r = decltype(rc)::value;
      if constexpr (r < 4) f.x[r] = ds_b128<KS * 1024 + r * 2048>(c.x);
      else { constexpr int n = (r - 4) >> 1, o = KS * 8192 + (n >> 1) * 2048; if constexpr (r & 1) f.hi[n] = ds_tr<o + 512>(c.w[n & 1]); else f.lo[n] = ds_tr<o>(c.w[n & 1]); }
    };
    auto send = [&](int m) {
      __builtin_amdgcn_raw_buffer_store_b128((u32x4){pk_bf16(acc[2][m][0], acc[2][m][1]), pk_bf16(acc[2][m][2], acc[2][m][3]), pk_bf16(acc[3][m][0], acc[3][m][1]), pk_bf16(acc[3][m][2], acc[3][m][3])},
                                             rS, slb + m * 1024, 0, 16); };
    auto tile = [&](FragE& c0, FragE& c1, FragE& n0, FragE& n1, auto NCc, auto NNc, auto&& post) {
      constexpr int NC = decltype(NCc)::value, NN = decltype(NNc)::value;
      constexpr bool PF = KN_PF && NC == 4 && NN == 2;
      constexpr int R2 = 8 + 4 * NN, NP = 4 * NC;
      const Ctx c = cn;
      static_for<0, 2 * NP>([&](auto ic) {
        constexpr int I = decltype(ic)::value, P = I >> 1, Q = PF && P > 8 ? P - 8 : P;
        constexpr int n = PF && P >= 1 && P <= 8 ? 2 + ((P >> 1) & 1) : Q / 4, m = PF && P >= 1 && P <= 8 ? (P - 1) >> 1 : ((n & 1) ? 3 - Q % 4 : Q % 4);
        constexpr int SP = NC == 4 ? (NN == 4 ? KN_SP4 : 20) : 12;
        constexpr int before = I < 1 ? 0 : ((I - 1) * R2 / SP < R2 ? (I - 1) * R2 / SP : R2), after = I * R2 / SP < R2 ? I * R2 / SP : R2;
        FragE& u = ((P ^ I) & 1) ? c1 : c0;
        acc[n][m] = __builtin_amdgcn_mfma_f32_16x16x32_bf16(__builtin_shufflevector(u.lo[n], u.hi[n], 0, 1, 2, 3, 4, 5, 6, 7), u.x[m], acc[n][m], 0, 0, 0);
        __builtin_amdgcn_sched_barrier(0);
        if constexpr (I == 1) { asm volatile("s_waitcnt lgkmcnt(0)" ::: "memory"); pin(c0, NCc); pin(c1, NCc); raw_barrier(); post(); }
        static_for<before, after>([&](auto rc) { constexpr int r = decltype(rc)::value;
          constexpr int h = r < 6 ? r / 3 : (r < 12 ? r & 1 : (r >> 1) & 1), q = r < 6 ? (r % 3 == 2 ? 0 : 4 + r % 3) : (r < 12 ? (r - 4) >> 1 : 4 + ((r - 8) >> 2) * 2 + (r & 1));
          if constexpr (h) rd1(n1, c, I1{}, std::integral_constant<int, q>{}); else rd1(n0, c, I0{}, std::integral_constant<int, q>{}); });
        if constexpr (PF && I >= 9 && I <= 21 && (I - 9) % 4 == 0) send((I - 9) / 4);
        if constexpr (I == 2 * NP - 4) { sw = (sw + 16384) & (SW * 16384 - 1); asm volatile("" : "+s"(sw)); }
        if constexpr (I == 2 * NP - 3) { cn.w[0] = wa[0] + sw; cn.w[1] = wa[1] + sw; asm volatile("" : "+v"(cn.w[0]), "+v"(cn.w[1])); }
        if constexpr (I == 2 * NP - 5) { sx = sx + 16384; asm volatile("" : "+s"(sx)); }
        if constexpr (I == 2 * NP - 2) { sx = sx == SX * 16384 ? 0 : sx; asm volatile("" : "+s"(sx)); }
        if constexpr (I == 2 * NP - 1) { cn.x = xa + sx; asm volatile("" : "+v"(cn.x)); }
        __builtin_amdgcn_sched_barrier(0);
      });
      asm volatile("s_waitcnt lgkmcnt(%0)" :: "n"(R2 - 6 < 15 ? R2 - 6 : 15) : "memory");
      asm volatile("" : "+v"(n0.x[0]), "+v"(n0.lo[0]), "+v"(n0.hi[0]), "+v"(n1.x[0]), "+v"(n1.lo[0]), "+v"(n1.hi[0]));
    };
    auto nop = [&]() {};
    if constexpr (KN_D & 1) tile(fc, fd, fa, fb, I4{}, I4{}, nop);
#pragma unroll 1
    for (int t = 0; t < A - 3; t += 2) {
      tile(fa, fb, fc, fd, I4{}, I4{}, [&]() { if (t == 2) { if (tid == 256) set_flag(pcnt + 1, tag); } });
      tile(fc, fd, fa, fb, I4{}, I4{}, nop);
      if (t == 0) wait_vm<0>();
    }
    tile(fa, fb, fc, fd, I4{}, I4{}, nop); tile(fc, fd, fa, fb, I4{}, I2{}, nop);
    STAMP(10);
    if (!KN_PF) { send(0); send(1); send(2); send(3); }
    auto pairs = [&](int n) {
#pragma unroll 1
      for (int t = 0; t < n; t += 2) { tile(fa, fb, fc, fd, I2{}, I2{}, nop); tile(fc, fd, fa, fb, I2{}, I2{}, nop); } };
    const unsigned* flag = a.sync + FPR(bid);
    u32x4 pp[4];
    auto ldP = [&]() {
#pragma unroll
      for (int i = 0; i < 4; ++i) pp[i] = __builtin_amdgcn_raw_buffer_load_b128(rS, sla + i * 1024, 0, 16);
    };
    {
    pairs(TA);
    STAMPV(11);
    wait_vm<0>();
    STAMPV(12);
    tile(fa, fb, fc, fd, I2{}, I2{}, [&]() { if (tid == 256) set_flag(a.sync + FPR(bid ^ 1), tag); });
    tile(fc, fd, fa, fb, I2{}, I2{}, nop);
    if constexpr (TB > TA) pairs(TB - TA);
    unsigned fv = 1u;
    if (wave == 4) fv = __hip_atomic_load((gu32)flag, __ATOMIC_RELAXED, __HIP_MEMORY_SCOPE_AGENT);
    tile(fa, fb, fc, fd, I2{}, I2{}, nop);
    tile(fc, fd, fa, fb, I2{}, I2{}, nop);
    if (wave == 4) {
      if (__builtin_amdgcn_readfirstlane(fv) != tag) wait_flags(flag, 0, tag, 1ull);
    }
    STAMPV(4);
    tile(fa, fb, fc, fd, I2{}, I2{}, [&]() {
#pragma unroll
      for (int i = 0; i < 4; ++i) pp[i] = __builtin_amdgcn_raw_buffer_load_b128(rS, sla + i * 1024, 0, 16);
      res_loads();
    });
    tile(fc, fd, fa, fb, I2{}, I2{}, nop);
    }
    asm volatile("s_waitcnt lgkmcnt(0)" ::: "memory");
    pin(fa, I2{}); pin(fb, I2{});
    STAMP(2);
#pragma unroll
    for (int m = 0; m < 4; ++m)
#pragma unroll
      for (int n = 0; n < 2; ++n) {
        const unsigned q0 = pp[m][n * 2], q1 = pp[m][n * 2 + 1];
        const f32x4 v = acc[n][m] + (f32x4){bflo(q0), bfhi(q0), bflo(q1), bfhi(q1)};
        *(u32x2*)(smem + STG + (wm * 64 + m * 16 + fr) * 128 + ((((wn * 2 + n) * 2 + (fq >> 1)) ^ (fr & 7)) << 4) + (fq & 1) * 8) = (u32x2){pk_bf16(v[0], v[1]), pk_bf16(v[2], v[3])};
      }
    STAMPV(5);
    asm volatile("" : "+v"(res[0]), "+v"(res[1]));
  }
  lds_barrier();
  {
    const auto rO = __builtin_amdgcn_make_buffer_rsrc((void*)a.O, 0, 0x7fffffff, 0x00020000);
    u32x4 o[2];
    int lz = lane; asm volatile("" : "+v"(lz));
#pragma unroll
    for (int i = 0; i < 2; ++i) {
      const u32x4 v = *(const u32x4*)(smem + STG + (wave * 16 + i * 8 + (lz >> 3)) * 128 + (((lz & 7) ^ (lz >> 3)) << 4));
#pragma unroll
      for (int j = 0; j < 4; ++j) { const unsigned r = res[i][j], q = v[j]; o[i][j] = pk_bf16(bflo(r) + bflo(q), bfhi(r) + bfhi(q)); }
    }
    asm volatile("" : "+v"(o[0]), "+v"(o[1]));
#pragma unroll
#define KN_OAUX 16
    for (int i = 0; i < 2; ++i) __builtin_amdgcn_raw_buffer_store_b128(o[i], rO, orow, i * 8 * D_ * 2, KN_OAUX);
    __builtin_amdgcn_sched_barrier(0);
    asm volatile("s_nop 1" ::: "memory");
    __builtin_amdgcn_sched_barrier(0);
    if (bid == 0 && tid == 0) set_flag(a.sync + FEP, tag);
  }
  STAMP(7);
  STAMP(8);
  STAMP(3);
#endif
}

struct Ptrs { const bf16_t *x, *wqkv, *wo, *wg, *wu, *wd; bf16_t* out; char* ws; };
#define DBG(i)
#define WS_O ((bf16_t*)(p.ws + (8u << 20)))
#define WS_SL ((float*)WS_O)
#define WS_H ((bf16_t*)(p.ws + (16u << 20)))
#define WS_ST ((float*)(p.ws + (24u << 20)))
#define WS_CS ((float*)(p.ws + (25u << 20)))
#define WS_SY ((unsigned*)(p.ws + (26u << 20)))
#define WS_A ((bf16_t*)(p.ws + (28u << 20)))
#if defined(__HIP_DEVICE_COMPILE__) && !defined(KN_NOWARM)
#define ARG_WARM(v) do { asm volatile("s_load_dword %0, %1, 0x0" : "=s"(v) : "s"(__builtin_amdgcn_kernarg_segment_ptr())); __builtin_amdgcn_sched_barrier(0); } while (0)
#else
#define ARG_WARM(v)
#endif
struct Args4 { GemmArgs q, b, c, d; };
extern "C" __global__ __launch_bounds__(512, 2) void k_all(Ptrs p) {
  unsigned warm = 0;
  ARG_WARM(warm);
  p.ws += -(unsigned)(size_t)p.ws & 0x1fffffu;
  const Args4 a{GemmArgs{p.x, p.wqkv, nullptr, nullptr, WS_O, (float*)WS_H, p.wg, p.wu, WS_CS, p.wd, p.wo, WS_SY, (float*)WS_A, warm DBG(0)},
    GemmArgs{WS_O, p.wo, nullptr, p.x, WS_H, WS_ST, nullptr, nullptr, nullptr, p.wd, nullptr, WS_SY, nullptr, 0u DBG(1)},
    GemmArgs{WS_H, p.wg, p.wu, p.out, WS_A, WS_ST, nullptr, nullptr, WS_CS, p.wd, nullptr, WS_SY, WS_SL, 0u DBG(2)},
    GemmArgs{WS_A, p.wd, nullptr, WS_H, p.out, nullptr, nullptr, nullptr, nullptr, nullptr, nullptr, WS_SY, WS_SL, 0u DBG(3)}};
  const unsigned tag = k1_phase(a.q);
  wo_phase(a.b, a.c, tag);
#define KN_EH 10
#define KN_TA 2
#define KN_TB 2
  auto pre = [&](auto tc) { dn_phase<1, KN_EH, KN_TA, KN_TB, decltype(tc)::value>(a.d, tag); };
  glu_phase(a.c, tag, pre);
  dn_phase<2, KN_EH, KN_TA, KN_TB>(a.d, tag);
#if defined(DIAG) && defined(__HIP_DEVICE_COMPILE__)
  lds_barrier();
  if (threadIdx.x < 64 && threadIdx.x != 12) { int v = ds_r32(SNAP + 320 + threadIdx.x * 4); asm volatile("s_waitcnt lgkmcnt(0)" : "+v"(v)); a.q.dbg[(threadIdx.x >> 4) * 8192 + blockIdx.x * 16 + (threadIdx.x & 15)] = (unsigned)v; }
#endif
}

extern "C" __global__ void k_zend() {}

extern "C" void kernel_launch(void* const* d_in, const int* in_sizes, int n_in, void* d_out, int out_size, void* d_ws,
                              size_t ws_size, hipStream_t stream) {
  const Ptrs p{(const bf16_t*)d_in[0], (const bf16_t*)d_in[1], (const bf16_t*)d_in[2], (const bf16_t*)d_in[4], (const bf16_t*)d_in[5], (const bf16_t*)d_in[6], (bf16_t*)d_out, (char*)d_ws};
  constexpr int L_all = SNAP + 64 + 512;
  static_assert(SNAP >= 3 * 256 * 128 + 4 * 14592 + 4096 + 1024 + 256 + 512 + 128 + 64 && L_all <= 163840, "LDS");
  (void)hipFuncSetAttribute((const void*)k_all, hipFuncAttributeMaxDynamicSharedMemorySize, L_all);
  k_all<<<256, 512, L_all, stream>>>(p);
}
```

```cpp
#include <hip/hip_runtime.h>
#include <hip/hip_bf16.h>
#include <stdint.h>
#include <type_traits>

typedef unsigned short bf16_t;
typedef short bf16x8 __attribute__((ext_vector_type(8)));
typedef short s16x4 __attribute__((ext_vector_type(4)));
typedef float f32x4 __attribute__((ext_vector_type(4)));
typedef float f32x2 __attribute__((ext_vector_type(2)));
typedef unsigned u32x2 __attribute__((ext_vector_type(2)));
typedef unsigned u32x4 __attribute__((ext_vector_type(4)));
typedef __bf16 bf16x2_t __attribute__((ext_vector_type(2)));
typedef __attribute__((address_space(3))) void* lds_void_ptr;
typedef __attribute__((address_space(3))) char* lds_char_ptr;

constexpr int S_ = 2048, D_ = 1024, DF_ = 3584;
#define KN_CT 7
#define KN_HQ 5
#define KN_KT 4
#define KN_RSA 262144
#define KN_NUMA 3
constexpr int RSH = (KN_NUMA & 1) ? 65536 : 32768, RSA = (KN_NUMA & 2) ? KN_RSA : 114688;
__device__ __forceinline__ int rbH(int rb, int sd) { return (KN_NUMA & 1) ? (rb & 63) * RSH + (((sd ^ (rb >> 5)) & 1) << 13) : rb * RSH; }
__device__ __forceinline__ int rbA(int rb, int sd) { return (KN_NUMA & 2) ? ((rb >> 3) & 7) * 2097152 + (rb & 7) * RSA + (((sd ^ (rb >> 3)) & 1) << 13) : rb * RSA; }
__device__ __forceinline__ constexpr int omH(int o) { return (KN_NUMA & 1) ? o + (o & ~8191) : o; }
__device__ __forceinline__ constexpr int omA(int o) { return (KN_NUMA & 2) ? o + (o & ~8191) : o; }

__device__ __forceinline__ float bflo(unsigned v) { return __uint_as_float(v << 16); }
__device__ __forceinline__ float bfhi(unsigned v) { return __uint_as_float(v & 0xffff0000u); }
__device__ __forceinline__ unsigned pk_bf16(float lo, float hi) {
  f32x2 v = {lo, hi};
  bf16x2_t b = __builtin_convertvector(v, bf16x2_t);
  return __builtin_bit_cast(unsigned, b);
}
__device__ __forceinline__ float rbf(float v) { return bflo(pk_bf16(v, v)); }

template <int STRIDE> __device__ __forceinline__ int wswz(int row) {
  if constexpr (STRIDE % 256 == 0) return ((row & 3) | (((row >> 3) & 1) << 2)) << 1;
  else if constexpr (STRIDE % 256 == 128) return (((row >> 1) & 1) | (((row >> 3) & 1) << 1)) << 1;
  else return ((row >> 3) & 1) << 1;
}

template <int OFF> __device__ __forceinline__ s16x4 ds_tr(unsigned addr) {
  s16x4 r; asm volatile("ds_read_b64_tr_b16 %0, %1 offset:%2" : "=v"(r) : "v"(addr), "n"(OFF)); return r;
}
template <int OFF> __device__ __forceinline__ bf16x8 ds_b128(unsigned addr) {
  bf16x8 r; asm volatile("ds_read_b128 %0, %1 offset:%2" : "=v"(r) : "v"(addr), "n"(OFF)); return r;
}
template <int OFF> __device__ __forceinline__ f32x2 ds_b64(unsigned addr) {
  f32x2 r; asm volatile("ds_read_b64 %0, %1 offset:%2" : "=v"(r) : "v"(addr), "n"(OFF)); return r;
}
__device__ __forceinline__ float sum8(float v) {
  v += __int_as_float(__builtin_amdgcn_update_dpp(0, __float_as_int(v), 0x128, 0xf, 0xf, false));
  auto a = __builtin_amdgcn_permlane16_swap(__float_as_uint(v), __float_as_uint(v), false, false);
  v = __uint_as_float(a[0]) + __uint_as_float(a[1]);
  auto b = __builtin_amdgcn_permlane32_swap(__float_as_uint(v), __float_as_uint(v), false, false);
  return __uint_as_float(b[0]) + __uint_as_float(b[1]);
}
__device__ __forceinline__ float sum8l(float v) {
  v += __int_as_float(__builtin_amdgcn_update_dpp(0, __float_as_int(v), 0xb1, 0xf, 0xf, false));
  v += __int_as_float(__builtin_amdgcn_update_dpp(0, __float_as_int(v), 0x4e, 0xf, 0xf, false));
  return v + __int_as_float(__builtin_amdgcn_update_dpp(0, __float_as_int(v), 0x141, 0xf, 0xf, false));
}
__device__ __forceinline__ int ds_r32(unsigned addr) { int r; asm volatile("ds_read_b32 %0, %1" : "=v"(r) : "v"(addr)); return r; }
__device__ __forceinline__ void ds_w32(unsigned addr, int v) { asm volatile("ds_write_b32 %0, %1" :: "v"(addr), "v"(v) : "memory"); }
template <int N> __device__ __forceinline__ void wait_vm() { asm volatile("s_waitcnt vmcnt(%0)" ::"n"(N) : "memory"); }
__device__ __forceinline__ void raw_barrier() { asm volatile("" ::: "memory"); __builtin_amdgcn_s_barrier(); asm volatile("" ::: "memory"); }
__device__ __forceinline__ void loop_barrier() { raw_barrier(); }
__device__ __forceinline__ void lds_barrier() { asm volatile("s_waitcnt lgkmcnt(0)" ::: "memory"); __builtin_amdgcn_s_barrier(); asm volatile("" ::: "memory"); }

__device__ __forceinline__ u32x4 mk_rsrc(const void* p) { const unsigned long long v = (unsigned long long)p; return (u32x4){(unsigned)v, (unsigned)(v >> 32) & 0xffffu, 0x7fffffffu, 0x00020000u}; }
#define LD1K(v, so, off, sc) "buffer_load_dwordx4 " v ", %1, " so " offen offset:" off sc " lds\n\t"
template <int WAIT> __device__ __forceinline__ void ld_w4(unsigned m, u32x4 r, const unsigned (&v)[4], unsigned s0, unsigned s1) {
  if constexpr (WAIT >= 0) asm volatile("s_mov_b32 m0, %0\n\ts_waitcnt vmcnt(%8)\n\ts_barrier\n\t" LD1K("%2", "%6", "0", "") LD1K("%3", "%7", "1024", "") LD1K("%4", "%6", "2048", "") LD1K("%5", "%7", "3072", "")
                                        :: "s"(m), "s"(r), "v"(v[0]), "v"(v[1]), "v"(v[2]), "v"(v[3]), "s"(s0), "s"(s1), "n"(WAIT) : "memory", "m0");
  else asm volatile("s_mov_b32 m0, %0\n\ts_nop 0\n\t" LD1K("%2", "%6", "0", "") LD1K("%3", "%7", "1024", "") LD1K("%4", "%6", "2048", "") LD1K("%5", "%7", "3072", "")
                    :: "s"(m), "s"(r), "v"(v[0]), "v"(v[1]), "v"(v[2]), "v"(v[3]), "s"(s0), "s"(s1) : "memory", "m0");
}
template <int WAIT> __device__ __forceinline__ void ld_w2(unsigned m, u32x4 r, const unsigned (&v)[4], unsigned s0, unsigned s1) {
  if constexpr (WAIT >= 0) asm volatile("s_mov_b32 m0, %0\n\ts_waitcnt vmcnt(%6)\n\ts_barrier\n\t" LD1K("%2", "%4", "0", "") LD1K("%3", "%5", "1024", "")
                                        :: "s"(m), "s"(r), "v"(v[0]), "v"(v[1]), "s"(s0), "s"(s1), "n"(WAIT) : "memory", "m0");
  else asm volatile("s_mov_b32 m0, %0\n\ts_nop 0\n\t" LD1K("%2", "%4", "0", "") LD1K("%3", "%5", "1024", "") :: "s"(m), "s"(r), "v"(v[0]), "v"(v[1]), "s"(s0), "s"(s1) : "memory", "m0");
}
__device__ __forceinline__ void ld_x4(unsigned m, u32x4 r, unsigned v0, unsigned v1, unsigned so) {
  asm volatile("s_mov_b32 m0, %0\n\ts_nop 0\n\t" LD1K("%2", "%4", "0", " sc1") LD1K("%2", "%4", "1024", " sc1") LD1K("%3", "%4", "2048", " sc1") LD1K("%3", "%4", "3072", " sc1")
               :: "s"(m), "s"(r), "v"(v0), "v"(v1), "s"(so) : "memory", "m0");
}
__device__ __forceinline__ unsigned long long ld_x4c(unsigned m, u32x4 r, unsigned v0, unsigned v1, unsigned so, unsigned ca) {
  u32x4 a, b, c, d; unsigned long long f;
  asm volatile("ds_read_b128 %1, %10\n\tds_read_b128 %2, %10 offset:1024\n\tds_read_b128 %3, %10 offset:2048\n\tds_read_b128 %4, %10 offset:3072\n\ts_mov_b32 m0, %5\n\ts_nop 0\n\t"
               "buffer_load_dwordx4 %7, %6, %9 offen sc1 lds\n\tbuffer_load_dwordx4 %7, %6, %9 offen offset:1024 sc1 lds\n\ts_waitcnt lgkmcnt(0)"
               : "=s"(f), "=&v"(a), "=&v"(b), "=&v"(c), "=&v"(d) : "s"(m), "s"(r), "v"(v0), "v"(v1), "s"(so), "v"(ca) : "memory", "m0");
  unsigned x = max(max(a[0], a[1]), a[2]), y = max(max(b[0], b[1]), b[2]); x = max(max(x, a[3]), c[0]); y = max(max(y, b[3]), d[0]);
  asm volatile("s_mov_b32 m0, %2\n\ts_nop 0\n\tbuffer_load_dwordx4 %4, %3, %5 offen offset:2048 sc1 lds" : "+v"(x), "+v"(y) : "s"(m), "s"(r), "v"(v1), "s"(so) : "memory", "m0");
  x = max(max(x, c[1]), c[2]); y = max(max(y, d[1]), d[2]); x = max(max(x, c[3]), d[3]);
  f = __builtin_amdgcn_ballot_w64(max(x, y) == 0xffffffffu);
  asm volatile("s_mov_b32 m0, %1\n\ts_nop 0\n\tbuffer_load_dwordx4 %3, %2, %4 offen offset:3072 sc1 lds" : "+s"(f) : "s"(m), "s"(r), "v"(v1), "s"(so) : "memory", "m0");
  return f;
}
template <int LO, int SO, bool SUB, bool SC = false> __device__ __forceinline__ void ld_ca(unsigned mb, u32x4 r, unsigned v, unsigned sd) {
  unsigned t;
  if constexpr (SUB) asm volatile("s_add_i32 m0, %1, %4\n\ts_sub_i32 %0, %5, %6\n\tbuffer_load_dwordx4 %3, %2, %0 offen lds" : "=&s"(t) : "s"(mb), "s"(r), "v"(v), "n"(LO), "n"(SO), "s"(sd) : "memory", "m0", "scc");
  else if constexpr (SC) asm volatile("s_add_i32 m0, %1, %4\n\ts_add_i32 %0, %6, %5\n\tbuffer_load_dwordx4 %3, %2, %0 offen sc1 lds" : "=&s"(t) : "s"(mb), "s"(r), "v"(v), "n"(LO), "n"(SO), "s"(sd) : "memory", "m0", "scc");
  else asm volatile("s_add_i32 m0, %1, %4\n\ts_add_i32 %0, %6, %5\n\tbuffer_load_dwordx4 %3, %2, %0 offen lds" : "=&s"(t) : "s"(mb), "s"(r), "v"(v), "n"(LO), "n"(SO), "s"(sd) : "memory", "m0", "scc");
}
template <int LO, int SO> __device__ __forceinline__ void ld_cx(unsigned mb, u32x4 r, unsigned v, unsigned sx) {
  unsigned t;
  asm volatile("s_add_i32 m0, %1, %4\n\ts_xor_b32 %0, %6, %5\n\tbuffer_load_dwordx4 %3, %2, %0 offen lds" : "=&s"(t) : "s"(mb), "s"(r), "v"(v), "n"(LO), "n"(SO), "s"(sx) : "memory", "m0", "scc");
}
template <int I, int N, class F> __device__ __forceinline__ void static_for(F&& f) {
  if constexpr (I < N) { f(std::integral_constant<int, I>{}); static_for<I + 1, N>(f); }
}
#define CODE_ADDR(v, sym) asm volatile("s_getpc_b64 s[40:41]\n\ts_add_u32 s40, s40, " sym "@rel32@lo+4\n\ts_addc_u32 s41, s41, " sym "@rel32@hi+12\n\ts_mov_b64 %0, s[40:41]" : "=s"(v) :: "s40", "s41", "scc")
#define MERGED 1
constexpr int SNAP = 162752;
#define KN_S1C 8
#define KN_S1P 3
constexpr int STAB = 786432, STL = 3584;
#define FHM(p) ((p) * 256)
#define FRC(p) ((p) * 256 + 64)
#define FAM(p) ((p) * 256 + 128)
#define FCS(t) (2048 + (t) * 32)
#define FLN(k, i) ((1 + 2 * (k) + (((i) >> 6) & 1)) * 4096 + ((~sd & 1) << 11) + ((i) & 63) * 32)
#define FLO(t) FLN(0, (t) * 16)
#define FHF(p) FLN(1, (p) * 32)
#define FAE(p) FLN(2, (p) * 32)
#define FPR(b) FLN(3, ((b) & 3) * 32 + ((b) >> 3))
#define SIDE (((xcd >> 2) ^ tag) & 1)
constexpr int VTAB = STAB + 32768, PRB = 1048576;
constexpr int FEP = 3072;
constexpr unsigned long long LO32 = 0xffffffffull, HI32 = LO32 << 32;
#define KN_V 5
#define KN_D 2
#define KN_PF 0
#define KN_TRY 6
typedef __attribute__((address_space(1))) unsigned* gu32;
__device__ __forceinline__ void set_flag(unsigned* p, unsigned tag) { __hip_atomic_store((gu32)p, tag, __ATOMIC_RELAXED, __HIP_MEMORY_SCOPE_AGENT); }
__device__ __forceinline__ bool missing(unsigned v, unsigned tag, unsigned long long mask) { return (__builtin_amdgcn_ballot_w64(v != tag) & mask) != 0; }
__device__ __forceinline__ void wait_flags(const unsigned* p, unsigned off, unsigned tag, unsigned long long mask) {
  unsigned v, polls = 0;
  do { asm volatile("s_nop 4\n\tglobal_load_dword %0, %1, %2 sc1\n\ts_waitcnt vmcnt(0)" : "=v"(v) : "v"(off), "s"(p) : "memory"); } while (missing(v, tag, mask) && ++polls < 4000000u);
}
__device__ __forceinline__ unsigned lane4() { unsigned l = __builtin_amdgcn_mbcnt_hi(~0u, __builtin_amdgcn_mbcnt_lo(~0u, 0u)) * 4u; asm volatile("" : "+v"(l)); return l; }
__device__ __forceinline__ bool lds_missing(unsigned addr, unsigned tag, unsigned long long mask) {
  int v = ds_r32(addr + lane4());
  asm volatile("s_waitcnt lgkmcnt(0)" : "+v"(v) :: "memory");
  return missing((unsigned)v, tag, mask);
}
constexpr int ACNT = SNAP - 60;
__device__ __forceinline__ void lds_inc(unsigned addr) { if (__builtin_amdgcn_mbcnt_hi(~0u, __builtin_amdgcn_mbcnt_lo(~0u, 0u)) == 0) asm volatile("ds_add_u32 %0, %1" :: "v"(addr), "v"(1) : "memory"); }
constexpr int FPAD = SNAP - 704;
__device__ __forceinline__ void lds_wait8(unsigned addr, int n = 8) {
  int dv; unsigned polls = 0;
  do { dv = ds_r32(addr); asm volatile("s_waitcnt lgkmcnt(0)" : "+v"(dv) :: "memory"); } while (__builtin_amdgcn_readfirstlane(dv) < n && ++polls < 40000000u);
}
template <int N> __device__ __forceinline__ bool marked_in(bf16x8 (&v)[N]) {
  asm volatile("s_waitcnt lgkmcnt(0)" ::: "memory");
  unsigned m = 0;
#pragma unroll
  for (int i = 0; i < N; ++i) {
    asm volatile("" : "+v"(v[i]));
    const u32x4 u = __builtin_bit_cast(u32x4, v[i]);
    m = max(max(m, u[0]), u[1]); m = max(max(m, u[2]), u[3]);
  }
  return __builtin_amdgcn_ballot_w64(m == 0xffffffffu) != 0;
}
template <int N, int STR = 4096> __device__ __forceinline__ bool marked(unsigned addr) {
  bf16x8 v[N];
  static_for<0, N>([&](auto ic) { constexpr int i = decltype(ic)::value; v[i] = ds_b128<i * STR>(addr); });
  return marked_in<N>(v);
}

struct GemmArgs {
  const bf16_t* X;
  const bf16_t* W0;
  const bf16_t* W1;
  const bf16_t* R;
  bf16_t* O;
  float* st;
  const bf16_t* CW0;
  const bf16_t* CW1;
  float* cs;
  const bf16_t* PF0;
  const bf16_t* PF1;
  unsigned* sync;
  float* slab;
  unsigned warm;
};
#define STAMP(i)
#define STAMPW(i, w)

#define STAMPV(i) STAMP(i)
template <int MF, int NF> struct Frags { bf16x8 x[MF]; s16x4 lo[NF], hi[NF]; };

constexpr int WOB = 55296;
constexpr int wo_pos(int s) { return s < 6 ? WOB + s * 16384 : (s - 6) * 16384; }
#define KN_GP 7
#define KN_GX 1
#define KN_GL 0
#define KN_EW 1
#define KN_VP 6
#define KN_VQ 0
#define KN_GR 24
#define KN_GE 26
template <int NF, int GR> struct SchedP {
  static constexpr int NP = 4 * NF, NR = NP + 8;
  static constexpr int pa(int p) { return (p + NP - 12) % NP; }
  static constexpr int pn(int a) { return a < 8 ? (a ^ (a >> 1)) & 1 : (a < NP - 8 ? a / 4 : NF - 2 + ((a ^ (a >> 1)) & 1)); }
  static constexpr int pm(int a) { return a < 8 ? a >> 1 : (a < NP - 8 ? ((a & 4) ? a & 3 : 3 - (a & 3)) : (a - NP + 8) >> 1); }
  static constexpr int gap(int r) { return r < 8 ? 2 + r : (r < 12 ? 12 + 4 * (r - 8) : (r < NP ? 26 + (r - 12) * GR / (NP - 12) : 11 + 2 * ((r - NP) & ~1) - (r & 1))); }
  static constexpr int need(int r) { return r < 8 ? 12 : (r < NP ? (12 + (r & ~3) < NP ? 12 + (r & ~3) : NP) : 12 + ((r - NP) & ~1)); }
  static constexpr int cnt(int p, int e = 0) {
    int g = -1, q = -1;
    for (int r = 0; r < NR; ++r) if (need(r) == p && (gap(r) > g || (gap(r) == g && r > q))) { g = gap(r); q = r; }
    if (g < 0) return -1;
    int c = 0;
    for (int r = 0; r < NR; ++r) if (gap(r) < 2 * p - e - (e > 0) && (gap(r) > g || (gap(r) == g && r > q))) ++c;
    return c < 15 ? c : 15;
  }
  static constexpr bool last(int a) { for (int b = a + 1; b < NP; ++b) if (pn(b) == pn(a)) return false; return true; }
};
using SchedG = SchedP<7, KN_GR>;
template <class HK>
__device__ __forceinline__ void glu_phase(const GemmArgs& a, unsigned tag, HK hook) {
#if defined(__HIP_DEVICE_COMPILE__)
  constexpr int LDW = DF_, KU = 32, NT = 16, MF = 4, NF = 7, NLW = 4, XI = 4, WI = 7;
  constexpr int UPOFF = KU * 224 + 16;
  constexpr int XT = 256 * 128, WU = (UPOFF + KU * 224 + 255) / 256 * 256;
  constexpr int XBASE = 0, WBASE = 3 * XT, RING = WBASE + 4 * WU;
  extern __shared__ __attribute__((aligned(16))) char smem[];
  STAMP(0);
  const unsigned lds0 = (unsigned)(size_t)(lds_char_ptr)smem;
  const int tid = threadIdx.x, lane = tid & 63;
  const int wave = __builtin_amdgcn_readfirstlane(tid >> 6);
  const int wm = wave >> 1, wn = wave & 1;
  const int bid = blockIdx.x, xcd = bid & 7, idx = bid >> 3;
  const int tm = (xcd >> 2) * 4 + (idx >> 3), tn = (xcd & 3) * 8 + (idx & 7);
  const int m0 = tm * 256, sd = SIDE;
  const int krot = (tm >> 2) * 8;
  const int fr = lane & 15, fq = lane >> 4;
  const int wq = (lane >> 2) & 3, wp = lane & 3;

  const int panel = m0 >> 8;
  constexpr int TCH = RING + 5120;
  constexpr int DONE = TCH + 896;
  auto done_inc = [&]() { if (lane == 0) asm volatile("ds_add_u32 %0, %1" :: "v"(lds0 + DONE), "v"(1) : "memory"); };
  auto touch = [&](const void* base, unsigned voff, unsigned soff, unsigned lim = 0x7fffffffu) {
    __builtin_amdgcn_raw_ptr_buffer_load_lds(__builtin_amdgcn_make_buffer_rsrc((void*)base, 0, lim, 0x00020000), (lds_void_ptr)(size_t)(lds0 + TCH), 4, voff, soff, 0, 0);
  };
  const auto rO = __builtin_amdgcn_make_buffer_rsrc((void*)a.O, 0, 0x7fffffff, 0x00020000);

  unsigned xsrc[XI], wsrc[WI];
#pragma unroll
  for (int i = 0; i < XI; ++i) { const int j = (wave & 3) + NLW * i; xsrc[i] = (unsigned)(rbH(m0 >> 4, sd) + (j >> 1) * RSH + (j & 1) * 1024 + lane * 16); }
#pragma unroll
  for (int i = 0; i < WI; ++i) {
    const int b = i * 1024 + lane * 16, p = b / 224, pos = (b % 224) >> 4;
    const int row = 8 * ((p & 15) >> 2) + (p & 3) + 4 * (p >> 4);
    wsrc[i] = (unsigned)(row * LDW + tn * 112 + pos * 8) * 2u;
  }
  auto ldX = [&](int i, int tile, int part, int slot) {
    __builtin_amdgcn_raw_ptr_buffer_load_lds(__builtin_amdgcn_make_buffer_rsrc((void*)a.X, 0, 0x7fffffff, 0x00020000), (lds_void_ptr)(size_t)(lds0 + XBASE + slot * XT + (part * 16 + (wave & 3) + NLW * i) * 1024), 16, xsrc[i],
                                             omH(((tile + krot) & 15) * 2048) + part * 8 * RSH, 0, 16);
  };
  auto ldW = [&](int i, int tile, int slot) {
    __builtin_amdgcn_raw_ptr_buffer_load_lds(__builtin_amdgcn_make_buffer_rsrc((void*)((wave & 1) ? a.W1 : a.W0), 0, 0x7fffffff, 0x00020000),
                                             (lds_void_ptr)(size_t)(lds0 + WBASE + (slot + (wave >> 1)) * WU + (wave & 1) * UPOFF + i * 1024), 16, wsrc[i],
                                             (((tile + krot) & 15) * 2 + (wave >> 1)) * KU * LDW * 2, 0, 0);
  };
  auto issueX = [&](int tile, int part, int slot) {
#pragma unroll
    for (int i = 0; i < XI; ++i) ldX(i, tile, part, slot);
  };

  f32x4 acc[NF][MF];
#pragma unroll
  for (int n = 0; n < NF; ++n)
#pragma unroll
    for (int m = 0; m < MF; ++m) acc[n][m] = (f32x4){0.f, 0.f, 0.f, 0.f};
  f32x4 accs = {0.f, 0.f, 0.f, 0.f};

  const unsigned xa0 = lds0 + XBASE + wm * MF * 2048 + lane * 16;
  unsigned wa[2];
#pragma unroll
  for (int c = 0; c < 2; ++c) wa[c] = lds0 + WBASE + (4 * fq + wq) * 224 + wn * 112 + (c == 0 ? wp * 8 : 96 + (wp & 1) * 8 + (wp >> 1) * UPOFF);

  using I0 = std::integral_constant<int, 0>; using I1 = std::integral_constant<int, 1>;
  using T = std::true_type; using F = std::false_type;
  typedef Frags<MF, NF> FR;
  struct RdCtx { unsigned xa, wb[2]; };
  auto mkctx = [&](unsigned xoffs, unsigned woffs) { return RdCtx{xa0 + xoffs, {wa[0] + woffs, wa[1] + woffs}}; };
  FR fa, fb;
  auto rdg = [&](const RdCtx& c, auto rc) {
    constexpr int r = decltype(rc)::value;
    if constexpr (r >= 28) { constexpr int m = (r - 28) >> 1; if constexpr (r & 1) fb.x[m] = ds_b128<m * 2048 + 1024>(c.xa); else fa.x[m] = ds_b128<m * 2048>(c.xa); }
    else {
      constexpr int n = r >> 2, cc = n == 6, o = (n == 6 ? 0 : (n >> 1) * 32 + (n & 1) * UPOFF) + ((r >> 1) & 1) * WU + (r & 1) * 16 * 224;
      FR& f = (r & 2) ? fb : fa;
      if constexpr (r & 1) f.hi[n] = ds_tr<o>(c.wb[cc]); else f.lo[n] = ds_tr<o>(c.wb[cc]);
    }
  };
  auto ping = [&](auto pc) {
    static_for<0, 36>([&](auto rc) { constexpr int r = decltype(rc)::value;
      if constexpr (SchedG::need(r) == decltype(pc)::value) {
        FR& f = (r >= 28 ? r & 1 : r & 2) ? fb : fa;
        if constexpr (r >= 28) { bf16x8& q = f.x[(r - 28) >> 1]; asm volatile("" : "+v"(q)); } else { s16x4& q = (r & 1) ? f.hi[r >> 2] : f.lo[r >> 2]; asm volatile("" : "+v"(q)); } } });
  };
  auto pairs = [&](auto PBc, auto PEc, auto RDc, auto CSc, const RdCtx& c, auto&& gapf) {
    constexpr bool RD = decltype(RDc)::value; constexpr int CS = decltype(CSc)::value;
    static_for<decltype(PBc)::value, decltype(PEc)::value>([&](auto pc) {
      constexpr int p = decltype(pc)::value, a = (p + 16) % 28, n = SchedG::pn(a), m = SchedG::pm(a);
      if constexpr (SchedG::cnt(p) >= 0) { asm volatile("s_waitcnt lgkmcnt(%0)" :: "n"(SchedG::cnt(p)) : "memory"); ping(pc); }
      static_for<0, 2>([&](auto hc) {
        constexpr int g = 2 * p + decltype(hc)::value;
        FR& u = ((a ^ g) & 1) ? fb : fa;
        acc[n][m] = __builtin_amdgcn_mfma_f32_16x16x32_bf16(__builtin_shufflevector(u.lo[n], u.hi[n], 0, 1, 2, 3, 4, 5, 6, 7), u.x[m], acc[n][m], 0, 0, 0);
        __builtin_amdgcn_sched_barrier(0);
        if constexpr (CS >= 0 && SchedG::last(a) && (g & 1)) {
          const unsigned sv = fr == n ? 0x3f803f80u : 0u;
          const u32x4 s4 = {sv, sv, sv, sv};
          FR& w = CS ? fb : fa;
          accs = __builtin_amdgcn_mfma_f32_16x16x32_bf16(__builtin_shufflevector(w.lo[n], w.hi[n], 0, 1, 2, 3, 4, 5, 6, 7), __builtin_bit_cast(bf16x8, s4), accs, 0, 0, 0);
          __builtin_amdgcn_sched_barrier(0);
        }
        if constexpr (RD) static_for<0, 36>([&](auto rc) { if constexpr (SchedG::gap(decltype(rc)::value) == g) rdg(c, rc); });
        if constexpr (g & 1) gapf(pc);
        __builtin_amdgcn_sched_barrier(0);
      });
    });
    if constexpr (RD) { asm volatile("s_waitcnt lgkmcnt(0)" ::: "memory"); ping(std::integral_constant<int, 28>{}); }
  };

  f32x4* tb_row = (f32x4*)(smem + RING);
  float* tb_cs = (float*)(tb_row + 256);
  constexpr int RTAB = 524288;
  auto glu_stats_issue = [&]() {
    {
      const auto rS = __builtin_amdgcn_make_buffer_rsrc((void*)a.st, 0, 0x7fffffff, 0x00020000);
      if (wave == 7) __builtin_amdgcn_raw_ptr_buffer_load_lds(rS, (lds_void_ptr)(size_t)(lds0 + RING + 3072), 16, ((lane >> 2) * S_ + m0 + tn * 8 + (lane & 3) * 2) * 8, 0, 0, 16);
    }
  };
  auto glu_stats = [&]() {
    {
      int lx = lane; asm volatile("" : "+v"(lx));
      const unsigned sa = lds0 + RING + 3072 + lx * 8;
      f32x2 p0 = ds_b64<0>(sa), p1 = ds_b64<512>(sa);
      asm volatile("s_waitcnt lgkmcnt(0)" : "+v"(p0), "+v"(p1) :: "memory");
      float s0 = p0[0] + p1[0]; asm volatile("" : "+v"(s0), "+v"(p0));
      const float mu = sum8(s0) * (1.f / 16.f);
      const float v = sum8((p0[1] + p1[1]) + 64.f * ((p0[0] - mu) * (p0[0] - mu) + (p1[0] - mu) * (p1[0] - mu))) * (1.f / 1024.f) + 1e-5f;
      float rs = __builtin_amdgcn_rsqf(v), k2 = -2.3022082f * rs; asm volatile("" : "+v"(k2), "+v"(rs));
      const f32x4 rv = {mu, -0.10294324f * rs * rs * rs, k2, v};
      __builtin_amdgcn_raw_buffer_store_b128(__builtin_bit_cast(u32x4, rv), __builtin_amdgcn_make_buffer_rsrc((void*)a.cs, 0, 0x7fffffff, 0x00020000),
                                             lx < 8 ? (unsigned)(RTAB + (m0 + tn * 8 + lx) * 16) : 0x80000000u, 0, 16);
    }
  };
  auto epilogue = [&](auto EAc) __attribute__((always_inline)) {
  STAMP(2);

  {
    constexpr bool EARLY = decltype(EAc)::value;
    static_assert(DONE + 4 == ACNT && TCH + 256 == FPAD, "LDS");
#define KN_EP 1
    if constexpr (!EARLY) {
      wait_vm<0>();
      lds_wait8(lds0 + DONE);
      if (KN_EP) __builtin_amdgcn_s_setprio(2);
      STAMPW(9, 4);
      hook(I0{});
      STAMPW(10, 4);
    } else { if (KN_EP) __builtin_amdgcn_s_setprio(3); hook(I1{}); }
    asm volatile("" ::: "memory");
    f32x4 cg[4], cu[4], rw[MF], rw6[2];
    int lz = lane; asm volatile("" : "+v"(lz));
    const int fr = lz & 15, fq = lz >> 4;
#pragma unroll
    for (int p = 0; p < 4; ++p) {
      const int o = wn * 56 + p * 16 + (p == 3 ? (fq & 1) : fq) * 4;
      cg[p] = *(const f32x4*)(tb_cs + o); cu[p] = *(const f32x4*)(tb_cs + 112 + o);
    }
#pragma unroll
    for (int m = 0; m < MF; ++m) rw[m] = tb_row[(wm * MF + m) * 16 + fr];
#pragma unroll
    for (int q = 0; q < 2; ++q) rw6[q] = tb_row[(wm * MF + 2 * q + (fq >> 1)) * 16 + fr];
    const int gz = rbA((m0 >> 4) + wm * 4, sd) + fr * 16, g = tn * 2 + wn, g1 = omA(g * 1024);
    const int go0 = gz + (fq & 1) * RSA + (fq >> 1) * 256, go6 = gz + ((fq & 1) * 2 + (fq >> 1)) * RSA;
    unsigned hv[MF][NF];
    auto put = [&](unsigned a0, unsigned a1, unsigned b0, unsigned b1, int vo, int so) {
      const auto s0 = __builtin_amdgcn_permlane16_swap(a0, b0, false, false), s1 = __builtin_amdgcn_permlane16_swap(a1, b1, false, false);
      __builtin_amdgcn_raw_buffer_store_b128((u32x4){s0[0], s1[0], s0[1], s1[1]}, rO, vo, so, 16);
      __builtin_amdgcn_sched_barrier(0);
      asm volatile("s_nop 1" ::: "memory");
      __builtin_amdgcn_sched_barrier(0);
    };
#define STAGE(body) _Pragma("unroll") for (int i = 0; i < 2 * NN; ++i) { const int m = MB + i / NN, n = N0 + i % NN; const f32x4 k = n == 6 ? rw6[m >> 1] : rw[m]; body; } __builtin_amdgcn_sched_barrier(0)
    auto grp = [&](auto MBc, auto N0c, auto NNc, auto&& mid) {
      constexpr int MB = decltype(MBc)::value, N0 = decltype(N0c)::value, NN = decltype(NNc)::value;
      f32x2 G[2 * NN], U[2 * NN], P[2 * NN];
      __builtin_amdgcn_sched_barrier(0);
      STAGE({ const int e = (n == 6 ? m & 1 : n & 1) * 2; const f32x4 x = n == 6 ? acc[6][m & 2] : acc[n & 6][m]; G[i] = ((f32x2){x[e], x[e + 1]}) - k[0] * ((f32x2){cg[n >> 1][e], cg[n >> 1][e + 1]}); });
      STAGE(P[i] = G[i] * G[i]);
      STAGE(P[i] = P[i] * k[1] + k[2]);
      STAGE(P[i] = G[i] * P[i]);
      STAGE({ const int e = (n == 6 ? m & 1 : n & 1) * 2; const f32x4 x = n == 6 ? acc[6][m | 1] : acc[n | 1][m]; U[i] = ((f32x2){x[e], x[e + 1]}) - k[0] * ((f32x2){cu[n >> 1][e], cu[n >> 1][e + 1]}); });
      STAGE(P[i] = ((f32x2){__builtin_amdgcn_exp2f(P[i][0]), __builtin_amdgcn_exp2f(P[i][1])}));
      mid();
      __builtin_amdgcn_sched_barrier(0);
      STAGE(P[i] = P[i] * k[3] + k[3]);
      STAGE(P[i] = ((f32x2){__builtin_amdgcn_rcpf(P[i][0]), __builtin_amdgcn_rcpf(P[i][1])}));
      STAGE(U[i] = G[i] * U[i]);
      STAGE(U[i] = U[i] * P[i]);
      STAGE({ hv[m][n] = pk_bf16(U[i][0], U[i][1]); asm volatile("" : "+v"(hv[m][n])); });
    };
#undef STAGE
    using I2 = std::integral_constant<int, 2>; using I3 = std::integral_constant<int, 3>; using I4 = std::integral_constant<int, 4>;
    grp(I0{}, I0{}, I4{}, [&]() {});
    grp(I2{}, I0{}, I4{}, [&]() {
#pragma unroll
      for (int p = 0; p < 2; ++p) put(hv[0][2 * p], hv[0][2 * p + 1], hv[1][2 * p], hv[1][2 * p + 1], go0 + p * 512, g1); });
#pragma unroll
    for (int p = 0; p < 2; ++p) put(hv[2][2 * p], hv[2][2 * p + 1], hv[3][2 * p], hv[3][2 * p + 1], go0 + p * 512, g1 + 2 * RSA);
    asm volatile("" ::: "memory");
    __builtin_amdgcn_sched_barrier(0);
    STAMP(4);
    STAMPW(11, 4);
    if (KN_EP) __builtin_amdgcn_s_setprio(EARLY ? 1 : 0);
#define KN_AW 1
    auto arrived = [&](int w) { if (!EARLY && w == KN_AW) { wait_vm<0>(); lds_inc(lds0 + ACNT); STAMPW(6, 4); } };
    arrived(0);
#pragma unroll
    for (int q = 0; q < 2; ++q)
#pragma unroll
      for (int e = 0; e < 4; ++e) {
        const float x0 = acc[6][2 * q][e], x1 = acc[6][2 * q + 1][e];
        auto sw = __builtin_amdgcn_permlane32_swap(__float_as_uint(x0), __float_as_uint(x1), false, false);
        acc[6][2 * q][e] = __uint_as_float(sw[0]); acc[6][2 * q + 1][e] = __uint_as_float(sw[1]);
      }
    const int sb = omA((256 + g * 3) * 256), go4 = go0 + (fq >> 1) * (omA((257 + g * 3) * 256) - sb - 256);
    grp(I0{}, I4{}, I3{}, [&]() { arrived(1); });
    arrived(2);
    grp(I2{}, I4{}, I3{}, [&]() { arrived(3); put(hv[0][4], hv[0][5], hv[1][4], hv[1][5], go4, sb); });
    put(hv[2][4], hv[2][5], hv[3][4], hv[3][5], go4, sb + 2 * RSA);
    put(hv[0][6], hv[1][6], hv[2][6], hv[3][6], go6, omA((258 + g * 3) * 256));
    asm volatile("" ::: "memory");
    if constexpr (!EARLY) lds_inc(lds0 + ACNT + 4);
  }
  };
  using IM1 = std::integral_constant<int, -1>;

  {
    constexpr int WR = 2;
    if (wave == 5) { ds_w32(lds0 + DONE, 0); ds_w32(lds0 + DONE + 4, 0); ds_w32(lds0 + DONE + 8, 0); }
    auto check_tile = [&](int t) {
      int lx = lane; asm volatile("" : "+v"(lx));
      const unsigned ad = lds0 + XBASE + t * XT + (wave & 3) * 1024 + lx * 16;
      if (__builtin_expect(marked<8>(ad), 0)) {
        for (int tries = 0;; ++tries) {
          if (tries >= KN_TRY) wait_flags(a.sync + FHF(panel), (lane4() & 127) * 32, tag, LO32);
          issueX(t, 0, t); issueX(t, 1, t);
          wait_vm<0>();
          if (tries >= KN_TRY || !marked<8>(ad)) break;
        }
      }
    };
    auto first = [&](auto ic) { constexpr int i = decltype(ic)::value; if constexpr (i < 7) ldW(i, 1, 2 ^ WR); else ldX((i - 7) % XI, 2, (i - 7) / XI, 2); };
    auto run = [&](auto LDRc) __attribute__((always_inline)) {
      constexpr bool LDR = decltype(LDRc)::value;
      if constexpr (LDR) static_for<0, KN_GP>(first);
      int xs = 1;
#define KN_CTJ 11
      unsigned long long tb = (unsigned long long)a.PF0; unsigned tv = 0, tl = 0x7fffffffu, ts = 1835008u, tmask = 0; int tj = 1, tsh = 1;
      if constexpr (!LDR) {
        unsigned long long c0, c1; CODE_ADDR(c0, "k_all"); CODE_ADDR(c1, "k_zend");
#define KN_WJ 1
#define KN_WE 8
#define KN_DJ 6
        tj = KN_DJ;
        if (wave == 4) { tb = c0; tl = c1 - c0 < 262144ull ? (unsigned)(c1 - c0) : 0u; ts = 0; tmask = 1u << KN_CTJ; }
        if (wave == 5) tmask = 0x55u << (KN_DJ + (idx & 1));
        if (wave == 6) { tb = (unsigned long long)((idx & 16) ? a.W1 : a.W0); ts = 64 * LDW * 2; tmask = ((1u << (KN_WE - KN_KT)) - 1u) << KN_WJ; tj = KN_WJ - krot - KN_KT; tsh = 0; }
        const int l4 = lane / 14;
        tv = wave == 4 ? (unsigned)((lane * 32 + idx) * 128)
           : (lane >= 56 ? 0x80000000u : (wave == 5 ? (unsigned)(bid * 7168 + lane * 128) : (unsigned)(((idx & 15) * 4 + l4) * (LDW * 2) + (xcd & 3) * 1792 + (lane - l4 * 14) * 128)));
      }
      const int js = 2 * (tm & 3) + 1;
      const auto rC = __builtin_amdgcn_make_buffer_rsrc((void*)a.cs, 0, 0x7fffffff, 0x00020000);
      unsigned* ccnt = a.sync + FCS(tn);
#define KN_JR 10
#define KN_JS 2
      const int jf = wave == 4 ? NT - 4 : (wave == 7 ? KN_JR : 64);
      const unsigned* fcnt = wave == 4 ? ccnt : a.sync + FRC(panel);
      const int jm = (idx >> 4) == ((xcd >> 1) & 1) && (idx & 7) < KN_V ? 3 : 64;
      int mo = rbA((m0 >> 4) + wave * 4, sd) + omA(tn * 2048);
      unsigned evm = tmask | (8u << js) | (wave == 4 ? 3u << (NT - 4) : 0u) | (wave == 7 ? ((5u << KN_JS) | 2u | (11u << KN_JR)) : 0u);
      if constexpr (!LDR) asm volatile("" : "+s"(evm), "+s"(tmask), "+s"(tb), "+s"(tl), "+s"(ts), "+s"(tj), "+s"(tsh), "+v"(tv));
      wait_vm<LDR ? KN_GP : 0>();
      STAMPW(5, 0);
      check_tile(LDR ? 0 : 1);
      lds_barrier();
      if constexpr (!LDR) { if (wave == 4) __builtin_amdgcn_raw_ptr_buffer_load_lds(__builtin_amdgcn_make_buffer_rsrc((void*)(a.sync + FHF(panel)), 0, 4096, 0x00020000), (lds_void_ptr)(size_t)(lds0 + TCH + 256), 4, lane * 128, 0, 0, 16); }
      using I12 = std::integral_constant<int, 12>; using I28 = std::integral_constant<int, 28>;
      { const RdCtx c = mkctx(0, WR * WU);
        static_for<0, 24>([&](auto gc) { static_for<0, 36>([&](auto rc) { if constexpr (SchedG::gap(decltype(rc)::value) == decltype(gc)::value) rdg(c, rc); }); });
        STAMP(1);
        pairs(I12{}, I28{}, T{}, IM1{}, c, [&](auto pc) { if constexpr (LDR) static_for<KN_GP, 15>([&](auto ic) { if constexpr (12 + (decltype(ic)::value - KN_GP) * 15 / (15 - KN_GP) == decltype(pc)::value) first(ic); }); }); }
      constexpr int CFL = TCH + 256, CPB = XBASE + ((NT - 3) % 3) * XT;
      auto iter = [&](int j, auto LWc, auto LXc, auto WAITc, auto CSc) {
        constexpr bool LW = decltype(LWc)::value && LDR, LX = decltype(LXc)::value && LDR, LP = LW && !LX;
        if constexpr (LDR) {
          wait_vm<decltype(WAITc)::value>();
          if constexpr (LX) { if (__builtin_expect(j == 2, 0)) check_tile(2); }
          if constexpr (LX) {
            if ((unsigned)(j - jm) < 8u) {
              int lx = lane; asm volatile("" : "+v"(lx));
              unsigned on = 0xffffffffu; asm volatile("" : "+v"(on));
              __builtin_amdgcn_raw_buffer_store_b128((u32x4){on, on, on, on}, rO, lx * 16, mo, 16);
              mo += (j & 1) ? 1024 : RSA - 1024;
            }
            if (j == jm + 9) { if (lane == 0) set_flag(a.sync + FAM(panel) + (tn & 15) * 4 + wave, tag); }
          }
        }
        if constexpr (!LDR) {
          if (__builtin_expect((evm >> j) & 1, 0)) {
            if (KN_VP) __builtin_amdgcn_s_setprio(2);
            if (j == js + 3) {
              if ((tmask >> (j - 1)) & 1) wait_vm<1>(); else wait_vm<0>();
              if (lane == 0) set_flag(ccnt + tm * 4 + wave - 4, tag);
            }
            if (j == 1) glu_stats_issue();
            if (j == KN_JS && wave == 7) { wait_vm<0>(); glu_stats(); }
            if (j == KN_JS + 2 && wave == 7) {
              if (j == js + 2) wait_vm<1>(); else wait_vm<0>();
              if (lane == 0) set_flag((unsigned*)fcnt + tn, tag);
            }
            {
              int lx = lane; asm volatile("" : "+v"(lx));
              if (j == jf) __builtin_amdgcn_raw_ptr_buffer_load_lds(__builtin_amdgcn_make_buffer_rsrc((void*)fcnt, 0, 128, 0x00020000), (lds_void_ptr)(size_t)(lds0 + CFL + (wave & 1) * 256), 4, lx * 4, 0, 0, 16);
              if (j == jf + 1) {
                wait_vm<0>();
                if (lds_missing(lds0 + CFL + (wave & 1) * 256, tag, LO32)) wait_flags(fcnt, lx * 4, tag, LO32);
                if (wave == 7) {
#pragma unroll
                  for (int i = 0; i < 4; ++i) __builtin_amdgcn_raw_ptr_buffer_load_lds(rC, (lds_void_ptr)(size_t)(lds0 + RING + i * 1024), 16, lx * 16, RTAB + m0 * 16 + i * 1024, 0, 16);
                }
              }
              if (j == jf + 3) wait_vm<0>();
            }
            if ((tmask >> j) & 1) touch((const void*)tb, tv, (((j - tj) >> tsh) & 15) * ts, tl);
            if (KN_VP == 1) __builtin_amdgcn_s_setprio(0);
          }
          __builtin_amdgcn_sched_barrier(0);
        }
        int xp = xs + 2; if (xp >= 3) xp -= 3;
        const int ws = ((j & 1) * 2) ^ WR;
        auto lda = [&](int i) { ldW(i, j + 1, ws ^ 2); };
        auto ldb = [&](int i) {
            if constexpr (LX) ldX(i % XI, j + 2, i / XI, xp);
            else __builtin_amdgcn_raw_ptr_buffer_load_lds(rC, (lds_void_ptr)(size_t)(lds0 + CPB + wave * 8192 + i * 1024), 16, lane * 16, (tn * 2 + wn) * 7168 + i * 1024, 0, 16); };
        using NLA = std::integral_constant<int, LW ? WI : 0>; using NLB = std::integral_constant<int, LX ? 2 * XI : (LP ? 7 : 0)>;
        { const RdCtx c = mkctx(xs * XT, ws * WU);
          pairs(I0{}, I28{}, T{}, CSc, c, [&](auto pc) { constexpr int p = decltype(pc)::value;
            if constexpr (LDR && p == 0) loop_barrier();
            if constexpr (!LDR && KN_VP > 1 && p == KN_VP) __builtin_amdgcn_s_setprio(0);
            if constexpr (!LDR && KN_VQ && p == KN_VQ) __builtin_amdgcn_s_setprio(1);
            if constexpr (!LDR && KN_VQ && p == KN_GE) __builtin_amdgcn_s_setprio(0);
            if constexpr (!LDR && p == KN_GE) { asm volatile("s_waitcnt lgkmcnt(0)" ::: "memory"); if (decltype(WAITc)::value == 0 || j + 1 < NT) loop_barrier(); }
            static_for<0, NLA::value>([&](auto ic) { constexpr int i = decltype(ic)::value; if constexpr (i * 13 / 7 == p) lda(i); });
            static_for<0, NLB::value>([&](auto ic) { constexpr int i = decltype(ic)::value; if constexpr (14 + i * 13 / 8 == p) ldb(i); }); }); }
        xs = xs + 1; if (xs >= 3) xs = 0;
      };
      using WX_ = std::integral_constant<int, 2 * XI>;
      bf16x8 pv[8];
      if constexpr (LDR) {
        for (int j = 1; j < NT - 2; ++j) iter(j, T{}, T{}, WX_{}, IM1{});
#define KN_PRI 0
        if (KN_PRI == 2) __builtin_amdgcn_s_setprio(3);
        iter(NT - 2, T{}, F{}, WX_{}, IM1{});
        if (KN_PRI == 1) __builtin_amdgcn_s_setprio(3);
        iter(NT - 1, F{}, F{}, std::integral_constant<int, 7>{}, IM1{});
        wait_vm<0>();
        int lc = lane; asm volatile("" : "+v"(lc));
        const unsigned ca = lds0 + CPB + wave * 8192 + (lc >> 4) * 1792 + ((lc & 15) < 14 ? lc & 15 : 0) * 16;
        static_for<0, 8>([&](auto ic) { constexpr int i = decltype(ic)::value; pv[i] = ds_b128<i * 224>(ca); });
      } else {
        int j = 1;
        {
          if (wave == 4) {
            wait_vm<0>();
            if (lds_missing(lds0 + TCH + 256, tag, LO32)) wait_flags(a.sync + FHF(panel), (lane4() & 127) * 32, tag, LO32);
          }
        }
        loop_barrier();
#pragma unroll 1
        for (; j < js; ++j) iter(j, F{}, F{}, I0{}, IM1{});
        const int ja = wm == 2 ? js + 2 : js;
#pragma unroll 1
        for (; j < ja; ++j) iter(j, F{}, F{}, I0{}, I0{});
#pragma unroll 1
        for (; j < js + 2; ++j) iter(j, F{}, F{}, I0{}, I1{});
        { int lx = lane; asm volatile("" : "+v"(lx));
          __builtin_amdgcn_raw_buffer_store_b128(__builtin_bit_cast(u32x4, accs), rC, (lx & 15) < NF ? (unsigned)((tn * 4 + wn * 2) * 3584 + (tm * 2 + (wm & 1)) * 224
                  + ((lx & 15) < 6 ? (lx & 1) * 3584 + ((lx & 15) >> 1) * 64 + (lx >> 4) * 16 : (lx >> 5) * 3584 + 192 + (lx & 16))) : 0x80000000u, 0, 16); }
#pragma unroll 1
        for (; j < NT; ++j) iter(j, F{}, F{}, I1{}, IM1{});
        done_inc();
      }
      { const RdCtx c = mkctx(0, 0); pairs(I0{}, I12{}, F{}, IM1{}, c, [&](auto pc) {
          if constexpr (LDR && decltype(pc)::value == 4) {
            asm volatile("s_waitcnt lgkmcnt(0)" ::: "memory");
#pragma unroll
            for (int k = 0; k < 8; ++k) { bf16x8& q = pv[k]; asm volatile("" : "+v"(q)); }
            f32x4 cv = ((__builtin_bit_cast(f32x4, pv[0]) + __builtin_bit_cast(f32x4, pv[1])) + (__builtin_bit_cast(f32x4, pv[2]) + __builtin_bit_cast(f32x4, pv[3])))
                     + ((__builtin_bit_cast(f32x4, pv[4]) + __builtin_bit_cast(f32x4, pv[5])) + (__builtin_bit_cast(f32x4, pv[6]) + __builtin_bit_cast(f32x4, pv[7])));
#pragma unroll
            for (int k = 0; k < 4; ++k) { const float t = cv[k]; auto sw = __builtin_amdgcn_permlane16_swap(__float_as_uint(t), __float_as_uint(t), false, false); cv[k] = __uint_as_float(sw[0]) + __uint_as_float(sw[1]); }
            if ((lane & 31) < 14) *(f32x4*)(tb_cs + (lane >> 5) * 112 + wn * 56 + fr * 4) = cv;
            done_inc();
          } }); }
      if constexpr (LDR) __builtin_amdgcn_s_setprio(0);
      epilogue(LDRc);
    };
    if (wave < NLW) run(T{}); else run(F{});
  }
  STAMP(7);
  STAMP(8);
  STAMPW(12, 4);
  STAMP(3);
#endif
}

#define KN_PT 13
#define KN_QR 0
#define KN_PS 0
#define KN_PG -1
#define KN_FW 1
#define KN_GQ 6
#define KN_G0 4
#define KN_OC 12
#define KN_TG 4
#define KN_PQ 1
#define KN_PZ 0
#define KN_PN 4
#define KN_H0 0
#define KN_HS 0
#define KN_HN 7
#define KN_X2 0
#define KN_MP 0
#define KN_HD 0
#define KN_HM 7
#define KN_HT 1
#define KN_QO1 3
#define KN_QO2 3
#define KN_QO3 3
#define KN_QO4 16
#define KN_QP 2
#define KN_QS 18
#define KN_QLB 13

constexpr int QXT = 144 * 128, QWU = 24576, QWB = 3 * QXT, QRING = QWB + 4 * QWU;
constexpr int QMU = QRING + 64, QRS = QMU + 576, QFL = QRS + 576, QTCH = QRING + 2304;
constexpr int QCS = 2 * QXT, QKH = QCS + 2560, QVH = QKH + 4096;
#define KN_QWC 8
struct SchedQ {
  static constexpr int ns(int j) { return j < 2 ? 2 : 0; }
  static constexpr int nx(int j) { return (j + 2 < 16 ? 4 : 0) + ns(j); }
  static constexpr int wh(int j) { if (j < 0) return 12; int v = wh(j - 1) + KN_QWC - ns(j); if (v > 6 * (j + 4)) v = 6 * (j + 4); return v > 96 ? 96 : v; }
  static constexpr int oh(int j) { return j < 13 ? 0 : 2 * (j == 13 ? KN_QO1 : (j == 14 ? KN_QO2 : KN_QO3)); }
  static constexpr int tot(int j) { return j < 0 ? 20 : tot(j - 1) + nx(j) + wh(j) - wh(j - 1) + oh(j) - oh(j - 1); }
  static constexpr int cnt(int j) {
    int pos = j < 2 ? 10 * (j + 1) : tot(j - 3) + 4;
    for (int i = 0; i < j; ++i) if (wh(i - 1) < 6 * (j + 1) && wh(i) >= 6 * (j + 1)) { const int q = tot(i - 1) + nx(i) + 6 * (j + 1) - wh(i - 1); if (q > pos) pos = q; }
    return tot(j - 1) - pos;
  }
};
static_assert(SchedQ::cnt(0) == 10 && SchedQ::cnt(8) == 16 && KN_QO1 <= 3 && KN_QO2 <= 6 && KN_QO3 <= 9 && KN_QWC >= 8, "schedule");
static_assert(SchedQ::cnt(KN_S1P) <= SchedQ::tot(KN_S1P - 1) - SchedQ::tot(0) - 6 && KN_S1C > KN_S1P + 2, "statistics");
struct FragQ { bf16x8 x[4]; s16x4 lo[6], hi[6]; };
__device__ __forceinline__ f32x2 ds_r2(unsigned addr) { f32x2 r; asm volatile("ds_read2_b32 %0, %1 offset1:16" : "=v"(r) : "v"(addr)); return r; }
__device__ __forceinline__ float fma1(float a, float b, float c) { float r; asm("v_fma_f32 %0, %1, %2, %3" : "=v"(r) : "v"(a), "v"(b), "v"(c)); return r; }
__device__ __forceinline__ unsigned k1_phase(const GemmArgs& a) {
  unsigned tag = 0;
#if defined(__HIP_DEVICE_COMPILE__)
  constexpr int K = D_, LDW = 3 * D_, NT = 16, XI = 5, XIMIN = 4, WI = 6;
  extern __shared__ __attribute__((aligned(16))) char smem[];
  STAMP(0);
  const unsigned lds0 = (unsigned)(size_t)(lds_char_ptr)smem;
  const int tid = threadIdx.x, lane = tid & 63;
  const int wave = __builtin_amdgcn_readfirstlane(tid >> 6);
  const int bid = blockIdx.x, xcd = bid & 7, idx = bid >> 3;
  const int tm = (xcd >> 2) * 8 + (idx >> 2), tn = (xcd & 3) * 4 + (idx & 3), m0 = tm * 128;
  const int fr = lane & 15, fq = lane >> 4, wq = (lane >> 2) & 3, wp = lane & 3;
  using I0 = std::integral_constant<int, 0>; using I1 = std::integral_constant<int, 1>; using I2 = std::integral_constant<int, 2>; using I3 = std::integral_constant<int, 3>;
  using T = std::true_type; using F = std::false_type;
  auto touch = [&](const void* base, unsigned voff, unsigned soff, unsigned lim = 0x7fffffffu) {
    __builtin_amdgcn_raw_ptr_buffer_load_lds(__builtin_amdgcn_make_buffer_rsrc((void*)base, 0, lim, 0x00020000), (lds_void_ptr)(size_t)(lds0 + QTCH), 4, voff, soff, 0, 0);
  };
  auto code_touch = [&](int line) {
    unsigned long long c0, c1; CODE_ADDR(c0, "k_all"); CODE_ADDR(c1, "k_zend");
    const unsigned len = c1 - c0 < 262144ull ? (unsigned)(c1 - c0) : 0u;
    touch((const void*)c0, (unsigned)(line * 128), 0, len);
  };
  int hj1, hj2; unsigned htv;
  auto hset = [&]() {
    const int slc = (3 - ((unsigned)(size_t)a.W0 >> 7) - (xcd & 3) * 4) & 7;
    const int hid = (idx >> 2) * 3 + (idx & 3) - ((idx & 3) > slc ? 1 : 0);
    const bool hlp = slc < 4 && (idx & 3) != slc;
    hj1 = hlp ? hid / 3 - KN_HQ : -100; hj2 = hlp ? hid / 3 + 8 - KN_HQ : -100;
    htv = (unsigned)(lane * (LDW * 2) + (hid % 3) * 2048 + ((xcd & 3) * 4 + slc) * 128);
  };
  const unsigned xo = fr * 128 + ((fq ^ ((fr >> 1) & 7)) << 4);
  auto wfrag = [&](int seg) { const int row = 8 * fq + wq; return lds0 + QWB + row * 128 + (((seg << 1) ^ wswz<128>(row)) << 4) + wp * 8; };
  const f32x4 z4 = {0.f, 0.f, 0.f, 0.f};
  auto cat = [](s16x4 lo, s16x4 hi) { return __builtin_shufflevector(lo, hi, 0, 1, 2, 3, 4, 5, 6, 7); };
  unsigned xsrc[XI], wsrc[WI];
  { const int lr = lane >> 3, w4 = wave & 3;
    const unsigned xv = (unsigned)(lr * (K * 2) + (((lane & 7) ^ ((w4 * 4 + (lr >> 1)) & 7)) << 4));
    const int r0 = m0 - 16 + w4 * 8;
#pragma unroll
    for (int i = 0; i < XI; ++i) xsrc[i] = xv + (unsigned)((i == 0 ? (r0 < 0 ? 0 : r0) : r0 + 32 * i) * (K * 2));
    const unsigned wv = (unsigned)((w4 * 8 + lr) * (LDW * 2) + tn * 128 + (((lane & 7) ^ wswz<128>(w4 * 8 + lr)) << 4));
#pragma unroll
    for (int i = 0; i < WI; ++i) wsrc[i] = wv + (unsigned)((i & 1) * 32 * (LDW * 2) + (i >> 1) * 2048); }
  unsigned mb = lds0 + wave * 1024, rx = KN_QR ? ((tm ^ tn) & 1) * 1024 : 0, rw = rx * (64 * LDW * 2 / 128);
  asm volatile("s_waitcnt lgkmcnt(0)" : "+s"(mb), "+s"(rx), "+s"(rw) : "s"(a.warm), "v"(wsrc[0]), "v"(wsrc[1]), "v"(wsrc[2]), "v"(wsrc[3]), "v"(wsrc[4]), "v"(wsrc[5]), "v"(xsrc[0]), "v"(xsrc[1]), "v"(xsrc[2]), "v"(xsrc[3]), "v"(xsrc[4]));
  __builtin_amdgcn_sched_barrier(0);
  const u32x4 qX = mk_rsrc(a.X), qW = mk_rsrc(a.W0);
  auto ldX = [&](auto ic, auto tc) { constexpr int i = decltype(ic)::value, t = decltype(tc)::value; if (i < 4 || wave < 2) ld_cx<t % 3 * QXT + i * 4096, t * 128>(mb, qX, xsrc[i], rx); };
  auto ldW = [&](auto qc) { constexpr int i = decltype(qc)::value % WI, t = decltype(qc)::value / WI; ld_ca<QWB + (t & 3) * QWU + i * 4096, t * (64 * LDW * 2), (t >= 8)>(mb, qW, wsrc[i], rw); };
  if (__builtin_expect(wave < 4, 1)) {
    static_for<0, WI>(ldW);
    static_for<0, XI>([&](auto ic) { ldX(ic, I0{}); });
  }
  __builtin_amdgcn_sched_barrier(0);
  if (__builtin_expect(wave < 4, 1)) {
    unsigned seed;
    asm volatile("s_load_dword %0, %1, %2" : "=s"(seed) : "s"(a.sync), "n"(FEP * 4));
    auto sb0 = []() { __builtin_amdgcn_sched_barrier(0); };
    static_for<WI, WI + 2>(ldW); sb0();
    unsigned xh0[3], xh1[3], wb[2], l16 = lane * 16;
#pragma unroll
    for (int i = 0; i < 3; ++i) { xh0[i] = fr == 0 ? lds0 + QRING : lds0 + i * QXT + xo; xh1[i] = fr == 0 ? lds0 + QRING : lds0 + i * QXT + (xo ^ 64); asm volatile("" : "+v"(xh0[i]), "+v"(xh1[i])); }
    sb0(); static_for<WI + 2, WI + 4>(ldW); sb0();
    wb[0] = wfrag(wave); wb[1] = wb[0] + 2 * QWU;
    u32x4 one1; { const unsigned o = fr == 0 ? 0x3f803f80u : 0u; one1 = (u32x4){o, o, o, o}; }
    const char* srows = (const char*)a.X + (size_t)(m0 + tn * 8 + wave * 2) * (K * 2);
    asm volatile("" : "+v"(wb[0]), "+v"(wb[1]), "+v"(one1), "+v"(l16), "+s"(srows));
    sb0(); static_for<WI + 4, 2 * WI>(ldW); sb0();
    hset();
    asm volatile("" : "+s"(hj1), "+s"(hj2), "+v"(htv));
    sb0(); static_for<0, 2>([&](auto ic) { ldX(ic, I1{}); }); sb0();
    unsigned long long tb = (unsigned long long)((wave == 3 || wave == 0) ? a.PF1 : a.W0);
    unsigned tv, ts, to, evm;
    { const int t1 = (bid + 256) / 48 - KN_GQ, t2 = (bid + 512) / 48 - KN_GQ;
      const unsigned e0 = KN_OC ? 1u << KN_OC : 0u, e1 = KN_GQ ? (1u << (t1 > 0 ? t1 : 0)) | (1u << t2) : 0u, e2 = (hj1 >= 0 ? 1u << hj1 : 0u) | (hj2 > 0 ? 1u << hj2 : 0u), e3 = 2u << (idx % 12);
      evm = wave == 0 ? e0 : (wave == 1 ? e1 : (wave == 2 ? e2 : e3));
      ts = wave == 1 ? 2u << 20 : 8 * 64 * LDW * 2;
      to = wave == 1 ? 2u << 20 : (wave == 2 ? ((hj1 >= 0 ? hj1 : hj2) + KN_HQ) * (64 * LDW * 2) : 0); }
    asm volatile("" : "+s"(evm), "+s"(ts), "+s"(to), "+s"(tb));
    sb0(); static_for<2, 4>([&](auto ic) { ldX(ic, I1{}); }); sb0();
    tv = (wave & 1) ? (unsigned)((bid * 64 + lane) * 128)
       : (wave ? htv : (lane < 32 ? (unsigned)((idx * 32 + lane) * 2048 + (xcd & 1) * 1024 + ((3 - ((unsigned)(size_t)a.PF1 >> 7)) & 7) * 128) : 0x80000000u));
    asm volatile("" : "+v"(tv));
    sb0(); ldX(std::integral_constant<int, 4>{}, I1{}); sb0();
    asm volatile("s_waitcnt lgkmcnt(0)\n\ts_mul_i32 %0, %1, 0x9e3779b1\n\ts_add_u32 %0, %0, 0x7f4a7c15" : "=s"(tag) : "s"(seed) : "scc");
    unsigned osrc, gv, sdst, ssrc, sld, smu, srs, fa0, fa1, fa2, mko;
    auto ldO = [&](auto qc) { constexpr int i = decltype(qc)::value >> 1, h = decltype(qc)::value & 1;
      ld_cx<wo_pos(i >> 1) + (i & 1) * 8192 + h * 4096, (((i & 1) * 8 + (i >> 1)) * 2 + h) * (32 * D_ * 2)>(mb, mk_rsrc(a.PF1), osrc, (xcd & 3) * 8 * (32 * D_ * 2)); };
    u32x4 sv[4]; f32x4 s1s[2], s1q[2]; float sa[4], sb;
    auto dpp = [](float t, auto c) { return __int_as_float(__builtin_amdgcn_update_dpp(0, __float_as_int(t), decltype(c)::value, 0xf, 0xf, false)); };
    auto stat = [&](auto kc) {
      constexpr int k = decltype(kc)::value;
      if constexpr (k == 0) asm volatile("" : "+v"(sv[0]), "+v"(sv[1]), "+v"(sv[2]), "+v"(sv[3]));
      if constexpr (k < 8) {
        constexpr int r = (k >> 1) & 1, h = k >> 2;
        const u32x4 one4 = {0x3f803f80u, 0x3f803f80u, 0x3f803f80u, 0x3f803f80u};
        const bf16x8 x = __builtin_bit_cast(bf16x8, sv[2 * r + h]);
        if constexpr (k & 1) s1q[r] = __builtin_amdgcn_mfma_f32_16x16x32_bf16(x, x, h ? s1q[r] : z4, 0, 0, 0);
        else s1s[r] = __builtin_amdgcn_mfma_f32_16x16x32_bf16(__builtin_bit_cast(bf16x8, one4), x, h ? s1s[r] : z4, 0, 0, 0);
      }
      if constexpr (k == 8 || k == 9) {
        constexpr int r = k - 8;
        const f32x4 g = s1q[r];
        const float d = (lane & 2) ? ((lane & 1) ? g[3] : g[2]) : ((lane & 1) ? g[1] : g[0]);
        sa[r] = fq == 0 ? s1s[r][0] : 0.f;
        sa[2 + r] = fq == wq ? d : 0.f;
        asm volatile("" : "+v"(sa[r]), "+v"(sa[2 + r]));
      }
      if constexpr (k == 10 || k == 11) { constexpr int i = (k - 10) * 2; sa[i] = ((lane & 1) ? sa[i + 1] : sa[i]) + dpp((lane & 1) ? sa[i] : sa[i + 1], std::integral_constant<int, 0xb1>{}); asm volatile("" : "+v"(sa[i])); }
      if constexpr (k == 12) sb = ((lane & 2) ? sa[2] : sa[0]) + dpp((lane & 2) ? sa[0] : sa[2], std::integral_constant<int, 0x4e>{});
      if constexpr (k == 13) { sb += dpp(sb, std::integral_constant<int, 0x124>{}); sb += dpp(sb, std::integral_constant<int, 0x128>{}); }
      if constexpr (k == 14) { auto q = __builtin_amdgcn_permlane16_swap(__float_as_uint(sb), __float_as_uint(sb), false, false); sb = __uint_as_float(q[0]) + __uint_as_float(q[1]); }
      if constexpr (k == 15) { auto q = __builtin_amdgcn_permlane32_swap(__float_as_uint(sb), __float_as_uint(sb), false, false); sb = (__uint_as_float(q[0]) + __uint_as_float(q[1])) * (1.f / 1024.f); }
      if constexpr (k == 16) { const float var = dpp(sb, std::integral_constant<int, 0x4e>{}) - sb * sb; sa[0] = __builtin_amdgcn_rsqf((var > 0.f ? var : 0.f) + 1e-5f); asm volatile("" : "+v"(sa[0])); }
      if constexpr (k >= 12 && k < 16) asm volatile("" : "+v"(sb));
      if constexpr (k == 17) {
        __builtin_amdgcn_raw_buffer_store_b128((u32x4){__float_as_uint(-sb), __float_as_uint(sa[0]), tag, ~tag}, __builtin_amdgcn_make_buffer_rsrc((void*)a.cs, 0, 0x7fffffff, 0x00020000), sdst, 0, 16);
        asm volatile("s_nop 1" ::: "memory");
      }
    };
    f32x4 acq = z4, ack = z4, acv = z4;
    const auto rC = __builtin_amdgcn_make_buffer_rsrc((void*)a.cs, 0, 0x7fffffff, 0x00020000);
    auto fetch = [&]() { __builtin_amdgcn_raw_ptr_buffer_load_lds(rC, (lds_void_ptr)(size_t)(lds0 + QRING + STL + wave * 1024), 16, ssrc, 0, 0, 16); };
    u32x4 ent; f32x4 hm4; int hm1, htg;
    auto olane = [&]() { int lx = lane; asm volatile("" : "+v"(lx)); return lx; };
    auto chore = [&](auto jc, auto gc) {
      constexpr int j = decltype(jc)::value, g = decltype(gc)::value;
      if constexpr (j == 1 && g == -1) ent = __builtin_bit_cast(u32x4, ds_b128<0>(lds0 + QFL + 16));
      if constexpr (j == 1 && g == 5) {
        asm volatile("" : "+v"(ent));
        if (wave == 3) __builtin_amdgcn_raw_buffer_store_b32((tag & ~1u) | (((ent[0] + ent[2] > ent[1] + ent[3] ? 0u : 1u) ^ (xcd >> 2)) & 1u), rC, lane == 0 ? (unsigned)(VTAB + bid * 4) : 0x80000000u, 0, 16);
      }
      if constexpr (j == 2 && g == 1) { const int lx = olane(); sdst = lx < 2 ? (unsigned)(STAB + (m0 + tn * 8 + wave * 2 + lx) * 16) : 0x80000000u; asm volatile("" : "+v"(sdst)); }
      if constexpr (j == KN_S1P && g >= 0 && g < 4) {
        if (KN_MP) { __builtin_amdgcn_s_setprio(3); __builtin_amdgcn_sched_barrier(0); }
        stat(std::integral_constant<int, 2 * g>{}); stat(std::integral_constant<int, 2 * g + 1>{});
        if (KN_MP) { __builtin_amdgcn_sched_barrier(0); __builtin_amdgcn_s_setprio(0); } }
      if constexpr (j == KN_S1P && (g == 9 || g == 10)) stat(std::integral_constant<int, g - 1>{});
      if constexpr (j == KN_S1P + 1 && g >= 0 && g < 8) stat(std::integral_constant<int, 10 + g>{});
      if constexpr (j == 5 && g == 1) { const int se = wave * 64 + olane(), sr = m0 - 16 + se;
        ssrc = se < 144 ? (unsigned)(STAB + (sr < 0 ? se : sr) * 16) : (wave == 3 ? (unsigned)(VTAB - 3072 + se * 16) : 0x80000000u); sld = lds0 + QRING + STL + se * 16; asm volatile("" : "+v"(ssrc), "+v"(sld)); }
      if constexpr (j == 5 && g == 5) { const int se = wave * 64 + olane();
        smu = lds0 + (se < 144 ? QMU + se * 4 : QTCH); srs = lds0 + (se < 144 ? QRS + (se & 1) * 288 + (se >> 1) * 4 : QTCH); asm volatile("" : "+v"(smu), "+v"(srs)); }
      if constexpr (j == 6 && g == 1) { const int lx = olane(); osrc = (unsigned)((wave * 8 + (lx >> 3)) * D_ + ((xcd & 1) * 8 + (idx & 7)) * 64 + ((lx & 7) ^ ((lx >> 3) & 6)) * 8) * 2u; asm volatile("" : "+v"(osrc)); }
      if constexpr (j == 6 && g == 5) { const int lx = olane(); gv = lx < 56 ? (unsigned)(((bid & 1) * 512 + (wave >> 1) * 128 + (bid >> 1)) * (DF_ * 2) + lx * 128) : 0x80000000u; asm volatile("" : "+v"(gv)); }
      if constexpr (j == 7 && g == 1) { const int lx = olane();
        fa0 = lds0 + QCS + ((lx & 15) == 0 ? wave * 64 + lx : 1024 + lx * 16); fa1 = lds0 + QCS + (lx < 16 ? 512 + wave * 64 : 1024) + lx * 4; asm volatile("" : "+v"(fa0), "+v"(fa1)); }
      if constexpr (j == 7 && g == 5) { fa2 = lds0 + (wave >> 1) * 1024 + (wave & 1) * 8 + l16;
        const int p2 = (xcd >> 1) * 2 + (idx >> 4), mq = (xcd & 1) * 16 + (idx & 15);
        mko = rbH(p2 * 16 + (mq >> 1), xcd >> 2) + omH((xcd >> 2) * 16384) + (mq & 1) * 3072; asm volatile("" : "+v"(fa2), "+s"(mko)); }
      if constexpr (j == KN_S1C && g == 10) fetch();
      if constexpr (j == KN_S1C + 3 && g == -1) ent = __builtin_bit_cast(u32x4, ds_b128<0>(sld));
      if constexpr (j == KN_S1C + 3 && g == 5) {
        static_assert(SchedQ::cnt(j) <= SchedQ::tot(j - 1) - SchedQ::tot(KN_S1C) && KN_S1C >= 8, "table");
        asm volatile("" : "+v"(ent));
        auto stale = [&]() { return __builtin_amdgcn_ballot_w64(wave == 3 ? ((ent[0] ^ tag) | (ent[1] ^ tag) | (ent[2] ^ tag) | (ent[3] ^ tag)) > 1u : (int)ssrc >= 0 && (ent[2] != tag || ent[3] != ~tag)) != 0; };
        if (__builtin_expect(stale(), 0)) {
          unsigned polls = 0;
          do {
            fetch();
            wait_vm<0>();
            ent = __builtin_bit_cast(u32x4, ds_b128<0>(sld));
            asm volatile("s_waitcnt lgkmcnt(0)" : "+v"(ent) :: "memory");
          } while (stale() && ++polls < 4000000u);
        }
      }
      if constexpr (j == KN_S1C + 3 && g == 8) {
        ds_w32(smu, (int)ent[0]); ds_w32(srs, (int)ent[1]);
        if (wave == 3) {
          int pc = 0;
#pragma unroll
          for (int i = 0; i < 4; ++i) pc += __builtin_popcountll(__builtin_amdgcn_ballot_w64(ent[i] & 1));
          tag = (tag & ~1u) | (pc > 128 ? 1u : 0u);
          ds_w32(lds0 + QFL + 8, (int)tag);
        }
      }
      if constexpr (j == KN_S1C + 4 && g >= 0 && g % 3 == 2) {
        if (wave == 3) {
          __builtin_amdgcn_raw_buffer_store_b128((u32x4){~0u, ~0u, ~0u, ~0u}, __builtin_amdgcn_make_buffer_rsrc((void*)a.st, 0, 0x7fffffff, 0x00020000), l16 + g / 3 * 1024,
                                                 mko ^ ((KN_NUMA & 1) ? ((SIDE ^ (xcd >> 2)) & 1) << 13 : 0), 16);
          asm volatile("s_nop 1" ::: "memory");
        }
      }
      if constexpr (j == NT - 2 && g == -1) { const int lx = olane(); hm4 = __builtin_bit_cast(f32x4, ds_b128<0>(lds0 + QMU + (lx & 48))); hm1 = ds_r32(lds0 + QMU + (lx & 15) * 4); htg = ds_r32(lds0 + QFL + 8); }
      if constexpr (j == NT - 2 && g == 5) { asm volatile("" : "+v"(hm4), "+v"(hm1), "+v"(htg)); tag = __builtin_amdgcn_readfirstlane(htg); }
      if constexpr (j == NT - 3 && g == 8 && (KN_CT & 2)) { if (wave == 2) code_touch(lane * 32 + idx); }
    };
    static_assert(KN_S1C + 4 <= NT - 4 && SchedQ::cnt(NT - 1) <= SchedQ::tot(NT - 2) - SchedQ::tot(KN_S1C + 4) && KN_H0 == 0, "marks");
    struct FragH { bf16x8 x[2]; s16x4 lo[2][3], hi[2][3]; } hf[2];
    auto mm = [&](FragH& f, auto ic) { constexpr int n = decltype(ic)::value;
      asm volatile("" : "+v"(f.lo[0][n]), "+v"(f.hi[0][n]), "+v"(f.lo[1][n]), "+v"(f.hi[1][n]));
      if constexpr (n == 1) asm volatile("" : "+v"(f.x[0]), "+v"(f.x[1]));
#pragma unroll
      for (int h = 0; h < 2; ++h) {
        if constexpr (n == 0) acq = __builtin_amdgcn_mfma_f32_16x16x32_bf16(cat(f.lo[h][0], f.hi[h][0]), __builtin_bit_cast(bf16x8, one1), acq, 0, 0, 0);
        if constexpr (n == 1) ack = __builtin_amdgcn_mfma_f32_16x16x32_bf16(cat(f.lo[h][1], f.hi[h][1]), f.x[h], ack, 0, 0, 0);
        if constexpr (n == 2) acv = __builtin_amdgcn_mfma_f32_16x16x32_bf16(f.x[h], cat(f.lo[h][2], f.hi[h][2]), acv, 0, 0, 0);
      } };
    unsigned tsq = 0;
    using SQ = SchedQ;
    static_for<0, NT>([&](auto jc) {
      constexpr int j = decltype(jc)::value, NX = (j + 2 < NT ? XI : 0) + SQ::ns(j), NW = SQ::wh(j) - SQ::wh(j - 1), NL = NX + NW + SQ::oh(j) - SQ::oh(j - 1);
      wait_vm<SQ::cnt(j)>();
      lds_barrier();
      auto rd = [&](auto rc) { constexpr int r = decltype(rc)::value, q = r < 4 ? r : r - 2, n = q >> 2, h = (q >> 1) & 1, o = (j & 1) * QWU + h * 4096 + n * 8192;
        FragH& f = hf[j & 1];
        if constexpr (r == 4 || r == 5) f.x[r - 4] = ds_b128<0>(r == 5 ? xh1[j % 3] : xh0[j % 3]);
        else if constexpr (q & 1) f.hi[h][n] = ds_tr<o + 512>(wb[(j >> 1) & 1]); else f.lo[h][n] = ds_tr<o>(wb[(j >> 1) & 1]); };
      chore(jc, std::integral_constant<int, -1>{});
      constexpr int H0 = j < NT - 1 ? KN_H0 : 0, NG = NL > 10 + H0 ? NL : 10 + H0;
      if constexpr (KN_HS > 0) asm volatile("s_nop %0" :: "n"(KN_HS - 1));
      static_for<0, NG>([&](auto gc) {
        constexpr int g = decltype(gc)::value, h = g - H0;
        __builtin_amdgcn_sched_barrier(0);
        if constexpr (g < NX - SQ::ns(j)) ldX(gc, std::integral_constant<int, j + 2>{});
        else if constexpr (g < NX) {
          constexpr int q = 2 * j + g - XI;
          asm volatile("s_nop 4\n\tglobal_load_dwordx4 %0, %1, %2 offset:%3" : "=&v"(sv[q]) : "v"(l16), "s"(srows), "n"(q * 1024) : "memory");
        }
        else if constexpr (g < NX + NW) ldW(std::integral_constant<int, SQ::wh(j - 1) + g - NX>{});
        else if constexpr (g < NL) ldO(std::integral_constant<int, SQ::oh(j - 1) + g - NX - NW>{});
        else if constexpr (h < 9) asm volatile("s_nop %0" :: "n"(KN_HN));
        if constexpr (j <= 12 && g == KN_TG) { if (__builtin_expect((evm >> j) & 1, 0)) { touch((const void*)tb, tv, to); to += ts; } }
        if constexpr (g == (KN_PG < 0 ? NG - 1 : KN_PG) && j <= KN_PT && j >= KN_PT - 3 * KN_PS && (KN_PS == 0 || (KN_PT - j) % (KN_PS ? KN_PS : 1) == 0)) {
          if (KN_PS == 0 || wave == 3 - (KN_PT - j) / (KN_PS ? KN_PS : 1)) touch(wave & 1 ? a.CW1 : a.CW0, gv, 0);
        }
        __builtin_amdgcn_sched_barrier(0);
        if constexpr (h >= 0 && h < 7) { rd(std::integral_constant<int, 2 * h>{}); rd(std::integral_constant<int, 2 * h + 1>{}); }
        if constexpr (j >= 1 && j < KN_HD + 1 && g >= KN_HM && g < KN_HM + 3 * KN_HT && (g - KN_HM) % KN_HT == 0) mm(hf[~j & 1], std::integral_constant<int, (g - KN_HM) / KN_HT>{});
        if constexpr (j >= KN_HD && (h == 4 || h == 7 || h == 9)) { asm volatile("s_waitcnt lgkmcnt(%0)" :: "n"(h == 4 ? 6 : (h == 7 ? 4 : 0)) : "memory"); mm(hf[j & 1], std::integral_constant<int, h == 4 ? 0 : (h == 7 ? 1 : 2)>{}); }
        if constexpr (g == 5 && (j == 1 || j == KN_S1C + 3 || j == NT - 2)) asm volatile("s_waitcnt lgkmcnt(12)" ::: "memory");
        __builtin_amdgcn_sched_barrier(0);
        chore(jc, gc);
      });
      __builtin_amdgcn_sched_barrier(0);
    });
    {
      f32x4 ck;
#pragma unroll
      for (int e = 0; e < 4; ++e) ck[e] = dpp(ack[e], std::integral_constant<int, 0x150>{});
      const auto c1 = __builtin_amdgcn_permlane16_swap(__float_as_uint(acv[0]), __float_as_uint(acv[0]), false, false);
      const auto c2 = __builtin_amdgcn_permlane32_swap(c1[0], c1[0], false, false);
      const float nm = __int_as_float(hm1), nc = __uint_as_float(c2[0]);
      asm volatile("ds_write_b128 %0, %1\n\tds_write_b128 %0, %2 offset:256\n\tds_write_b32 %3, %4" :: "v"(fa0), "v"(acq), "v"(ck), "v"(fa1), "v"(nc) : "memory");
      const u32x2 kk = {pk_bf16(fma1(nm, ck[0], ack[0]), fma1(nm, ck[1], ack[1])), pk_bf16(fma1(nm, ck[2], ack[2]), fma1(nm, ck[3], ack[3]))};
      const u32x2 vv = {pk_bf16(fma1(nc, hm4[0], acv[0]), fma1(nc, hm4[1], acv[1])), pk_bf16(fma1(nc, hm4[2], acv[2]), fma1(nc, hm4[3], acv[3]))};
      asm volatile("ds_write_b64 %0, %1 offset:%3\n\tds_write_b64 %0, %2 offset:%4" :: "v"(fa2), "v"(kk), "v"(vv), "n"(QKH), "n"(QVH) : "memory");
    }
    lds_barrier();
    if (tid == 192) set_flag(a.sync + FHM((xcd >> 1) * 2 + (idx >> 4)) + (xcd & 1) * 16 + (idx & 15), tag);
    static_for<SQ::oh(NT - 1), 2 * KN_QO4>(ldO);
    wait_vm<2 * (KN_QO4 - 2)>();
    const int sd = SIDE;
    unsigned fo = tid == 0 ? (unsigned)(FLO(tm) + tn * 32) * 4u : 0x80000000u, ft = tag;
    const auto rF = __builtin_amdgcn_make_buffer_rsrc((void*)a.sync, 0, 147456, 0x00020000);
    asm volatile("" : "+v"(fo), "+v"(ft) : "s"(rF));
    raw_barrier();
    if (wave == 0) __builtin_amdgcn_raw_buffer_store_b32(ft, rF, fo, 0, 16);
    static_for<2 * KN_QO4, 32>(ldO);
  } else {
    const int wm = (wave >> 1) & 1, hd = wave & 1;
    if (wave == 4) {
      if (lane < 4) { *(unsigned*)(smem + QRING + lane * 4) = 0x3f803f80u; if (lane < 2) *(unsigned*)(smem + QFL + lane * 4) = 0u; }
      if (KN_FW) __builtin_amdgcn_raw_ptr_buffer_load_lds(__builtin_amdgcn_make_buffer_rsrc((void*)a.sync, 0, 147456, 0x00020000), (lds_void_ptr)(size_t)(lds0 + QTCH), 4, lane * 32768 + bid * 128, 0, 0, 16);
    }
    if (KN_X2 && wave == 5 && bid < 32 * KN_X2) touch(a.X, (unsigned)((bid * (64 / (KN_X2 ? KN_X2 : 1)) + lane / (KN_X2 ? KN_X2 : 1)) * (K * 2) + 256 + lane % (KN_X2 ? KN_X2 : 1) * 128), 0);
    if (wave == 6) { hset(); if (hj1 < 0 && hj1 > -100) touch(a.W0, htv, (hj1 + KN_HQ) * (64 * LDW * 2));
      if (KN_G0 && bid >= KN_G0 * 48) touch(a.W0, (bid * 64 + lane) * 128, 0); }
    if ((KN_CT & 1) && wave == 7) code_touch(lane * 32 + idx);
    {
      const int id = bid * 2 + wm;
      const char* pb = (const char*)a.sync + PRB + (id >> 3) * 16384 + hd * 8192 + (id & 7) * (KN_PN == 8 ? 128 : 256);
      unsigned long long p0, p1; unsigned t;
#define SL4(a, b) "s_load_dword %1, %3, 0x" a "00\n\ts_load_dword %1, %3, 0x" b "00\n\ts_load_dword %1, %3, 0x1" a "00\n\ts_load_dword %1, %3, 0x1" b "00\n\t"
      if (KN_PN == 8) asm volatile("s_sleep %5\n\ts_memtime %0\n\ts_waitcnt lgkmcnt(0)\n\t" SL4("0", "8") SL4("4", "c") "s_waitcnt lgkmcnt(%4)\n\ts_memtime %2\n\ts_waitcnt lgkmcnt(0)" : "=&s"(p0), "=&s"(t), "=&s"(p1) : "s"(pb), "n"(KN_PQ), "n"(KN_PZ));
      else asm volatile("s_sleep %5\n\ts_memtime %0\n\ts_waitcnt lgkmcnt(0)\n\t" SL4("0", "8") "s_waitcnt lgkmcnt(%4)\n\ts_memtime %2\n\ts_waitcnt lgkmcnt(0)" : "=&s"(p0), "=&s"(t), "=&s"(p1) : "s"(pb), "n"(KN_PQ), "n"(KN_PZ));
      ds_w32(lds0 + QFL + 16 + (wave - 4) * 4, (int)((unsigned)p1 - (unsigned)p0));
    }
    f32x2 bias[2], bias0[2];
    {
      const int srow = m0 + wm * 64 + fr;
      const unsigned mk = 0x5555u & ((2u << (srow < 15 ? srow : 15)) - 1u), sft = (fr & 14) - fq * 4 + 14;
      const unsigned b0 = ~(0x5555u >> sft), b1 = ~(0x55550000u >> sft), c0 = ~(mk >> sft), c1 = ~((mk << 16) >> sft);
#pragma unroll
      for (int j = 0; j < 4; ++j) {
        bias[j >> 1][j & 1] = __uint_as_float((unsigned)__builtin_amdgcn_sbfe((int)(j < 2 ? b0 : b1), 2 - 2 * (j & 1), 1) & 0xff61b1e6u);
        bias0[j >> 1][j & 1] = __uint_as_float((unsigned)__builtin_amdgcn_sbfe((int)(j < 2 ? c0 : c1), 2 - 2 * (j & 1), 1) & 0xff61b1e6u);
      }
    }
    unsigned pl = (fr & 1) ? 0x01000c0cu : 0x0c0c0100u, ph = (fr & 1) ? 0x03020c0cu : 0x0c0c0302u;
    unsigned ta = lds0 + QRING + STL + (16 + wm * 64 + fr) * 16, tm4 = lds0 + QMU + (16 + wm * 64 + fq * 4) * 4, trk = lds0 + QRS + (fr & 1) * 288 + (wm * 32 + fq * 2) * 4;
    unsigned tcs = lds0 + hd * 128 + fq * 16, tcv = lds0 + QCS + 512 + hd * 128 + fr * 4, tex = lds0 + hd * 1024 + lane * 16, tfl = lds0 + QFL + hd * 4, odst = rbH((m0 >> 4) + wm * 4, xcd >> 2) + omH(tn * 2048) + hd * 1024 + lane * 16;
    asm volatile("" : "+v"(bias[0]), "+v"(bias[1]), "+v"(bias0[0]), "+v"(bias0[1]), "+v"(pl), "+v"(ph), "+v"(ta), "+v"(tm4), "+v"(trk), "+v"(tcs), "+v"(tcv), "+v"(tex), "+v"(tfl), "+v"(odst));
    if (KN_MP) __builtin_amdgcn_s_setprio(1);
    f32x4 acc[6][4];
#pragma unroll
    for (int n = 0; n < 6; ++n)
#pragma unroll
      for (int m = 0; m < 4; ++m) acc[n][m] = z4;
    const unsigned xa[2] = {lds0 + (16 + wm * 64) * 128 + xo, lds0 + (16 + wm * 64) * 128 + (xo ^ 64)}, wa[2] = {wfrag(hd * 2), wfrag(hd * 2 + 1)};
    struct Ctx { unsigned x[2], w[2]; } c = {{xa[0], xa[1]}, {wa[0], wa[1]}};
    using SP = SchedP<6, KN_QS>;
    FragQ fa, fb;
    auto rd = [&](auto rc) {
      constexpr int r = decltype(rc)::value;
      if constexpr (r >= 24) { constexpr int m = (r - 24) >> 1; if constexpr (r & 1) fb.x[m] = ds_b128<m * 2048>(c.x[1]); else fa.x[m] = ds_b128<m * 2048>(c.x[0]); }
      else { constexpr int n = r >> 2, o = ((r >> 1) & 1) * 4096 + (n >> 1) * 8192 + (r & 1) * 512; FragQ& f = (r & 2) ? fb : fa;
        if constexpr (r & 1) f.hi[n] = ds_tr<o>(c.w[n & 1]); else f.lo[n] = ds_tr<o>(c.w[n & 1]); }
    };
    auto pin = [&](auto pc) {
      static_for<0, 32>([&](auto rc) { constexpr int r = decltype(rc)::value;
        if constexpr (SP::need(r) == decltype(pc)::value) {
          FragQ& f = (r >= 24 ? r & 1 : r & 2) ? fb : fa;
          if constexpr (r >= 24) { bf16x8& q = f.x[(r - 24) >> 1]; asm volatile("" : "+v"(q)); } else { s16x4& q = (r & 1) ? f.hi[r >> 2] : f.lo[r >> 2]; asm volatile("" : "+v"(q)); } } });
    };
    int sx = QXT, sw = QWU;
    auto pairs = [&](auto PBc, auto PEc, auto RDc, auto&& gap) {
      constexpr bool RD = decltype(RDc)::value;
      static_for<decltype(PBc)::value, decltype(PEc)::value>([&](auto pc) {
        constexpr int p = decltype(pc)::value, A = SP::pa(p), n = SP::pn(A), m = SP::pm(A);
        if constexpr (SP::cnt(p) >= 0 && (!KN_EW || p == decltype(PBc)::value)) { asm volatile("s_waitcnt lgkmcnt(%0)" :: "n"(SP::cnt(p)) : "memory"); pin(pc); }
        static_for<2 * p, 2 * p + 2>([&](auto gc) {
          constexpr int g = decltype(gc)::value;
          FragQ& u = ((A ^ g) & 1) ? fb : fa;
          if constexpr (n < 4) acc[n][m] = __builtin_amdgcn_mfma_f32_16x16x32_bf16(cat(u.lo[n], u.hi[n]), u.x[m], acc[n][m], 0, 0, 0);
          else acc[n][m] = __builtin_amdgcn_mfma_f32_16x16x32_bf16(u.x[m], cat(u.lo[n], u.hi[n]), acc[n][m], 0, 0, 0);
          __builtin_amdgcn_sched_barrier(0);
          if constexpr (KN_EW && RD && !(g & 1) && SP::cnt(p + 1, 1) >= 0) { asm volatile("s_waitcnt lgkmcnt(%0)" :: "n"(SP::cnt(p + 1, 1)) : "memory"); pin(std::integral_constant<int, p + 1>{}); }
          gap(gc);
          if constexpr (RD) {
            static_for<0, 32>([&](auto rc) { if constexpr (SP::gap(decltype(rc)::value) == g) rd(rc); });
            if constexpr (g == 13) { sx = sx + QXT; sw = sw + QWU; asm volatile("" : "+s"(sx), "+s"(sw)); }
            if constexpr (g == 17) { sx = sx == 3 * QXT ? 0 : sx; asm volatile("" : "+s"(sx)); }
            if constexpr (g == 21) { sw = sw == 4 * QWU ? 0 : sw; asm volatile("" : "+s"(sw)); }
            if constexpr (g == 44) { c.x[0] = xa[0] + sx; c.x[1] = xa[1] + sx; asm volatile("" : "+v"(c.x[0]), "+v"(c.x[1])); }
            if constexpr (g == 45) { c.w[0] = wa[0] + sw; c.w[1] = wa[1] + sw; asm volatile("" : "+v"(c.w[0]), "+v"(c.w[1])); }
          }
          __builtin_amdgcn_sched_barrier(0);
        });
      });
      if constexpr (RD && !KN_EW) { asm volatile("s_waitcnt lgkmcnt(0)" ::: "memory"); pin(std::integral_constant<int, 24>{}); }
      static_assert(SP::cnt(24, 1) == 0 && SP::gap(23) < 46, "all reads");
    };
    static_assert(SP::gap(23) <= 44, "addresses");
    using I12 = std::integral_constant<int, 12>; using I24 = std::integral_constant<int, 24>;
    lds_barrier();
    static_for<0, 24>([&](auto gc) { static_for<0, 32>([&](auto rc) { if constexpr (SP::gap(decltype(rc)::value) == decltype(gc)::value) rd(rc); }); });
    STAMP(1);
    pairs(I12{}, I24{}, T{}, [&](auto) {});
#pragma unroll 1
    for (int j = 1; j < NT; ++j) pairs(I0{}, I24{}, T{}, [&](auto pc) { if constexpr (decltype(pc)::value == 1) raw_barrier(); });
#define ES(i)
    ES(0);
    if (KN_QP) __builtin_amdgcn_s_setprio(KN_QP);
    f32x2 st[4], rk[5], cv, fs[4][2]; f32x4 m4[4], cs[4]; int tg;
    pairs(I0{}, I12{}, F{}, [&](auto ic) { constexpr int I = decltype(ic)::value;
      if constexpr (I < 4) st[I] = ds_b64<I * 256>(ta);
      if constexpr (I == 4) tg = ds_r32(lds0 + QFL + 8);
      if constexpr (I < 5) rk[I] = ds_b64<I * 32>(trk);
      if constexpr (I >= 5 && I < 9) m4[I - 5] = __builtin_bit_cast(f32x4, ds_b128<(I - 5) * 64>(tm4));
      if constexpr (I == 9) { asm volatile("s_waitcnt lgkmcnt(4)" : "+v"(st[0]), "+v"(st[1]), "+v"(st[2]), "+v"(st[3]), "+v"(rk[0]), "+v"(rk[1]), "+v"(rk[2]), "+v"(rk[3]), "+v"(rk[4]) :: "memory"); }
      if constexpr (I == 9 || I == 10) { constexpr int m = (I - 9) * 2; const float sc2 = 0.17677669529663687f * 1.4426950408889634f; st[m][1] *= sc2; st[m + 1][1] *= sc2; asm volatile("" : "+v"(st[m]), "+v"(st[m + 1])); }
      if constexpr (I >= 11 && I < 19) { constexpr int m = (I - 11) >> 1, h = (I - 11) & 1; fs[m][h] = rk[m + h] * st[m][1]; asm volatile("" : "+v"(fs[m][h])); }
      if constexpr (I == 19) { int& q = tg; asm volatile("" : "+v"(q)); tag = __builtin_amdgcn_readfirstlane(tg); if (KN_NUMA & 1) odst ^= ((SIDE ^ (xcd >> 2)) & 1) << 13; asm volatile("" : "+v"(odst)); }
      if constexpr (I == 4) ES(1);
      if constexpr (I == KN_QLB) { ES(2); raw_barrier(); ES(3); }
      if constexpr (I >= 14 && I < 18) { constexpr int q = (I - 14) ^ 2; cs[q] = __builtin_bit_cast(f32x4, ds_b128<QCS + (q >> 1) * 256 + (q & 1) * 64>(tcs)); }
      if constexpr (I == 18) cv = ds_r2(tcv); });
    static_assert(KN_QLB <= 14, "barrier");
    STAMP(2);
    ES(4);
    asm volatile("s_waitcnt lgkmcnt(0)" : "+v"(cs[0]), "+v"(cs[1]), "+v"(cs[2]), "+v"(cs[3]), "+v"(cv), "+v"(m4[0]), "+v"(m4[1]), "+v"(m4[2]), "+v"(m4[3]) :: "memory");
    u32x4 Q[4], Kf[5], V[5];
    auto nrm = [&](f32x4 x, f32x4 y, float sc) {
      const f32x2 p = (f32x2){y[0], y[1]} * sc + (f32x2){x[0], x[1]}, q = (f32x2){y[2], y[3]} * sc + (f32x2){x[2], x[3]};
      return (u32x2){pk_bf16(p[0], p[1]), pk_bf16(q[0], q[1])}; };
    auto c4 = [](u32x2 p, u32x2 q) { return (u32x4){p[0], p[1], q[0], q[1]}; };
    auto mkK = [&](int m) { Kf[m + 1] = c4(nrm(acc[2][m], cs[2], st[m][0]), nrm(acc[3][m], cs[3], st[m][0])); };
    auto mkQ = [&](int m) { Q[m] = c4(nrm(acc[0][m], cs[0], st[m][0]), nrm(acc[1][m], cs[1], st[m][0])); };
    auto mkV = [&](int m) { V[m + 1] = c4(nrm(acc[4][m], m4[m], cv[0]), nrm(acc[5][m], m4[m], cv[1])); };
    mkK(3); mkV(3);
    if (wm == 0) asm volatile("ds_write_b128 %0, %1 offset:%5\n\tds_write_b128 %0, %2 offset:%6\n\tds_write_b32 %3, %4" :: "v"(tex), "v"(Kf[4]), "v"(V[4]), "v"(tfl), "v"(pl), "n"(QKH + 2048), "n"(QVH + 2048) : "memory");
    __builtin_amdgcn_sched_barrier(0);
    f32x4 sc[4][2]; f32x2 lg[4][2]; float mx[4];
    auto qk = [&](int m, int h) { sc[m][h] = __builtin_amdgcn_mfma_f32_16x16x32_bf16(__builtin_bit_cast(bf16x8, Kf[m + h]), __builtin_bit_cast(bf16x8, Q[m]), z4, 0, 0, 0); };
    mkQ(3); mkK(2); qk(3, 1); qk(3, 0); mkQ(2); mkK(1); qk(2, 1); qk(2, 0); mkQ(1); mkK(0); qk(1, 1); qk(1, 0);
    __builtin_amdgcn_sched_barrier(0);
    int fl; bf16x8 k0, v0;
    tex += wm * 2048;
    auto front = [&]() { fl = ds_r32(tfl); k0 = ds_b128<QKH>(tex); v0 = ds_b128<QVH>(tex); };
    front();
    __builtin_amdgcn_sched_barrier(0);
    mkQ(0); qk(0, 1); mkV(2); mkV(1);
    __builtin_amdgcn_sched_barrier(0);
    asm volatile("s_waitcnt lgkmcnt(0)" : "+v"(fl), "+v"(k0), "+v"(v0), "+v"(V[3]), "+v"(V[2]) :: "memory");
    if (__builtin_expect(wm && __builtin_amdgcn_readfirstlane(fl) == 0, 0)) {
      unsigned polls = 0;
      do { front(); asm volatile("s_waitcnt lgkmcnt(0)" : "+v"(fl), "+v"(k0), "+v"(v0) :: "memory"); } while (__builtin_amdgcn_readfirstlane(fl) == 0 && ++polls < 40000000u);
    }
    Kf[0] = __builtin_bit_cast(u32x4, k0); V[0] = __builtin_bit_cast(u32x4, v0);
    qk(0, 0); mkV(0);
    STAMP(4);
    __builtin_amdgcn_sched_barrier(0); ES(5);
    __builtin_amdgcn_sched_barrier(0);
    const bool par = pl & 4;
#pragma unroll
    for (int m = 3; m >= 0; --m) {
#pragma unroll
      for (int h = 0; h < 2; ++h) {
        float e[4] = {sc[m][h][0], sc[m][h][1], sc[m][h][2], sc[m][h][3]};
        asm("" : "+v"(e[0]), "+v"(e[2]));
        lg[m][h] = (f32x2){par ? e[1] : e[0], par ? e[3] : e[2]} * fs[m][h] + (m == 0 ? bias0[h] : bias[h]);
      }
      mx[m] = fmaxf(fmaxf(lg[m][0][0], lg[m][0][1]), fmaxf(lg[m][1][0], lg[m][1][1]));
    }
    __builtin_amdgcn_sched_barrier(0);
    auto s16 = [](float x, float y) { auto p = __builtin_amdgcn_permlane16_swap(__float_as_uint(x), __float_as_uint(y), false, false); return (f32x2){__uint_as_float(p[0]), __uint_as_float(p[1])}; };
    auto s32 = [](float x, float y) { auto p = __builtin_amdgcn_permlane32_swap(__float_as_uint(x), __float_as_uint(y), false, false); return (f32x2){__uint_as_float(p[0]), __uint_as_float(p[1])}; };
    auto spread = [&](float r) { const f32x2 t = s32(r, r), u = s16(t[0], t[0]), w = s16(t[1], t[1]); mx[0] = u[0]; mx[1] = u[1]; mx[2] = w[0]; mx[3] = w[1]; };
    { const f32x2 p = s16(mx[0], mx[1]), q = s16(mx[2], mx[3]), r = s32(fmaxf(p[0], p[1]), fmaxf(q[0], q[1])); spread(fmaxf(r[0], r[1])); }
    __builtin_amdgcn_sched_barrier(0);
#pragma unroll
    for (int m = 3; m >= 0; --m) {
#pragma unroll
      for (int h = 0; h < 2; ++h) { const f32x2 d = lg[m][h] - mx[m]; lg[m][h] = (f32x2){__builtin_amdgcn_exp2f(d[0]), __builtin_amdgcn_exp2f(d[1])}; }
      const f32x2 t = lg[m][0] + lg[m][1];
      mx[m] = t[0] + t[1];
    }
    __builtin_amdgcn_sched_barrier(0);
    { const f32x2 p = s16(mx[0], mx[1]), q = s16(mx[2], mx[3]);
#pragma unroll
      for (int m = 3; m >= 0; --m) lg[m][0] = lg[m][0] * rk[m];
      const f32x2 r = s32(p[0] + p[1], q[0] + q[1]);
#pragma unroll
      for (int m = 3; m >= 0; --m) lg[m][1] = lg[m][1] * rk[m + 1];
      spread(__builtin_amdgcn_rcpf(r[0] + r[1])); }
    __builtin_amdgcn_sched_barrier(0);
    auto half = [](u32x4 v, int h) { return __builtin_bit_cast(s16x4, (u32x2){v[2 * h], v[2 * h + 1]}); };
#pragma unroll
    for (int m = 3; m >= 0; --m) {
      s16x4 pf[2];
#pragma unroll
      for (int h = 0; h < 2; ++h) { const f32x2 t = lg[m][h] * mx[m]; const unsigned u = pk_bf16(t[0], t[1]); pf[h] = __builtin_bit_cast(s16x4, (u32x2){__builtin_amdgcn_perm(0u, u, pl), __builtin_amdgcn_perm(0u, u, ph)}); }
#pragma unroll
      for (int f = 0; f < 2; ++f) sc[m][f] = __builtin_amdgcn_mfma_f32_16x16x16bf16_1k(half(V[m], f), pf[0], z4, 0, 0, 0);
#pragma unroll
      for (int f = 0; f < 2; ++f) sc[m][f] = __builtin_amdgcn_mfma_f32_16x16x16bf16_1k(half(V[m + 1], f), pf[1], sc[m][f], 0, 0, 0);
    }
    __builtin_amdgcn_sched_barrier(0);
    STAMP(10);
    ES(6);
    const auto rO = __builtin_amdgcn_make_buffer_rsrc((void*)a.O, 0, 0x7fffffff, 0x00020000);
    u32x4 ov[4];
#pragma unroll
    for (int m = 3; m >= 0; --m) {
      ov[m] = (u32x4){pk_bf16(sc[m][0][0], sc[m][0][1]), pk_bf16(sc[m][0][2], sc[m][0][3]), pk_bf16(sc[m][1][0], sc[m][1][1]), pk_bf16(sc[m][1][2], sc[m][1][3])};
      __builtin_amdgcn_raw_buffer_store_b128(ov[m], rO, odst, m * RSH, 16);
    }
    asm volatile("" :: "v"(ov[0]), "v"(ov[1]), "v"(ov[2]), "v"(ov[3]));
    if (KN_QP) __builtin_amdgcn_s_setprio(0);
    STAMP(7);
    ES(7);
    wait_vm<0>();
    STAMP(8);
    ES(8);
    raw_barrier();
    ES(9);
  }
  STAMP(3);
#endif
  return tag;
}

struct FragW { s16x4 lo[4], hi[4]; };
__device__ __forceinline__ void wo_phase(const GemmArgs& a, const GemmArgs& nx, unsigned tag) {
#if defined(__HIP_DEVICE_COMPILE__)
  extern __shared__ __attribute__((aligned(16))) char smem[];
  STAMP(0);
  const unsigned lds0 = (unsigned)(size_t)(lds_char_ptr)smem;
  const int tid = threadIdx.x, lane = tid & 63;
  const int wave = __builtin_amdgcn_readfirstlane(tid >> 6);
  const int bid = blockIdx.x, xcd = bid & 7, idx = bid >> 3;
  const int tm = (xcd >> 1) * 4 + (idx >> 3), tn = (xcd & 1) * 8 + (idx & 7);
  const int m0 = tm * 128, panel = m0 >> 8, rot = (xcd & 3) * 4, sd = SIDE;
  {
    constexpr int PAD = QTCH;
    const int g = wave >> 2, rw = wave & 3;
    const int fr = lane & 15, fq = lane >> 4, wq = (lane >> 2) & 3, wp = lane & 3;
    unsigned wa[4];
#pragma unroll
    for (int c = 0; c < 4; ++c) { const int row = 4 * fq + wq; wa[c] = lds0 + g * 8192 + row * 128 + (((c << 1) ^ (row & 6)) << 4) + wp * 8; }
    FragW f[3];
    auto rd = [&](auto ic, auto rc) {
      constexpr int i = decltype(ic)::value, r = decltype(rc)::value, P = wo_pos(i >> 1) + (i & 1) * 4096, B = P & ~32767, o = P & 32767;
      if constexpr (r & 1) f[i % 3].hi[r >> 1] = ds_tr<o + 2048>(wa[r >> 1] + B); else f[i % 3].lo[r >> 1] = ds_tr<o>(wa[r >> 1] + B);
    };
    auto pin = [&](FragW& q) {
#pragma unroll
      for (int n = 0; n < 4; ++n) asm volatile("" : "+v"(q.lo[n]), "+v"(q.hi[n]));
    };
    static_for<0, 16>([&](auto rc) { constexpr int r = decltype(rc)::value; rd(std::integral_constant<int, r / 8>{}, std::integral_constant<int, r % 8>{}); });
    if constexpr (KN_CT & 4) { if (wave == 2) {
      unsigned long long c0, c1; CODE_ADDR(c0, "k_all"); CODE_ADDR(c1, "k_zend");
      wait_vm<0>();
      __builtin_amdgcn_raw_ptr_buffer_load_lds(__builtin_amdgcn_make_buffer_rsrc((void*)c0, 0, c1 - c0 < 262144ull ? (unsigned)(c1 - c0) : 0u, 0x00020000), (lds_void_ptr)(size_t)(lds0 + PAD), 4, (unsigned)((lane * 32 + idx) * 128), 0, 0, 0); } }
    if (wave == 1) wait_flags(a.sync + FLO(tm), (lane & 15) * 128, tag, 0xffffull);
    {
      if (wave == 3) {
        wait_flags(a.sync, (FHM(lane >= 32 ? (xcd >> 2) * 4 + (idx >> 3) : panel) + (lane & 31)) * 4, tag, ~0ull);
      }
    }
    const auto rX = __builtin_amdgcn_make_buffer_rsrc((void*)a.X, 0, 0x7fffffff, 0x00020000);
    const int rb = (m0 >> 4) + rw * 2 + g;
    unsigned xs[2] = {(unsigned)(rbH(rb, sd) + lane * 16), (unsigned)(rbH(rb ^ 1, sd) + lane * 16)};
    const int ck = (fq & 1) * 2 + (fq >> 1);
    unsigned orow = (unsigned)((rb * 16 + fr) * (D_ * 2) + tn * 128 + ck * 16), ofrg = (unsigned)(rbH(rb, sd) + omH(tn * 2048) + ck * 256 + fr * 16);
    asm volatile("" : "+v"(xs[0]), "+v"(xs[1]), "+v"(orow), "+v"(ofrg));
    STAMP(9);
    if (!(KN_CT & 4) || wave != 2) wait_vm<0>();
    lds_barrier();
    STAMP(6);
    pin(f[0]); pin(f[1]);
    u32x4 xr[16][2], res[2];
#define KN_XD 4
    auto ldX = [&](int i, int m) {
      if (i < 16) xr[i][m] = __builtin_amdgcn_raw_buffer_load_b128(rX, xs[m] + (i & 1) * 1024, omH((i >> 1) * 2048) ^ omH(((g * 8) ^ rot) * 2048), 16);
      else if (i == 16) res[m] = __builtin_amdgcn_raw_buffer_load_b128(__builtin_amdgcn_make_buffer_rsrc((void*)a.R, 0, 0x7fffffff, 0x00020000), orow + m * 64, 0, 0);
    };
#pragma unroll
    for (int i = 0; i < KN_XD; ++i) { ldX(i, 0); ldX(i, 1); __builtin_amdgcn_sched_barrier(0); }
    unsigned gsrc[2];
#pragma unroll
    for (int i = 0; i < 2; ++i) {
      const int j = wave + 8 * i, jj = j >= 7 ? j - 7 : j;
      const int b = jj * 1024 + lane * 16, q = b / 224, pos = (b % 224) >> 4;
      gsrc[i] = (unsigned)((8 * ((q & 15) >> 2) + (q & 3) + 4 * (q >> 4)) * DF_ + ((xcd & 3) * 8 + (idx & 7)) * 112 + pos * 8) * 2u;
      asm volatile("" : "+v"(gsrc[i]));
    }
    f32x4 acc[4][2];
#pragma unroll
    for (int n = 0; n < 4; ++n) { acc[n][0] = (f32x4){0.f, 0.f, 0.f, 0.f}; acc[n][1] = acc[n][0]; }
    __builtin_amdgcn_sched_barrier(0);
    static_for<0, 16>([&](auto ic) {
      constexpr int i = decltype(ic)::value;
      if constexpr (i >= 2) { asm volatile("s_waitcnt lgkmcnt(%0)" :: "n"(i < 15 ? 8 : 0) : "memory"); pin(f[i % 3]); }
      static_for<0, 8>([&](auto jc) {
        constexpr int j = decltype(jc)::value, n = j >> 1, m = j & 1;
        const bf16x8 w = __builtin_shufflevector(f[i % 3].lo[n], f[i % 3].hi[n], 0, 1, 2, 3, 4, 5, 6, 7);
        acc[n][m] = __builtin_amdgcn_mfma_f32_16x16x32_bf16(w, __builtin_bit_cast(bf16x8, xr[i][m]), acc[n][m], 0, 0, 0);
        if constexpr (i + 2 < 16) rd(std::integral_constant<int, i + 2>{}, jc);
        if constexpr (j == 1 || j == 5) ldX(i + KN_XD, j >> 2);
        __builtin_amdgcn_sched_barrier(0);
      });
      if constexpr (i == 0) STAMP(1);
    });
    STAMP(2);
    auto slot = [&](int w) { return 32768 + w * 4096 + lane * 16; };
#pragma unroll
    for (int n = 0; n < 4; ++n) *(f32x4*)(smem + slot(wave) + n * 1024) = acc[n][1];
    asm volatile("" : "+v"(res[0]), "+v"(res[1]));
    lds_barrier();
    STAMP(4);
    f32x4 pa[4];
    static_for<0, 4>([&](auto nc) { constexpr int n = decltype(nc)::value; pa[n] = __builtin_bit_cast(f32x4, ds_b128<n * 1024>(lds0 + slot(wave ^ 4))); });
    auto ldGs = [&]() {
      auto ldG = [&](int i, int u) { const int j = wave + 8 * i;
        __builtin_amdgcn_raw_ptr_buffer_load_lds(__builtin_amdgcn_make_buffer_rsrc((void*)(j >= 7 ? nx.W1 : nx.W0), 0, 0x7fffffff, 0x00020000),
                          (lds_void_ptr)(size_t)(lds0 + 98304 + (u ^ 2) * 14592 + (j >= 7 ? 7184 + (j - 7) * 1024 : j * 1024)), 16, gsrc[i], (u + (xcd >> 2) * 16) * 32 * DF_ * 2, 0, 0); };
      ldG(0, 0); ldG(0, 1);
      if (wave < 6) { ldG(1, 0); ldG(1, 1); }
    };
    if (!KN_GL) ldGs();
    asm volatile("s_waitcnt lgkmcnt(0)" : "+v"(pa[0]), "+v"(pa[1]), "+v"(pa[2]), "+v"(pa[3]) :: "memory");
    const auto rO = __builtin_amdgcn_make_buffer_rsrc((void*)a.O, 0, 0x7fffffff, 0x00020000);
    u32x4 o[2];
#pragma unroll
    for (int i = 0; i < 2; ++i) {
      const f32x4 u = acc[2 * i][0] + pa[2 * i], v = acc[2 * i + 1][0] + pa[2 * i + 1];
      const auto s0 = __builtin_amdgcn_permlane16_swap(pk_bf16(u[0], u[1]), pk_bf16(v[0], v[1]), false, false);
      const auto s1 = __builtin_amdgcn_permlane16_swap(pk_bf16(u[2], u[3]), pk_bf16(v[2], v[3]), false, false);
      const u32x4 q4 = {s0[0], s1[0], s0[1], s1[1]};
#pragma unroll
      for (int j = 0; j < 4; ++j) { const unsigned r = res[i][j], q = q4[j]; o[i][j] = pk_bf16(bflo(r) + bflo(q), bfhi(r) + bfhi(q)); }
      __builtin_amdgcn_raw_buffer_store_b128(o[i], rO, ofrg + i * 1024, 0, 16);
    }
    if (KN_GL) { __builtin_amdgcn_sched_barrier(0); asm volatile("s_nop 1" ::: "memory"); ldGs(); __builtin_amdgcn_sched_barrier(0); }
    auto sum4 = [](float v) {
      auto p = __builtin_amdgcn_permlane16_swap(__float_as_uint(v), __float_as_uint(v), false, false); v = __uint_as_float(p[0]) + __uint_as_float(p[1]);
      auto q = __builtin_amdgcn_permlane32_swap(__float_as_uint(v), __float_as_uint(v), false, false); return __uint_as_float(q[0]) + __uint_as_float(q[1]); };
    f32x2 e[8], sm = {0.f, 0.f};
#pragma unroll
    for (int j = 0; j < 8; ++j) { const unsigned q = o[j >> 2][j & 3]; e[j] = (f32x2){bflo(q), bfhi(q)}; sm += e[j]; }
    const float mu = sum4(sm[0] + sm[1]) * (1.f / 64.f);
    f32x2 t2 = {0.f, 0.f};
#pragma unroll
    for (int j = 0; j < 8; ++j) { const f32x2 d = e[j] - mu; t2 += d * d; }
    const float q2 = sum4(t2[0] + t2[1]);
    __builtin_amdgcn_raw_buffer_store_b64((u32x2){__float_as_uint(mu), __float_as_uint(q2)}, __builtin_amdgcn_make_buffer_rsrc((void*)a.st, 0, 0x7fffffff, 0x00020000),
                                          lane < 16 ? (unsigned)((tn * S_ + rb * 16 + lane) * 8) : 0x80000000u, 0, 16);
    STAMP(7);
    STAMP(8);
    unsigned fo = lane == 0 ? (unsigned)(FHF(panel) + ((tm & 1) * 16 + tn) * 32) * 4u : 0x80000000u, ft = tag;
    unsigned gx = rbH(((xcd >> 2) * 4 + (idx >> 3)) * 16, sd) + (rw >> 1) * RSH + (rw & 1) * 1024 + lane * 16, gm = lds0 + g * 32768 + rw * 1024, gs = omH((xcd >> 2) * 16384 + g * 2048);
    const auto rF = __builtin_amdgcn_make_buffer_rsrc((void*)a.sync, 0, 147456, 0x00020000);
    const u32x4 qH = mk_rsrc(a.O);
    asm volatile("s_waitcnt vmcnt(0) lgkmcnt(0)" : "+v"(fo), "+v"(ft), "+v"(gx), "+s"(gm), "+s"(gs) : "s"(rF), "s"(qH) : "memory");
    raw_barrier();
    if (wave == 4) __builtin_amdgcn_raw_buffer_store_b32(ft, rF, fo, 0, 16);
    if (KN_GX > 1 && g) __builtin_amdgcn_s_sleep(KN_GX - 1);
    static_for<0, 8>([&](auto ic) { constexpr int i = decltype(ic)::value; ld_ca<i * 4096, i * 2 * RSH, false, true>(gm, qH, gx, gs); });
    STAMP(3);
  }
#endif
}

struct FragE { bf16x8 x[4]; s16x4 lo[4], hi[4]; };
template <int MODE, int HTD, int TA, int TB, int TL = 0>
__device__ __forceinline__ void dn_phase(const GemmArgs& a, unsigned tag) {
#if defined(__HIP_DEVICE_COMPILE__)
  constexpr int SX = 5, SW = 4, WB = SX * 16384, PAD = WB + SW * 16384, V = KN_V;
  static_assert(TA == 2 && TB == 2, "schedule");
  extern __shared__ __attribute__((aligned(16))) char smem[];
  if constexpr (MODE != 1) STAMP(0);
  const unsigned lds0 = (unsigned)(size_t)(lds_char_ptr)smem;
  const int tid = threadIdx.x;
  int lane = tid & 63; asm volatile("" : "+v"(lane));
  const int wave = __builtin_amdgcn_readfirstlane(tid >> 6);
  const int bid = blockIdx.x, xcd = bid & 7, idx = bid >> 3;
  const int tm = (xcd >> 1) * 4 + (idx >> 3), tn = (idx & 7) * 2 + (xcd & 1), A = (xcd & 1) ? 24 - KN_D : 24 + KN_D;
  const int m0 = tm * 128, panel = m0 >> 8, sd = SIDE;
  unsigned* pcnt = a.sync + FAE((xcd >> 2) * 4 + (idx >> 3)) + ((xcd & 3) * 8 + (idx & 7)) * 32;
  const unsigned orow = (unsigned)((m0 + wave * 16 + (lane >> 3)) * (D_ * 2) + tn * 128 + (lane & 7) * 16);
  const unsigned ofrg = (unsigned)(rbH((m0 >> 4) + wave, sd) + omH(tn * 2048) + (lane & 7) * 256 + (lane >> 3) * 16);
  u32x4 res[2];
  auto res_loads = [&]() {
    const auto rR = __builtin_amdgcn_make_buffer_rsrc((void*)a.R, 0, 0x7fffffff, 0x00020000);
#pragma unroll
    for (int i = 0; i < 2; ++i) res[i] = __builtin_amdgcn_raw_buffer_load_b128(rR, ofrg, i * 128, 16);
  };
  constexpr int STG = 0;
  if (MODE == 1 || wave < 4) {
    const int lw = wave & 3;
    const int kf = (xcd & 3) * 8;
    auto ktile = [&](int t) { const int l = (xcd & 2) * 6 + (t < A ? t - 16 + (xcd & 1) * (8 + KN_D) : t - A + 16); return t < 16 ? (kf ^ ((t & 8) * 2)) + (t & 7) : (l < 24 ? l + 32 : l + 8); };
    auto wrow = [&](int kt, int j) { const int p = kt * 8 + j, b = p - 256, v1 = p + 3 * (p >> 2), v2 = b + 4 * ((b * 21846) >> 16) + 4, m = b >> 31; return ((v1 & m) | (v2 & ~m)) * 8; };
    const auto rW = __builtin_amdgcn_make_buffer_rsrc((void*)a.W0, 0, 0x7fffffff, 0x00020000);
    const u32x4 qX = mk_rsrc(a.X), qW = mk_rsrc((const char*)a.W0 - 4096);
    const unsigned xv0 = (unsigned)(rbA((m0 >> 4) + lw * 2, sd) + lane * 16), xv1 = xv0 + (RSA - 2048);
    unsigned wv[4];
    { const unsigned w0 = (unsigned)((lane >> 3) * D_ + tn * 64 + ((lane & 7) ^ wswz<128>(lane >> 3)) * 8) * 2u;
#pragma unroll
      for (int i = 0; i < 4; ++i) wv[i] = (w0 ^ (i * 64)) + (4096 - i * 1024); }
    const unsigned mX = lds0 + lw * 4096, mW = mX + WB;
    auto ldXo = [&](int so, int slot) { ld_x4(mX + slot * 16384, qX, xv0, xv1, so); };
    int tbx = 0, tb0 = 0, tb1 = 0;
    auto tables = [&]() { const int kt = ktile(lane < A + 8 ? lane : 0); tbx = omA(kt * 2048); tb0 = wrow(kt, 2 * lw) * (D_ * 2); tb1 = wrow(kt, 2 * lw + 1) * (D_ * 2); asm volatile("" : "+v"(tbx), "+v"(tb0), "+v"(tb1)); };
    int o0, o1, ox;
    auto offs = [&](int t) { o0 = __builtin_amdgcn_readlane(tb0, t + 4); o1 = __builtin_amdgcn_readlane(tb1, t + 4); ox = __builtin_amdgcn_readlane(tbx, t + 4); asm volatile("s_nop 1" : "+s"(o0), "+s"(o1), "+s"(ox)); };
    const unsigned* fcnt = a.sync + FAE(panel);
    const unsigned fof = (lane & 31) * 128 + (lane >> 5) * 4;
    auto ldF = [&]() { __builtin_amdgcn_raw_ptr_buffer_load_lds(__builtin_amdgcn_make_buffer_rsrc((void*)fcnt, 0, 4096, 0x00020000), (lds_void_ptr)(size_t)(lds0 + PAD + 256), 4, fof, 0, 0, 16); };
    int ht; unsigned htv;
    auto hset = [&]() {
      int idh = idx; asm volatile("" : "+s"(idh));
      const int slc = (3 - ((unsigned)(size_t)a.W0 >> 7)) & 7, pr = idh & 3;
      const bool hlp = pr != (slc >> 1) && lw < 3;
      const int htau = 2 + lw * 12 + (idh >> 3) * 3 + pr - (pr > (slc >> 1) ? 1 : 0);
      ht = (hlp && htau < (((slc ^ xcd) & 1) ? A : A + 8)) ? htau - HTD : 1000;
      htv = (unsigned)((wrow(ktile(htau < A + 8 ? htau : 0), lane >> 3) + (lane & 7)) * (D_ * 2) + ((idh & 4) * 2 + slc) * 128);
    };
    auto help = [&]() { __builtin_amdgcn_raw_ptr_buffer_load_lds(rW, (lds_void_ptr)(size_t)(lds0 + FPAD - 256), 4, htv, 0, 0, 0); };
    auto ldW0 = [&](int i) { const int so = ((kf + i) * 112 + (lw >> 1) * 56 + (lw & 1) * 16) * (D_ * 2); ld_w4<-1>(mW + i * 16384, qW, wv, so, so + 8 * D_ * 2); };
    if constexpr (MODE == 1) {
      if constexpr (TL == 1) __builtin_amdgcn_raw_ptr_buffer_load_lds(__builtin_amdgcn_make_buffer_rsrc((void*)(a.sync + FAM(panel)), 0, 256, 0x00020000), (lds_void_ptr)(size_t)(lds0 + FPAD), 4, lane * 4, 0, 0, 16);
      if constexpr (TL == 0) { ldW0(0); ldW0(1); }
      return;
    }
#define TS0
#define TS1(T)
#define KN_XW 4
    wait_vm<3>();
    lds_inc(lds0 + ACNT);
    STAMPW(4, 0);
    if (KN_XW && KN_XW < 4) { hset(); tables(); asm volatile("" :: "s"(ht), "v"(htv)); }
    if (KN_XW == 3) ldW0(2);
    const bool early = !lds_missing(lds0 + FPAD, tag, ((1ull << (4 * V)) - 1) << ((xcd & 1) * 32));
    lds_wait8(lds0 + ACNT);
    STAMPW(9, 0);
    if (tid == 192) set_flag(pcnt, tag);
    asm volatile("" : "+v"(lane) :: "memory");
    if (!KN_XW) { hset(); tables(); asm volatile("" :: "s"(ht), "v"(htv)); }
    if (KN_XW <= 1 || KN_XW == 5) lds_wait8(lds0 + ACNT + 4, 4);
    STAMPW(5, 0);
    ldXo(omA(kf * 2048), 0); ldXo(omA(kf * 2048) + 2048, 1); if (KN_XW != 3) ldW0(2);
    if (KN_XW >= 4) { asm volatile("" : "+v"(lane) :: "memory"); hset(); tables(); asm volatile("" :: "s"(ht), "v"(htv)); }
    static_assert(V >= 5 && V <= 8, "first tiles");
    unsigned sx = mX + 2 * 16384, sw = 3 * 16384;
    auto next = [&]() { sw = (sw + 16384) & 65535; sx = sx + 16384; sx = sx >= mX + SX * 16384 ? mX : sx; asm volatile("" : "+s"(sw), "+s"(sx)); };
    { unsigned sc = mX + lane * 16;
      const int te = lw == 0 ? V - 4 : 1000;
      if (ht < -1) ht = -1;
      ox = omA(kf * 2048) + 4096;
      wait_vm<KN_XW == 3 ? 4 : 8>();
#pragma unroll 1
      for (int t = -1; t < V - 1; ++t) {
        const int oy = ox;
        offs(t);
        if (KN_XW == 3 && t == 0) wait_vm<8>(); else wait_vm<12>();
        if (__builtin_expect(ld_x4c(sx, qX, xv0, xv1, oy, sc) != 0 || !early, 0)) {
          for (int tries = early ? 0 : KN_TRY;; ++tries) {
            if (tries >= KN_TRY) wait_flags(fcnt, fof, tag, LO32);
            int so = __builtin_amdgcn_readlane(tbx, t + 1), sm = __builtin_amdgcn_readfirstlane(sc); asm volatile("s_nop 3" : "+s"(so), "+s"(sm));
            ld_x4(sm, qX, xv0, xv1, so);
            wait_vm<0>();
            if (tries >= KN_TRY || !marked<4, 1024>(sc)) break;
          }
        }
        if (t == -1) STAMPW(6, 0);
        if (__builtin_expect(t == te, 0)) { wait_vm<12>(); if (lds_missing(lds0 + PAD + 256, tag, LO32)) wait_flags(fcnt, fof, tag, LO32); }
        ld_w4<63>(mW + sw, qW, wv, o0, o1);
        TS0;
        next();
        if (__builtin_expect(t == te - 2, 0)) { ldF(); asm volatile("" ::: "memory"); }
        if (__builtin_expect(t == ht, 0)) help();
        sc = t == 3 ? sc - 4 * 16384 : sc + 16384;
        TS1(t + 1);
      }
    }
    ld_x4(sx, qX, xv0, xv1, ox);
    sx = sx + 16384; sx = sx >= mX + SX * 16384 ? mX : sx;
    const int tf = lw == 0 ? 9 : 1000;
    offs(V - 1);
    auto round = [&](int t, auto P1c, auto WAITc) {
      constexpr bool P1 = decltype(P1c)::value; constexpr int WAIT = decltype(WAITc)::value;
      if constexpr (P1) { if (__builtin_expect(t == tf + 3, 0)) { wait_vm<WAIT>(); if (lds_missing(lds0 + PAD + 256, tag, HI32)) wait_flags(fcnt, fof, tag, HI32); } }
      if constexpr (P1) ld_w4<WAIT>(mW + sw, qW, wv, o0, o1); else ld_w2<WAIT>(mW + sw, qW, wv, o0, o1);
      TS0;
      __builtin_amdgcn_sched_barrier(0);
      if (__builtin_expect(t == ht, 0)) help();
      if constexpr (P1) { if (__builtin_expect(t == tf, 0)) ldF(); }
      const int oy = ox; const unsigned sy = sx;
      offs(t + 1);
      next();
      TS1(t + 1);
      __builtin_amdgcn_sched_barrier(0);
      ld_x4(sy, qX, xv0, xv1, oy);
    };
    int t = V - 1;
#pragma unroll 1
    for (; t < A - 4; ++t) round(t, std::true_type{}, std::integral_constant<int, 16>{});
    round(t++, std::false_type{}, std::integral_constant<int, 16>{});
    round(t++, std::false_type{}, std::integral_constant<int, 14>{});
#pragma unroll 1
    for (; t < A + 4; ++t) round(t, std::false_type{}, std::integral_constant<int, 12>{});
    wait_vm<12>(); raw_barrier(); TS0; TS1(t + 1);
    res_loads();
    wait_vm<8>(); raw_barrier(); TS0; TS1(t + 2);
    wait_vm<2>(); raw_barrier(); TS0; TS1(t + 3);
    raw_barrier(); TS0; TS1(t + 4);
    asm volatile("" : "+v"(res[0]), "+v"(res[1]));
  } else {
    if constexpr (MODE == 1) return;
    const int fr = lane & 15, fq = lane >> 4, wq = (lane >> 2) & 3, wp = lane & 3, cw = wave & 3, wm = cw >> 1, wn = cw & 1;
#define KN_DP 0
    if (KN_DP) __builtin_amdgcn_s_setprio(KN_DP);
    f32x4 acc[4][4];
#pragma unroll
    for (int n = 0; n < 4; ++n)
#pragma unroll
      for (int m = 0; m < 4; ++m) acc[n][m] = (f32x4){0.f, 0.f, 0.f, 0.f};
    const unsigned xa = lds0 + wm * 8192 + lane * 16;
    unsigned wa[2];
#pragma unroll
    for (int c = 0; c < 2; ++c) { const int row = 8 * fq + wq; wa[c] = lds0 + WB + (row >> 4) * 4096 + (row & 15) * 128 + ((((wn * 2 + c) << 1) ^ wswz<128>(row)) << 4) + wp * 8; }
    struct Ctx { unsigned x, w[2]; };
    auto mk = [&](unsigned xo, unsigned wo) { Ctx c; c.x = xa + xo; c.w[0] = wa[0] + wo; c.w[1] = wa[1] + wo; return c; };
    auto rd = [&](FragE& f, const Ctx& c, auto KSc, auto kc) {
      constexpr int KS = decltype(KSc)::value, k = decltype(kc)::value;
      if constexpr (k < 4) f.x[k] = ds_b128<KS * 1024 + k * 2048>(c.x);
      else { constexpr int n = k - 4, o = KS * 8192 + (n >> 1) * 2048; f.lo[n] = ds_tr<o>(c.w[n & 1]); f.hi[n] = ds_tr<o + 512>(c.w[n & 1]); }
    };
    auto pin = [&](FragE& f, auto Nc) {
#pragma unroll
      for (int m = 0; m < 4; ++m) asm volatile("" : "+v"(f.x[m]));
#pragma unroll
      for (int n = 0; n < decltype(Nc)::value; ++n) asm volatile("" : "+v"(f.lo[n]), "+v"(f.hi[n]));
    };
    using I0 = std::integral_constant<int, 0>; using I1 = std::integral_constant<int, 1>; using I2 = std::integral_constant<int, 2>; using I4 = std::integral_constant<int, 4>;
    FragE fa, fb, fc, fd;
    const auto rS = __builtin_amdgcn_make_buffer_rsrc((void*)a.slab, 0, 0x7fffffff, 0x00020000);
    unsigned sla = rbH(tm * 8 + (tn >> 1), sd) + omH((tn & 1) * 16384 + cw * 4096) + lane * 16, slb = sla ^ omH(16384);
    asm volatile("" : "+v"(sla), "+v"(slb));
    raw_barrier();
    { FragE& f0 = (KN_D & 1) ? fc : fa; FragE& f1 = (KN_D & 1) ? fd : fb;
      const Ctx c = mk(0, 0); static_for<0, 8>([&](auto kc) { rd(f0, c, I0{}, kc); }); asm volatile("s_waitcnt lgkmcnt(4)" ::: "memory"); static_for<0, 8>([&](auto kc) { rd(f1, c, I1{}, kc); });
      asm volatile("s_waitcnt lgkmcnt(0)" ::: "memory");
      pin(f0, I4{}); pin(f1, I4{}); }
    STAMP(1);
    static_assert((SW & (SW - 1)) == 0, "W ring");
    int sx = 16384, sw = 16384;
    Ctx cn = mk(sx, sw);
#define KN_SP4 26
    auto rd1 = [&](FragE& f, const Ctx& c, auto KSc, auto rc) {
      constexpr int KS = decltype(KSc)::value, r = decltype(rc)::value;
      if constexpr (r < 4) f.x[r] = ds_b128<KS * 1024 + r * 2048>(c.x);
      else { constexpr int n = (r - 4) >> 1, o = KS * 8192 + (n >> 1) * 2048; if constexpr (r & 1) f.hi[n] = ds_tr<o + 512>(c.w[n & 1]); else f.lo[n] = ds_tr<o>(c.w[n & 1]); }
    };
    auto send = [&](int m) {
      __builtin_amdgcn_raw_buffer_store_b128((u32x4){pk_bf16(acc[2][m][0], acc[2][m][1]), pk_bf16(acc[2][m][2], acc[2][m][3]), pk_bf16(acc[3][m][0], acc[3][m][1]), pk_bf16(acc[3][m][2], acc[3][m][3])},
                                             rS, slb + m * 1024, 0, 16); };
    auto tile = [&](FragE& c0, FragE& c1, FragE& n0, FragE& n1, auto NCc, auto NNc, auto&& post) {
      constexpr int NC = decltype(NCc)::value, NN = decltype(NNc)::value;
      constexpr bool PF = KN_PF && NC == 4 && NN == 2;
      constexpr int R2 = 8 + 4 * NN, NP = 4 * NC;
      const Ctx c = cn;
      static_for<0, 2 * NP>([&](auto ic) {
        constexpr int I = decltype(ic)::value, P = I >> 1, Q = PF && P > 8 ? P - 8 : P;
        constexpr int n = PF && P >= 1 && P <= 8 ? 2 + ((P >> 1) & 1) : Q / 4, m = PF && P >= 1 && P <= 8 ? (P - 1) >> 1 : ((n & 1) ? 3 - Q % 4 : Q % 4);
        constexpr int SP = NC == 4 ? (NN == 4 ? KN_SP4 : 20) : 12;
        constexpr int before = I < 1 ? 0 : ((I - 1) * R2 / SP < R2 ? (I - 1) * R2 / SP : R2), after = I * R2 / SP < R2 ? I * R2 / SP : R2;
        FragE& u = ((P ^ I) & 1) ? c1 : c0;
        acc[n][m] = __builtin_amdgcn_mfma_f32_16x16x32_bf16(__builtin_shufflevector(u.lo[n], u.hi[n], 0, 1, 2, 3, 4, 5, 6, 7), u.x[m], acc[n][m], 0, 0, 0);
        __builtin_amdgcn_sched_barrier(0);
        if constexpr (I == 1) { asm volatile("s_waitcnt lgkmcnt(0)" ::: "memory"); pin(c0, NCc); pin(c1, NCc); raw_barrier(); post(); }
        static_for<before, after>([&](auto rc) { constexpr int r = decltype(rc)::value;
          constexpr int h = r < 6 ? r / 3 : (r < 12 ? r & 1 : (r >> 1) & 1), q = r < 6 ? (r % 3 == 2 ? 0 : 4 + r % 3) : (r < 12 ? (r - 4) >> 1 : 4 + ((r - 8) >> 2) * 2 + (r & 1));
          if constexpr (h) rd1(n1, c, I1{}, std::integral_constant<int, q>{}); else rd1(n0, c, I0{}, std::integral_constant<int, q>{}); });
        if constexpr (PF && I >= 9 && I <= 21 && (I - 9) % 4 == 0) send((I - 9) / 4);
        if constexpr (I == 2 * NP - 4) { sw = (sw + 16384) & (SW * 16384 - 1); asm volatile("" : "+s"(sw)); }
        if constexpr (I == 2 * NP - 3) { cn.w[0] = wa[0] + sw; cn.w[1] = wa[1] + sw; asm volatile("" : "+v"(cn.w[0]), "+v"(cn.w[1])); }
        if constexpr (I == 2 * NP - 5) { sx = sx + 16384; asm volatile("" : "+s"(sx)); }
        if constexpr (I == 2 * NP - 2) { sx = sx == SX * 16384 ? 0 : sx; asm volatile("" : "+s"(sx)); }
        if constexpr (I == 2 * NP - 1) { cn.x = xa + sx; asm volatile("" : "+v"(cn.x)); }
        __builtin_amdgcn_sched_barrier(0);
      });
      asm volatile("s_waitcnt lgkmcnt(%0)" :: "n"(R2 - 6 < 15 ? R2 - 6 : 15) : "memory");
      asm volatile("" : "+v"(n0.x[0]), "+v"(n0.lo[0]), "+v"(n0.hi[0]), "+v"(n1.x[0]), "+v"(n1.lo[0]), "+v"(n1.hi[0]));
    };
    auto nop = [&]() {};
    if constexpr (KN_D & 1) tile(fc, fd, fa, fb, I4{}, I4{}, nop);
#pragma unroll 1
    for (int t = 0; t < A - 3; t += 2) {
      tile(fa, fb, fc, fd, I4{}, I4{}, [&]() { if (t == 2) { if (tid == 256) set_flag(pcnt + 1, tag); } });
      tile(fc, fd, fa, fb, I4{}, I4{}, nop);
      if (t == 0) wait_vm<0>();
    }
    tile(fa, fb, fc, fd, I4{}, I4{}, nop); tile(fc, fd, fa, fb, I4{}, I2{}, nop);
    STAMP(10);
    if (!KN_PF) { send(0); send(1); send(2); send(3); }
    auto pairs = [&](int n) {
#pragma unroll 1
      for (int t = 0; t < n; t += 2) { tile(fa, fb, fc, fd, I2{}, I2{}, nop); tile(fc, fd, fa, fb, I2{}, I2{}, nop); } };
    const unsigned* flag = a.sync + FPR(bid);
    u32x4 pp[4];
    auto ldP = [&]() {
#pragma unroll
      for (int i = 0; i < 4; ++i) pp[i] = __builtin_amdgcn_raw_buffer_load_b128(rS, sla + i * 1024, 0, 16);
    };
    {
    pairs(TA);
    STAMPV(11);
    wait_vm<0>();
    STAMPV(12);
    tile(fa, fb, fc, fd, I2{}, I2{}, [&]() { if (tid == 256) set_flag(a.sync + FPR(bid ^ 1), tag); });
    tile(fc, fd, fa, fb, I2{}, I2{}, nop);
    if constexpr (TB > TA) pairs(TB - TA);
    unsigned fv = 1u;
    if (wave == 4) fv = __hip_atomic_load((gu32)flag, __ATOMIC_RELAXED, __HIP_MEMORY_SCOPE_AGENT);
    tile(fa, fb, fc, fd, I2{}, I2{}, nop);
    tile(fc, fd, fa, fb, I2{}, I2{}, nop);
    if (wave == 4) {
      if (__builtin_amdgcn_readfirstlane(fv) != tag) wait_flags(flag, 0, tag, 1ull);
    }
    STAMPV(4);
    tile(fa, fb, fc, fd, I2{}, I2{}, [&]() {
#pragma unroll
      for (int i = 0; i < 4; ++i) pp[i] = __builtin_amdgcn_raw_buffer_load_b128(rS, sla + i * 1024, 0, 16);
      res_loads();
    });
    tile(fc, fd, fa, fb, I2{}, I2{}, nop);
    }
    asm volatile("s_waitcnt lgkmcnt(0)" ::: "memory");
    pin(fa, I2{}); pin(fb, I2{});
    STAMP(2);
#pragma unroll
    for (int m = 0; m < 4; ++m)
#pragma unroll
      for (int n = 0; n < 2; ++n) {
        const unsigned q0 = pp[m][n * 2], q1 = pp[m][n * 2 + 1];
        const f32x4 v = acc[n][m] + (f32x4){bflo(q0), bfhi(q0), bflo(q1), bfhi(q1)};
        *(u32x2*)(smem + STG + (wm * 64 + m * 16 + fr) * 128 + ((((wn * 2 + n) * 2 + (fq >> 1)) ^ (fr & 7)) << 4) + (fq & 1) * 8) = (u32x2){pk_bf16(v[0], v[1]), pk_bf16(v[2], v[3])};
      }
    STAMPV(5);
    asm volatile("" : "+v"(res[0]), "+v"(res[1]));
  }
  lds_barrier();
  {
    const auto rO = __builtin_amdgcn_make_buffer_rsrc((void*)a.O, 0, 0x7fffffff, 0x00020000);
    u32x4 o[2];
    int lz = lane; asm volatile("" : "+v"(lz));
#pragma unroll
    for (int i = 0; i < 2; ++i) {
      const u32x4 v = *(const u32x4*)(smem + STG + (wave * 16 + i * 8 + (lz >> 3)) * 128 + (((lz & 7) ^ (lz >> 3)) << 4));
#pragma unroll
      for (int j = 0; j < 4; ++j) { const unsigned r = res[i][j], q = v[j]; o[i][j] = pk_bf16(bflo(r) + bflo(q), bfhi(r) + bfhi(q)); }
    }
    asm volatile("" : "+v"(o[0]), "+v"(o[1]));
#pragma unroll
#define KN_OAUX 16
    for (int i = 0; i < 2; ++i) __builtin_amdgcn_raw_buffer_store_b128(o[i], rO, orow, i * 8 * D_ * 2, KN_OAUX);
    __builtin_amdgcn_sched_barrier(0);
    asm volatile("s_nop 1" ::: "memory");
    __builtin_amdgcn_sched_barrier(0);
    if (bid == 0 && tid == 0) set_flag(a.sync + FEP, tag);
  }
  STAMP(7);
  STAMP(8);
  STAMP(3);
#endif
}

struct Ptrs { const bf16_t *x, *wqkv, *wo, *wg, *wu, *wd; bf16_t* out; char* ws; };
#define DBG(i)
#define WS_O ((bf16_t*)(p.ws + (8u << 20)))
#define WS_SL ((float*)WS_O)
#define WS_H ((bf16_t*)(p.ws + (16u << 20)))
#define WS_ST ((float*)(p.ws + (24u << 20)))
#define WS_CS ((float*)(p.ws + (25u << 20)))
#define WS_SY ((unsigned*)(p.ws + (26u << 20)))
#define WS_A ((bf16_t*)(p.ws + (28u << 20)))
#if defined(__HIP_DEVICE_COMPILE__) && !defined(KN_NOWARM)
#define ARG_WARM(v) do { asm volatile("s_load_dword %0, %1, 0x0" : "=s"(v) : "s"(__builtin_amdgcn_kernarg_segment_ptr())); __builtin_amdgcn_sched_barrier(0); } while (0)
#else
#define ARG_WARM(v)
#endif
struct Args4 { GemmArgs q, b, c, d; };
extern "C" __global__ __launch_bounds__(512, 2) void k_all(Ptrs p) {
  unsigned warm = 0;
  ARG_WARM(warm);
  p.ws += -(unsigned)(size_t)p.ws & 0x1fffffu;
  const Args4 a{GemmArgs{p.x, p.wqkv, nullptr, nullptr, WS_O, (float*)WS_H, p.wg, p.wu, WS_CS, p.wd, p.wo, WS_SY, (float*)WS_A, warm DBG(0)},
    GemmArgs{WS_O, p.wo, nullptr, p.x, WS_H, WS_ST, nullptr, nullptr, nullptr, p.wd, nullptr, WS_SY, nullptr, 0u DBG(1)},
    GemmArgs{WS_H, p.wg, p.wu, p.out, WS_A, WS_ST, nullptr, nullptr, WS_CS, p.wd, nullptr, WS_SY, WS_SL, 0u DBG(2)},
    GemmArgs{WS_A, p.wd, nullptr, WS_H, p.out, nullptr, nullptr, nullptr, nullptr, nullptr, nullptr, WS_SY, WS_SL, 0u DBG(3)}};
  const unsigned tag = k1_phase(a.q);
  wo_phase(a.b, a.c, tag);
#define KN_EH 10
#define KN_TA 2
#define KN_TB 2
  auto pre = [&](auto tc) { dn_phase<1, KN_EH, KN_TA, KN_TB, decltype(tc)::value>(a.d, tag); };
  glu_phase(a.c, tag, pre);
  dn_phase<2, KN_EH, KN_TA, KN_TB>(a.d, tag);
#if defined(DIAG) && defined(__HIP_DEVICE_COMPILE__)
  lds_barrier();
  if (threadIdx.x < 64 && threadIdx.x != 12) { int v = ds_r32(SNAP + 320 + threadIdx.x * 4); asm volatile("s_waitcnt lgkmcnt(0)" : "+v"(v)); a.q.dbg[(threadIdx.x >> 4) * 8192 + blockIdx.x * 16 + (threadIdx.x & 15)] = (unsigned)v; }
#endif
}

extern "C" __global__ void k_zend() {}

extern "C" void kernel_launch(void* const* d_in, const int* in_sizes, int n_in, void* d_out, int out_size, void* d_ws,
                              size_t ws_size, hipStream_t stream) {
  const Ptrs p{(const bf16_t*)d_in[0], (const bf16_t*)d_in[1], (const bf16_t*)d_in[2], (const bf16_t*)d_in[4], (const bf16_t*)d_in[5], (const bf16_t*)d_in[6], (bf16_t*)d_out, (char*)d_ws};
  constexpr int L_all = SNAP + 64 + 512;
  static_assert(SNAP >= 3 * 256 * 128 + 4 * 14592 + 4096 + 1024 + 256 + 512 + 128 + 64 && L_all <= 163840, "LDS");
  (void)hipFuncSetAttribute((const void*)k_all, hipFuncAttributeMaxDynamicSharedMemorySize, L_all);
  k_all<<<256, 512, L_all, stream>>>(p);
}
```

```cpp
#include <hip/hip_runtime.h>
#include <hip/hip_bf16.h>
#include <stdint.h>
#include <type_traits>

typedef unsigned short bf16_t;
typedef short bf16x8 __attribute__((ext_vector_type(8)));
typedef short s16x4 __attribute__((ext_vector_type(4)));
typedef float f32x4 __attribute__((ext_vector_type(4)));
typedef float f32x2 __attribute__((ext_vector_type(2)));
typedef unsigned u32x2 __attribute__((ext_vector_type(2)));
typedef unsigned u32x4 __attribute__((ext_vector_type(4)));
typedef __bf16 bf16x2_t __attribute__((ext_vector_type(2)));
typedef __attribute__((address_space(3))) void* lds_void_ptr;
typedef __attribute__((address_space(3))) char* lds_char_ptr;

constexpr int S_ = 2048, D_ = 1024, DF_ = 3584;
#define KN_CT 7
#define KN_HQ 5
#define KN_KT 4
#define KN_RSA 262144
#define KN_NUMA 3
constexpr int RSH = (KN_NUMA & 1) ? 65536 : 32768, RSA = (KN_NUMA & 2) ? KN_RSA : 114688;
__device__ __forceinline__ int rbH(int rb, int sd) { return (KN_NUMA & 1) ? (rb & 63) * RSH + (((sd ^ (rb >> 5)) & 1) << 13) : rb * RSH; }
__device__ __forceinline__ int rbA(int rb, int sd) { return (KN_NUMA & 2) ? ((rb >> 3) & 7) * 2097152 + (rb & 7) * RSA + (((sd ^ (rb >> 3)) & 1) << 13) : rb * RSA; }
__device__ __forceinline__ constexpr int omH(int o) { return (KN_NUMA & 1) ? o + (o & ~8191) : o; }
__device__ __forceinline__ constexpr int omA(int o) { return (KN_NUMA & 2) ? o + (o & ~8191) : o; }

__device__ __forceinline__ float bflo(unsigned v) { return __uint_as_float(v << 16); }
__device__ __forceinline__ float bfhi(unsigned v) { return __uint_as_float(v & 0xffff0000u); }
__device__ __forceinline__ unsigned pk_bf16(float lo, float hi) {
  f32x2 v = {lo, hi};
  bf16x2_t b = __builtin_convertvector(v, bf16x2_t);
  return __builtin_bit_cast(unsigned, b);
}
__device__ __forceinline__ float rbf(float v) { return bflo(pk_bf16(v, v)); }

template <int STRIDE> __device__ __forceinline__ int wswz(int row) {
  if constexpr (STRIDE % 256 == 0) return ((row & 3) | (((row >> 3) & 1) << 2)) << 1;
  else if constexpr (STRIDE % 256 == 128) return (((row >> 1) & 1) | (((row >> 3) & 1) << 1)) << 1;
  else return ((row >> 3) & 1) << 1;
}

template <int OFF> __device__ __forceinline__ s16x4 ds_tr(unsigned addr) {
  s16x4 r; asm volatile("ds_read_b64_tr_b16 %0, %1 offset:%2" : "=v"(r) : "v"(addr), "n"(OFF)); return r;
}
template <int OFF> __device__ __forceinline__ bf16x8 ds_b128(unsigned addr) {
  bf16x8 r; asm volatile("ds_read_b128 %0, %1 offset:%2" : "=v"(r) : "v"(addr), "n"(OFF)); return r;
}
template <int OFF> __device__ __forceinline__ f32x2 ds_b64(unsigned addr) {
  f32x2 r; asm volatile("ds_read_b64 %0, %1 offset:%2" : "=v"(r) : "v"(addr), "n"(OFF)); return r;
}
__device__ __forceinline__ float sum8(float v) {
  v += __int_as_float(__builtin_amdgcn_update_dpp(0, __float_as_int(v), 0x128, 0xf, 0xf, false));
  auto a = __builtin_amdgcn_permlane16_swap(__float_as_uint(v), __float_as_uint(v), false, false);
  v = __uint_as_float(a[0]) + __uint_as_float(a[1]);
  auto b = __builtin_amdgcn_permlane32_swap(__float_as_uint(v), __float_as_uint(v), false, false);
  return __uint_as_float(b[0]) + __uint_as_float(b[1]);
}
__device__ __forceinline__ float sum8l(float v) {
  v += __int_as_float(__builtin_amdgcn_update_dpp(0, __float_as_int(v), 0xb1, 0xf, 0xf, false));
  v += __int_as_float(__builtin_amdgcn_update_dpp(0, __float_as_int(v), 0x4e, 0xf, 0xf, false));
  return v + __int_as_float(__builtin_amdgcn_update_dpp(0, __float_as_int(v), 0x141, 0xf, 0xf, false));
}
__device__ __forceinline__ int ds_r32(unsigned addr) { int r; asm volatile("ds_read_b32 %0, %1" : "=v"(r) : "v"(addr)); return r; }
__device__ __forceinline__ void ds_w32(unsigned addr, int v) { asm volatile("ds_write_b32 %0, %1" :: "v"(addr), "v"(v) : "memory"); }
template <int N> __device__ __forceinline__ void wait_vm() { asm volatile("s_waitcnt vmcnt(%0)" ::"n"(N) : "memory"); }
__device__ __forceinline__ void raw_barrier() { asm volatile("" ::: "memory"); __builtin_amdgcn_s_barrier(); asm volatile("" ::: "memory"); }
__device__ __forceinline__ void loop_barrier() { raw_barrier(); }
__device__ __forceinline__ void lds_barrier() { asm volatile("s_waitcnt lgkmcnt(0)" ::: "memory"); __builtin_amdgcn_s_barrier(); asm volatile("" ::: "memory"); }

__device__ __forceinline__ u32x4 mk_rsrc(const void* p) { const unsigned long long v = (unsigned long long)p; return (u32x4){(unsigned)v, (unsigned)(v >> 32) & 0xffffu, 0x7fffffffu, 0x00020000u}; }
#define LD1K(v, so, off, sc) "buffer_load_dwordx4 " v ", %1, " so " offen offset:" off sc " lds\n\t"
template <int WAIT> __device__ __forceinline__ void ld_w4(unsigned m, u32x4 r, const unsigned (&v)[4], unsigned s0, unsigned s1) {
  if constexpr (WAIT >= 0) asm volatile("s_mov_b32 m0, %0\n\ts_waitcnt vmcnt(%8)\n\ts_barrier\n\t" LD1K("%2", "%6", "0", "") LD1K("%3", "%7", "1024", "") LD1K("%4", "%6", "2048", "") LD1K("%5", "%7", "3072", "")
                                        :: "s"(m), "s"(r), "v"(v[0]), "v"(v[1]), "v"(v[2]), "v"(v[3]), "s"(s0), "s"(s1), "n"(WAIT) : "memory", "m0");
  else asm volatile("s_mov_b32 m0, %0\n\ts_nop 0\n\t" LD1K("%2", "%6", "0", "") LD1K("%3", "%7", "1024", "") LD1K("%4", "%6", "2048", "") LD1K("%5", "%7", "3072", "")
                    :: "s"(m), "s"(r), "v"(v[0]), "v"(v[1]), "v"(v[2]), "v"(v[3]), "s"(s0), "s"(s1) : "memory", "m0");
}
template <int WAIT> __device__ __forceinline__ void ld_w2(unsigned m, u32x4 r, const unsigned (&v)[4], unsigned s0, unsigned s1) {
  if constexpr (WAIT >= 0) asm volatile("s_mov_b32 m0, %0\n\ts_waitcnt vmcnt(%6)\n\ts_barrier\n\t" LD1K("%2", "%4", "0", "") LD1K("%3", "%5", "1024", "")
                                        :: "s"(m), "s"(r), "v"(v[0]), "v"(v[1]), "s"(s0), "s"(s1), "n"(WAIT) : "memory", "m0");
  else asm volatile("s_mov_b32 m0, %0\n\ts_nop 0\n\t" LD1K("%2", "%4", "0", "") LD1K("%3", "%5", "1024", "") :: "s"(m), "s"(r), "v"(v[0]), "v"(v[1]), "s"(s0), "s"(s1) : "memory", "m0");
}
__device__ __forceinline__ void ld_x4(unsigned m, u32x4 r, unsigned v0, unsigned v1, unsigned so) {
  asm volatile("s_mov_b32 m0, %0\n\ts_nop 0\n\t" LD1K("%2", "%4", "0", " sc1") LD1K("%2", "%4", "1024", " sc1") LD1K("%3", "%4", "2048", " sc1") LD1K("%3", "%4", "3072", " sc1")
               :: "s"(m), "s"(r), "v"(v0), "v"(v1), "s"(so) : "memory", "m0");
}
__device__ __forceinline__ unsigned long long ld_x4c(unsigned m, u32x4 r, unsigned v0, unsigned v1, unsigned so, unsigned ca) {
  u32x4 a, b, c, d; unsigned long long f;
  asm volatile("ds_read_b128 %1, %10\n\tds_read_b128 %2, %10 offset:1024\n\tds_read_b128 %3, %10 offset:2048\n\tds_read_b128 %4, %10 offset:3072\n\ts_mov_b32 m0, %5\n\ts_nop 0\n\t"
               "buffer_load_dwordx4 %7, %6, %9 offen sc1 lds\n\tbuffer_load_dwordx4 %7, %6, %9 offen offset:1024 sc1 lds\n\ts_waitcnt lgkmcnt(0)"
               : "=s"(f), "=&v"(a), "=&v"(b), "=&v"(c), "=&v"(d) : "s"(m), "s"(r), "v"(v0), "v"(v1), "s"(so), "v"(ca) : "memory", "m0");
  unsigned x = max(max(a[0], a[1]), a[2]), y = max(max(b[0], b[1]), b[2]); x = max(max(x, a[3]), c[0]); y = max(max(y, b[3]), d[0]);
  asm volatile("s_mov_b32 m0, %2\n\ts_nop 0\n\tbuffer_load_dwordx4 %4, %3, %5 offen offset:2048 sc1 lds" : "+v"(x), "+v"(y) : "s"(m), "s"(r), "v"(v1), "s"(so) : "memory", "m0");
  x = max(max(x, c[1]), c[2]); y = max(max(y, d[1]), d[2]); x = max(max(x, c[3]), d[3]);
  f = __builtin_amdgcn_ballot_w64(max(x, y) == 0xffffffffu);
  asm volatile("s_mov_b32 m0, %1\n\ts_nop 0\n\tbuffer_load_dwordx4 %3, %2, %4 offen offset:3072 sc1 lds" : "+s"(f) : "s"(m), "s"(r), "v"(v1), "s"(so) : "memory", "m0");
  return f;
}
template <int LO, int SO, bool SUB, bool SC = false> __device__ __forceinline__ void ld_ca(unsigned mb, u32x4 r, unsigned v, unsigned sd) {
  unsigned t;
  if constexpr (SUB) asm volatile("s_add_i32 m0, %1, %4\n\ts_sub_i32 %0, %5, %6\n\tbuffer_load_dwordx4 %3, %2, %0 offen lds" : "=&s"(t) : "s"(mb), "s"(r), "v"(v), "n"(LO), "n"(SO), "s"(sd) : "memory", "m0", "scc");
  else if constexpr (SC) asm volatile("s_add_i32 m0, %1, %4\n\ts_add_i32 %0, %6, %5\n\tbuffer_load_dwordx4 %3, %2, %0 offen sc1 lds" : "=&s"(t) : "s"(mb), "s"(r), "v"(v), "n"(LO), "n"(SO), "s"(sd) : "memory", "m0", "scc");
  else asm volatile("s_add_i32 m0, %1, %4\n\ts_add_i32 %0, %6, %5\n\tbuffer_load_dwordx4 %3, %2, %0 offen lds" : "=&s"(t) : "s"(mb), "s"(r), "v"(v), "n"(LO), "n"(SO), "s"(sd) : "memory", "m0", "scc");
}
template <int LO, int SO> __device__ __forceinline__ void ld_cx(unsigned mb, u32x4 r, unsigned v, unsigned sx) {
  unsigned t;
  asm volatile("s_add_i32 m0, %1, %4\n\ts_xor_b32 %0, %6, %5\n\tbuffer_load_dwordx4 %3, %2, %0 offen lds" : "=&s"(t) : "s"(mb), "s"(r), "v"(v), "n"(LO), "n"(SO), "s"(sx) : "memory", "m0", "scc");
}
template <int I, int N, class F> __device__ __forceinline__ void static_for(F&& f) {
  if constexpr (I < N) { f(std::integral_constant<int, I>{}); static_for<I + 1, N>(f); }
}
#define CODE_ADDR(v, sym) asm volatile("s_getpc_b64 s[40:41]\n\ts_add_u32 s40, s40, " sym "@rel32@lo+4\n\ts_addc_u32 s41, s41, " sym "@rel32@hi+12\n\ts_mov_b64 %0, s[40:41]" : "=s"(v) :: "s40", "s41", "scc")
#define MERGED 1
#define DELAY_AT(i)
constexpr int SNAP = 162752;
#define KN_S1C 8
#define KN_S1P 3
constexpr int STAB = 786432, STL = 3584;
#define FHM(p) ((p) * 256)
#define FRC(p) ((p) * 256 + 64)
#define FAM(p) ((p) * 256 + 128)
#define FCS(t) (2048 + (t) * 32)
#define FLN(k, i) ((1 + 2 * (k) + (((i) >> 6) & 1)) * 4096 + ((~sd & 1) << 11) + ((i) & 63) * 32)
#define FLO(t) FLN(0, (t) * 16)
#define FHF(p) FLN(1, (p) * 32)
#define FAE(p) FLN(2, (p) * 32)
#define FPR(b) FLN(3, ((b) & 3) * 32 + ((b) >> 3))
#define SIDE (((xcd >> 2) ^ tag) & 1)
constexpr int VTAB = STAB + 32768, PRB = 1048576;
constexpr int FEP = 3072;
constexpr unsigned long long LO32 = 0xffffffffull, HI32 = LO32 << 32;
#define KN_V 5
#define KN_D 2
#define KN_PF 0
#define KN_TRY 6
typedef __attribute__((address_space(1))) unsigned* gu32;
__device__ __forceinline__ void set_flag(unsigned* p, unsigned tag) { __hip_atomic_store((gu32)p, tag, __ATOMIC_RELAXED, __HIP_MEMORY_SCOPE_AGENT); }
__device__ __forceinline__ bool missing(unsigned v, unsigned tag, unsigned long long mask) { return (__builtin_amdgcn_ballot_w64(v != tag) & mask) != 0; }
__device__ __forceinline__ void wait_flags(const unsigned* p, unsigned off, unsigned tag, unsigned long long mask) {
  unsigned v, polls = 0;
  do { asm volatile("s_nop 4\n\tglobal_load_dword %0, %1, %2 sc1\n\ts_waitcnt vmcnt(0)" : "=v"(v) : "v"(off), "s"(p) : "memory"); } while (missing(v, tag, mask) && ++polls < 4000000u);
}
__device__ __forceinline__ unsigned lane4() { unsigned l = __builtin_amdgcn_mbcnt_hi(~0u, __builtin_amdgcn_mbcnt_lo(~0u, 0u)) * 4u; asm volatile("" : "+v"(l)); return l; }
__device__ __forceinline__ bool lds_missing(unsigned addr, unsigned tag, unsigned long long mask) {
  int v = ds_r32(addr + lane4());
  asm volatile("s_waitcnt lgkmcnt(0)" : "+v"(v) :: "memory");
  return missing((unsigned)v, tag, mask);
}
constexpr int ACNT = SNAP - 60;
__device__ __forceinline__ void lds_inc(unsigned addr) { if (__builtin_amdgcn_mbcnt_hi(~0u, __builtin_amdgcn_mbcnt_lo(~0u, 0u)) == 0) asm volatile("ds_add_u32 %0, %1" :: "v"(addr), "v"(1) : "memory"); }
constexpr int FPAD = SNAP - 704;
__device__ __forceinline__ void lds_wait8(unsigned addr, int n = 8) {
  int dv; unsigned polls = 0;
  do { dv = ds_r32(addr); asm volatile("s_waitcnt lgkmcnt(0)" : "+v"(dv) :: "memory"); } while (__builtin_amdgcn_readfirstlane(dv) < n && ++polls < 40000000u);
}
template <int N> __device__ __forceinline__ bool marked_in(bf16x8 (&v)[N]) {
  asm volatile("s_waitcnt lgkmcnt(0)" ::: "memory");
  unsigned m = 0;
#pragma unroll
  for (int i = 0; i < N; ++i) {
    asm volatile("" : "+v"(v[i]));
    const u32x4 u = __builtin_bit_cast(u32x4, v[i]);
    m = max(max(m, u[0]), u[1]); m = max(max(m, u[2]), u[3]);
  }
  return __builtin_amdgcn_ballot_w64(m == 0xffffffffu) != 0;
}
template <int N, int STR = 4096> __device__ __forceinline__ bool marked(unsigned addr) {
  bf16x8 v[N];
  static_for<0, N>([&](auto ic) { constexpr int i = decltype(ic)::value; v[i] = ds_b128<i * STR>(addr); });
  return marked_in<N>(v);
}

struct GemmArgs {
  const bf16_t* X;
  const bf16_t* W0;
  const bf16_t* W1;
  const bf16_t* R;
  bf16_t* O;
  float* st;
  const bf16_t* CW0;
  const bf16_t* CW1;
  float* cs;
  const bf16_t* PF0;
  const bf16_t* PF1;
  unsigned* sync;
  float* slab;
  unsigned warm;
};
#define STAMP(i)
#define STAMPW(i, w)

#define STAMPV(i) STAMP(i)
template <int MF, int NF> struct Frags { bf16x8 x[MF]; s16x4 lo[NF], hi[NF]; };

constexpr int WOB = 55296;
constexpr int wo_pos(int s) { return s < 6 ? WOB + s * 16384 : (s - 6) * 16384; }
#define KN_GP 7
#define KN_GX 1
#define KN_GL 0
#define KN_EW 1
#define KN_VP 6
#define KN_VQ 0
#define KN_WP 1
#define KN_GR 24
#define KN_GE 26
template <int NF, int GR> struct SchedP {
  static constexpr int NP = 4 * NF, NR = NP + 8;
  static constexpr int pa(int p) { return (p + NP - 12) % NP; }
  static constexpr int pn(int a) { return a < 8 ? (a ^ (a >> 1)) & 1 : (a < NP - 8 ? a / 4 : NF - 2 + ((a ^ (a >> 1)) & 1)); }
  static constexpr int pm(int a) { return a < 8 ? a >> 1 : (a < NP - 8 ? ((a & 4) ? a & 3 : 3 - (a & 3)) : (a - NP + 8) >> 1); }
  static constexpr int gap(int r) { return r < 8 ? 2 + r : (r < 12 ? 12 + 4 * (r - 8) : (r < NP ? 26 + (r - 12) * GR / (NP - 12) : 11 + 2 * ((r - NP) & ~1) - (r & 1))); }
  static constexpr int need(int r) { return r < 8 ? 12 : (r < NP ? (12 + (r & ~3) < NP ? 12 + (r & ~3) : NP) : 12 + ((r - NP) & ~1)); }
  static constexpr int cnt(int p, int e = 0) {
    int g = -1, q = -1;
    for (int r = 0; r < NR; ++r) if (need(r) == p && (gap(r) > g || (gap(r) == g && r > q))) { g = gap(r); q = r; }
    if (g < 0) return -1;
    int c = 0;
    for (int r = 0; r < NR; ++r) if (gap(r) < 2 * p - e - (e > 0) && (gap(r) > g || (gap(r) == g && r > q))) ++c;
    return c < 15 ? c : 15;
  }
  static constexpr bool last(int a) { for (int b = a + 1; b < NP; ++b) if (pn(b) == pn(a)) return false; return true; }
};
using SchedG = SchedP<7, KN_GR>;
template <class HK>
__device__ __forceinline__ void glu_phase(const GemmArgs& a, unsigned tag, HK hook) {
#if defined(__HIP_DEVICE_COMPILE__)
  constexpr int LDW = DF_, KU = 32, NT = 16, MF = 4, NF = 7, NLW = 4, XI = 4, WI = 7;
  constexpr int UPOFF = KU * 224 + 16;
  constexpr int XT = 256 * 128, WU = (UPOFF + KU * 224 + 255) / 256 * 256;
  constexpr int XBASE = 0, WBASE = 3 * XT, RING = WBASE + 4 * WU;
  extern __shared__ __attribute__((aligned(16))) char smem[];
  STAMP(0);
  const unsigned lds0 = (unsigned)(size_t)(lds_char_ptr)smem;
  const int tid = threadIdx.x, lane = tid & 63;
  const int wave = __builtin_amdgcn_readfirstlane(tid >> 6);
  const int wm = wave >> 1, wn = wave & 1;
  const int bid = blockIdx.x, xcd = bid & 7, idx = bid >> 3;
  const int tm = (xcd >> 2) * 4 + (idx >> 3), tn = (xcd & 3) * 8 + (idx & 7);
  const int m0 = tm * 256, sd = SIDE;
  const int krot = (tm >> 2) * 8;
  const int fr = lane & 15, fq = lane >> 4;
  const int wq = (lane >> 2) & 3, wp = lane & 3;

  const int panel = m0 >> 8;
  constexpr int TCH = RING + 5120;
  constexpr int DONE = TCH + 896;
  auto done_inc = [&]() { if (lane == 0) asm volatile("ds_add_u32 %0, %1" :: "v"(lds0 + DONE), "v"(1) : "memory"); };
  auto touch = [&](const void* base, unsigned voff, unsigned soff, unsigned lim = 0x7fffffffu) {
    __builtin_amdgcn_raw_ptr_buffer_load_lds(__builtin_amdgcn_make_buffer_rsrc((void*)base, 0, lim, 0x00020000), (lds_void_ptr)(size_t)(lds0 + TCH), 4, voff, soff, 0, 0);
  };
  const auto rO = __builtin_amdgcn_make_buffer_rsrc((void*)a.O, 0, 0x7fffffff, 0x00020000);

  unsigned xsrc[XI], wsrc[WI];
#pragma unroll
  for (int i = 0; i < XI; ++i) { const int j = (wave & 3) + NLW * i; xsrc[i] = (unsigned)(rbH(m0 >> 4, sd) + (j >> 1) * RSH + (j & 1) * 1024 + lane * 16); }
#pragma unroll
  for (int i = 0; i < WI; ++i) {
    const int b = i * 1024 + lane * 16, p = b / 224, pos = (b % 224) >> 4;
    const int row = 8 * ((p & 15) >> 2) + (p & 3) + 4 * (p >> 4);
    wsrc[i] = (unsigned)(row * LDW + tn * 112 + pos * 8) * 2u;
  }
  auto ldX = [&](int i, int tile, int part, int slot) {
    __builtin_amdgcn_raw_ptr_buffer_load_lds(__builtin_amdgcn_make_buffer_rsrc((void*)a.X, 0, 0x7fffffff, 0x00020000), (lds_void_ptr)(size_t)(lds0 + XBASE + slot * XT + (part * 16 + (wave & 3) + NLW * i) * 1024), 16, xsrc[i],
                                             omH(((tile + krot) & 15) * 2048) + part * 8 * RSH, 0, 16);
  };
  auto ldW = [&](int i, int tile, int slot) {
    __builtin_amdgcn_raw_ptr_buffer_load_lds(__builtin_amdgcn_make_buffer_rsrc((void*)((wave & 1) ? a.W1 : a.W0), 0, 0x7fffffff, 0x00020000),
                                             (lds_void_ptr)(size_t)(lds0 + WBASE + (slot + (wave >> 1)) * WU + (wave & 1) * UPOFF + i * 1024), 16, wsrc[i],
                                             (((tile + krot) & 15) * 2 + (wave >> 1)) * KU * LDW * 2, 0, 0);
  };
  auto issueX = [&](int tile, int part, int slot) {
#pragma unroll
    for (int i = 0; i < XI; ++i) ldX(i, tile, part, slot);
  };

  f32x4 acc[NF][MF];
#pragma unroll
  for (int n = 0; n < NF; ++n)
#pragma unroll
    for (int m = 0; m < MF; ++m) acc[n][m] = (f32x4){0.f, 0.f, 0.f, 0.f};
  f32x4 accs = {0.f, 0.f, 0.f, 0.f};

  const unsigned xa0 = lds0 + XBASE + wm * MF * 2048 + lane * 16;
  unsigned wa[2];
#pragma unroll
  for (int c = 0; c < 2; ++c) wa[c] = lds0 + WBASE + (4 * fq + wq) * 224 + wn * 112 + (c == 0 ? wp * 8 : 96 + (wp & 1) * 8 + (wp >> 1) * UPOFF);

  using I0 = std::integral_constant<int, 0>; using I1 = std::integral_constant<int, 1>;
  using T = std::true_type; using F = std::false_type;
  typedef Frags<MF, NF> FR;
  struct RdCtx { unsigned xa, wb[2]; };
  auto mkctx = [&](unsigned xoffs, unsigned woffs) { return RdCtx{xa0 + xoffs, {wa[0] + woffs, wa[1] + woffs}}; };
  FR fa, fb;
  auto rdg = [&](const RdCtx& c, auto rc) {
    constexpr int r = decltype(rc)::value;
    if constexpr (r >= 28) { constexpr int m = (r - 28) >> 1; if constexpr (r & 1) fb.x[m] = ds_b128<m * 2048 + 1024>(c.xa); else fa.x[m] = ds_b128<m * 2048>(c.xa); }
    else {
      constexpr int n = r >> 2, cc = n == 6, o = (n == 6 ? 0 : (n >> 1) * 32 + (n & 1) * UPOFF) + ((r >> 1) & 1) * WU + (r & 1) * 16 * 224;
      FR& f = (r & 2) ? fb : fa;
      if constexpr (r & 1) f.hi[n] = ds_tr<o>(c.wb[cc]); else f.lo[n] = ds_tr<o>(c.wb[cc]);
    }
  };
  auto ping = [&](auto pc) {
    static_for<0, 36>([&](auto rc) { constexpr int r = decltype(rc)::value;
      if constexpr (SchedG::need(r) == decltype(pc)::value) {
        FR& f = (r >= 28 ? r & 1 : r & 2) ? fb : fa;
        if constexpr (r >= 28) { bf16x8& q = f.x[(r - 28) >> 1]; asm volatile("" : "+v"(q)); } else { s16x4& q = (r & 1) ? f.hi[r >> 2] : f.lo[r >> 2]; asm volatile("" : "+v"(q)); } } });
  };
  auto pairs = [&](auto PBc, auto PEc, auto RDc, auto CSc, const RdCtx& c, auto&& gapf) {
    constexpr bool RD = decltype(RDc)::value; constexpr int CS = decltype(CSc)::value;
    static_for<decltype(PBc)::value, decltype(PEc)::value>([&](auto pc) {
      constexpr int p = decltype(pc)::value, a = (p + 16) % 28, n = SchedG::pn(a), m = SchedG::pm(a);
      if constexpr (SchedG::cnt(p) >= 0) { asm volatile("s_waitcnt lgkmcnt(%0)" :: "n"(SchedG::cnt(p)) : "memory"); ping(pc); }
      static_for<0, 2>([&](auto hc) {
        constexpr int g = 2 * p + decltype(hc)::value;
        FR& u = ((a ^ g) & 1) ? fb : fa;
        acc[n][m] = __builtin_amdgcn_mfma_f32_16x16x32_bf16(__builtin_shufflevector(u.lo[n], u.hi[n], 0, 1, 2, 3, 4, 5, 6, 7), u.x[m], acc[n][m], 0, 0, 0);
        __builtin_amdgcn_sched_barrier(0);
        if constexpr (CS >= 0 && SchedG::last(a) && (g & 1)) {
          const unsigned sv = fr == n ? 0x3f803f80u : 0u;
          const u32x4 s4 = {sv, sv, sv, sv};
          FR& w = CS ? fb : fa;
          accs = __builtin_amdgcn_mfma_f32_16x16x32_bf16(__builtin_shufflevector(w.lo[n], w.hi[n], 0, 1, 2, 3, 4, 5, 6, 7), __builtin_bit_cast(bf16x8, s4), accs, 0, 0, 0);
          __builtin_amdgcn_sched_barrier(0);
        }
        if constexpr (RD) static_for<0, 36>([&](auto rc) { if constexpr (SchedG::gap(decltype(rc)::value) == g) rdg(c, rc); });
        if constexpr (g & 1) gapf(pc);
        __builtin_amdgcn_sched_barrier(0);
      });
    });
    if constexpr (RD) { asm volatile("s_waitcnt lgkmcnt(0)" ::: "memory"); ping(std::integral_constant<int, 28>{}); }
  };

  f32x4* tb_row = (f32x4*)(smem + RING);
  float* tb_cs = (float*)(tb_row + 256);
  constexpr int RTAB = 524288;
  auto glu_stats_issue = [&]() {
    {
      const auto rS = __builtin_amdgcn_make_buffer_rsrc((void*)a.st, 0, 0x7fffffff, 0x00020000);
      if (wave == 7) __builtin_amdgcn_raw_ptr_buffer_load_lds(rS, (lds_void_ptr)(size_t)(lds0 + RING + 3072), 16, ((lane >> 2) * S_ + m0 + tn * 8 + (lane & 3) * 2) * 8, 0, 0, 16);
    }
  };
  auto glu_stats = [&]() {
    {
      int lx = lane; asm volatile("" : "+v"(lx));
      const unsigned sa = lds0 + RING + 3072 + lx * 8;
      f32x2 p0 = ds_b64<0>(sa), p1 = ds_b64<512>(sa);
      asm volatile("s_waitcnt lgkmcnt(0)" : "+v"(p0), "+v"(p1) :: "memory");
      float s0 = p0[0] + p1[0]; asm volatile("" : "+v"(s0), "+v"(p0));
      const float mu = sum8(s0) * (1.f / 16.f);
      const float v = sum8((p0[1] + p1[1]) + 64.f * ((p0[0] - mu) * (p0[0] - mu) + (p1[0] - mu) * (p1[0] - mu))) * (1.f / 1024.f) + 1e-5f;
      float rs = __builtin_amdgcn_rsqf(v), k2 = -2.3022082f * rs; asm volatile("" : "+v"(k2), "+v"(rs));
      const f32x4 rv = {mu, -0.10294324f * rs * rs * rs, k2, v};
      __builtin_amdgcn_raw_buffer_store_b128(__builtin_bit_cast(u32x4, rv), __builtin_amdgcn_make_buffer_rsrc((void*)a.cs, 0, 0x7fffffff, 0x00020000),
                                             lx < 8 ? (unsigned)(RTAB + (m0 + tn * 8 + lx) * 16) : 0x80000000u, 0, 16);
    }
  };
  auto epilogue = [&](auto EAc) __attribute__((always_inline)) {
  STAMP(2);

  {
    constexpr bool EARLY = decltype(EAc)::value;
    static_assert(DONE + 4 == ACNT && TCH + 256 == FPAD, "LDS");
#define KN_EP 1
    if constexpr (!EARLY) {
      wait_vm<0>();
      lds_wait8(lds0 + DONE);
      if (KN_EP) __builtin_amdgcn_s_setprio(2);
      STAMPW(9, 4);
      hook(I0{});
      STAMPW(10, 4);
    } else { if (KN_EP) __builtin_amdgcn_s_setprio(3); hook(I1{}); }
    asm volatile("" ::: "memory");
    f32x4 cg[4], cu[4], rw[MF], rw6[2];
    int lz = lane; asm volatile("" : "+v"(lz));
    const int fr = lz & 15, fq = lz >> 4;
#pragma unroll
    for (int p = 0; p < 4; ++p) {
      const int o = wn * 56 + p * 16 + (p == 3 ? (fq & 1) : fq) * 4;
      cg[p] = *(const f32x4*)(tb_cs + o); cu[p] = *(const f32x4*)(tb_cs + 112 + o);
    }
#pragma unroll
    for (int m = 0; m < MF; ++m) rw[m] = tb_row[(wm * MF + m) * 16 + fr];
#pragma unroll
    for (int q = 0; q < 2; ++q) rw6[q] = tb_row[(wm * MF + 2 * q + (fq >> 1)) * 16 + fr];
    const int gz = rbA((m0 >> 4) + wm * 4, sd) + fr * 16, g = tn * 2 + wn, g1 = omA(g * 1024);
    const int go0 = gz + (fq & 1) * RSA + (fq >> 1) * 256, go6 = gz + ((fq & 1) * 2 + (fq >> 1)) * RSA;
    unsigned hv[MF][NF];
    auto put = [&](unsigned a0, unsigned a1, unsigned b0, unsigned b1, int vo, int so) {
      const auto s0 = __builtin_amdgcn_permlane16_swap(a0, b0, false, false), s1 = __builtin_amdgcn_permlane16_swap(a1, b1, false, false);
      __builtin_amdgcn_raw_buffer_store_b128((u32x4){s0[0], s1[0], s0[1], s1[1]}, rO, vo, so, 16);
      __builtin_amdgcn_sched_barrier(0);
      asm volatile("s_nop 1" ::: "memory");
      __builtin_amdgcn_sched_barrier(0);
    };
#define STAGE(body) _Pragma("unroll") for (int i = 0; i < 2 * NN; ++i) { const int m = MB + i / NN, n = N0 + i % NN; const f32x4 k = n == 6 ? rw6[m >> 1] : rw[m]; body; } __builtin_amdgcn_sched_barrier(0)
    auto grp = [&](auto MBc, auto N0c, auto NNc, auto&& mid) {
      constexpr int MB = decltype(MBc)::value, N0 = decltype(N0c)::value, NN = decltype(NNc)::value;
      f32x2 G[2 * NN], U[2 * NN], P[2 * NN];
      __builtin_amdgcn_sched_barrier(0);
      STAGE({ const int e = (n == 6 ? m & 1 : n & 1) * 2; const f32x4 x = n == 6 ? acc[6][m & 2] : acc[n & 6][m]; G[i] = ((f32x2){x[e], x[e + 1]}) - k[0] * ((f32x2){cg[n >> 1][e], cg[n >> 1][e + 1]}); });
      STAGE(P[i] = G[i] * G[i]);
      STAGE(P[i] = P[i] * k[1] + k[2]);
      STAGE(P[i] = G[i] * P[i]);
      STAGE({ const int e = (n == 6 ? m & 1 : n & 1) * 2; const f32x4 x = n == 6 ? acc[6][m | 1] : acc[n | 1][m]; U[i] = ((f32x2){x[e], x[e + 1]}) - k[0] * ((f32x2){cu[n >> 1][e], cu[n >> 1][e + 1]}); });
      STAGE(P[i] = ((f32x2){__builtin_amdgcn_exp2f(P[i][0]), __builtin_amdgcn_exp2f(P[i][1])}));
      mid();
      __builtin_amdgcn_sched_barrier(0);
      STAGE(P[i] = P[i] * k[3] + k[3]);
      STAGE(P[i] = ((f32x2){__builtin_amdgcn_rcpf(P[i][0]), __builtin_amdgcn_rcpf(P[i][1])}));
      STAGE(U[i] = G[i] * U[i]);
      STAGE(U[i] = U[i] * P[i]);
      STAGE({ hv[m][n] = pk_bf16(U[i][0], U[i][1]); asm volatile("" : "+v"(hv[m][n])); });
    };
#undef STAGE
    using I2 = std::integral_constant<int, 2>; using I3 = std::integral_constant<int, 3>; using I4 = std::integral_constant<int, 4>;
    grp(I0{}, I0{}, I4{}, [&]() {});
    grp(I2{}, I0{}, I4{}, [&]() {
#pragma unroll
      for (int p = 0; p < 2; ++p) put(hv[0][2 * p], hv[0][2 * p + 1], hv[1][2 * p], hv[1][2 * p + 1], go0 + p * 512, g1); });
#pragma unroll
    for (int p = 0; p < 2; ++p) put(hv[2][2 * p], hv[2][2 * p + 1], hv[3][2 * p], hv[3][2 * p + 1], go0 + p * 512, g1 + 2 * RSA);
    asm volatile("" ::: "memory");
    __builtin_amdgcn_sched_barrier(0);
    STAMP(4);
    STAMPW(11, 4);
    if (KN_EP) __builtin_amdgcn_s_setprio(EARLY ? 1 : 0);
#define KN_AW 1
    auto arrived = [&](int w) { if (!EARLY && w == KN_AW) { wait_vm<0>(); lds_inc(lds0 + ACNT); STAMPW(6, 4); } };
    DELAY_AT(7);
    arrived(0);
#pragma unroll
    for (int q = 0; q < 2; ++q)
#pragma unroll
      for (int e = 0; e < 4; ++e) {
        const float x0 = acc[6][2 * q][e], x1 = acc[6][2 * q + 1][e];
        auto sw = __builtin_amdgcn_permlane32_swap(__float_as_uint(x0), __float_as_uint(x1), false, false);
        acc[6][2 * q][e] = __uint_as_float(sw[0]); acc[6][2 * q + 1][e] = __uint_as_float(sw[1]);
      }
    const int sb = omA((256 + g * 3) * 256), go4 = go0 + (fq >> 1) * (omA((257 + g * 3) * 256) - sb - 256);
    grp(I0{}, I4{}, I3{}, [&]() { arrived(1); });
    arrived(2);
    grp(I2{}, I4{}, I3{}, [&]() { arrived(3); put(hv[0][4], hv[0][5], hv[1][4], hv[1][5], go4, sb); });
    put(hv[2][4], hv[2][5], hv[3][4], hv[3][5], go4, sb + 2 * RSA);
    put(hv[0][6], hv[1][6], hv[2][6], hv[3][6], go6, omA((258 + g * 3) * 256));
    asm volatile("" ::: "memory");
    if constexpr (!EARLY) lds_inc(lds0 + ACNT + 4);
  }
  };
  using IM1 = std::integral_constant<int, -1>;

  {
    constexpr int WR = 2;
    if (wave == 5) { ds_w32(lds0 + DONE, 0); ds_w32(lds0 + DONE + 4, 0); ds_w32(lds0 + DONE + 8, 0); }
    auto check_tile = [&](int t) {
      int lx = lane; asm volatile("" : "+v"(lx));
      const unsigned ad = lds0 + XBASE + t * XT + (wave & 3) * 1024 + lx * 16;
      if (__builtin_expect(marked<8>(ad), 0)) {
        for (int tries = 0;; ++tries) {
          if (tries >= KN_TRY) wait_flags(a.sync + FHF(panel), (lane4() & 127) * 32, tag, LO32);
          issueX(t, 0, t); issueX(t, 1, t);
          wait_vm<0>();
          if (tries >= KN_TRY || !marked<8>(ad)) break;
        }
      }
    };
    auto first = [&](auto ic) { constexpr int i = decltype(ic)::value; if constexpr (i < 7) ldW(i, 1, 2 ^ WR); else ldX((i - 7) % XI, 2, (i - 7) / XI, 2); };
    auto run = [&](auto LDRc) __attribute__((always_inline)) {
      constexpr bool LDR = decltype(LDRc)::value;
      if constexpr (LDR) static_for<0, KN_GP>(first);
      int xs = 1;
#define KN_CTJ 11
      unsigned long long tb = (unsigned long long)a.PF0; unsigned tv = 0, tl = 0x7fffffffu, ts = 1835008u, tmask = 0; int tj = 1, tsh = 1;
      if constexpr (!LDR) {
        unsigned long long c0, c1; CODE_ADDR(c0, "k_all"); CODE_ADDR(c1, "k_zend");
#define KN_WJ 1
#define KN_WE 8
#define KN_DJ 6
        tj = KN_DJ;
        if (wave == 4) { tb = c0; tl = c1 - c0 < 262144ull ? (unsigned)(c1 - c0) : 0u; ts = 0; tmask = 1u << KN_CTJ; }
        if (wave == 5) tmask = 0x55u << (KN_DJ + (idx & 1));
        if (wave == 6) { tb = (unsigned long long)((idx & 16) ? a.W1 : a.W0); ts = 64 * LDW * 2; tmask = ((1u << (KN_WE - KN_KT)) - 1u) << KN_WJ; tj = KN_WJ - krot - KN_KT; tsh = 0; }
        const int l4 = lane / 14;
        tv = wave == 4 ? (unsigned)((lane * 32 + idx) * 128)
           : (lane >= 56 ? 0x80000000u : (wave == 5 ? (unsigned)(bid * 7168 + lane * 128) : (unsigned)(((idx & 15) * 4 + l4) * (LDW * 2) + (xcd & 3) * 1792 + (lane - l4 * 14) * 128)));
      }
      const int js = 2 * (tm & 3) + 1;
      const auto rC = __builtin_amdgcn_make_buffer_rsrc((void*)a.cs, 0, 0x7fffffff, 0x00020000);
      unsigned* ccnt = a.sync + FCS(tn);
#define KN_JR 10
#define KN_JS 2
      const int jf = wave == 4 ? NT - 4 : (wave == 7 ? KN_JR : 64);
      const unsigned* fcnt = wave == 4 ? ccnt : a.sync + FRC(panel);
      const int jm = (idx >> 4) == ((xcd >> 1) & 1) && (idx & 7) < KN_V ? 3 : 64;
      int mo = rbA((m0 >> 4) + wave * 4, sd) + omA(tn * 2048);
      unsigned evm = tmask | (8u << js) | (wave == 4 ? 3u << (NT - 4) : 0u) | (wave == 7 ? ((5u << KN_JS) | 2u | (11u << KN_JR)) : 0u);
      if constexpr (!LDR) asm volatile("" : "+s"(evm), "+s"(tmask), "+s"(tb), "+s"(tl), "+s"(ts), "+s"(tj), "+s"(tsh), "+v"(tv));
      wait_vm<LDR ? KN_GP : 0>();
      STAMPW(5, 0);
      check_tile(LDR ? 0 : 1);
      lds_barrier();
      if constexpr (!LDR) { if (wave == 4) __builtin_amdgcn_raw_ptr_buffer_load_lds(__builtin_amdgcn_make_buffer_rsrc((void*)(a.sync + FHF(panel)), 0, 4096, 0x00020000), (lds_void_ptr)(size_t)(lds0 + TCH + 256), 4, lane * 128, 0, 0, 16); }
      using I12 = std::integral_constant<int, 12>; using I28 = std::integral_constant<int, 28>;
      { const RdCtx c = mkctx(0, WR * WU);
        static_for<0, 24>([&](auto gc) { static_for<0, 36>([&](auto rc) { if constexpr (SchedG::gap(decltype(rc)::value) == decltype(gc)::value) rdg(c, rc); }); });
        STAMP(1);
        pairs(I12{}, I28{}, T{}, IM1{}, c, [&](auto pc) { if constexpr (LDR) static_for<KN_GP, 15>([&](auto ic) { if constexpr (12 + (decltype(ic)::value - KN_GP) * 15 / (15 - KN_GP) == decltype(pc)::value) first(ic); }); }); }
      constexpr int CFL = TCH + 256, CPB = XBASE + ((NT - 3) % 3) * XT;
      auto iter = [&](int j, auto LWc, auto LXc, auto WAITc, auto CSc) {
        constexpr bool LW = decltype(LWc)::value && LDR, LX = decltype(LXc)::value && LDR, LP = LW && !LX;
        if constexpr (LDR) {
          wait_vm<decltype(WAITc)::value>();
          if constexpr (LX) { if (__builtin_expect(j == 2, 0)) check_tile(2); }
          if constexpr (LX) {
            if ((unsigned)(j - jm) < 8u) {
              int lx = lane; asm volatile("" : "+v"(lx));
              unsigned on = 0xffffffffu; asm volatile("" : "+v"(on));
              __builtin_amdgcn_raw_buffer_store_b128((u32x4){on, on, on, on}, rO, lx * 16, mo, 16);
              mo += (j & 1) ? 1024 : RSA - 1024;
            }
            if (j == jm + 9) { if (lane == 0) set_flag(a.sync + FAM(panel) + (tn & 15) * 4 + wave, tag); }
          }
        }
        if constexpr (!LDR) {
          if (__builtin_expect((evm >> j) & 1, 0)) {
            if (KN_VP) __builtin_amdgcn_s_setprio(2);
            if (j == js + 3) {
              if ((tmask >> (j - 1)) & 1) wait_vm<1>(); else wait_vm<0>();
              if (lane == 0) set_flag(ccnt + tm * 4 + wave - 4, tag);
            }
            if (j == 1) glu_stats_issue();
            if (j == KN_JS && wave == 7) { wait_vm<0>(); glu_stats(); }
            if (j == KN_JS + 2 && wave == 7) {
              if (j == js + 2) wait_vm<1>(); else wait_vm<0>();
              if (lane == 0) set_flag((unsigned*)fcnt + tn, tag);
            }
            {
              int lx = lane; asm volatile("" : "+v"(lx));
              if (j == jf) __builtin_amdgcn_raw_ptr_buffer_load_lds(__builtin_amdgcn_make_buffer_rsrc((void*)fcnt, 0, 128, 0x00020000), (lds_void_ptr)(size_t)(lds0 + CFL + (wave & 1) * 256), 4, lx * 4, 0, 0, 16);
              if (j == jf + 1) {
                wait_vm<0>();
                if (lds_missing(lds0 + CFL + (wave & 1) * 256, tag, LO32)) wait_flags(fcnt, lx * 4, tag, LO32);
                if (wave == 7) {
#pragma unroll
                  for (int i = 0; i < 4; ++i) __builtin_amdgcn_raw_ptr_buffer_load_lds(rC, (lds_void_ptr)(size_t)(lds0 + RING + i * 1024), 16, lx * 16, RTAB + m0 * 16 + i * 1024, 0, 16);
                }
              }
              if (j == jf + 3) wait_vm<0>();
            }
            if ((tmask >> j) & 1) touch((const void*)tb, tv, (((j - tj) >> tsh) & 15) * ts, tl);
            if (KN_VP == 1) __builtin_amdgcn_s_setprio(0);
          }
          __builtin_amdgcn_sched_barrier(0);
        }
        int xp = xs + 2; if (xp >= 3) xp -= 3;
        const int ws = ((j & 1) * 2) ^ WR;
        auto lda = [&](int i) { ldW(i, j + 1, ws ^ 2); };
        auto ldb = [&](int i) {
            if constexpr (LX) ldX(i % XI, j + 2, i / XI, xp);
            else __builtin_amdgcn_raw_ptr_buffer_load_lds(rC, (lds_void_ptr)(size_t)(lds0 + CPB + wave * 8192 + i * 1024), 16, lane * 16, (tn * 2 + wn) * 7168 + i * 1024, 0, 16); };
        using NLA = std::integral_constant<int, LW ? WI : 0>; using NLB = std::integral_constant<int, LX ? 2 * XI : (LP ? 7 : 0)>;
        { const RdCtx c = mkctx(xs * XT, ws * WU);
          pairs(I0{}, I28{}, T{}, CSc, c, [&](auto pc) { constexpr int p = decltype(pc)::value;
            if constexpr (LDR && p == 0) loop_barrier();
            if constexpr (!LDR && KN_VP > 1 && p == KN_VP) __builtin_amdgcn_s_setprio(0);
            if constexpr (!LDR && KN_VQ && p == KN_VQ) __builtin_amdgcn_s_setprio(1);
            if constexpr (!LDR && KN_VQ && p == KN_GE) __builtin_amdgcn_s_setprio(0);
            if constexpr (!LDR && p == KN_GE) { asm volatile("s_waitcnt lgkmcnt(0)" ::: "memory"); if (decltype(WAITc)::value == 0 || j + 1 < NT) loop_barrier(); }
            static_for<0, NLA::value>([&](auto ic) { constexpr int i = decltype(ic)::value; if constexpr (i * 13 / 7 == p) lda(i); });
            static_for<0, NLB::value>([&](auto ic) { constexpr int i = decltype(ic)::value; if constexpr (14 + i * 13 / 8 == p) ldb(i); }); }); }
        xs = xs + 1; if (xs >= 3) xs = 0;
      };
      using WX_ = std::integral_constant<int, 2 * XI>;
      bf16x8 pv[8];
      if constexpr (LDR) {
        for (int j = 1; j < NT - 2; ++j) iter(j, T{}, T{}, WX_{}, IM1{});
#define KN_PRI 0
        if (KN_PRI == 2) __builtin_amdgcn_s_setprio(3);
        iter(NT - 2, T{}, F{}, WX_{}, IM1{});
        if (KN_PRI == 1) __builtin_amdgcn_s_setprio(3);
        iter(NT - 1, F{}, F{}, std::integral_constant<int, 7>{}, IM1{});
        wait_vm<0>();
        int lc = lane; asm volatile("" : "+v"(lc));
        const unsigned ca = lds0 + CPB + wave * 8192 + (lc >> 4) * 1792 + ((lc & 15) < 14 ? lc & 15 : 0) * 16;
        static_for<0, 8>([&](auto ic) { constexpr int i = decltype(ic)::value; pv[i] = ds_b128<i * 224>(ca); });
      } else {
        int j = 1;
        {
          if (wave == 4) {
            wait_vm<0>();
            if (lds_missing(lds0 + TCH + 256, tag, LO32)) wait_flags(a.sync + FHF(panel), (lane4() & 127) * 32, tag, LO32);
          }
        }
        loop_barrier();
#pragma unroll 1
        for (; j < js; ++j) iter(j, F{}, F{}, I0{}, IM1{});
        const int ja = wm == 2 ? js + 2 : js;
#pragma unroll 1
        for (; j < ja; ++j) iter(j, F{}, F{}, I0{}, I0{});
#pragma unroll 1
        for (; j < js + 2; ++j) iter(j, F{}, F{}, I0{}, I1{});
        { int lx = lane; asm volatile("" : "+v"(lx));
          __builtin_amdgcn_raw_buffer_store_b128(__builtin_bit_cast(u32x4, accs), rC, (lx & 15) < NF ? (unsigned)((tn * 4 + wn * 2) * 3584 + (tm * 2 + (wm & 1)) * 224
                  + ((lx & 15) < 6 ? (lx & 1) * 3584 + ((lx & 15) >> 1) * 64 + (lx >> 4) * 16 : (lx >> 5) * 3584 + 192 + (lx & 16))) : 0x80000000u, 0, 16); }
#pragma unroll 1
        for (; j < NT; ++j) iter(j, F{}, F{}, I1{}, IM1{});
        done_inc();
      }
      { const RdCtx c = mkctx(0, 0); pairs(I0{}, I12{}, F{}, IM1{}, c, [&](auto pc) {
          if constexpr (LDR && decltype(pc)::value == 4) {
            asm volatile("s_waitcnt lgkmcnt(0)" ::: "memory");
#pragma unroll
            for (int k = 0; k < 8; ++k) { bf16x8& q = pv[k]; asm volatile("" : "+v"(q)); }
            f32x4 cv = ((__builtin_bit_cast(f32x4, pv[0]) + __builtin_bit_cast(f32x4, pv[1])) + (__builtin_bit_cast(f32x4, pv[2]) + __builtin_bit_cast(f32x4, pv[3])))
                     + ((__builtin_bit_cast(f32x4, pv[4]) + __builtin_bit_cast(f32x4, pv[5])) + (__builtin_bit_cast(f32x4, pv[6]) + __builtin_bit_cast(f32x4, pv[7])));
#pragma unroll
            for (int k = 0; k < 4; ++k) { const float t = cv[k]; auto sw = __builtin_amdgcn_permlane16_swap(__float_as_uint(t), __float_as_uint(t), false, false); cv[k] = __uint_as_float(sw[0]) + __uint_as_float(sw[1]); }
            if ((lane & 31) < 14) *(f32x4*)(tb_cs + (lane >> 5) * 112 + wn * 56 + fr * 4) = cv;
            done_inc();
          } }); }
      if constexpr (LDR) __builtin_amdgcn_s_setprio(0);
      DELAY_AT(3);
      epilogue(LDRc);
    };
    if (wave < NLW) run(T{}); else run(F{});
  }
  STAMP(7);
  STAMP(8);
  STAMPW(12, 4);
  STAMP(3);
#endif
}

#define KN_PT 13
#define KN_QR 0
#define KN_PS 0
#define KN_PG -1
#define KN_FW 1
#define KN_GQ 6
#define KN_G0 4
#define KN_OC 12
#define KN_TG 4
#define KN_PQ 1
#define KN_PZ 0
#define KN_PN 4
#define KN_H0 0
#define KN_HS 0
#define KN_HN 7
#define KN_X2 0
#define KN_MP 0
#define KN_HD 0
#define KN_HM 7
#define KN_HT 1
#define KN_QO1 3
#define KN_QO2 3
#define KN_QO3 3
#define KN_QO4 16
#define KN_QP 2
#define KN_QS 18
#define KN_QLB 13

constexpr int QXT = 144 * 128, QWU = 24576, QWB = 3 * QXT, QRING = QWB + 4 * QWU;
constexpr int QMU = QRING + 64, QRS = QMU + 576, QFL = QRS + 576, QTCH = QRING + 2304;
constexpr int QCS = 2 * QXT, QKH = QCS + 2560, QVH = QKH + 4096;
#define KN_QWC 8
struct SchedQ {
  static constexpr int ns(int j) { return j < 2 ? 2 : 0; }
  static constexpr int nx(int j) { return (j + 2 < 16 ? 4 : 0) + ns(j); }
  static constexpr int wh(int j) { if (j < 0) return 12; int v = wh(j - 1) + KN_QWC - ns(j); if (v > 6 * (j + 4)) v = 6 * (j + 4); return v > 96 ? 96 : v; }
  static constexpr int oh(int j) { return j < 13 ? 0 : 2 * (j == 13 ? KN_QO1 : (j == 14 ? KN_QO2 : KN_QO3)); }
  static constexpr int tot(int j) { return j < 0 ? 20 : tot(j - 1) + nx(j) + wh(j) - wh(j - 1) + oh(j) - oh(j - 1); }
  static constexpr int cnt(int j) {
    int pos = j < 2 ? 10 * (j + 1) : tot(j - 3) + 4;
    for (int i = 0; i < j; ++i) if (wh(i - 1) < 6 * (j + 1) && wh(i) >= 6 * (j + 1)) { const int q = tot(i - 1) + nx(i) + 6 * (j + 1) - wh(i - 1); if (q > pos) pos = q; }
    return tot(j - 1) - pos;
  }
};
static_assert(SchedQ::cnt(0) == 10 && SchedQ::cnt(8) == 16 && KN_QO1 <= 3 && KN_QO2 <= 6 && KN_QO3 <= 9 && KN_QWC >= 8, "schedule");
static_assert(SchedQ::cnt(KN_S1P) <= SchedQ::tot(KN_S1P - 1) - SchedQ::tot(0) - 6 && KN_S1C > KN_S1P + 2, "statistics");
struct FragQ { bf16x8 x[4]; s16x4 lo[6], hi[6]; };
__device__ __forceinline__ f32x2 ds_r2(unsigned addr) { f32x2 r; asm volatile("ds_read2_b32 %0, %1 offset1:16" : "=v"(r) : "v"(addr)); return r; }
__device__ __forceinline__ float fma1(float a, float b, float c) { float r; asm("v_fma_f32 %0, %1, %2, %3" : "=v"(r) : "v"(a), "v"(b), "v"(c)); return r; }
__device__ __forceinline__ unsigned k1_phase(const GemmArgs& a) {
  unsigned tag = 0;
#if defined(__HIP_DEVICE_COMPILE__)
  constexpr int K = D_, LDW = 3 * D_, NT = 16, XI = 5, XIMIN = 4, WI = 6;
  extern __shared__ __attribute__((aligned(16))) char smem[];
  STAMP(0);
  const unsigned lds0 = (unsigned)(size_t)(lds_char_ptr)smem;
  const int tid = threadIdx.x, lane = tid & 63;
  const int wave = __builtin_amdgcn_readfirstlane(tid >> 6);
  const int bid = blockIdx.x, xcd = bid & 7, idx = bid >> 3;
  const int tm = (xcd >> 2) * 8 + (idx >> 2), tn = (xcd & 3) * 4 + (idx & 3), m0 = tm * 128;
  const int fr = lane & 15, fq = lane >> 4, wq = (lane >> 2) & 3, wp = lane & 3;
  using I0 = std::integral_constant<int, 0>; using I1 = std::integral_constant<int, 1>; using I2 = std::integral_constant<int, 2>; using I3 = std::integral_constant<int, 3>;
  using T = std::true_type; using F = std::false_type;
  auto touch = [&](const void* base, unsigned voff, unsigned soff, unsigned lim = 0x7fffffffu) {
    __builtin_amdgcn_raw_ptr_buffer_load_lds(__builtin_amdgcn_make_buffer_rsrc((void*)base, 0, lim, 0x00020000), (lds_void_ptr)(size_t)(lds0 + QTCH), 4, voff, soff, 0, 0);
  };
  auto code_touch = [&](int line) {
    unsigned long long c0, c1; CODE_ADDR(c0, "k_all"); CODE_ADDR(c1, "k_zend");
    const unsigned len = c1 - c0 < 262144ull ? (unsigned)(c1 - c0) : 0u;
    touch((const void*)c0, (unsigned)(line * 128), 0, len);
  };
  int hj1, hj2; unsigned htv;
  auto hset = [&]() {
    const int slc = (3 - ((unsigned)(size_t)a.W0 >> 7) - (xcd & 3) * 4) & 7;
    const int hid = (idx >> 2) * 3 + (idx & 3) - ((idx & 3) > slc ? 1 : 0);
    const bool hlp = slc < 4 && (idx & 3) != slc;
    hj1 = hlp ? hid / 3 - KN_HQ : -100; hj2 = hlp ? hid / 3 + 8 - KN_HQ : -100;
    htv = (unsigned)(lane * (LDW * 2) + (hid % 3) * 2048 + ((xcd & 3) * 4 + slc) * 128);
  };
  const unsigned xo = fr * 128 + ((fq ^ ((fr >> 1) & 7)) << 4);
  auto wfrag = [&](int seg) { const int row = 8 * fq + wq; return lds0 + QWB + row * 128 + (((seg << 1) ^ wswz<128>(row)) << 4) + wp * 8; };
  const f32x4 z4 = {0.f, 0.f, 0.f, 0.f};
  auto cat = [](s16x4 lo, s16x4 hi) { return __builtin_shufflevector(lo, hi, 0, 1, 2, 3, 4, 5, 6, 7); };
  unsigned xsrc[XI], wsrc[WI];
  { const int lr = lane >> 3, w4 = wave & 3;
    const unsigned xv = (unsigned)(lr * (K * 2) + (((lane & 7) ^ ((w4 * 4 + (lr >> 1)) & 7)) << 4));
    const int r0 = m0 - 16 + w4 * 8;
#pragma unroll
    for (int i = 0; i < XI; ++i) xsrc[i] = xv + (unsigned)((i == 0 ? (r0 < 0 ? 0 : r0) : r0 + 32 * i) * (K * 2));
    const unsigned wv = (unsigned)((w4 * 8 + lr) * (LDW * 2) + tn * 128 + (((lane & 7) ^ wswz<128>(w4 * 8 + lr)) << 4));
#pragma unroll
    for (int i = 0; i < WI; ++i) wsrc[i] = wv + (unsigned)((i & 1) * 32 * (LDW * 2) + (i >> 1) * 2048); }
  unsigned mb = lds0 + wave * 1024, rx = KN_QR ? ((tm ^ tn) & 1) * 1024 : 0, rw = rx * (64 * LDW * 2 / 128);
  asm volatile("s_waitcnt lgkmcnt(0)" : "+s"(mb), "+s"(rx), "+s"(rw) : "s"(a.warm), "v"(wsrc[0]), "v"(wsrc[1]), "v"(wsrc[2]), "v"(wsrc[3]), "v"(wsrc[4]), "v"(wsrc[5]), "v"(xsrc[0]), "v"(xsrc[1]), "v"(xsrc[2]), "v"(xsrc[3]), "v"(xsrc[4]));
  __builtin_amdgcn_sched_barrier(0);
  const u32x4 qX = mk_rsrc(a.X), qW = mk_rsrc(a.W0);
  auto ldX = [&](auto ic, auto tc) { constexpr int i = decltype(ic)::value, t = decltype(tc)::value; if (i < 4 || wave < 2) ld_cx<t % 3 * QXT + i * 4096, t * 128>(mb, qX, xsrc[i], rx); };
  auto ldW = [&](auto qc) { constexpr int i = decltype(qc)::value % WI, t = decltype(qc)::value / WI; ld_ca<QWB + (t & 3) * QWU + i * 4096, t * (64 * LDW * 2), (t >= 8)>(mb, qW, wsrc[i], rw); };
  if (__builtin_expect(wave < 4, 1)) {
    static_for<0, WI>(ldW);
    static_for<0, XI>([&](auto ic) { ldX(ic, I0{}); });
  }
  __builtin_amdgcn_sched_barrier(0);
  if (__builtin_expect(wave < 4, 1)) {
    unsigned seed;
    asm volatile("s_load_dword %0, %1, %2" : "=s"(seed) : "s"(a.sync), "n"(FEP * 4));
    auto sb0 = []() { __builtin_amdgcn_sched_barrier(0); };
    static_for<WI, WI + 2>(ldW); sb0();
    unsigned xh0[3], xh1[3], wb[2], l16 = lane * 16;
#pragma unroll
    for (int i = 0; i < 3; ++i) { xh0[i] = fr == 0 ? lds0 + QRING : lds0 + i * QXT + xo; xh1[i] = fr == 0 ? lds0 + QRING : lds0 + i * QXT + (xo ^ 64); asm volatile("" : "+v"(xh0[i]), "+v"(xh1[i])); }
    sb0(); static_for<WI + 2, WI + 4>(ldW); sb0();
    wb[0] = wfrag(wave); wb[1] = wb[0] + 2 * QWU;
    u32x4 one1; { const unsigned o = fr == 0 ? 0x3f803f80u : 0u; one1 = (u32x4){o, o, o, o}; }
    const char* srows = (const char*)a.X + (size_t)(m0 + tn * 8 + wave * 2) * (K * 2);
    asm volatile("" : "+v"(wb[0]), "+v"(wb[1]), "+v"(one1), "+v"(l16), "+s"(srows));
    sb0(); static_for<WI + 4, 2 * WI>(ldW); sb0();
    hset();
    asm volatile("" : "+s"(hj1), "+s"(hj2), "+v"(htv));
    sb0(); static_for<0, 2>([&](auto ic) { ldX(ic, I1{}); }); sb0();
    unsigned long long tb = (unsigned long long)((wave == 3 || wave == 0) ? a.PF1 : a.W0);
    unsigned tv, ts, to, evm;
    { const int t1 = (bid + 256) / 48 - KN_GQ, t2 = (bid + 512) / 48 - KN_GQ;
      const unsigned e0 = KN_OC ? 1u << KN_OC : 0u, e1 = KN_GQ ? (1u << (t1 > 0 ? t1 : 0)) | (1u << t2) : 0u, e2 = (hj1 >= 0 ? 1u << hj1 : 0u) | (hj2 > 0 ? 1u << hj2 : 0u), e3 = 2u << (idx % 12);
      evm = wave == 0 ? e0 : (wave == 1 ? e1 : (wave == 2 ? e2 : e3));
      ts = wave == 1 ? 2u << 20 : 8 * 64 * LDW * 2;
      to = wave == 1 ? 2u << 20 : (wave == 2 ? ((hj1 >= 0 ? hj1 : hj2) + KN_HQ) * (64 * LDW * 2) : 0); }
    asm volatile("" : "+s"(evm), "+s"(ts), "+s"(to), "+s"(tb));
    sb0(); static_for<2, 4>([&](auto ic) { ldX(ic, I1{}); }); sb0();
    tv = (wave & 1) ? (unsigned)((bid * 64 + lane) * 128)
       : (wave ? htv : (lane < 32 ? (unsigned)((idx * 32 + lane) * 2048 + (xcd & 1) * 1024 + ((3 - ((unsigned)(size_t)a.PF1 >> 7)) & 7) * 128) : 0x80000000u));
    asm volatile("" : "+v"(tv));
    sb0(); ldX(std::integral_constant<int, 4>{}, I1{}); sb0();
    asm volatile("s_waitcnt lgkmcnt(0)\n\ts_mul_i32 %0, %1, 0x9e3779b1\n\ts_add_u32 %0, %0, 0x7f4a7c15" : "=s"(tag) : "s"(seed) : "scc");
    unsigned osrc, gv, sdst, ssrc, sld, smu, srs, fa0, fa1, fa2, mko;
    auto ldO = [&](auto qc) { constexpr int i = decltype(qc)::value >> 1, h = decltype(qc)::value & 1;
      ld_cx<wo_pos(i >> 1) + (i & 1) * 8192 + h * 4096, (((i & 1) * 8 + (i >> 1)) * 2 + h) * (32 * D_ * 2)>(mb, mk_rsrc(a.PF1), osrc, (xcd & 3) * 8 * (32 * D_ * 2)); };
    u32x4 sv[4]; f32x4 s1s[2], s1q[2]; float sa[4], sb;
    auto dpp = [](float t, auto c) { return __int_as_float(__builtin_amdgcn_update_dpp(0, __float_as_int(t), decltype(c)::value, 0xf, 0xf, false)); };
    auto stat = [&](auto kc) {
      constexpr int k = decltype(kc)::value;
      if constexpr (k == 0) asm volatile("" : "+v"(sv[0]), "+v"(sv[1]), "+v"(sv[2]), "+v"(sv[3]));
      if constexpr (k < 8) {
        constexpr int r = (k >> 1) & 1, h = k >> 2;
        const u32x4 one4 = {0x3f803f80u, 0x3f803f80u, 0x3f803f80u, 0x3f803f80u};
        const bf16x8 x = __builtin_bit_cast(bf16x8, sv[2 * r + h]);
        if constexpr (k & 1) s1q[r] = __builtin_amdgcn_mfma_f32_16x16x32_bf16(x, x, h ? s1q[r] : z4, 0, 0, 0);
        else s1s[r] = __builtin_amdgcn_mfma_f32_16x16x32_bf16(__builtin_bit_cast(bf16x8, one4), x, h ? s1s[r] : z4, 0, 0, 0);
      }
      if constexpr (k == 8 || k == 9) {
        constexpr int r = k - 8;
        const f32x4 g = s1q[r];
        const float d = (lane & 2) ? ((lane & 1) ? g[3] : g[2]) : ((lane & 1) ? g[1] : g[0]);
        sa[r] = fq == 0 ? s1s[r][0] : 0.f;
        sa[2 + r] = fq == wq ? d : 0.f;
        asm volatile("" : "+v"(sa[r]), "+v"(sa[2 + r]));
      }
      if constexpr (k == 10 || k == 11) { constexpr int i = (k - 10) * 2; sa[i] = ((lane & 1) ? sa[i + 1] : sa[i]) + dpp((lane & 1) ? sa[i] : sa[i + 1], std::integral_constant<int, 0xb1>{}); asm volatile("" : "+v"(sa[i])); }
      if constexpr (k == 12) sb = ((lane & 2) ? sa[2] : sa[0]) + dpp((lane & 2) ? sa[0] : sa[2], std::integral_constant<int, 0x4e>{});
      if constexpr (k == 13) { sb += dpp(sb, std::integral_constant<int, 0x124>{}); sb += dpp(sb, std::integral_constant<int, 0x128>{}); }
      if constexpr (k == 14) { auto q = __builtin_amdgcn_permlane16_swap(__float_as_uint(sb), __float_as_uint(sb), false, false); sb = __uint_as_float(q[0]) + __uint_as_float(q[1]); }
      if constexpr (k == 15) { auto q = __builtin_amdgcn_permlane32_swap(__float_as_uint(sb), __float_as_uint(sb), false, false); sb = (__uint_as_float(q[0]) + __uint_as_float(q[1])) * (1.f / 1024.f); }
      if constexpr (k == 16) { const float var = dpp(sb, std::integral_constant<int, 0x4e>{}) - sb * sb; sa[0] = __builtin_amdgcn_rsqf((var > 0.f ? var : 0.f) + 1e-5f); asm volatile("" : "+v"(sa[0])); }
      if constexpr (k >= 12 && k < 16) asm volatile("" : "+v"(sb));
      if constexpr (k == 17) {
        __builtin_amdgcn_raw_buffer_store_b128((u32x4){__float_as_uint(-sb), __float_as_uint(sa[0]), tag, ~tag}, __builtin_amdgcn_make_buffer_rsrc((void*)a.cs, 0, 0x7fffffff, 0x00020000), sdst, 0, 16);
        asm volatile("s_nop 1" ::: "memory");
      }
    };
    f32x4 acq = z4, ack = z4, acv = z4;
    const auto rC = __builtin_amdgcn_make_buffer_rsrc((void*)a.cs, 0, 0x7fffffff, 0x00020000);
    auto fetch = [&]() { __builtin_amdgcn_raw_ptr_buffer_load_lds(rC, (lds_void_ptr)(size_t)(lds0 + QRING + STL + wave * 1024), 16, ssrc, 0, 0, 16); };
    u32x4 ent; f32x4 hm4; int hm1, htg;
    auto olane = [&]() { int lx = lane; asm volatile("" : "+v"(lx)); return lx; };
    auto chore = [&](auto jc, auto gc) {
      constexpr int j = decltype(jc)::value, g = decltype(gc)::value;
      if constexpr (j == 1 && g == -1) ent = __builtin_bit_cast(u32x4, ds_b128<0>(lds0 + QFL + 16));
      if constexpr (j == 1 && g == 5) {
        asm volatile("" : "+v"(ent));
        if (wave == 3) __builtin_amdgcn_raw_buffer_store_b32((tag & ~1u) | (((ent[0] + ent[2] > ent[1] + ent[3] ? 0u : 1u) ^ (xcd >> 2)) & 1u), rC, lane == 0 ? (unsigned)(VTAB + bid * 4) : 0x80000000u, 0, 16);
      }
      if constexpr (j == 2 && g == 1) { const int lx = olane(); sdst = lx < 2 ? (unsigned)(STAB + (m0 + tn * 8 + wave * 2 + lx) * 16) : 0x80000000u; asm volatile("" : "+v"(sdst)); }
      if constexpr (j == KN_S1P && g >= 0 && g < 4) {
        if (KN_MP) { __builtin_amdgcn_s_setprio(3); __builtin_amdgcn_sched_barrier(0); }
        stat(std::integral_constant<int, 2 * g>{}); stat(std::integral_constant<int, 2 * g + 1>{});
        if (KN_MP) { __builtin_amdgcn_sched_barrier(0); __builtin_amdgcn_s_setprio(0); } }
      if constexpr (j == KN_S1P && (g == 9 || g == 10)) stat(std::integral_constant<int, g - 1>{});
      if constexpr (j == KN_S1P + 1 && g >= 0 && g < 8) stat(std::integral_constant<int, 10 + g>{});
      if constexpr (j == 5 && g == 1) { const int se = wave * 64 + olane(), sr = m0 - 16 + se;
        ssrc = se < 144 ? (unsigned)(STAB + (sr < 0 ? se : sr) * 16) : (wave == 3 ? (unsigned)(VTAB - 3072 + se * 16) : 0x80000000u); sld = lds0 + QRING + STL + se * 16; asm volatile("" : "+v"(ssrc), "+v"(sld)); }
      if constexpr (j == 5 && g == 5) { const int se = wave * 64 + olane();
        smu = lds0 + (se < 144 ? QMU + se * 4 : QTCH); srs = lds0 + (se < 144 ? QRS + (se & 1) * 288 + (se >> 1) * 4 : QTCH); asm volatile("" : "+v"(smu), "+v"(srs)); }
      if constexpr (j == 6 && g == 1) { const int lx = olane(); osrc = (unsigned)((wave * 8 + (lx >> 3)) * D_ + ((xcd & 1) * 8 + (idx & 7)) * 64 + ((lx & 7) ^ ((lx >> 3) & 6)) * 8) * 2u; asm volatile("" : "+v"(osrc)); }
      if constexpr (j == 6 && g == 5) { const int lx = olane(); gv = lx < 56 ? (unsigned)(((bid & 1) * 512 + (wave >> 1) * 128 + (bid >> 1)) * (DF_ * 2) + lx * 128) : 0x80000000u; asm volatile("" : "+v"(gv)); }
      if constexpr (j == 7 && g == 1) { const int lx = olane();
        fa0 = lds0 + QCS + ((lx & 15) == 0 ? wave * 64 + lx : 1024 + lx * 16); fa1 = lds0 + QCS + (lx < 16 ? 512 + wave * 64 : 1024) + lx * 4; asm volatile("" : "+v"(fa0), "+v"(fa1)); }
      if constexpr (j == 7 && g == 5) { fa2 = lds0 + (wave >> 1) * 1024 + (wave & 1) * 8 + l16;
        const int p2 = (xcd >> 1) * 2 + (idx >> 4), mq = (xcd & 1) * 16 + (idx & 15);
        mko = rbH(p2 * 16 + (mq >> 1), xcd >> 2) + omH((xcd >> 2) * 16384) + (mq & 1) * 3072; asm volatile("" : "+v"(fa2), "+s"(mko)); }
      if constexpr (j == KN_S1C && g == 10) fetch();
      if constexpr (j == KN_S1C + 3 && g == -1) ent = __builtin_bit_cast(u32x4, ds_b128<0>(sld));
      if constexpr (j == KN_S1C + 3 && g == 5) {
        static_assert(SchedQ::cnt(j) <= SchedQ::tot(j - 1) - SchedQ::tot(KN_S1C) && KN_S1C >= 8, "table");
        asm volatile("" : "+v"(ent));
        auto stale = [&]() { return __builtin_amdgcn_ballot_w64(wave == 3 ? ((ent[0] ^ tag) | (ent[1] ^ tag) | (ent[2] ^ tag) | (ent[3] ^ tag)) > 1u : (int)ssrc >= 0 && (ent[2] != tag || ent[3] != ~tag)) != 0; };
        if (__builtin_expect(stale(), 0)) {
          unsigned polls = 0;
          do {
            fetch();
            wait_vm<0>();
            ent = __builtin_bit_cast(u32x4, ds_b128<0>(sld));
            asm volatile("s_waitcnt lgkmcnt(0)" : "+v"(ent) :: "memory");
          } while (stale() && ++polls < 4000000u);
        }
      }
      if constexpr (j == KN_S1C + 3 && g == 8) {
        ds_w32(smu, (int)ent[0]); ds_w32(srs, (int)ent[1]);
        if (wave == 3) {
          int pc = 0;
#pragma unroll
          for (int i = 0; i < 4; ++i) pc += __builtin_popcountll(__builtin_amdgcn_ballot_w64(ent[i] & 1));
          tag = (tag & ~1u) | (pc > 128 ? 1u : 0u);
          ds_w32(lds0 + QFL + 8, (int)tag);
        }
      }
      if constexpr (j == KN_S1C + 4 && g >= 0 && g % 3 == 2) {
        if (wave == 3) {
          __builtin_amdgcn_raw_buffer_store_b128((u32x4){~0u, ~0u, ~0u, ~0u}, __builtin_amdgcn_make_buffer_rsrc((void*)a.st, 0, 0x7fffffff, 0x00020000), l16 + g / 3 * 1024,
                                                 mko ^ ((KN_NUMA & 1) ? ((SIDE ^ (xcd >> 2)) & 1) << 13 : 0), 16);
          asm volatile("s_nop 1" ::: "memory");
        }
      }
      if constexpr (j == NT - 2 && g == -1) { const int lx = olane(); hm4 = __builtin_bit_cast(f32x4, ds_b128<0>(lds0 + QMU + (lx & 48))); hm1 = ds_r32(lds0 + QMU + (lx & 15) * 4); htg = ds_r32(lds0 + QFL + 8); }
      if constexpr (j == NT - 2 && g == 5) { asm volatile("" : "+v"(hm4), "+v"(hm1), "+v"(htg)); tag = __builtin_amdgcn_readfirstlane(htg); }
      if constexpr (j == NT - 3 && g == 8 && (KN_CT & 2)) { if (wave == 2) code_touch(lane * 32 + idx); }
    };
    static_assert(KN_S1C + 4 <= NT - 4 && SchedQ::cnt(NT - 1) <= SchedQ::tot(NT - 2) - SchedQ::tot(KN_S1C + 4) && KN_H0 == 0, "marks");
    struct FragH { bf16x8 x[2]; s16x4 lo[2][3], hi[2][3]; } hf[2];
    auto mm = [&](FragH& f, auto ic) { constexpr int n = decltype(ic)::value;
      asm volatile("" : "+v"(f.lo[0][n]), "+v"(f.hi[0][n]), "+v"(f.lo[1][n]), "+v"(f.hi[1][n]));
      if constexpr (n == 1) asm volatile("" : "+v"(f.x[0]), "+v"(f.x[1]));
#pragma unroll
      for (int h = 0; h < 2; ++h) {
        if constexpr (n == 0) acq = __builtin_amdgcn_mfma_f32_16x16x32_bf16(cat(f.lo[h][0], f.hi[h][0]), __builtin_bit_cast(bf16x8, one1), acq, 0, 0, 0);
        if constexpr (n == 1) ack = __builtin_amdgcn_mfma_f32_16x16x32_bf16(cat(f.lo[h][1], f.hi[h][1]), f.x[h], ack, 0, 0, 0);
        if constexpr (n == 2) acv = __builtin_amdgcn_mfma_f32_16x16x32_bf16(f.x[h], cat(f.lo[h][2], f.hi[h][2]), acv, 0, 0, 0);
      } };
    unsigned tsq = 0;
    using SQ = SchedQ;
    static_for<0, NT>([&](auto jc) {
      constexpr int j = decltype(jc)::value, NX = (j + 2 < NT ? XI : 0) + SQ::ns(j), NW = SQ::wh(j) - SQ::wh(j - 1), NL = NX + NW + SQ::oh(j) - SQ::oh(j - 1);
      if constexpr (j == 12) DELAY_AT(5);
      if constexpr (j == 3) DELAY_AT(6);
      wait_vm<SQ::cnt(j)>();
      lds_barrier();
      auto rd = [&](auto rc) { constexpr int r = decltype(rc)::value, q = r < 4 ? r : r - 2, n = q >> 2, h = (q >> 1) & 1, o = (j & 1) * QWU + h * 4096 + n * 8192;
        FragH& f = hf[j & 1];
        if constexpr (r == 4 || r == 5) f.x[r - 4] = ds_b128<0>(r == 5 ? xh1[j % 3] : xh0[j % 3]);
        else if constexpr (q & 1) f.hi[h][n] = ds_tr<o + 512>(wb[(j >> 1) & 1]); else f.lo[h][n] = ds_tr<o>(wb[(j >> 1) & 1]); };
      chore(jc, std::integral_constant<int, -1>{});
      constexpr int H0 = j < NT - 1 ? KN_H0 : 0, NG = NL > 10 + H0 ? NL : 10 + H0;
      if constexpr (KN_HS > 0) asm volatile("s_nop %0" :: "n"(KN_HS - 1));
      static_for<0, NG>([&](auto gc) {
        constexpr int g = decltype(gc)::value, h = g - H0;
        __builtin_amdgcn_sched_barrier(0);
        if constexpr (g < NX - SQ::ns(j)) ldX(gc, std::integral_constant<int, j + 2>{});
        else if constexpr (g < NX) {
          constexpr int q = 2 * j + g - XI;
          asm volatile("s_nop 4\n\tglobal_load_dwordx4 %0, %1, %2 offset:%3" : "=&v"(sv[q]) : "v"(l16), "s"(srows), "n"(q * 1024) : "memory");
        }
        else if constexpr (g < NX + NW) ldW(std::integral_constant<int, SQ::wh(j - 1) + g - NX>{});
        else if constexpr (g < NL) ldO(std::integral_constant<int, SQ::oh(j - 1) + g - NX - NW>{});
        else if constexpr (h < 9) asm volatile("s_nop %0" :: "n"(KN_HN));
        if constexpr (j <= 12 && g == KN_TG) { if (__builtin_expect((evm >> j) & 1, 0)) { touch((const void*)tb, tv, to); to += ts; } }
        if constexpr (g == (KN_PG < 0 ? NG - 1 : KN_PG) && j <= KN_PT && j >= KN_PT - 3 * KN_PS && (KN_PS == 0 || (KN_PT - j) % (KN_PS ? KN_PS : 1) == 0)) {
          if (KN_PS == 0 || wave == 3 - (KN_PT - j) / (KN_PS ? KN_PS : 1)) touch(wave & 1 ? a.CW1 : a.CW0, gv, 0);
        }
        __builtin_amdgcn_sched_barrier(0);
        if constexpr (h >= 0 && h < 7) { rd(std::integral_constant<int, 2 * h>{}); rd(std::integral_constant<int, 2 * h + 1>{}); }
        if constexpr (j >= 1 && j < KN_HD + 1 && g >= KN_HM && g < KN_HM + 3 * KN_HT && (g - KN_HM) % KN_HT == 0) mm(hf[~j & 1], std::integral_constant<int, (g - KN_HM) / KN_HT>{});
        if constexpr (j >= KN_HD && (h == 4 || h == 7 || h == 9)) { asm volatile("s_waitcnt lgkmcnt(%0)" :: "n"(h == 4 ? 6 : (h == 7 ? 4 : 0)) : "memory"); mm(hf[j & 1], std::integral_constant<int, h == 4 ? 0 : (h == 7 ? 1 : 2)>{}); }
        if constexpr (g == 5 && (j == 1 || j == KN_S1C + 3 || j == NT - 2)) asm volatile("s_waitcnt lgkmcnt(12)" ::: "memory");
        __builtin_amdgcn_sched_barrier(0);
        chore(jc, gc);
      });
      __builtin_amdgcn_sched_barrier(0);
    });
    {
      f32x4 ck;
#pragma unroll
      for (int e = 0; e < 4; ++e) ck[e] = dpp(ack[e], std::integral_constant<int, 0x150>{});
      const auto c1 = __builtin_amdgcn_permlane16_swap(__float_as_uint(acv[0]), __float_as_uint(acv[0]), false, false);
      const auto c2 = __builtin_amdgcn_permlane32_swap(c1[0], c1[0], false, false);
      const float nm = __int_as_float(hm1), nc = __uint_as_float(c2[0]);
      asm volatile("ds_write_b128 %0, %1\n\tds_write_b128 %0, %2 offset:256\n\tds_write_b32 %3, %4" :: "v"(fa0), "v"(acq), "v"(ck), "v"(fa1), "v"(nc) : "memory");
      const u32x2 kk = {pk_bf16(fma1(nm, ck[0], ack[0]), fma1(nm, ck[1], ack[1])), pk_bf16(fma1(nm, ck[2], ack[2]), fma1(nm, ck[3], ack[3]))};
      const u32x2 vv = {pk_bf16(fma1(nc, hm4[0], acv[0]), fma1(nc, hm4[1], acv[1])), pk_bf16(fma1(nc, hm4[2], acv[2]), fma1(nc, hm4[3], acv[3]))};
      asm volatile("ds_write_b64 %0, %1 offset:%3\n\tds_write_b64 %0, %2 offset:%4" :: "v"(fa2), "v"(kk), "v"(vv), "n"(QKH), "n"(QVH) : "memory");
    }
    lds_barrier();
    if (tid == 192) set_flag(a.sync + FHM((xcd >> 1) * 2 + (idx >> 4)) + (xcd & 1) * 16 + (idx & 15), tag);
    static_for<SQ::oh(NT - 1), 2 * KN_QO4>(ldO);
    wait_vm<2 * (KN_QO4 - 2)>();
    const int sd = SIDE;
    unsigned fo = tid == 0 ? (unsigned)(FLO(tm) + tn * 32) * 4u : 0x80000000u, ft = tag;
    const auto rF = __builtin_amdgcn_make_buffer_rsrc((void*)a.sync, 0, 147456, 0x00020000);
    asm volatile("" : "+v"(fo), "+v"(ft) : "s"(rF));
    raw_barrier();
    if (wave == 0) __builtin_amdgcn_raw_buffer_store_b32(ft, rF, fo, 0, 16);
    static_for<2 * KN_QO4, 32>(ldO);
  } else {
    const int wm = (wave >> 1) & 1, hd = wave & 1;
    if (wave == 4) {
      if (lane < 4) { *(unsigned*)(smem + QRING + lane * 4) = 0x3f803f80u; if (lane < 2) *(unsigned*)(smem + QFL + lane * 4) = 0u; }
      if (KN_FW) __builtin_amdgcn_raw_ptr_buffer_load_lds(__builtin_amdgcn_make_buffer_rsrc((void*)a.sync, 0, 147456, 0x00020000), (lds_void_ptr)(size_t)(lds0 + QTCH), 4, lane * 32768 + bid * 128, 0, 0, 16);
    }
    if (KN_X2 && wave == 5 && bid < 32 * KN_X2) touch(a.X, (unsigned)((bid * (64 / (KN_X2 ? KN_X2 : 1)) + lane / (KN_X2 ? KN_X2 : 1)) * (K * 2) + 256 + lane % (KN_X2 ? KN_X2 : 1) * 128), 0);
    if (wave == 6) { hset(); if (hj1 < 0 && hj1 > -100) touch(a.W0, htv, (hj1 + KN_HQ) * (64 * LDW * 2));
      if (KN_G0 && bid >= KN_G0 * 48) touch(a.W0, (bid * 64 + lane) * 128, 0); }
    if ((KN_CT & 1) && wave == 7) code_touch(lane * 32 + idx);
    {
      const int id = bid * 2 + wm;
      const char* pb = (const char*)a.sync + PRB + (id >> 3) * 16384 + hd * 8192 + (id & 7) * (KN_PN == 8 ? 128 : 256);
      unsigned long long p0, p1; unsigned t;
#define SL4(a, b) "s_load_dword %1, %3, 0x" a "00\n\ts_load_dword %1, %3, 0x" b "00\n\ts_load_dword %1, %3, 0x1" a "00\n\ts_load_dword %1, %3, 0x1" b "00\n\t"
      if (KN_PN == 8) asm volatile("s_sleep %5\n\ts_memtime %0\n\ts_waitcnt lgkmcnt(0)\n\t" SL4("0", "8") SL4("4", "c") "s_waitcnt lgkmcnt(%4)\n\ts_memtime %2\n\ts_waitcnt lgkmcnt(0)" : "=&s"(p0), "=&s"(t), "=&s"(p1) : "s"(pb), "n"(KN_PQ), "n"(KN_PZ));
      else asm volatile("s_sleep %5\n\ts_memtime %0\n\ts_waitcnt lgkmcnt(0)\n\t" SL4("0", "8") "s_waitcnt lgkmcnt(%4)\n\ts_memtime %2\n\ts_waitcnt lgkmcnt(0)" : "=&s"(p0), "=&s"(t), "=&s"(p1) : "s"(pb), "n"(KN_PQ), "n"(KN_PZ));
      ds_w32(lds0 + QFL + 16 + (wave - 4) * 4, (int)((unsigned)p1 - (unsigned)p0));
    }
    f32x2 bias[2], bias0[2];
    {
      const int srow = m0 + wm * 64 + fr;
      const unsigned mk = 0x5555u & ((2u << (srow < 15 ? srow : 15)) - 1u), sft = (fr & 14) - fq * 4 + 14;
      const unsigned b0 = ~(0x5555u >> sft), b1 = ~(0x55550000u >> sft), c0 = ~(mk >> sft), c1 = ~((mk << 16) >> sft);
#pragma unroll
      for (int j = 0; j < 4; ++j) {
        bias[j >> 1][j & 1] = __uint_as_float((unsigned)__builtin_amdgcn_sbfe((int)(j < 2 ? b0 : b1), 2 - 2 * (j & 1), 1) & 0xff61b1e6u);
        bias0[j >> 1][j & 1] = __uint_as_float((unsigned)__builtin_amdgcn_sbfe((int)(j < 2 ? c0 : c1), 2 - 2 * (j & 1), 1) & 0xff61b1e6u);
      }
    }
    unsigned pl = (fr & 1) ? 0x01000c0cu : 0x0c0c0100u, ph = (fr & 1) ? 0x03020c0cu : 0x0c0c0302u;
    unsigned ta = lds0 + QRING + STL + (16 + wm * 64 + fr) * 16, tm4 = lds0 + QMU + (16 + wm * 64 + fq * 4) * 4, trk = lds0 + QRS + (fr & 1) * 288 + (wm * 32 + fq * 2) * 4;
    unsigned tcs = lds0 + hd * 128 + fq * 16, tcv = lds0 + QCS + 512 + hd * 128 + fr * 4, tex = lds0 + hd * 1024 + lane * 16, tfl = lds0 + QFL + hd * 4, odst = rbH((m0 >> 4) + wm * 4, xcd >> 2) + omH(tn * 2048) + hd * 1024 + lane * 16;
    asm volatile("" : "+v"(bias[0]), "+v"(bias[1]), "+v"(bias0[0]), "+v"(bias0[1]), "+v"(pl), "+v"(ph), "+v"(ta), "+v"(tm4), "+v"(trk), "+v"(tcs), "+v"(tcv), "+v"(tex), "+v"(tfl), "+v"(odst));
    if (KN_MP) __builtin_amdgcn_s_setprio(1);
    f32x4 acc[6][4];
#pragma unroll
    for (int n = 0; n < 6; ++n)
#pragma unroll
      for (int m = 0; m < 4; ++m) acc[n][m] = z4;
    const unsigned xa[2] = {lds0 + (16 + wm * 64) * 128 + xo, lds0 + (16 + wm * 64) * 128 + (xo ^ 64)}, wa[2] = {wfrag(hd * 2), wfrag(hd * 2 + 1)};
    struct Ctx { unsigned x[2], w[2]; } c = {{xa[0], xa[1]}, {wa[0], wa[1]}};
    using SP = SchedP<6, KN_QS>;
    FragQ fa, fb;
    auto rd = [&](auto rc) {
      constexpr int r = decltype(rc)::value;
      if constexpr (r >= 24) { constexpr int m = (r - 24) >> 1; if constexpr (r & 1) fb.x[m] = ds_b128<m * 2048>(c.x[1]); else fa.x[m] = ds_b128<m * 2048>(c.x[0]); }
      else { constexpr int n = r >> 2, o = ((r >> 1) & 1) * 4096 + (n >> 1) * 8192 + (r & 1) * 512; FragQ& f = (r & 2) ? fb : fa;
        if constexpr (r & 1) f.hi[n] = ds_tr<o>(c.w[n & 1]); else f.lo[n] = ds_tr<o>(c.w[n & 1]); }
    };
    auto pin = [&](auto pc) {
      static_for<0, 32>([&](auto rc) { constexpr int r = decltype(rc)::value;
        if constexpr (SP::need(r) == decltype(pc)::value) {
          FragQ& f = (r >= 24 ? r & 1 : r & 2) ? fb : fa;
          if constexpr (r >= 24) { bf16x8& q = f.x[(r - 24) >> 1]; asm volatile("" : "+v"(q)); } else { s16x4& q = (r & 1) ? f.hi[r >> 2] : f.lo[r >> 2]; asm volatile("" : "+v"(q)); } } });
    };
    int sx = QXT, sw = QWU;
    auto pairs = [&](auto PBc, auto PEc, auto RDc, auto&& gap) {
      constexpr bool RD = decltype(RDc)::value;
      static_for<decltype(PBc)::value, decltype(PEc)::value>([&](auto pc) {
        constexpr int p = decltype(pc)::value, A = SP::pa(p), n = SP::pn(A), m = SP::pm(A);
        if constexpr (SP::cnt(p) >= 0 && (!KN_EW || p == decltype(PBc)::value)) { asm volatile("s_waitcnt lgkmcnt(%0)" :: "n"(SP::cnt(p)) : "memory"); pin(pc); }
        static_for<2 * p, 2 * p + 2>([&](auto gc) {
          constexpr int g = decltype(gc)::value;
          FragQ& u = ((A ^ g) & 1) ? fb : fa;
          if constexpr (n < 4) acc[n][m] = __builtin_amdgcn_mfma_f32_16x16x32_bf16(cat(u.lo[n], u.hi[n]), u.x[m], acc[n][m], 0, 0, 0);
          else acc[n][m] = __builtin_amdgcn_mfma_f32_16x16x32_bf16(u.x[m], cat(u.lo[n], u.hi[n]), acc[n][m], 0, 0, 0);
          __builtin_amdgcn_sched_barrier(0);
          if constexpr (KN_EW && RD && !(g & 1) && SP::cnt(p + 1, 1) >= 0) { asm volatile("s_waitcnt lgkmcnt(%0)" :: "n"(SP::cnt(p + 1, 1)) : "memory"); pin(std::integral_constant<int, p + 1>{}); }
          gap(gc);
          if constexpr (RD) {
            static_for<0, 32>([&](auto rc) { if constexpr (SP::gap(decltype(rc)::value) == g) rd(rc); });
            if constexpr (g == 13) { sx = sx + QXT; sw = sw + QWU; asm volatile("" : "+s"(sx), "+s"(sw)); }
            if constexpr (g == 17) { sx = sx == 3 * QXT ? 0 : sx; asm volatile("" : "+s"(sx)); }
            if constexpr (g == 21) { sw = sw == 4 * QWU ? 0 : sw; asm volatile("" : "+s"(sw)); }
            if constexpr (g == 44) { c.x[0] = xa[0] + sx; c.x[1] = xa[1] + sx; asm volatile("" : "+v"(c.x[0]), "+v"(c.x[1])); }
            if constexpr (g == 45) { c.w[0] = wa[0] + sw; c.w[1] = wa[1] + sw; asm volatile("" : "+v"(c.w[0]), "+v"(c.w[1])); }
          }
          __builtin_amdgcn_sched_barrier(0);
        });
      });
      if constexpr (RD && !KN_EW) { asm volatile("s_waitcnt lgkmcnt(0)" ::: "memory"); pin(std::integral_constant<int, 24>{}); }
      static_assert(SP::cnt(24, 1) == 0 && SP::gap(23) < 46, "all reads");
    };
    static_assert(SP::gap(23) <= 44, "addresses");
    using I12 = std::integral_constant<int, 12>; using I24 = std::integral_constant<int, 24>;
    lds_barrier();
    static_for<0, 24>([&](auto gc) { static_for<0, 32>([&](auto rc) { if constexpr (SP::gap(decltype(rc)::value) == decltype(gc)::value) rd(rc); }); });
    STAMP(1);
    pairs(I12{}, I24{}, T{}, [&](auto) {});
#pragma unroll 1
    for (int j = 1; j < NT; ++j) pairs(I0{}, I24{}, T{}, [&](auto pc) { if constexpr (decltype(pc)::value == 1) raw_barrier(); });
#define ES(i)
    ES(0);
    if (KN_QP) __builtin_amdgcn_s_setprio(KN_QP);
    f32x2 st[4], rk[5], cv, fs[4][2]; f32x4 m4[4], cs[4]; int tg;
    pairs(I0{}, I12{}, F{}, [&](auto ic) { constexpr int I = decltype(ic)::value;
      if constexpr (I < 4) st[I] = ds_b64<I * 256>(ta);
      if constexpr (I == 4) tg = ds_r32(lds0 + QFL + 8);
      if constexpr (I < 5) rk[I] = ds_b64<I * 32>(trk);
      if constexpr (I >= 5 && I < 9) m4[I - 5] = __builtin_bit_cast(f32x4, ds_b128<(I - 5) * 64>(tm4));
      if constexpr (I == 9) { asm volatile("s_waitcnt lgkmcnt(4)" : "+v"(st[0]), "+v"(st[1]), "+v"(st[2]), "+v"(st[3]), "+v"(rk[0]), "+v"(rk[1]), "+v"(rk[2]), "+v"(rk[3]), "+v"(rk[4]) :: "memory"); }
      if constexpr (I == 9 || I == 10) { constexpr int m = (I - 9) * 2; const float sc2 = 0.17677669529663687f * 1.4426950408889634f; st[m][1] *= sc2; st[m + 1][1] *= sc2; asm volatile("" : "+v"(st[m]), "+v"(st[m + 1])); }
      if constexpr (I >= 11 && I < 19) { constexpr int m = (I - 11) >> 1, h = (I - 11) & 1; fs[m][h] = rk[m + h] * st[m][1]; asm volatile("" : "+v"(fs[m][h])); }
      if constexpr (I == 19) { int& q = tg; asm volatile("" : "+v"(q)); tag = __builtin_amdgcn_readfirstlane(tg); if (KN_NUMA & 1) odst ^= ((SIDE ^ (xcd >> 2)) & 1) << 13; asm volatile("" : "+v"(odst)); }
      if constexpr (I == 4) ES(1);
      if constexpr (I == KN_QLB) { ES(2); raw_barrier(); ES(3); }
      if constexpr (I >= 14 && I < 18) { constexpr int q = (I - 14) ^ 2; cs[q] = __builtin_bit_cast(f32x4, ds_b128<QCS + (q >> 1) * 256 + (q & 1) * 64>(tcs)); }
      if constexpr (I == 18) cv = ds_r2(tcv); });
    static_assert(KN_QLB <= 14, "barrier");
    STAMP(2);
    ES(4);
    asm volatile("s_waitcnt lgkmcnt(0)" : "+v"(cs[0]), "+v"(cs[1]), "+v"(cs[2]), "+v"(cs[3]), "+v"(cv), "+v"(m4[0]), "+v"(m4[1]), "+v"(m4[2]), "+v"(m4[3]) :: "memory");
    u32x4 Q[4], Kf[5], V[5];
    auto nrm = [&](f32x4 x, f32x4 y, float sc) {
      const f32x2 p = (f32x2){y[0], y[1]} * sc + (f32x2){x[0], x[1]}, q = (f32x2){y[2], y[3]} * sc + (f32x2){x[2], x[3]};
      return (u32x2){pk_bf16(p[0], p[1]), pk_bf16(q[0], q[1])}; };
    auto c4 = [](u32x2 p, u32x2 q) { return (u32x4){p[0], p[1], q[0], q[1]}; };
    auto mkK = [&](int m) { Kf[m + 1] = c4(nrm(acc[2][m], cs[2], st[m][0]), nrm(acc[3][m], cs[3], st[m][0])); };
    auto mkQ = [&](int m) { Q[m] = c4(nrm(acc[0][m], cs[0], st[m][0]), nrm(acc[1][m], cs[1], st[m][0])); };
    auto mkV = [&](int m) { V[m + 1] = c4(nrm(acc[4][m], m4[m], cv[0]), nrm(acc[5][m], m4[m], cv[1])); };
    mkK(3); mkV(3);
    if (wm == 0) asm volatile("ds_write_b128 %0, %1 offset:%5\n\tds_write_b128 %0, %2 offset:%6\n\tds_write_b32 %3, %4" :: "v"(tex), "v"(Kf[4]), "v"(V[4]), "v"(tfl), "v"(pl), "n"(QKH + 2048), "n"(QVH + 2048) : "memory");
    __builtin_amdgcn_sched_barrier(0);
    f32x4 sc[4][2]; f32x2 lg[4][2]; float mx[4];
    auto qk = [&](int m, int h) { sc[m][h] = __builtin_amdgcn_mfma_f32_16x16x32_bf16(__builtin_bit_cast(bf16x8, Kf[m + h]), __builtin_bit_cast(bf16x8, Q[m]), z4, 0, 0, 0); };
    mkQ(3); mkK(2); qk(3, 1); qk(3, 0); mkQ(2); mkK(1); qk(2, 1); qk(2, 0); mkQ(1); mkK(0); qk(1, 1); qk(1, 0);
    __builtin_amdgcn_sched_barrier(0);
    int fl; bf16x8 k0, v0;
    tex += wm * 2048;
    auto front = [&]() { fl = ds_r32(tfl); k0 = ds_b128<QKH>(tex); v0 = ds_b128<QVH>(tex); };
    front();
    __builtin_amdgcn_sched_barrier(0);
    mkQ(0); qk(0, 1); mkV(2); mkV(1);
    __builtin_amdgcn_sched_barrier(0);
    asm volatile("s_waitcnt lgkmcnt(0)" : "+v"(fl), "+v"(k0), "+v"(v0), "+v"(V[3]), "+v"(V[2]) :: "memory");
    if (__builtin_expect(wm && __builtin_amdgcn_readfirstlane(fl) == 0, 0)) {
      unsigned polls = 0;
      do { front(); asm volatile("s_waitcnt lgkmcnt(0)" : "+v"(fl), "+v"(k0), "+v"(v0) :: "memory"); } while (__builtin_amdgcn_readfirstlane(fl) == 0 && ++polls < 40000000u);
    }
    Kf[0] = __builtin_bit_cast(u32x4, k0); V[0] = __builtin_bit_cast(u32x4, v0);
    qk(0, 0); mkV(0);
    STAMP(4);
    __builtin_amdgcn_sched_barrier(0); ES(5);
    __builtin_amdgcn_sched_barrier(0);
    const bool par = pl & 4;
#pragma unroll
    for (int m = 3; m >= 0; --m) {
#pragma unroll
      for (int h = 0; h < 2; ++h) {
        float e[4] = {sc[m][h][0], sc[m][h][1], sc[m][h][2], sc[m][h][3]};
        asm("" : "+v"(e[0]), "+v"(e[2]));
        lg[m][h] = (f32x2){par ? e[1] : e[0], par ? e[3] : e[2]} * fs[m][h] + (m == 0 ? bias0[h] : bias[h]);
      }
      mx[m] = fmaxf(fmaxf(lg[m][0][0], lg[m][0][1]), fmaxf(lg[m][1][0], lg[m][1][1]));
    }
    __builtin_amdgcn_sched_barrier(0);
    auto s16 = [](float x, float y) { auto p = __builtin_amdgcn_permlane16_swap(__float_as_uint(x), __float_as_uint(y), false, false); return (f32x2){__uint_as_float(p[0]), __uint_as_float(p[1])}; };
    auto s32 = [](float x, float y) { auto p = __builtin_amdgcn_permlane32_swap(__float_as_uint(x), __float_as_uint(y), false, false); return (f32x2){__uint_as_float(p[0]), __uint_as_float(p[1])}; };
    auto spread = [&](float r) { const f32x2 t = s32(r, r), u = s16(t[0], t[0]), w = s16(t[1], t[1]); mx[0] = u[0]; mx[1] = u[1]; mx[2] = w[0]; mx[3] = w[1]; };
    { const f32x2 p = s16(mx[0], mx[1]), q = s16(mx[2], mx[3]), r = s32(fmaxf(p[0], p[1]), fmaxf(q[0], q[1])); spread(fmaxf(r[0], r[1])); }
    __builtin_amdgcn_sched_barrier(0);
#pragma unroll
    for (int m = 3; m >= 0; --m) {
#pragma unroll
      for (int h = 0; h < 2; ++h) { const f32x2 d = lg[m][h] - mx[m]; lg[m][h] = (f32x2){__builtin_amdgcn_exp2f(d[0]), __builtin_amdgcn_exp2f(d[1])}; }
      const f32x2 t = lg[m][0] + lg[m][1];
      mx[m] = t[0] + t[1];
    }
    __builtin_amdgcn_sched_barrier(0);
    { const f32x2 p = s16(mx[0], mx[1]), q = s16(mx[2], mx[3]);
#pragma unroll
      for (int m = 3; m >= 0; --m) lg[m][0] = lg[m][0] * rk[m];
      const f32x2 r = s32(p[0] + p[1], q[0] + q[1]);
#pragma unroll
      for (int m = 3; m >= 0; --m) lg[m][1] = lg[m][1] * rk[m + 1];
      spread(__builtin_amdgcn_rcpf(r[0] + r[1])); }
    __builtin_amdgcn_sched_barrier(0);
    auto half = [](u32x4 v, int h) { return __builtin_bit_cast(s16x4, (u32x2){v[2 * h], v[2 * h + 1]}); };
#pragma unroll
    for (int m = 3; m >= 0; --m) {
      s16x4 pf[2];
#pragma unroll
      for (int h = 0; h < 2; ++h) { const f32x2 t = lg[m][h] * mx[m]; const unsigned u = pk_bf16(t[0], t[1]); pf[h] = __builtin_bit_cast(s16x4, (u32x2){__builtin_amdgcn_perm(0u, u, pl), __builtin_amdgcn_perm(0u, u, ph)}); }
#pragma unroll
      for (int f = 0; f < 2; ++f) sc[m][f] = __builtin_amdgcn_mfma_f32_16x16x16bf16_1k(half(V[m], f), pf[0], z4, 0, 0, 0);
#pragma unroll
      for (int f = 0; f < 2; ++f) sc[m][f] = __builtin_amdgcn_mfma_f32_16x16x16bf16_1k(half(V[m + 1], f), pf[1], sc[m][f], 0, 0, 0);
    }
    __builtin_amdgcn_sched_barrier(0);
    DELAY_AT(1);
    STAMP(10);
    ES(6);
    const auto rO = __builtin_amdgcn_make_buffer_rsrc((void*)a.O, 0, 0x7fffffff, 0x00020000);
    u32x4 ov[4];
#pragma unroll
    for (int m = 3; m >= 0; --m) {
      ov[m] = (u32x4){pk_bf16(sc[m][0][0], sc[m][0][1]), pk_bf16(sc[m][0][2], sc[m][0][3]), pk_bf16(sc[m][1][0], sc[m][1][1]), pk_bf16(sc[m][1][2], sc[m][1][3])};
      __builtin_amdgcn_raw_buffer_store_b128(ov[m], rO, odst, m * RSH, 16);
    }
    asm volatile("" :: "v"(ov[0]), "v"(ov[1]), "v"(ov[2]), "v"(ov[3]));
    if (KN_QP) __builtin_amdgcn_s_setprio(0);
    STAMP(7);
    ES(7);
    wait_vm<0>();
    STAMP(8);
    ES(8);
    raw_barrier();
    ES(9);
  }
  STAMP(3);
#endif
  return tag;
}

struct FragW { s16x4 lo[4], hi[4]; };
__device__ __forceinline__ void wo_phase(const GemmArgs& a, const GemmArgs& nx, unsigned tag) {
#if defined(__HIP_DEVICE_COMPILE__)
  extern __shared__ __attribute__((aligned(16))) char smem[];
  STAMP(0);
  const unsigned lds0 = (unsigned)(size_t)(lds_char_ptr)smem;
  const int tid = threadIdx.x, lane = tid & 63;
  const int wave = __builtin_amdgcn_readfirstlane(tid >> 6);
  const int bid = blockIdx.x, xcd = bid & 7, idx = bid >> 3;
  const int tm = (xcd >> 1) * 4 + (idx >> 3), tn = (xcd & 1) * 8 + (idx & 7);
  const int m0 = tm * 128, panel = m0 >> 8, rot = (xcd & 3) * 4, sd = SIDE;
  {
    constexpr int PAD = QTCH;
    const int g = wave >> 2, rw = wave & 3;
    const int fr = lane & 15, fq = lane >> 4, wq = (lane >> 2) & 3, wp = lane & 3;
    unsigned wa[4];
#pragma unroll
    for (int c = 0; c < 4; ++c) { const int row = 4 * fq + wq; wa[c] = lds0 + g * 8192 + row * 128 + (((c << 1) ^ (row & 6)) << 4) + wp * 8; }
    FragW f[3];
    auto rd = [&](auto ic, auto rc) {
      constexpr int i = decltype(ic)::value, r = decltype(rc)::value, P = wo_pos(i >> 1) + (i & 1) * 4096, B = P & ~32767, o = P & 32767;
      if constexpr (r & 1) f[i % 3].hi[r >> 1] = ds_tr<o + 2048>(wa[r >> 1] + B); else f[i % 3].lo[r >> 1] = ds_tr<o>(wa[r >> 1] + B);
    };
    auto pin = [&](FragW& q) {
#pragma unroll
      for (int n = 0; n < 4; ++n) asm volatile("" : "+v"(q.lo[n]), "+v"(q.hi[n]));
    };
    static_for<0, 16>([&](auto rc) { constexpr int r = decltype(rc)::value; rd(std::integral_constant<int, r / 8>{}, std::integral_constant<int, r % 8>{}); });
    const auto rX = __builtin_amdgcn_make_buffer_rsrc((void*)a.X, 0, 0x7fffffff, 0x00020000);
    const int rb = (m0 >> 4) + rw * 2 + g;
    unsigned xs[2] = {(unsigned)(rbH(rb, sd) + lane * 16), (unsigned)(rbH(rb ^ 1, sd) + lane * 16)};
    const int ck = (fq & 1) * 2 + (fq >> 1);
    unsigned orow = (unsigned)((rb * 16 + fr) * (D_ * 2) + tn * 128 + ck * 16), ofrg = (unsigned)(rbH(rb, sd) + omH(tn * 2048) + ck * 256 + fr * 16);
    asm volatile("" : "+v"(xs[0]), "+v"(xs[1]), "+v"(orow), "+v"(ofrg));
    u32x4 res[2];
#pragma unroll
    for (int m = 0; m < 2; ++m) res[m] = __builtin_amdgcn_raw_buffer_load_b128(__builtin_amdgcn_make_buffer_rsrc((void*)a.R, 0, 0x7fffffff, 0x00020000), orow + m * 64, 0, 0);
    __builtin_amdgcn_sched_barrier(0);
    if constexpr (KN_CT & 4) { if (wave == 2) {
      unsigned long long c0, c1; CODE_ADDR(c0, "k_all"); CODE_ADDR(c1, "k_zend");
      wait_vm<0>();
      __builtin_amdgcn_raw_ptr_buffer_load_lds(__builtin_amdgcn_make_buffer_rsrc((void*)c0, 0, c1 - c0 < 262144ull ? (unsigned)(c1 - c0) : 0u, 0x00020000), (lds_void_ptr)(size_t)(lds0 + PAD), 4, (unsigned)((lane * 32 + idx) * 128), 0, 0, 0); } }
    if (wave == 1) wait_flags(a.sync + FLO(tm), (lane & 15) * 128, tag, 0xffffull);
    {
      if (wave == 3) {
        wait_flags(a.sync, (FHM(lane >= 32 ? (xcd >> 2) * 4 + (idx >> 3) : panel) + (lane & 31)) * 4, tag, ~0ull);
      }
    }
    STAMP(9);
    if (wave == 0 || wave == 3) wait_vm<2>();
    lds_barrier();
    STAMP(6);
    pin(f[0]); pin(f[1]);
    u32x4 xr[16][2];
#define KN_XD 4
    auto ldX = [&](int i, int m) {
      if (i < 16) xr[i][m] = __builtin_amdgcn_raw_buffer_load_b128(rX, xs[m] + (i & 1) * 1024, omH((i >> 1) * 2048) ^ omH(((g * 8) ^ rot) * 2048), 16);
    };
#pragma unroll
    for (int i = 0; i < KN_XD; ++i) { ldX(i, 0); ldX(i, 1); __builtin_amdgcn_sched_barrier(0); }
    unsigned gsrc[2];
#pragma unroll
    for (int i = 0; i < 2; ++i) {
      const int j = wave + 8 * i, jj = j >= 7 ? j - 7 : j;
      const int b = jj * 1024 + lane * 16, q = b / 224, pos = (b % 224) >> 4;
      gsrc[i] = (unsigned)((8 * ((q & 15) >> 2) + (q & 3) + 4 * (q >> 4)) * DF_ + ((xcd & 3) * 8 + (idx & 7)) * 112 + pos * 8) * 2u;
      asm volatile("" : "+v"(gsrc[i]));
    }
    f32x4 acc[4][2];
#pragma unroll
    for (int n = 0; n < 4; ++n) { acc[n][0] = (f32x4){0.f, 0.f, 0.f, 0.f}; acc[n][1] = acc[n][0]; }
    __builtin_amdgcn_sched_barrier(0);
    static_for<0, 16>([&](auto ic) {
      constexpr int i = decltype(ic)::value;
      if constexpr (i >= 2) { asm volatile("s_waitcnt lgkmcnt(%0)" :: "n"(i < 15 ? 8 : 0) : "memory"); pin(f[i % 3]); }
      if constexpr (KN_WP == 1 && i % 4 == 0) __builtin_amdgcn_s_setprio(3 - i / 4);
      if constexpr (KN_WP == 2 && i % 3 == 0 && i < 12) __builtin_amdgcn_s_setprio(3 - i / 3);
      if constexpr (KN_WP == 3 && i == 0) { if (g) __builtin_amdgcn_s_setprio(1); }
      if constexpr (KN_WP == 3 && i == 8) __builtin_amdgcn_s_setprio(0);
      static_for<0, 8>([&](auto jc) {
        constexpr int j = decltype(jc)::value, n = j >> 1, m = j & 1;
        const bf16x8 w = __builtin_shufflevector(f[i % 3].lo[n], f[i % 3].hi[n], 0, 1, 2, 3, 4, 5, 6, 7);
        acc[n][m] = __builtin_amdgcn_mfma_f32_16x16x32_bf16(w, __builtin_bit_cast(bf16x8, xr[i][m]), acc[n][m], 0, 0, 0);
        if constexpr (i + 2 < 16) rd(std::integral_constant<int, i + 2>{}, jc);
        if constexpr (j == 1 || j == 5) ldX(i + KN_XD, j >> 2);
        __builtin_amdgcn_sched_barrier(0);
      });
      if constexpr (i == 0) STAMP(1);
    });
    STAMP(2);
    auto slot = [&](int w) { return 32768 + w * 4096 + lane * 16; };
#pragma unroll
    for (int n = 0; n < 4; ++n) *(f32x4*)(smem + slot(wave) + n * 1024) = acc[n][1];
    asm volatile("" : "+v"(res[0]), "+v"(res[1]));
    lds_barrier();
    DELAY_AT(2);
    STAMP(4);
    f32x4 pa[4];
    static_for<0, 4>([&](auto nc) { constexpr int n = decltype(nc)::value; pa[n] = __builtin_bit_cast(f32x4, ds_b128<n * 1024>(lds0 + slot(wave ^ 4))); });
    auto ldGs = [&]() {
      auto ldG = [&](int i, int u) { const int j = wave + 8 * i;
        __builtin_amdgcn_raw_ptr_buffer_load_lds(__builtin_amdgcn_make_buffer_rsrc((void*)(j >= 7 ? nx.W1 : nx.W0), 0, 0x7fffffff, 0x00020000),
                          (lds_void_ptr)(size_t)(lds0 + 98304 + (u ^ 2) * 14592 + (j >= 7 ? 7184 + (j - 7) * 1024 : j * 1024)), 16, gsrc[i], (u + (xcd >> 2) * 16) * 32 * DF_ * 2, 0, 0); };
      ldG(0, 0); ldG(0, 1);
      if (wave < 6) { ldG(1, 0); ldG(1, 1); }
    };
    if (!KN_GL) ldGs();
    asm volatile("s_waitcnt lgkmcnt(0)" : "+v"(pa[0]), "+v"(pa[1]), "+v"(pa[2]), "+v"(pa[3]) :: "memory");
    const auto rO = __builtin_amdgcn_make_buffer_rsrc((void*)a.O, 0, 0x7fffffff, 0x00020000);
    u32x4 o[2];
#pragma unroll
    for (int i = 0; i < 2; ++i) {
      const f32x4 u = acc[2 * i][0] + pa[2 * i], v = acc[2 * i + 1][0] + pa[2 * i + 1];
      const auto s0 = __builtin_amdgcn_permlane16_swap(pk_bf16(u[0], u[1]), pk_bf16(v[0], v[1]), false, false);
      const auto s1 = __builtin_amdgcn_permlane16_swap(pk_bf16(u[2], u[3]), pk_bf16(v[2], v[3]), false, false);
      const u32x4 q4 = {s0[0], s1[0], s0[1], s1[1]};
#pragma unroll
      for (int j = 0; j < 4; ++j) { const unsigned r = res[i][j], q = q4[j]; o[i][j] = pk_bf16(bflo(r) + bflo(q), bfhi(r) + bfhi(q)); }
      __builtin_amdgcn_raw_buffer_store_b128(o[i], rO, ofrg + i * 1024, 0, 16);
    }
    if (KN_GL) { __builtin_amdgcn_sched_barrier(0); asm volatile("s_nop 1" ::: "memory"); ldGs(); __builtin_amdgcn_sched_barrier(0); }
    auto sum4 = [](float v) {
      auto p = __builtin_amdgcn_permlane16_swap(__float_as_uint(v), __float_as_uint(v), false, false); v = __uint_as_float(p[0]) + __uint_as_float(p[1]);
      auto q = __builtin_amdgcn_permlane32_swap(__float_as_uint(v), __float_as_uint(v), false, false); return __uint_as_float(q[0]) + __uint_as_float(q[1]); };
    f32x2 e[8], sm = {0.f, 0.f};
#pragma unroll
    for (int j = 0; j < 8; ++j) { const unsigned q = o[j >> 2][j & 3]; e[j] = (f32x2){bflo(q), bfhi(q)}; sm += e[j]; }
    const float mu = sum4(sm[0] + sm[1]) * (1.f / 64.f);
    f32x2 t2 = {0.f, 0.f};
#pragma unroll
    for (int j = 0; j < 8; ++j) { const f32x2 d = e[j] - mu; t2 += d * d; }
    const float q2 = sum4(t2[0] + t2[1]);
    __builtin_amdgcn_raw_buffer_store_b64((u32x2){__float_as_uint(mu), __float_as_uint(q2)}, __builtin_amdgcn_make_buffer_rsrc((void*)a.st, 0, 0x7fffffff, 0x00020000),
                                          lane < 16 ? (unsigned)((tn * S_ + rb * 16 + lane) * 8) : 0x80000000u, 0, 16);
    STAMP(7);
    STAMP(8);
    unsigned fo = lane == 0 ? (unsigned)(FHF(panel) + ((tm & 1) * 16 + tn) * 32) * 4u : 0x80000000u, ft = tag;
    unsigned gx = rbH(((xcd >> 2) * 4 + (idx >> 3)) * 16, sd) + (rw >> 1) * RSH + (rw & 1) * 1024 + lane * 16, gm = lds0 + g * 32768 + rw * 1024, gs = omH((xcd >> 2) * 16384 + g * 2048);
    const auto rF = __builtin_amdgcn_make_buffer_rsrc((void*)a.sync, 0, 147456, 0x00020000);
    const u32x4 qH = mk_rsrc(a.O);
    asm volatile("s_waitcnt vmcnt(0) lgkmcnt(0)" : "+v"(fo), "+v"(ft), "+v"(gx), "+s"(gm), "+s"(gs) : "s"(rF), "s"(qH) : "memory");
    raw_barrier();
    if (wave == 4) __builtin_amdgcn_raw_buffer_store_b32(ft, rF, fo, 0, 16);
    if (KN_GX > 1 && g) __builtin_amdgcn_s_sleep(KN_GX - 1);
    static_for<0, 8>([&](auto ic) { constexpr int i = decltype(ic)::value; ld_ca<i * 4096, i * 2 * RSH, false, true>(gm, qH, gx, gs); });
    STAMP(3);
  }
#endif
}

struct FragE { bf16x8 x[4]; s16x4 lo[4], hi[4]; };
template <int MODE, int HTD, int TA, int TB, int TL = 0>
__device__ __forceinline__ void dn_phase(const GemmArgs& a, unsigned tag) {
#if defined(__HIP_DEVICE_COMPILE__)
  constexpr int SX = 5, SW = 4, WB = SX * 16384, PAD = WB + SW * 16384, V = KN_V;
  static_assert(TA == 2 && TB == 2, "schedule");
  extern __shared__ __attribute__((aligned(16))) char smem[];
  if constexpr (MODE != 1) STAMP(0);
  const unsigned lds0 = (unsigned)(size_t)(lds_char_ptr)smem;
  const int tid = threadIdx.x;
  int lane = tid & 63; asm volatile("" : "+v"(lane));
  const int wave = __builtin_amdgcn_readfirstlane(tid >> 6);
  const int bid = blockIdx.x, xcd = bid & 7, idx = bid >> 3;
  const int tm = (xcd >> 1) * 4 + (idx >> 3), tn = (idx & 7) * 2 + (xcd & 1), A = (xcd & 1) ? 24 - KN_D : 24 + KN_D;
  const int m0 = tm * 128, panel = m0 >> 8, sd = SIDE;
  unsigned* pcnt = a.sync + FAE((xcd >> 2) * 4 + (idx >> 3)) + ((xcd & 3) * 8 + (idx & 7)) * 32;
  const unsigned orow = (unsigned)((m0 + wave * 16 + (lane >> 3)) * (D_ * 2) + tn * 128 + (lane & 7) * 16);
  const unsigned ofrg = (unsigned)(rbH((m0 >> 4) + wave, sd) + omH(tn * 2048) + (lane & 7) * 256 + (lane >> 3) * 16);
  u32x4 res[2];
  auto res_loads = [&]() {
    const auto rR = __builtin_amdgcn_make_buffer_rsrc((void*)a.R, 0, 0x7fffffff, 0x00020000);
#pragma unroll
    for (int i = 0; i < 2; ++i) res[i] = __builtin_amdgcn_raw_buffer_load_b128(rR, ofrg, i * 128, 16);
  };
  constexpr int STG = 0;
  if (MODE == 1 || wave < 4) {
    const int lw = wave & 3;
    const int kf = (xcd & 3) * 8;
    auto ktile = [&](int t) { const int l = (xcd & 2) * 6 + (t < A ? t - 16 + (xcd & 1) * (8 + KN_D) : t - A + 16); return t < 16 ? (kf ^ ((t & 8) * 2)) + (t & 7) : (l < 24 ? l + 32 : l + 8); };
    auto wrow = [&](int kt, int j) { const int p = kt * 8 + j, b = p - 256, v1 = p + 3 * (p >> 2), v2 = b + 4 * ((b * 21846) >> 16) + 4, m = b >> 31; return ((v1 & m) | (v2 & ~m)) * 8; };
    const auto rW = __builtin_amdgcn_make_buffer_rsrc((void*)a.W0, 0, 0x7fffffff, 0x00020000);
    const u32x4 qX = mk_rsrc(a.X), qW = mk_rsrc((const char*)a.W0 - 4096);
    const unsigned xv0 = (unsigned)(rbA((m0 >> 4) + lw * 2, sd) + lane * 16), xv1 = xv0 + (RSA - 2048);
    unsigned wv[4];
    { const unsigned w0 = (unsigned)((lane >> 3) * D_ + tn * 64 + ((lane & 7) ^ wswz<128>(lane >> 3)) * 8) * 2u;
#pragma unroll
      for (int i = 0; i < 4; ++i) wv[i] = (w0 ^ (i * 64)) + (4096 - i * 1024); }
    const unsigned mX = lds0 + lw * 4096, mW = mX + WB;
    auto ldXo = [&](int so, int slot) { ld_x4(mX + slot * 16384, qX, xv0, xv1, so); };
    int tbx = 0, tb0 = 0, tb1 = 0;
    auto tables = [&]() { const int kt = ktile(lane < A + 8 ? lane : 0); tbx = omA(kt * 2048); tb0 = wrow(kt, 2 * lw) * (D_ * 2); tb1 = wrow(kt, 2 * lw + 1) * (D_ * 2); asm volatile("" : "+v"(tbx), "+v"(tb0), "+v"(tb1)); };
    int o0, o1, ox;
    auto offs = [&](int t) { o0 = __builtin_amdgcn_readlane(tb0, t + 4); o1 = __builtin_amdgcn_readlane(tb1, t + 4); ox = __builtin_amdgcn_readlane(tbx, t + 4); asm volatile("s_nop 1" : "+s"(o0), "+s"(o1), "+s"(ox)); };
    const unsigned* fcnt = a.sync + FAE(panel);
    const unsigned fof = (lane & 31) * 128 + (lane >> 5) * 4;
    auto ldF = [&]() { __builtin_amdgcn_raw_ptr_buffer_load_lds(__builtin_amdgcn_make_buffer_rsrc((void*)fcnt, 0, 4096, 0x00020000), (lds_void_ptr)(size_t)(lds0 + PAD + 256), 4, fof, 0, 0, 16); };
    int ht; unsigned htv;
    auto hset = [&]() {
      int idh = idx; asm volatile("" : "+s"(idh));
      const int slc = (3 - ((unsigned)(size_t)a.W0 >> 7)) & 7, pr = idh & 3;
      const bool hlp = pr != (slc >> 1) && lw < 3;
      const int htau = 2 + lw * 12 + (idh >> 3) * 3 + pr - (pr > (slc >> 1) ? 1 : 0);
      ht = (hlp && htau < (((slc ^ xcd) & 1) ? A : A + 8)) ? htau - HTD : 1000;
      htv = (unsigned)((wrow(ktile(htau < A + 8 ? htau : 0), lane >> 3) + (lane & 7)) * (D_ * 2) + ((idh & 4) * 2 + slc) * 128);
    };
    auto help = [&]() { __builtin_amdgcn_raw_ptr_buffer_load_lds(rW, (lds_void_ptr)(size_t)(lds0 + FPAD - 256), 4, htv, 0, 0, 0); };
    auto ldW0 = [&](int i) { const int so = ((kf + i) * 112 + (lw >> 1) * 56 + (lw & 1) * 16) * (D_ * 2); ld_w4<-1>(mW + i * 16384, qW, wv, so, so + 8 * D_ * 2); };
    if constexpr (MODE == 1) {
      if constexpr (TL == 1) __builtin_amdgcn_raw_ptr_buffer_load_lds(__builtin_amdgcn_make_buffer_rsrc((void*)(a.sync + FAM(panel)), 0, 256, 0x00020000), (lds_void_ptr)(size_t)(lds0 + FPAD), 4, lane * 4, 0, 0, 16);
      if constexpr (TL == 0) { ldW0(0); ldW0(1); }
      return;
    }
#define TS0
#define TS1(T)
#define KN_XW 4
    wait_vm<3>();
    lds_inc(lds0 + ACNT);
    STAMPW(4, 0);
    if (KN_XW && KN_XW < 4) { hset(); tables(); asm volatile("" :: "s"(ht), "v"(htv)); }
    if (KN_XW == 3) ldW0(2);
    const bool early = !lds_missing(lds0 + FPAD, tag, ((1ull << (4 * V)) - 1) << ((xcd & 1) * 32));
    lds_wait8(lds0 + ACNT);
    STAMPW(9, 0);
    if (tid == 192) set_flag(pcnt, tag);
    asm volatile("" : "+v"(lane) :: "memory");
    if (!KN_XW) { hset(); tables(); asm volatile("" :: "s"(ht), "v"(htv)); }
    if (KN_XW <= 1 || KN_XW == 5) lds_wait8(lds0 + ACNT + 4, 4);
    STAMPW(5, 0);
    ldXo(omA(kf * 2048), 0); ldXo(omA(kf * 2048) + 2048, 1); if (KN_XW != 3) ldW0(2);
    if (KN_XW >= 4) { asm volatile("" : "+v"(lane) :: "memory"); hset(); tables(); asm volatile("" :: "s"(ht), "v"(htv)); }
    static_assert(V >= 5 && V <= 8, "first tiles");
    unsigned sx = mX + 2 * 16384, sw = 3 * 16384;
    auto next = [&]() { sw = (sw + 16384) & 65535; sx = sx + 16384; sx = sx >= mX + SX * 16384 ? mX : sx; asm volatile("" : "+s"(sw), "+s"(sx)); };
    { unsigned sc = mX + lane * 16;
      const int te = lw == 0 ? V - 4 : 1000;
      if (ht < -1) ht = -1;
      ox = omA(kf * 2048) + 4096;
      wait_vm<KN_XW == 3 ? 4 : 8>();
#pragma unroll 1
      for (int t = -1; t < V - 1; ++t) {
        const int oy = ox;
        offs(t);
        if (KN_XW == 3 && t == 0) wait_vm<8>(); else wait_vm<12>();
        if (__builtin_expect(ld_x4c(sx, qX, xv0, xv1, oy, sc) != 0 || !early, 0)) {
          for (int tries = early ? 0 : KN_TRY;; ++tries) {
            if (tries >= KN_TRY) wait_flags(fcnt, fof, tag, LO32);
            int so = __builtin_amdgcn_readlane(tbx, t + 1), sm = __builtin_amdgcn_readfirstlane(sc); asm volatile("s_nop 3" : "+s"(so), "+s"(sm));
            ld_x4(sm, qX, xv0, xv1, so);
            wait_vm<0>();
            if (tries >= KN_TRY || !marked<4, 1024>(sc)) break;
          }
        }
        if (t == -1) STAMPW(6, 0);
        if (__builtin_expect(t == te, 0)) { wait_vm<12>(); if (lds_missing(lds0 + PAD + 256, tag, LO32)) wait_flags(fcnt, fof, tag, LO32); }
        ld_w4<63>(mW + sw, qW, wv, o0, o1);
        TS0;
        next();
        if (__builtin_expect(t == te - 2, 0)) { ldF(); asm volatile("" ::: "memory"); }
        if (__builtin_expect(t == ht, 0)) help();
        sc = t == 3 ? sc - 4 * 16384 : sc + 16384;
        TS1(t + 1);
      }
    }
    ld_x4(sx, qX, xv0, xv1, ox);
    sx = sx + 16384; sx = sx >= mX + SX * 16384 ? mX : sx;
    const int tf = lw == 0 ? 9 : 1000;
    offs(V - 1);
    auto round = [&](int t, auto P1c, auto WAITc) {
      constexpr bool P1 = decltype(P1c)::value; constexpr int WAIT = decltype(WAITc)::value;
      if constexpr (P1) { if (__builtin_expect(t == tf + 3, 0)) { wait_vm<WAIT>(); if (lds_missing(lds0 + PAD + 256, tag, HI32)) wait_flags(fcnt, fof, tag, HI32); } }
      if constexpr (P1) ld_w4<WAIT>(mW + sw, qW, wv, o0, o1); else ld_w2<WAIT>(mW + sw, qW, wv, o0, o1);
      TS0;
      __builtin_amdgcn_sched_barrier(0);
      if (__builtin_expect(t == ht, 0)) help();
      if constexpr (P1) { if (__builtin_expect(t == tf, 0)) ldF(); }
      const int oy = ox; const unsigned sy = sx;
      offs(t + 1);
      next();
      TS1(t + 1);
      __builtin_amdgcn_sched_barrier(0);
      ld_x4(sy, qX, xv0, xv1, oy);
    };
    int t = V - 1;
#pragma unroll 1
    for (; t < A - 4; ++t) round(t, std::true_type{}, std::integral_constant<int, 16>{});
    round(t++, std::false_type{}, std::integral_constant<int, 16>{});
    round(t++, std::false_type{}, std::integral_constant<int, 14>{});
#pragma unroll 1
    for (; t < A + 4; ++t) round(t, std::false_type{}, std::integral_constant<int, 12>{});
    wait_vm<12>(); raw_barrier(); TS0; TS1(t + 1);
    res_loads();
    wait_vm<8>(); raw_barrier(); TS0; TS1(t + 2);
    wait_vm<2>(); raw_barrier(); TS0; TS1(t + 3);
    raw_barrier(); TS0; TS1(t + 4);
    asm volatile("" : "+v"(res[0]), "+v"(res[1]));
  } else {
    if constexpr (MODE == 1) return;
    const int fr = lane & 15, fq = lane >> 4, wq = (lane >> 2) & 3, wp = lane & 3, cw = wave & 3, wm = cw >> 1, wn = cw & 1;
#define KN_DP 0
    if (KN_DP) __builtin_amdgcn_s_setprio(KN_DP);
    f32x4 acc[4][4];
#pragma unroll
    for (int n = 0; n < 4; ++n)
#pragma unroll
      for (int m = 0; m < 4; ++m) acc[n][m] = (f32x4){0.f, 0.f, 0.f, 0.f};
    const unsigned xa = lds0 + wm * 8192 + lane * 16;
    unsigned wa[2];
#pragma unroll
    for (int c = 0; c < 2; ++c) { const int row = 8 * fq + wq; wa[c] = lds0 + WB + (row >> 4) * 4096 + (row & 15) * 128 + ((((wn * 2 + c) << 1) ^ wswz<128>(row)) << 4) + wp * 8; }
    struct Ctx { unsigned x, w[2]; };
    auto mk = [&](unsigned xo, unsigned wo) { Ctx c; c.x = xa + xo; c.w[0] = wa[0] + wo; c.w[1] = wa[1] + wo; return c; };
    auto rd = [&](FragE& f, const Ctx& c, auto KSc, auto kc) {
      constexpr int KS = decltype(KSc)::value, k = decltype(kc)::value;
      if constexpr (k < 4) f.x[k] = ds_b128<KS * 1024 + k * 2048>(c.x);
      else { constexpr int n = k - 4, o = KS * 8192 + (n >> 1) * 2048; f.lo[n] = ds_tr<o>(c.w[n & 1]); f.hi[n] = ds_tr<o + 512>(c.w[n & 1]); }
    };
    auto pin = [&](FragE& f, auto Nc) {
#pragma unroll
      for (int m = 0; m < 4; ++m) asm volatile("" : "+v"(f.x[m]));
#pragma unroll
      for (int n = 0; n < decltype(Nc)::value; ++n) asm volatile("" : "+v"(f.lo[n]), "+v"(f.hi[n]));
    };
    using I0 = std::integral_constant<int, 0>; using I1 = std::integral_constant<int, 1>; using I2 = std::integral_constant<int, 2>; using I4 = std::integral_constant<int, 4>;
    FragE fa, fb, fc, fd;
    const auto rS = __builtin_amdgcn_make_buffer_rsrc((void*)a.slab, 0, 0x7fffffff, 0x00020000);
    unsigned sla = rbH(tm * 8 + (tn >> 1), sd) + omH((tn & 1) * 16384 + cw * 4096) + lane * 16, slb = sla ^ omH(16384);
    asm volatile("" : "+v"(sla), "+v"(slb));
    raw_barrier();
    { FragE& f0 = (KN_D & 1) ? fc : fa; FragE& f1 = (KN_D & 1) ? fd : fb;
      const Ctx c = mk(0, 0); static_for<0, 8>([&](auto kc) { rd(f0, c, I0{}, kc); }); asm volatile("s_waitcnt lgkmcnt(4)" ::: "memory"); static_for<0, 8>([&](auto kc) { rd(f1, c, I1{}, kc); });
      asm volatile("s_waitcnt lgkmcnt(0)" ::: "memory");
      pin(f0, I4{}); pin(f1, I4{}); }
    STAMP(1);
    static_assert((SW & (SW - 1)) == 0, "W ring");
    int sx = 16384, sw = 16384;
    Ctx cn = mk(sx, sw);
#define KN_SP4 26
    auto rd1 = [&](FragE& f, const Ctx& c, auto KSc, auto rc) {
      constexpr int KS = decltype(KSc)::value, r = decltype(rc)::value;
      if constexpr (r < 4) f.x[r] = ds_b128<KS * 1024 + r * 2048>(c.x);
      else { constexpr int n = (r - 4) >> 1, o = KS * 8192 + (n >> 1) * 2048; if constexpr (r & 1) f.hi[n] = ds_tr<o + 512>(c.w[n & 1]); else f.lo[n] = ds_tr<o>(c.w[n & 1]); }
    };
    auto send = [&](int m) {
      __builtin_amdgcn_raw_buffer_store_b128((u32x4){pk_bf16(acc[2][m][0], acc[2][m][1]), pk_bf16(acc[2][m][2], acc[2][m][3]), pk_bf16(acc[3][m][0], acc[3][m][1]), pk_bf16(acc[3][m][2], acc[3][m][3])},
                                             rS, slb + m * 1024, 0, 16); };
    auto tile = [&](FragE& c0, FragE& c1, FragE& n0, FragE& n1, auto NCc, auto NNc, auto&& post) {
      constexpr int NC = decltype(NCc)::value, NN = decltype(NNc)::value;
      constexpr bool PF = KN_PF && NC == 4 && NN == 2;
      constexpr int R2 = 8 + 4 * NN, NP = 4 * NC;
      const Ctx c = cn;
      static_for<0, 2 * NP>([&](auto ic) {
        constexpr int I = decltype(ic)::value, P = I >> 1, Q = PF && P > 8 ? P - 8 : P;
        constexpr int n = PF && P >= 1 && P <= 8 ? 2 + ((P >> 1) & 1) : Q / 4, m = PF && P >= 1 && P <= 8 ? (P - 1) >> 1 : ((n & 1) ? 3 - Q % 4 : Q % 4);
        constexpr int SP = NC == 4 ? (NN == 4 ? KN_SP4 : 20) : 12;
        constexpr int before = I < 1 ? 0 : ((I - 1) * R2 / SP < R2 ? (I - 1) * R2 / SP : R2), after = I * R2 / SP < R2 ? I * R2 / SP : R2;
        FragE& u = ((P ^ I) & 1) ? c1 : c0;
        acc[n][m] = __builtin_amdgcn_mfma_f32_16x16x32_bf16(__builtin_shufflevector(u.lo[n], u.hi[n], 0, 1, 2, 3, 4, 5, 6, 7), u.x[m], acc[n][m], 0, 0, 0);
        __builtin_amdgcn_sched_barrier(0);
        if constexpr (I == 1) { asm volatile("s_waitcnt lgkmcnt(0)" ::: "memory"); pin(c0, NCc); pin(c1, NCc); raw_barrier(); post(); }
        static_for<before, after>([&](auto rc) { constexpr int r = decltype(rc)::value;
          constexpr int h = r < 6 ? r / 3 : (r < 12 ? r & 1 : (r >> 1) & 1), q = r < 6 ? (r % 3 == 2 ? 0 : 4 + r % 3) : (r < 12 ? (r - 4) >> 1 : 4 + ((r - 8) >> 2) * 2 + (r & 1));
          if constexpr (h) rd1(n1, c, I1{}, std::integral_constant<int, q>{}); else rd1(n0, c, I0{}, std::integral_constant<int, q>{}); });
        if constexpr (PF && I >= 9 && I <= 21 && (I - 9) % 4 == 0) send((I - 9) / 4);
        if constexpr (I == 2 * NP - 4) { sw = (sw + 16384) & (SW * 16384 - 1); asm volatile("" : "+s"(sw)); }
        if constexpr (I == 2 * NP - 3) { cn.w[0] = wa[0] + sw; cn.w[1] = wa[1] + sw; asm volatile("" : "+v"(cn.w[0]), "+v"(cn.w[1])); }
        if constexpr (I == 2 * NP - 5) { sx = sx + 16384; asm volatile("" : "+s"(sx)); }
        if constexpr (I == 2 * NP - 2) { sx = sx == SX * 16384 ? 0 : sx; asm volatile("" : "+s"(sx)); }
        if constexpr (I == 2 * NP - 1) { cn.x = xa + sx; asm volatile("" : "+v"(cn.x)); }
        __builtin_amdgcn_sched_barrier(0);
      });
      asm volatile("s_waitcnt lgkmcnt(%0)" :: "n"(R2 - 6 < 15 ? R2 - 6 : 15) : "memory");
      asm volatile("" : "+v"(n0.x[0]), "+v"(n0.lo[0]), "+v"(n0.hi[0]), "+v"(n1.x[0]), "+v"(n1.lo[0]), "+v"(n1.hi[0]));
    };
    auto nop = [&]() {};
    if constexpr (KN_D & 1) tile(fc, fd, fa, fb, I4{}, I4{}, nop);
#pragma unroll 1
    for (int t = 0; t < A - 3; t += 2) {
      tile(fa, fb, fc, fd, I4{}, I4{}, [&]() { if (t == 2) { if (tid == 256) set_flag(pcnt + 1, tag); } });
      tile(fc, fd, fa, fb, I4{}, I4{}, nop);
      if (t == 0) wait_vm<0>();
    }
    tile(fa, fb, fc, fd, I4{}, I4{}, nop); tile(fc, fd, fa, fb, I4{}, I2{}, nop);
    STAMP(10);
    DELAY_AT(4);
    if (!KN_PF) { send(0); send(1); send(2); send(3); }
    auto pairs = [&](int n) {
#pragma unroll 1
      for (int t = 0; t < n; t += 2) { tile(fa, fb, fc, fd, I2{}, I2{}, nop); tile(fc, fd, fa, fb, I2{}, I2{}, nop); } };
    const unsigned* flag = a.sync + FPR(bid);
    u32x4 pp[4];
    auto ldP = [&]() {
#pragma unroll
      for (int i = 0; i < 4; ++i) pp[i] = __builtin_amdgcn_raw_buffer_load_b128(rS, sla + i * 1024, 0, 16);
    };
    {
    pairs(TA);
    STAMPV(11);
    wait_vm<0>();
    STAMPV(12);
    tile(fa, fb, fc, fd, I2{}, I2{}, [&]() { if (tid == 256) set_flag(a.sync + FPR(bid ^ 1), tag); });
    tile(fc, fd, fa, fb, I2{}, I2{}, nop);
    if constexpr (TB > TA) pairs(TB - TA);
    unsigned fv = 1u;
    if (wave == 4) fv = __hip_atomic_load((gu32)flag, __ATOMIC_RELAXED, __HIP_MEMORY_SCOPE_AGENT);
    tile(fa, fb, fc, fd, I2{}, I2{}, nop);
    tile(fc, fd, fa, fb, I2{}, I2{}, nop);
    if (wave == 4) {
      if (__builtin_amdgcn_readfirstlane(fv) != tag) wait_flags(flag, 0, tag, 1ull);
    }
    STAMPV(4);
    tile(fa, fb, fc, fd, I2{}, I2{}, [&]() {
#pragma unroll
      for (int i = 0; i < 4; ++i) pp[i] = __builtin_amdgcn_raw_buffer_load_b128(rS, sla + i * 1024, 0, 16);
      res_loads();
    });
    tile(fc, fd, fa, fb, I2{}, I2{}, nop);
    }
    asm volatile("s_waitcnt lgkmcnt(0)" ::: "memory");
    pin(fa, I2{}); pin(fb, I2{});
    STAMP(2);
#pragma unroll
    for (int m = 0; m < 4; ++m)
#pragma unroll
      for (int n = 0; n < 2; ++n) {
        const unsigned q0 = pp[m][n * 2], q1 = pp[m][n * 2 + 1];
        const f32x4 v = acc[n][m] + (f32x4){bflo(q0), bfhi(q0), bflo(q1), bfhi(q1)};
        *(u32x2*)(smem + STG + (wm * 64 + m * 16 + fr) * 128 + ((((wn * 2 + n) * 2 + (fq >> 1)) ^ (fr & 7)) << 4) + (fq & 1) * 8) = (u32x2){pk_bf16(v[0], v[1]), pk_bf16(v[2], v[3])};
      }
    STAMPV(5);
    asm volatile("" : "+v"(res[0]), "+v"(res[1]));
  }
  lds_barrier();
  {
    const auto rO = __builtin_amdgcn_make_buffer_rsrc((void*)a.O, 0, 0x7fffffff, 0x00020000);
    u32x4 o[2];
    int lz = lane; asm volatile("" : "+v"(lz));
#pragma unroll
    for (int i = 0; i < 2; ++i) {
      const u32x4 v = *(const u32x4*)(smem + STG + (wave * 16 + i * 8 + (lz >> 3)) * 128 + (((lz & 7) ^ (lz >> 3)) << 4));
#pragma unroll
      for (int j = 0; j < 4; ++j) { const unsigned r = res[i][j], q = v[j]; o[i][j] = pk_bf16(bflo(r) + bflo(q), bfhi(r) + bfhi(q)); }
    }
    asm volatile("" : "+v"(o[0]), "+v"(o[1]));
#pragma unroll
#define KN_OAUX 16
    for (int i = 0; i < 2; ++i) __builtin_amdgcn_raw_buffer_store_b128(o[i], rO, orow, i * 8 * D_ * 2, KN_OAUX);
    __builtin_amdgcn_sched_barrier(0);
    asm volatile("s_nop 1" ::: "memory");
    __builtin_amdgcn_sched_barrier(0);
    if (bid == 0 && tid == 0) set_flag(a.sync + FEP, tag);
  }
  STAMP(7);
  STAMP(8);
  STAMP(3);
#endif
}

struct Ptrs { const bf16_t *x, *wqkv, *wo, *wg, *wu, *wd; bf16_t* out; char* ws; };
#define DBG(i)
#define WS_O ((bf16_t*)(p.ws + (8u << 20)))
#define WS_SL ((float*)WS_O)
#define WS_H ((bf16_t*)(p.ws + (16u << 20)))
#define WS_ST ((float*)(p.ws + (24u << 20)))
#define WS_CS ((float*)(p.ws + (25u << 20)))
#define WS_SY ((unsigned*)(p.ws + (26u << 20)))
#define WS_A ((bf16_t*)(p.ws + (28u << 20)))
#if defined(__HIP_DEVICE_COMPILE__) && !defined(KN_NOWARM)
#define ARG_WARM(v) do { asm volatile("s_load_dword %0, %1, 0x0" : "=s"(v) : "s"(__builtin_amdgcn_kernarg_segment_ptr())); __builtin_amdgcn_sched_barrier(0); } while (0)
#else
#define ARG_WARM(v)
#endif
struct Args4 { GemmArgs q, b, c, d; };
extern "C" __global__ __launch_bounds__(512, 2) void k_all(Ptrs p) {
  unsigned warm = 0;
  DELAY_AT(0);
  ARG_WARM(warm);
  p.ws += -(unsigned)(size_t)p.ws & 0x1fffffu;
  const Args4 a{GemmArgs{p.x, p.wqkv, nullptr, nullptr, WS_O, (float*)WS_H, p.wg, p.wu, WS_CS, p.wd, p.wo, WS_SY, (float*)WS_A, warm DBG(0)},
    GemmArgs{WS_O, p.wo, nullptr, p.x, WS_H, WS_ST, nullptr, nullptr, nullptr, p.wd, nullptr, WS_SY, nullptr, 0u DBG(1)},
    GemmArgs{WS_H, p.wg, p.wu, p.out, WS_A, WS_ST, nullptr, nullptr, WS_CS, p.wd, nullptr, WS_SY, WS_SL, 0u DBG(2)},
    GemmArgs{WS_A, p.wd, nullptr, WS_H, p.out, nullptr, nullptr, nullptr, nullptr, nullptr, nullptr, WS_SY, WS_SL, 0u DBG(3)}};
  const unsigned tag = k1_phase(a.q);
  wo_phase(a.b, a.c, tag);
#define KN_EH 10
#define KN_TA 2
#define KN_TB 2
  DELAY_AT(8);
  auto pre = [&](auto tc) { dn_phase<1, KN_EH, KN_TA, KN_TB, decltype(tc)::value>(a.d, tag); };
  glu_phase(a.c, tag, pre);
  dn_phase<2, KN_EH, KN_TA, KN_TB>(a.d, tag);
#if defined(DIAG) && defined(__HIP_DEVICE_COMPILE__)
  lds_barrier();
  if (threadIdx.x < 64 && threadIdx.x != 12) { int v = ds_r32(SNAP + 320 + threadIdx.x * 4); asm volatile("s_waitcnt lgkmcnt(0)" : "+v"(v)); a.q.dbg[(threadIdx.x >> 4) * 8192 + blockIdx.x * 16 + (threadIdx.x & 15)] = (unsigned)v; }
#endif
}

extern "C" __global__ void k_zend() {}

extern "C" void kernel_launch(void* const* d_in, const int* in_sizes, int n_in, void* d_out, int out_size, void* d_ws,
                              size_t ws_size, hipStream_t stream) {
  const Ptrs p{(const bf16_t*)d_in[0], (const bf16_t*)d_in[1], (const bf16_t*)d_in[2], (const bf16_t*)d_in[4], (const bf16_t*)d_in[5], (const bf16_t*)d_in[6], (bf16_t*)d_out, (char*)d_ws};
  constexpr int L_all = SNAP + 64 + 512;
  static_assert(SNAP >= 3 * 256 * 128 + 4 * 14592 + 4096 + 1024 + 256 + 512 + 128 + 64 && L_all <= 163840, "LDS");
  (void)hipFuncSetAttribute((const void*)k_all, hipFuncAttributeMaxDynamicSharedMemorySize, L_all);
  k_all<<<256, 512, L_all, stream>>>(p);
}
```
